```python
import torch

def _softmax(x, axis=-1):
    return torch.softmax(x, dim=axis)
def _gelu(x):
    return torch.nn.functional.gelu(x, approximate="tanh")
def _silu(x):
    return torch.nn.functional.silu(x)
def _sigmoid(x):
    return torch.sigmoid(x)
def _layernorm(x, axis=-1, eps=1e-5):
    m = x.mean(dim=axis, keepdim=True)
    v = x.var(dim=axis, keepdim=True, unbiased=False)
    return (x - m) / torch.sqrt(v + eps)
def _scatter_add(tgt, idx, src, axis=0):
    return tgt.clone().index_add_(axis, idx, src)
def _segment_sum(src, seg, n):
    out = torch.zeros((n,) + src.shape[1:], dtype=src.dtype)
    return out.index_add_(0, seg, src)
def _pad(x, pw):
    flat = tuple(v for pair in reversed(pw) for v in pair)
    return torch.nn.functional.pad(x, flat)

kernel_name = 'recurrent_scan__sliding_block_cached__moe_arch_mod__prompt'

def setup_inputs():

    _w = (lambda a: (lambda t: t.to(torch.bfloat16) if t.is_floating_point() else t)(torch.as_tensor(a)))
    def _A_setup():
        B, Sq, D = 1, 2048, 640
        Hq, Hkv, Sk, Dh = 32, 32, 2048, 192
        assert Hq % Hkv == 0, (Hq, Hkv)
        return {
            "x": _w((lambda *s: torch.randn(*s, dtype=torch.bfloat16) * 0.05)(B, Sq, D)),
            "k_cache": _w((lambda *s: torch.randn(*s, dtype=torch.bfloat16) * 0.8)(B, Hkv, Sk, Dh)),
            "v_cache": _w((lambda *s: torch.randn(*s, dtype=torch.bfloat16) * 0.2)(B, Hkv, Sk, Dh)),
            "wq": _w((lambda *s: torch.randn(*s, dtype=torch.bfloat16) * 0.8)(D, Hq * Dh) * (D ** -0.5)),
            "wo": _w((lambda *s: torch.randn(*s, dtype=torch.bfloat16) * 0.2)(Hq * Dh, D) * ((Hq * Dh) ** -0.5)),
        }
    _A_in = _A_setup()

    def _B_setup():
        B, S, D, F, C = 1, 2048, 640, 1536, 2
        return {
            "x": _w((lambda *s: torch.randn(*s, dtype=torch.bfloat16) * 0.05)(B, S, D)),
            "w_r": _w((lambda *s: torch.randn(*s, dtype=torch.bfloat16) * 0.2)(D)),
            "w_gate": _w((lambda *s: torch.randn(*s, dtype=torch.bfloat16) * 0.1)(D, F)),
            "w_up": _w((lambda *s: torch.randn(*s, dtype=torch.bfloat16) * 0.1)(D, F)),
            "w_down": _w((lambda *s: torch.randn(*s, dtype=torch.bfloat16) * 0.1)(F, D)),
        }
    _B_in = _B_setup()

    return {
        "A_x": _A_in["x"],
        "A_k_cache": _A_in["k_cache"],
        "A_v_cache": _A_in["v_cache"],
        "A_wq": _A_in["wq"],
        "A_wo": _A_in["wo"],
        "B_x": _B_in["x"],
        "B_w_r": _B_in["w_r"],
        "B_w_gate": _B_in["w_gate"],
        "B_w_up": _B_in["w_up"],
        "B_w_down": _B_in["w_down"],
    }

def reference(A_x, A_k_cache, A_v_cache, A_wq, A_wo, B_x, B_w_r, B_w_gate, B_w_up, B_w_down):
    def _A_ref(x, k_cache, v_cache, wq, wo):
        B, Sq, D = x.shape
        Hq, Dh = 32, 192
        Hkv, Sk = k_cache.shape[1], k_cache.shape[2]
        W = min(4096, max(256, Sk // 4))
        rep = Hq // Hkv
        q = (lambda x, *d: x.permute(*d))((x @ wq).reshape(B, Sq, Hq, Dh), 0, 2, 1, 3)
        k = (lambda x, n, axis: torch.repeat_interleave(x, n, dim=axis))(k_cache[:, :, Sk - W:, :], rep, axis=1)
        v = (lambda x, n, axis: torch.repeat_interleave(x, n, dim=axis))(v_cache[:, :, Sk - W:, :], rep, axis=1)
        scores = torch.einsum("bhqd,bhkd->bhqk", q, k) * (Dh ** -0.5)
        p = _softmax(scores, axis=-1)
        o = torch.einsum("bhqk,bhkd->bhqd", p, v)
        return (lambda x, *d: x.permute(*d))(o, 0, 2, 1, 3).reshape(B, Sq, Hq * Dh) @ wo

    def _B_ref(x, w_r, w_gate, w_up, w_down):
        B, S, D = x.shape
        C = 2
        r = x @ w_r
        idx = torch.argsort(-r, axis=-1)[:, :C]
        idx_e = (lambda x, n, axis: torch.repeat_interleave(x, n, dim=axis))(idx.reshape(B, C, 1), D, axis=2)
        x_sel = (lambda x, i, axis: torch.take_along_dim(x, i, dim=axis))(x, idx_e, axis=1)
        g = _silu(x_sel @ w_gate)
        u = x_sel @ w_up
        y = (g * u) @ w_down
        r_sel = _sigmoid((lambda x, i, axis: torch.take_along_dim(x, i, dim=axis))(r, idx, axis=1))
        y = y * r_sel.reshape(B, C, 1)
        b_off = (torch.arange(B) * S).reshape(B, 1)
        flat_idx = (idx + b_off).reshape(B * C)
        zero = (x * 0.0).reshape(B * S, D)
        upd = _scatter_add(zero, flat_idx, y.reshape(B * C, D), axis=0)
        return x + upd.reshape(B, S, D)

    x = A_x
    xn = _layernorm(x, axis=-1)
    s = _A_ref(xn, A_k_cache, A_v_cache, A_wq, A_wo)
    g = _silu(_B_ref(xn, B_w_r, B_w_gate, B_w_up, B_w_down))
    return x + s * g
```

```cpp
#include <hip/hip_runtime.h>
#include <hip/hip_bf16.h>
#include <stdint.h>

typedef short bf16x8 __attribute__((ext_vector_type(8)));
typedef short s16x4 __attribute__((ext_vector_type(4)));
typedef float f32x16 __attribute__((ext_vector_type(16)));
typedef float f32x2 __attribute__((ext_vector_type(2)));
typedef __bf16 bf16x2_t __attribute__((ext_vector_type(2)));
typedef unsigned u32x4 __attribute__((ext_vector_type(4)));
typedef unsigned short u16;
#define DI __device__ __forceinline__
#define LAS __attribute__((address_space(3)))
#define MFMA(a, b, c) __builtin_amdgcn_mfma_f32_32x32x16_bf16((a), (b), (c), 0, 0, 0)

constexpr int SQ = 2048, DM = 640, NH = 32, DH = 192, SK = 2048, WIN = 512, FF = 1536;
constexpr int HD = NH * DH;

DI float bf2f(u16 b) { return __uint_as_float((unsigned)b << 16); }
DI unsigned cvtpk(float lo, float hi) { f32x2 v = {lo, hi}; bf16x2_t b = __builtin_convertvector(v, bf16x2_t); return __builtin_bit_cast(unsigned, b); }
DI float rbf(float x) { return __uint_as_float(cvtpk(x, 0.f) << 16); }
typedef short v4i16_t __attribute__((ext_vector_type(4)));
DI s16x4 trread(LAS const char* p) { return __builtin_bit_cast(s16x4, __builtin_amdgcn_ds_read_tr16_b64_v4i16((LAS v4i16_t*)p)); }
DI bf16x8 cat(s16x4 a, s16x4 b) { return __builtin_shufflevector(a, b, 0, 1, 2, 3, 4, 5, 6, 7); }

constexpr size_t WS_XNF = 0;
constexpr size_t WS_R   = WS_XNF + (size_t)SQ * DM * 2;
constexpr size_t WS_IDX = WS_R + SQ * 4;
constexpr size_t WS_H   = WS_IDX + 256;
constexpr size_t WS_Y   = WS_H + 2 * FF * 4;
constexpr size_t WS_OF  = (WS_Y + 2 * DM * 4 + 4095) / 4096 * 4096;

__global__ __launch_bounds__(128) void k0_ln(const u16* __restrict__ x, const u16* __restrict__ wr, u16* __restrict__ xnf, float* __restrict__ r) {
  const int tid = threadIdx.x, l16 = tid & 15, row = blockIdx.x * 8 + (tid >> 4);
  const u16* xr = x + (size_t)row * DM;
  float v[40];
  float s = 0.f;
#pragma unroll
  for (int i = 0; i < 5; ++i) {
    u32x4 w = *(const u32x4*)(xr + (i * 16 + l16) * 8);
#pragma unroll
    for (int e = 0; e < 4; ++e) { v[i * 8 + 2 * e] = __uint_as_float(w[e] << 16); v[i * 8 + 2 * e + 1] = __uint_as_float(w[e] & 0xffff0000u); }
  }
#pragma unroll
  for (int i = 0; i < 40; ++i) s += v[i];
#pragma unroll
  for (int o = 1; o < 16; o <<= 1) s += __shfl_xor(s, o, 64);
  const float mean = s * (1.f / DM);
  float ss = 0.f;
#pragma unroll
  for (int i = 0; i < 40; ++i) { float d = v[i] - mean; ss += d * d; }
#pragma unroll
  for (int o = 1; o < 16; o <<= 1) ss += __shfl_xor(ss, o, 64);
  const float mb = rbf(mean), vb = rbf(ss * (1.f / DM));
  const float sd = rbf(sqrtf(rbf(vb + 1e-5f)));
  float rs = 0.f;
#pragma unroll
  for (int i = 0; i < 5; ++i) {
    const int c = i * 16 + l16;
    u32x4 wv = *(const u32x4*)(wr + c * 8);
    u32x4 o;
#pragma unroll
    for (int e = 0; e < 4; ++e) {
      float a = rbf(v[i * 8 + 2 * e] - mb) / sd, b = rbf(v[i * 8 + 2 * e + 1] - mb) / sd;
      o[e] = cvtpk(a, b);
      rs += __uint_as_float(o[e] << 16) * __uint_as_float(wv[e] << 16) + __uint_as_float(o[e] & 0xffff0000u) * __uint_as_float(wv[e] & 0xffff0000u);
    }
    const size_t off = (((size_t)(row >> 5) * 40 + (c >> 1)) * 64 + (c & 1) * 32 + (row & 31)) * 8;
    *(u32x4*)(xnf + off) = o;
  }
#pragma unroll
  for (int o = 1; o < 16; o <<= 1) rs += __shfl_xor(rs, o, 64);
  if (l16 == 0) r[row] = rbf(rs);
}

DI float xn_at(const u16* xnf, int row, int k) { return bf2f(xnf[(((size_t)(row >> 5) * 40 + (k >> 4)) * 64 + ((k >> 3) & 1) * 32 + (row & 31)) * 8 + (k & 7)]); }

__global__ __launch_bounds__(256) void k_top2(const float* __restrict__ r, int* __restrict__ idx) {
  __shared__ float sv[256]; __shared__ int si[256];
  const int tid = threadIdx.x;
  int excl = -1;
  for (int pass = 0; pass < 2; ++pass) {
    float bv = -INFINITY; int bi = 0x7fffffff;
    for (int i = tid; i < SQ; i += 256) { float v = r[i]; if (i != excl && (v > bv || (v == bv && i < bi) || bi == 0x7fffffff)) { bv = v; bi = i; } }
    sv[tid] = bv; si[tid] = bi; __syncthreads();
    for (int o = 128; o > 0; o >>= 1) { if (tid < o) { float v = sv[tid + o]; int i = si[tid + o]; if (v > sv[tid] || (v == sv[tid] && i < si[tid])) { sv[tid] = v; si[tid] = i; } } __syncthreads(); }
    excl = si[0]; if (tid == 0) idx[pass] = excl; __syncthreads();
  }
}
__global__ __launch_bounds__(256) void k_ffn1(const u16* __restrict__ xnf, const int* __restrict__ idx, const u16* __restrict__ wg, const u16* __restrict__ wu, float* __restrict__ hout) {
  __shared__ float xs[2][DM]; __shared__ float red[4][4][64];
  const int tid = threadIdx.x, c = tid & 63, kg = tid >> 6, col = blockIdx.x * 64 + c;
  for (int i = tid; i < 2 * DM; i += 256) xs[i / DM][i % DM] = xn_at(xnf, idx[i / DM], i % DM);
  __syncthreads();
  float g0 = 0, g1 = 0, u0 = 0, u1 = 0;
  for (int k = kg * 160; k < kg * 160 + 160; ++k) { float a = bf2f(wg[(size_t)k * FF + col]), b = bf2f(wu[(size_t)k * FF + col]); g0 += xs[0][k] * a; g1 += xs[1][k] * a; u0 += xs[0][k] * b; u1 += xs[1][k] * b; }
  red[kg][0][c] = g0; red[kg][1][c] = g1; red[kg][2][c] = u0; red[kg][3][c] = u1; __syncthreads();
  if (tid < 128) { int t = tid >> 6; float g = 0, u = 0; for (int j = 0; j < 4; ++j) { g += red[j][t][c]; u += red[j][2 + t][c]; }
    g = rbf(g); u = rbf(u); g = rbf(g / (1.f + __expf(-g))); hout[t * FF + col] = rbf(g * u); }
}
__global__ __launch_bounds__(256) void k_ffn2(const float* __restrict__ hin, const float* __restrict__ r, const int* __restrict__ idx, const u16* __restrict__ wd, float* __restrict__ y) {
  __shared__ float red[4][2][64];
  const int tid = threadIdx.x, c = tid & 63, kg = tid >> 6, col = blockIdx.x * 64 + c;
  float y0 = 0, y1 = 0;
  for (int k = kg * 384; k < kg * 384 + 384; ++k) { float a = bf2f(wd[(size_t)k * DM + col]); y0 += hin[k] * a; y1 += hin[FF + k] * a; }
  red[kg][0][c] = y0; red[kg][1][c] = y1; __syncthreads();
  if (tid < 128) { int t = tid >> 6; float v = 0; for (int j = 0; j < 4; ++j) v += red[j][t][c];
    float rv = r[idx[t]]; float sg = rbf(1.f / (1.f + __expf(-rv))); y[t * DM + col] = rbf(rbf(v) * sg); }
}

DI void glds16(const void* g, LAS char* l) { __builtin_amdgcn_global_load_lds((const __attribute__((address_space(1))) void*)g, (LAS void*)l, 16, 0, 0); }

__global__ __launch_bounds__(512, 2) void k1_attn(const u16* __restrict__ xnf, const u16* __restrict__ wq, const u16* __restrict__ kc, const u16* __restrict__ vc, u16* __restrict__ of) {
  extern __shared__ __attribute__((aligned(16))) char smem[];
  LAS char* lds = (LAS char*)smem;
  const int tid = threadIdx.x, lane = tid & 63, wid = __builtin_amdgcn_readfirstlane(tid >> 6);
  const int b = blockIdx.x, h = (b & 7) * 4 + ((b >> 3) & 3), qb = b >> 5;
  const int hi = lane >> 5, blk = (lane >> 4) & 1, q4 = (lane & 15) >> 2, p = lane & 3, sp = ((p & 1) << 1) | (p >> 1);
  const int qb32 = qb * 8 + wid;
  f32x16 acc[6];
#pragma unroll
  for (int t = 0; t < 6; ++t) acc[t] = f32x16{};
  const u16* wsrc = wq + (size_t)(wid * 8 + ((lane >> 2) & 7)) * HD + h * DH + (lane >> 5) * 32 + (lane & 3) * 8;
  auto dma_w = [&](int st, int buf) {
#pragma unroll
    for (int c = 0; c < 3; ++c) glds16(wsrc + (size_t)st * 64 * HD + c * 64, lds + buf * 24576 + (wid * 6 + 2 * c) * 512);
  };
  const int trw = hi * 3072 + q4 * 64 + blk * 32 + sp * 8;
  const bf16x8* xsrc = (const bf16x8*)(xnf + ((size_t)qb32 * 40 * 64 + lane) * 8);
  dma_w(0, 0);
  for (int st = 0; st < 10; ++st) {
    __syncthreads();
    if (st + 1 < 10) dma_w(st + 1, (st + 1) & 1);
    LAS const char* wb = lds + (st & 1) * 24576 + trw;
#pragma unroll
    for (int s = 0; s < 4; ++s) {
      bf16x8 xb = xsrc[(st * 4 + s) * 64];
#pragma unroll
      for (int t = 0; t < 6; ++t) {
        bf16x8 a = cat(trread(wb + s * 6144 + t * 512), trread(wb + s * 6144 + t * 512 + 256));
        acc[t] = MFMA(a, xb, acc[t]);
      }
    }
  }
  constexpr float QS = 0.07216878364870322f * 1.4426950408889634f;
  bf16x8 qf[12];
#pragma unroll
  for (int t = 0; t < 6; ++t)
#pragma unroll
    for (int s = 0; s < 2; ++s) {
      u32x4 w;
#pragma unroll
      for (int e = 0; e < 4; ++e) w[e] = cvtpk(acc[t][8 * s + 2 * e] * QS, acc[t][8 * s + 2 * e + 1] * QS);
      qf[2 * t + s] = __builtin_bit_cast(bf16x8, w);
    }
  const u16* kbase = kc + ((size_t)h * SK + (SK - WIN)) * DH;
  const u16* vbase = vc + ((size_t)h * SK + (SK - WIN)) * DH;
  const int krow = wid * 8 + (lane >> 3);
  const u16* ksrc = kbase + (size_t)krow * DH + (((lane & 7) ^ ((krow >> 1) & 7)) * 8);
  const u16* vsrc = vbase + (size_t)(wid * 8 + ((lane >> 2) & 7)) * DH + (lane >> 5) * 32 + (lane & 3) * 8;
  auto dma_kv = [&](int tl, int buf) {
#pragma unroll
    for (int c = 0; c < 3; ++c) glds16(ksrc + (size_t)tl * 64 * DH + c * 64, lds + buf * 49152 + (wid * 3 + c) * 1024);
#pragma unroll
    for (int c = 0; c < 3; ++c) glds16(vsrc + (size_t)tl * 64 * DH + c * 64, lds + buf * 49152 + 24576 + (wid * 6 + 2 * c) * 512);
  };
  const int key = lane & 31;
  int kro[4];
#pragma unroll
  for (int j = 0; j < 4; ++j) kro[j] = (key >> 3) * 3072 + (key & 7) * 128 + (((2 * j + hi) ^ ((key >> 1) & 7)) * 16);
  const int trv = (4 * hi + q4) * 64 + blk * 32 + sp * 8;
  f32x16 o[6];
#pragma unroll
  for (int t = 0; t < 6; ++t) o[t] = f32x16{};
  float m = -1e30f, l = 0.f;
  __syncthreads();
  dma_kv(0, 0);
  for (int tl = 0; tl < 8; ++tl) {
    __syncthreads();
    if (tl + 1 < 8) dma_kv(tl + 1, (tl + 1) & 1);
    LAS const char* kb = lds + (tl & 1) * 49152;
    LAS const char* vb = kb + 24576 + trv;
    f32x16 s0 = f32x16{}, s1 = f32x16{};
#pragma unroll
    for (int ks = 0; ks < 12; ++ks) {
      bf16x8 a0 = *(LAS const bf16x8*)(kb + kro[ks & 3] + (ks >> 2) * 1024);
      bf16x8 a1 = *(LAS const bf16x8*)(kb + kro[ks & 3] + (ks >> 2) * 1024 + 12288);
      s0 = MFMA(a0, qf[ks], s0); s1 = MFMA(a1, qf[ks], s1);
    }
    float mx = s0[0];
#pragma unroll
    for (int r = 1; r < 16; ++r) mx = fmaxf(mx, s0[r]);
#pragma unroll
    for (int r = 0; r < 16; ++r) mx = fmaxf(mx, s1[r]);
    mx = fmaxf(mx, __shfl_xor(mx, 32, 64));
    const float mn = fmaxf(m, mx), al = __builtin_amdgcn_exp2f(m - mn);
    m = mn;
    float ps = 0.f;
#pragma unroll
    for (int r = 0; r < 16; ++r) { s0[r] = __builtin_amdgcn_exp2f(s0[r] - mn); s1[r] = __builtin_amdgcn_exp2f(s1[r] - mn); ps += s0[r] + s1[r]; }
    l = l * al + ps;
#pragma unroll
    for (int t = 0; t < 6; ++t)
#pragma unroll
      for (int r = 0; r < 16; ++r) o[t][r] *= al;
    bf16x8 pf[4];
#pragma unroll
    for (int s = 0; s < 2; ++s) {
      u32x4 w0, w1;
#pragma unroll
      for (int e = 0; e < 4; ++e) { w0[e] = cvtpk(s0[8 * s + 2 * e], s0[8 * s + 2 * e + 1]); w1[e] = cvtpk(s1[8 * s + 2 * e], s1[8 * s + 2 * e + 1]); }
      pf[s] = __builtin_bit_cast(bf16x8, w0); pf[2 + s] = __builtin_bit_cast(bf16x8, w1);
    }
#pragma unroll
    for (int s = 0; s < 4; ++s)
#pragma unroll
      for (int t = 0; t < 6; ++t) {
        bf16x8 a = cat(trread(vb + (2 * s) * 3072 + t * 512), trread(vb + (2 * s + 1) * 3072 + t * 512));
        o[t] = MFMA(a, pf[s], o[t]);
      }
  }
  l += __shfl_xor(l, 32, 64);
  const float il = 1.f / l;
  u32x4* odst = (u32x4*)(of + (((size_t)qb32 * 384 + h * 12) * 64 + lane) * 8);
#pragma unroll
  for (int t = 0; t < 6; ++t)
#pragma unroll
    for (int s = 0; s < 2; ++s) {
      u32x4 w;
#pragma unroll
      for (int e = 0; e < 4; ++e) w[e] = cvtpk(o[t][8 * s + 2 * e] * il, o[t][8 * s + 2 * e + 1] * il);
      odst[(2 * t + s) * 64] = w;
    }
}

__global__ __launch_bounds__(256) void k2_oproj(const u16* __restrict__ of, const u16* __restrict__ wo, const u16* __restrict__ x, const u16* __restrict__ xnf,
                                                const int* __restrict__ idx, const float* __restrict__ y, u16* __restrict__ out) {
  extern __shared__ __attribute__((aligned(16))) char smem[];
  LAS char* lds = (LAS char*)smem;
  const int tid = threadIdx.x, lane = tid & 63, wid = __builtin_amdgcn_readfirstlane(tid >> 6);
  const int qb32 = blockIdx.x >> 2, nb = blockIdx.x & 3, n0 = nb * 160;
  const int hi = lane >> 5, blk = (lane >> 4) & 1, q4 = (lane & 15) >> 2, p = lane & 3, sp = ((p & 1) << 1) | (p >> 1);
  LAS char* wl = lds + wid * 20480;
  f32x16 acc[5];
#pragma unroll
  for (int t = 0; t < 5; ++t) acc[t] = f32x16{};
  const int k0 = wid * 1536;
  auto dma = [&](int stg, int buf) {
#pragma unroll
    for (int i = 0; i < 10; ++i) {
      const int st = 2 * i + hi, kb8 = st / 5, t = st % 5;
      glds16(wo + (size_t)(k0 + stg * 32 + kb8 * 8 + ((lane >> 2) & 7)) * DM + n0 + t * 32 + (lane & 3) * 8, wl + buf * 10240 + i * 1024);
    }
  };
  const int trw = hi * 2560 + q4 * 64 + blk * 32 + sp * 8;
  const bf16x8* osrc = (const bf16x8*)(of + (((size_t)qb32 * 384 + wid * 96) * 64 + lane) * 8);
  dma(0, 0);
  for (int stg = 0; stg < 48; ++stg) {
    asm volatile("s_waitcnt vmcnt(0) lgkmcnt(0)" ::: "memory");
    if (stg + 1 < 48) dma(stg + 1, (stg + 1) & 1);
    LAS const char* wb = wl + (stg & 1) * 10240 + trw;
#pragma unroll
    for (int s = 0; s < 2; ++s) {
      bf16x8 ob = osrc[(stg * 2 + s) * 64];
#pragma unroll
      for (int t = 0; t < 5; ++t) {
        bf16x8 a = cat(trread(wb + s * 5120 + t * 512), trread(wb + s * 5120 + t * 512 + 256));
        acc[t] = MFMA(a, ob, acc[t]);
      }
    }
  }
  __syncthreads();
  float* red = (float*)smem;
#pragma unroll
  for (int t = 0; t < 5; ++t)
#pragma unroll
    for (int r = 0; r < 16; ++r) red[(wid * 80 + t * 16 + r) * 64 + lane] = acc[t][r];
  __syncthreads();
  const int row = qb32 * 32 + (lane & 31);
  const int i0 = idx[0], i1 = idx[1];
  for (int g = wid; g < 10; g += 4) {
    const int n = n0 + g * 16 + hi * 8;
    float sv[8];
#pragma unroll
    for (int j = 0; j < 8; ++j) { float v = 0; for (int w = 0; w < 4; ++w) v += red[(w * 80 + g * 8 + j) * 64 + lane]; sv[j] = v; }
    u32x4 xv = *(const u32x4*)(x + (size_t)row * DM + n);
    u32x4 xnv = *(const u32x4*)(xnf + (((size_t)qb32 * 40 + (n >> 4)) * 64 + lane) * 8);
    float res[8];
#pragma unroll
    for (int j = 0; j < 8; ++j) {
      float xx = (j & 1) ? __uint_as_float(xv[j >> 1] & 0xffff0000u) : __uint_as_float(xv[j >> 1] << 16);
      float xn = (j & 1) ? __uint_as_float(xnv[j >> 1] & 0xffff0000u) : __uint_as_float(xnv[j >> 1] << 16);
      if (row == i0) xn = rbf(xn + y[n + j]);
      if (row == i1) xn = rbf(xn + y[DM + n + j]);
      float gg = rbf(xn / (1.f + __expf(-xn)));
      res[j] = xx + rbf(rbf(sv[j]) * gg);
    }
    u32x4 ov = {cvtpk(res[0], res[1]), cvtpk(res[2], res[3]), cvtpk(res[4], res[5]), cvtpk(res[6], res[7])};
    *(u32x4*)(out + (size_t)row * DM + n) = ov;
  }
}

extern "C" void kernel_launch(void* const* d_in, const int* in_sizes, int n_in, void* d_out, int out_size, void* d_ws, size_t ws_size, hipStream_t stream) {
  const u16* x = (const u16*)d_in[0]; const u16* kc = (const u16*)d_in[1]; const u16* vc = (const u16*)d_in[2];
  const u16* wq = (const u16*)d_in[3]; const u16* wo = (const u16*)d_in[4];
  const u16* wr = (const u16*)d_in[6]; const u16* wg = (const u16*)d_in[7]; const u16* wu = (const u16*)d_in[8]; const u16* wd = (const u16*)d_in[9];
  char* ws = (char*)d_ws;
  u16* xnf = (u16*)(ws + WS_XNF); float* r = (float*)(ws + WS_R); int* idx = (int*)(ws + WS_IDX); float* hb = (float*)(ws + WS_H); float* y = (float*)(ws + WS_Y); u16* of = (u16*)(ws + WS_OF);
  hipFuncSetAttribute((const void*)k1_attn, hipFuncAttributeMaxDynamicSharedMemorySize, 98304);
  hipFuncSetAttribute((const void*)k2_oproj, hipFuncAttributeMaxDynamicSharedMemorySize, 81920);
  k0_ln<<<256, 128, 0, stream>>>(x, wr, xnf, r);
  k_top2<<<1, 256, 0, stream>>>(r, idx);
  k_ffn1<<<24, 256, 0, stream>>>(xnf, idx, wg, wu, hb);
  k_ffn2<<<10, 256, 0, stream>>>(hb, r, idx, wd, y);
  k1_attn<<<256, 512, 98304, stream>>>(xnf, wq, kc, vc, of);
  k2_oproj<<<256, 256, 81920, stream>>>(of, wo, x, xnf, idx, y, (u16*)d_out);
}
```

```cpp
#include <hip/hip_runtime.h>
#include <stdint.h>

#define CLOB_V "v1","v2","v3","v4","v5","v6","v7","v8","v9","v10","v11","v12","v13","v14","v15","v16","v17","v18","v19","v20","v21","v22","v23","v24","v25","v26","v27","v28","v29","v30","v31","v32","v33","v34","v35","v36","v37","v38","v39","v40","v41","v42","v43","v44","v45","v46","v47","v48","v49","v50","v51","v52","v53","v54","v55","v56","v57","v58","v59","v60","v61","v62","v63","v64","v65","v66","v67","v68","v69","v70","v71","v72","v73","v74","v75","v76","v77","v78","v79","v80","v81","v82","v83","v84","v85","v86","v87","v88","v89","v90","v91","v92","v93","v94","v95","v96","v97","v98","v99","v100","v101","v102","v103","v104","v105","v106","v107","v108","v109","v110","v111","v112","v113","v114","v115","v116","v117","v118","v119","v120","v121","v122","v123","v124","v125","v126","v127","v128","v129","v130","v131","v132","v133","v134","v135","v136","v137","v138","v139","v140","v141","v142","v143","v144","v145","v146","v147","v148","v149","v150","v151","v152","v153","v154","v155","v156","v157","v158","v159","v160","v161","v162","v163","v164","v165","v166","v167","v168","v169","v170","v171","v172","v173","v174","v175","v176","v177","v178","v179","v180","v181","v182","v183","v184","v185","v186","v187","v188","v189","v190","v191","v192","v193","v194","v195","v196","v197","v198","v199","v200","v201","v202","v203","v204","v205","v206","v207","v208","v209","v210","v211","v212","v213","v214","v215","v216","v217","v218","v219","v220","v221","v222","v223","v224","v225","v226","v227","v228","v229","v230","v231","v232","v233","v234","v235","v236","v237","v238","v239","v240","v241","v242","v243","v244","v245","v246","v247","v248","v249","v250","v251","v252","v253","v254","v255"
#define CLOB_A "a0","a1","a2","a3","a4","a5","a6","a7","a8","a9","a10","a11","a12","a13","a14","a15","a16","a17","a18","a19","a20","a21","a22","a23","a24","a25","a26","a27","a28","a29","a30","a31","a32","a33","a34","a35","a36","a37","a38","a39","a40","a41","a42","a43","a44","a45","a46","a47","a48","a49","a50","a51","a52","a53","a54","a55","a56","a57","a58","a59","a60","a61","a62","a63","a64","a65","a66","a67","a68","a69","a70","a71","a72","a73","a74","a75","a76","a77","a78","a79","a80","a81","a82","a83","a84","a85","a86","a87","a88","a89","a90","a91","a92","a93","a94","a95","a96","a97","a98","a99","a100","a101","a102","a103","a104","a105","a106","a107","a108","a109","a110","a111","a112","a113","a114","a115","a116","a117","a118","a119","a120","a121","a122","a123","a124","a125","a126","a127","a128","a129","a130","a131","a132","a133","a134","a135","a136","a137","a138","a139","a140","a141","a142","a143","a144","a145","a146","a147","a148","a149","a150","a151","a152","a153","a154","a155","a156","a157","a158","a159","a160","a161","a162","a163","a164","a165","a166","a167","a168","a169","a170","a171","a172","a173","a174","a175","a176","a177","a178","a179","a180","a181","a182","a183","a184","a185","a186","a187","a188","a189","a190","a191","a192","a193","a194","a195","a196","a197","a198","a199","a200","a201","a202","a203","a204","a205","a206","a207","a208","a209","a210","a211","a212","a213","a214","a215","a216","a217","a218","a219","a220","a221","a222","a223","a224","a225","a226","a227","a228","a229","a230","a231","a232","a233","a234","a235","a236","a237","a238","a239","a240","a241","a242","a243","a244","a245","a246","a247","a248","a249","a250","a251","a252","a253","a254","a255"
#define CLOB_S "s3","s4","s5","s6","s7","s8","s9","s10","s11","s12","s13","s14","s15","s16","s17","s18","s19","s20","s21","s22","s23","s24","s25","s26","s27","s28","s29","s30","s31","s33","s34","s35","s36","s37","s38","s39","s40","s41","s42","s43","s44","s45","s46","s47","s48","s49","s50","s51","s52","s53","s54","s55","s56","s57","s58","s59","s60","s61","s62","s63","s64","s65","s66","s67","s68","s69","s70","s71","s72","s73","s74","s75","s76","s77","s78","s79","s80","s81","s82","s83","s84","s85","s86","s87","s88","s89","s90","s91","s92","s93","s94","s95","s96","s97","s98","s99"

__global__ __launch_bounds__(256, 1) __attribute__((aligned(4096))) void k_fused(const void* x, unsigned xn, unsigned xf, void* ws, unsigned wsn, unsigned wsf,
    const void* wq, unsigned wqn, unsigned wqf, const void* kc, unsigned kn, unsigned kf, const void* vc, unsigned vn, unsigned vf,
    const void* wo, unsigned won, unsigned wof, const void* wr, const void* wg, const void* wu, const void* wd, void* out) {
  uint64_t karg = (uint64_t)__builtin_amdgcn_kernarg_segment_ptr();
  asm volatile(
"s_load_dwordx16 s[4:19], %[karg], 0\n\t"
"s_load_dwordx16 s[20:35], %[karg], 64\n\t"
"s_load_dwordx2 s[36:37], %[karg], 128\n\t"
".Lhere%=:\n"
"s_getpc_b64 s[64:65]\n\t"
"s_mov_b32 s66, .Lend%=-.Lhere%=\n\t"
"s_mov_b32 s67, 131072\n\t"
"s_and_b32 s65, s65, 65535\n\t"
"s_lshr_b32 s46, %[sB], 3\n\t"
"s_lshl_b32 s46, s46, 9\n\t"
"v_and_b32 v57, 63, %[vT]\n\t"
"v_and_b32 v58, 192, %[vT]\n\t"
"v_lshlrev_b32 v57, 14, v57\n\t"
"v_lshl_add_u32 v57, v58, 1, v57\n\t"
"buffer_load_dword v57, v57, s[64:67], s46 offen offset:0\n\t"
"v_lshrrev_b32 v49, 6, %[vT]\n\t"
"v_lshrrev_b32 v116, 5, %[vT]\n\t"
"v_and_b32 v117, 31, %[vT]\n\t"
"v_readfirstlane_b32 s38, v49\n\t"
"v_mul_u32_u24 v114, 1280, v116\n\t"
"v_lshlrev_b32 v115, 3, v117\n\t"
"v_add_u32 v114, v114, v115\n\t"
"v_add_u32 v119, v114, v115\n\t"
"v_lshlrev_b32 v120, 4, v117\n\t"
"s_and_b32 s46, %[sB], 7\n\t"
"s_lshl_b32 s46, s46, 2\n\t"
"s_bfe_u32 s75, %[sB], 131075\n\t"
"s_add_u32 s78, s46, s75\n\t"
"s_and_b32 s46, %[sB], 224\n\t"
"s_add_u32 s50, s46, s78\n\t"
"s_lshr_b32 s45, %[sB], 5\n\t"
"s_mul_i32 s77, s45, 5\n\t"
"s_lshr_b32 s77, s77, 3\n\t"
"s_mul_i32 s77, s77, 2\n\t"
"s_mul_i32 s75, s75, 0\n\t"
"s_and_b32 s46, %[sB], 7\n\t"
"s_mul_i32 s46, s46, 2\n\t"
"s_add_u32 s46, s46, s75\n\t"
"s_lshr_b32 s46, s46, 1\n\t"
"s_add_u32 s77, s77, s46\n\t"
"s_mul_i32 s46, s77, 26\n\t"
"s_lshr_b32 s46, s46, 8\n\t"
"s_mul_i32 s46, s46, 10\n\t"
"s_sub_u32 s77, s77, s46\n\t"
"s_lshl_b32 s45, s45, 2\n\t"
"s_add_u32 s45, s45, s38\n\t"
"s_mul_i32 s76, s45, 81920\n\t"
"s_mul_i32 s42, s38, 3080\n\t"
"s_and_b32 s46, %[sB], 3\n\t"
"s_mul_i32 s75, s46, 20480\n\t"
"s_add_u32 s76, s76, s75\n\t"
"s_and_b32 s98, s46, 1\n\t"
"s_mul_i32 s98, s98, -40960\n\t"
"s_add_u32 s98, s98, 20480\n\t"
"s_and_b32 s99, s46, 2\n\t"
"s_mul_i32 s99, s99, -40960\n\t"
"s_add_u32 s99, s99, 40960\n\t"
"v_and_b32 v48, 63, %[vT]\n\t"
"v_lshlrev_b32 v1, 4, v48\n\t"
"v_bfe_u32 v50, %[vT], 2, 3\n\t"
"v_lshl_add_u32 v50, v49, 3, v50\n\t"
"v_mul_u32_u24 v2, 12288, v50\n\t"
"v_bfe_u32 v50, %[vT], 5, 1\n\t"
"v_lshl_add_u32 v2, v50, 6, v2\n\t"
"v_and_b32 v50, 3, %[vT]\n\t"
"v_lshl_add_u32 v2, v50, 4, v2\n\t"
"v_add_u32 v2, 2048, v2\n\t"
"v_mov_b32 v118, 2646016\n\t"
"s_mov_b32 s49, 0\n\t"
"s_mov_b32 s55, 524288\n\t"
"v_subrev_u32 v8, 896, v2\n\t"
"v_subrev_u32 v9, 1792, v2\n\t"
"v_bfe_u32 v50, %[vT], 3, 3\n\t"
"v_mul_u32_u24 v13, 1280, v50\n\t"
"v_bfe_u32 v50, %[vT], 4, 2\n\t"
"v_and_b32 v51, 7, %[vT]\n\t"
"v_xor_b32 v51, v50, v51\n\t"
"v_lshl_add_u32 v13, v51, 4, v13\n\t"
"v_xor_b32 v14, 64, v13\n\t"
"v_add_u32 v14, 9216, v14\n\t"
"v_bfe_u32 v51, %[vT], 1, 3\n\t"
"v_xor_b32 v50, v50, v51\n\t"
"v_and_b32 v51, 15, %[vT]\n\t"
"v_lshlrev_b32 v51, 7, v51\n\t"
"v_lshl_add_u32 v15, v50, 4, v51\n\t"
"v_and_b32 v51, 192, %[vT]\n\t"
"v_lshl_add_u32 v15, v51, 7, v15\n\t"
"v_add_u32 v15, 77824, v15\n\t"
"v_xor_b32 v16, 64, v15\n\t"
"v_mov_b32 v24, 4286578688\n\t"
"v_mov_b32 v25, v24\n\t"
"v_mov_b32 v26, v24\n\t"
"v_mov_b32 v27, v24\n\t"
"s_mul_i32 s79, s50, 10240\n\t"
"s_mul_i32 s72, s78, 384\n\t"
"s_mul_i32 s85, s77, 786432\n\t"
"s_waitcnt lgkmcnt(0)\n\t"
"s_buffer_load_dword s3, s[12:15], s85\n\t"
"buffer_load_dwordx4 v[64:67], v119, s[4:7], s79 offen offset:0\n\t"
"buffer_load_dwordx4 v[68:71], v119, s[4:7], s79 offen offset:512\n\t"
"buffer_load_dwordx2 v[72:73], v114, s[4:7], s79 offen offset:1024\n\t"
"s_mov_b64 s[64:65], s[28:29]\n\t"
"s_mov_b32 s66, 1280\n\t"
"s_mov_b32 s67, 131072\n\t"
"buffer_load_dword v118, v118, s[8:11], 0 offen offset:0 sc1\n\t"
"buffer_load_dwordx4 v[74:77], v120, s[64:67], 0 offen offset:0\n\t"
"buffer_load_dwordx4 v[78:81], v120, s[64:67], 0 offen offset:512\n\t"
"buffer_load_dwordx2 v[82:83], v115, s[64:67], 0 offen offset:1024\n\t"
"s_sub_u32 s14, s14, s72\n\t"
"s_sub_u32 s72, s72, 2048\n\t"
"s_ashr_i32 s71, s72, 31\n\t"
"s_add_u32 s12, s12, s72\n\t"
"s_addc_u32 s13, s13, s71\n\t"
".Lstart%=:\n"
"s_lshl_b32 s40, s77, 7\n\t"
"s_add_u32 s40, s76, s40\n\t"
"s_mul_i32 s41, s77, 786432\n\t"
"s_add_u32 s48, s76, 1280\n\t"
"s_lshl_b32 s51, s38, 13\n\t"
"s_add_u32 s51, s51, 77824\n\t"
"s_mov_b32 s56, 393216\n\t"
"s_mul_i32 s79, s56, 20\n\t"
"s_add_u32 m0, s42, 0\n\t"
"s_nop 0\n\t"
"buffer_load_dwordx4 v2, s[12:15], s41 offen offset:0 lds\n\t"
"v_lshrrev_b32 v51, 3, %[vT]\n\t"
"v_mul_u32_u24 v51, 384, v51\n\t"
"v_and_b32 v52, 7, %[vT]\n\t"
"v_bfe_u32 v53, %[vT], 4, 3\n\t"
"v_xor_b32 v53, v52, v53\n\t"
"v_lshl_add_u32 v3, v53, 4, v51\n\t"
"v_add_u32 v3, 2048, v3\n\t"
"v_bfe_u32 v54, %[vT], 4, 2\n\t"
"v_lshlrev_b32 v53, 1, v54\n\t"
"buffer_load_dwordx4 v8, s[12:15], s41 offen offset:1024 lds\n\t"
"v_xor_b32 v53, v52, v53\n\t"
"v_lshl_add_u32 v4, v53, 4, v51\n\t"
"v_mul_u32_u24 v5, 3080, v54\n\t"
"v_and_b32 v53, 15, %[vT]\n\t"
"v_lshl_add_u32 v5, v53, 4, v5\n\t"
"v_bfe_u32 v53, %[vT], 3, 1\n\t"
"v_mul_u32_u24 v6, 3072, v53\n\t"
"v_lshl_add_u32 v6, v52, 7, v6\n\t"
"v_bfe_u32 v53, %[vT], 1, 3\n\t"
"v_xor_b32 v53, v54, v53\n\t"
"buffer_load_dwordx4 v9, s[12:15], s41 offen offset:2048 lds\n\t"
"s_add_u32 s41, s41, s56\n\t"
"v_lshl_add_u32 v6, v53, 4, v6\n\t"
"v_add_u32 v6, 77824, v6\n\t"
"v_bfe_u32 v53, %[vT], 2, 4\n\t"
"v_lshlrev_b32 v7, 7, v53\n\t"
"v_bfe_u32 v53, %[vT], 3, 2\n\t"
"v_lshl_add_u32 v7, v53, 5, v7\n\t"
"v_and_b32 v55, 3, %[vT]\n\t"
"v_lshl_add_u32 v7, v55, 3, v7\n\t"
"s_lshl_b32 s46, s50, 10\n\t"
"s_add_u32 s46, s46, 28340224\n\t"
"s_add_u32 m0, s51, 0\n\t"
"s_add_u32 s47, s40, 0\n\t"
"buffer_load_dwordx4 v13, s[4:7], s47 offen offset:0 lds\n\t"
"v_lshl_add_u32 v55, %[vT], 2, s46\n\t"
"v_cmp_gt_u32 vcc, 1, %[vT]\n\t"
"v_mov_b32 v56, 1073741824\n\t"
"s_lshl_b32 s46, s50, 6\n\t"
"v_cndmask_b32 v28, v56, v55, vcc\n\t"
"s_add_u32 s46, s46, 2621440\n\t"
"v_lshl_add_u32 v55, v116, 3, s46\n\t"
"v_cmp_eq_u32 vcc, 0, v117\n\t"
"s_and_b32 s46, s50, 192\n\t"
"s_add_u32 s46, s46, s50\n\t"
"buffer_load_dwordx4 v14, s[4:7], s47 offen offset:1024 lds\n\t"
"v_cndmask_b32 v63, v56, v55, vcc\n\t"
"s_lshl_b32 s46, s46, 4\n\t"
"s_and_b32 s75, s38, 2\n\t"
"s_lshl_b32 s75, s75, 9\n\t"
"s_add_u32 s46, s46, s75\n\t"
"s_and_b32 s75, s38, 1\n\t"
"s_lshl_b32 s75, s75, 3\n\t"
"s_add_u32 s46, s46, s75\n\t"
"s_add_u32 s46, s46, 2637824\n\t"
"v_writelane_b32 v63, s46, 1\n\t"
"s_addk_i32 m0, 2048\n\t"
"s_add_u32 s47, s40, s98\n\t"
"buffer_load_dwordx4 v13, s[4:7], s47 offen offset:0 lds\n\t"
"s_mov_b32 s86, 1065369472\n\t"
"s_mov_b32 s87, 4294901760\n\t"
"s_lshr_b32 s46, %[sB], 5\n\t"
"s_mul_i32 s75, s46, 5\n\t"
"s_lshr_b32 s75, s75, 3\n\t"
"s_mul_i32 s74, s78, 5\n\t"
"s_add_u32 s74, s74, s75\n\t"
"s_lshl_b32 s74, s74, 2\n\t"
"s_add_u32 s74, s74, s38\n\t"
"buffer_load_dwordx4 v14, s[4:7], s47 offen offset:1024 lds\n\t"
"s_mul_i32 s74, s74, 384\n\t"
"s_add_u32 s74, s74, 0\n\t"
"s_bitcmp1_b32 181, s46\n\t"
"s_cselect_b32 s74, s74, 1073741824\n\t"
"v_mov_b32 v55, s86\n\t"
"v_accvgpr_write_b32 a232, v55\n\t"
"v_accvgpr_write_b32 a233, v55\n\t"
"v_accvgpr_write_b32 a234, v55\n\t"
"v_accvgpr_write_b32 a235, v55\n\t"
"s_mul_i32 s46, s38, 3\n\t"
"s_lshr_b32 s75, s46, 1\n\t"
"s_addk_i32 m0, 2048\n\t"
"s_add_u32 s47, s40, s99\n\t"
"buffer_load_dwordx4 v13, s[4:7], s47 offen offset:0 lds\n\t"
"s_lshl_b32 s75, s75, 9\n\t"
"s_and_b32 s80, s46, 1\n\t"
"s_lshl_b32 s80, s80, 3\n\t"
"s_add_u32 s75, s75, s80\n\t"
"v_add_u32 v17, s75, v5\n\t"
"s_add_u32 s46, s46, 1\n\t"
"s_lshr_b32 s75, s46, 1\n\t"
"s_lshl_b32 s75, s75, 9\n\t"
"s_and_b32 s80, s46, 1\n\t"
"s_lshl_b32 s80, s80, 3\n\t"
"buffer_load_dwordx4 v14, s[4:7], s47 offen offset:1024 lds\n\t"
"s_add_u32 s75, s75, s80\n\t"
"v_add_u32 v18, s75, v5\n\t"
"s_add_u32 s46, s46, 1\n\t"
"s_lshr_b32 s75, s46, 1\n\t"
"s_lshl_b32 s75, s75, 9\n\t"
"s_and_b32 s80, s46, 1\n\t"
"s_lshl_b32 s80, s80, 3\n\t"
"s_add_u32 s75, s75, s80\n\t"
"v_add_u32 v19, s75, v5\n\t"
"s_add_u32 s46, s46, 1\n\t"
"s_addk_i32 m0, 2048\n\t"
"s_add_u32 s47, s47, s98\n\t"
"buffer_load_dwordx4 v13, s[4:7], s47 offen offset:0 lds\n\t"
"s_lshl_b32 s80, s45, 9\n\t"
"s_add_u32 s80, s80, 2621440\n\t"
"v_lshrrev_b32 v38, 1, v1\n\t"
"s_and_b32 s81, %[sB], 3\n\t"
"s_lshl_b32 s81, s81, 7\n\t"
"v_xor_b32 v38, s81, v38\n\t"
"v_add_u32 v38, s80, v38\n\t"
"s_lshl_b32 s80, s38, 11\n\t"
"s_add_u32 s80, s80, 2637824\n\t"
"v_add_u32 v39, s80, v1\n\t"
"buffer_load_dwordx4 v14, s[4:7], s47 offen offset:1024 lds\n\t"
"s_addk_i32 s40, 128\n\t"
"s_cmp_eq_u32 s40, s48\n\t"
"s_cselect_b32 s40, s76, s40\n\t"
"s_min_u32 s81, s38, 2\n\t"
"s_lshl_b32 s81, s81, 9\n\t"
"v_lshrrev_b32 v42, 1, v1\n\t"
"v_add_u32 v42, s81, v42\n\t"
"v_lshrrev_b32 v41, 1, v42\n\t"
"v_add_u32 v41, 74944, v41\n\t"
"s_mul_i32 s80, s78, 7680\n\t"
"s_add_u32 s80, s80, 0\n\t"
"v_add_u32 v42, s80, v42\n\t"
"v_add_u32 v43, 4608, v42\n\t"
"s_cmp_lg_u32 s49, 0\n\t"
"s_cbranch_scc1 .Lnoln%=\n\t"
"s_mul_i32 s46, s78, 786432\n\t"
"s_add_u32 s46, s46, 589824\n\t"
"s_add_u32 s20, s20, s46\n\t"
"s_addc_u32 s21, s21, 0\n\t"
"s_sub_u32 s46, s46, 2048\n\t"
"s_add_u32 s16, s16, s46\n\t"
"s_addc_u32 s17, s17, 0\n\t"
"s_mov_b64 exec, 0\n\t"
"s_cmp_eq_u32 s38, 0\n\t"
"s_cbranch_scc1 .Lpg1%=\n\t"
"s_cmp_eq_u32 s38, 1\n\t"
"s_cbranch_scc1 .Lpg2%=\n\t"
"s_cmp_eq_u32 s38, 2\n\t"
"s_cbranch_scc1 .Lpg3%=\n\t"
"s_branch .Lpg4%=\n\t"
".Lhr0%=:\n"
"s_mov_b64 exec, -1\n\t"
"s_nop 3\n\t"
"s_waitcnt vmcnt(15)\n\t"
"s_add_u32 m0, s42, 12320\n\t"
"s_nop 0\n\t"
"buffer_load_dwordx4 v2, s[12:15], s41 offen offset:0 lds\n\t"
"v_dot2_f32_bf16 v58, v64, s86, 0\n\t"
"v_dot2_f32_bf16 v59, v65, s86, 0\n\t"
"v_dot2_f32_bf16 v58, v66, s86, v58\n\t"
"v_dot2_f32_bf16 v59, v67, s86, v59\n\t"
"v_dot2_f32_bf16 v58, v68, s86, v58\n\t"
"v_dot2_f32_bf16 v59, v69, s86, v59\n\t"
"buffer_load_dwordx4 v8, s[12:15], s41 offen offset:1024 lds\n\t"
"v_dot2_f32_bf16 v58, v70, s86, v58\n\t"
"v_dot2_f32_bf16 v59, v71, s86, v59\n\t"
"v_dot2_f32_bf16 v58, v72, s86, v58\n\t"
"v_dot2_f32_bf16 v59, v73, s86, v59\n\t"
"v_lshlrev_b32 v84, 16, v64\n\t"
"v_and_b32 v85, s87, v64\n\t"
"buffer_load_dwordx4 v9, s[12:15], s41 offen offset:2048 lds\n\t"
"s_add_u32 s41, s41, s56\n\t"
"s_cmp_eq_u32 s41, s79\n\t"
"s_cselect_b32 s41, 0, s41\n\t"
"v_lshlrev_b32 v86, 16, v65\n\t"
"v_add_f32 v58, v58, v59\n\t"
"v_and_b32 v87, s87, v65\n\t"
"v_lshlrev_b32 v88, 16, v66\n\t"
"v_add_f32_dpp v58, v58, v58 quad_perm:[1,0,3,2] row_mask:0xf bank_mask:0xf\n\t"
"v_and_b32 v89, s87, v66\n\t"
"s_add_u32 m0, s51, 32768\n\t"
"s_add_u32 s47, s40, 0\n\t"
"buffer_load_dwordx4 v13, s[4:7], s47 offen offset:0 lds\n\t"
"v_lshlrev_b32 v90, 16, v67\n\t"
"v_add_f32_dpp v58, v58, v58 quad_perm:[2,3,0,1] row_mask:0xf bank_mask:0xf\n\t"
"v_and_b32 v91, s87, v67\n\t"
"v_lshlrev_b32 v92, 16, v68\n\t"
"v_add_f32_dpp v58, v58, v58 row_half_mirror row_mask:0xf bank_mask:0xf\n\t"
"v_and_b32 v93, s87, v68\n\t"
"buffer_load_dwordx4 v14, s[4:7], s47 offen offset:1024 lds\n\t"
"v_lshlrev_b32 v94, 16, v69\n\t"
"v_add_f32_dpp v58, v58, v58 row_mirror row_mask:0xf bank_mask:0xf\n\t"
"v_and_b32 v95, s87, v69\n\t"
"v_lshlrev_b32 v96, 16, v70\n\t"
"v_mov_b32 v59, v58\n\t"
"v_and_b32 v97, s87, v70\n\t"
"s_addk_i32 m0, 2048\n\t"
"s_add_u32 s47, s40, s98\n\t"
"buffer_load_dwordx4 v13, s[4:7], s47 offen offset:0 lds\n\t"
"v_lshlrev_b32 v98, 16, v71\n\t"
"v_permlane16_swap_b32 v58, v59\n\t"
"v_and_b32 v99, s87, v71\n\t"
"v_lshlrev_b32 v100, 16, v72\n\t"
"v_add_f32 v58, v58, v59\n\t"
"v_and_b32 v101, s87, v72\n\t"
"buffer_load_dwordx4 v14, s[4:7], s47 offen offset:1024 lds\n\t"
"v_lshlrev_b32 v102, 16, v73\n\t"
"v_and_b32 v103, s87, v73\n\t"
"v_mul_f32 v58, 0x3acccccd, v58\n\t"
"v_cvt_pk_bf16_f32 v48, v58, v58\n\t"
"v_lshlrev_b32 v48, 16, v48\n\t"
"v_pk_add_f32 v[84:85], v[84:85], v[48:49] op_sel_hi:[1,0] neg_lo:[0,1] neg_hi:[0,1]\n\t"
"s_addk_i32 m0, 2048\n\t"
"s_add_u32 s47, s40, s99\n\t"
"buffer_load_dwordx4 v13, s[4:7], s47 offen offset:0 lds\n\t"
"v_pk_add_f32 v[86:87], v[86:87], v[48:49] op_sel_hi:[1,0] neg_lo:[0,1] neg_hi:[0,1]\n\t"
"v_cvt_pk_bf16_f32 v104, v84, v85\n\t"
"v_cvt_pk_bf16_f32 v105, v86, v87\n\t"
"v_pk_add_f32 v[88:89], v[88:89], v[48:49] op_sel_hi:[1,0] neg_lo:[0,1] neg_hi:[0,1]\n\t"
"v_pk_add_f32 v[90:91], v[90:91], v[48:49] op_sel_hi:[1,0] neg_lo:[0,1] neg_hi:[0,1]\n\t"
"v_cvt_pk_bf16_f32 v106, v88, v89\n\t"
"buffer_load_dwordx4 v14, s[4:7], s47 offen offset:1024 lds\n\t"
"v_cvt_pk_bf16_f32 v107, v90, v91\n\t"
"v_pk_add_f32 v[92:93], v[92:93], v[48:49] op_sel_hi:[1,0] neg_lo:[0,1] neg_hi:[0,1]\n\t"
"v_pk_add_f32 v[94:95], v[94:95], v[48:49] op_sel_hi:[1,0] neg_lo:[0,1] neg_hi:[0,1]\n\t"
"v_cvt_pk_bf16_f32 v108, v92, v93\n\t"
"v_cvt_pk_bf16_f32 v109, v94, v95\n\t"
"v_pk_add_f32 v[96:97], v[96:97], v[48:49] op_sel_hi:[1,0] neg_lo:[0,1] neg_hi:[0,1]\n\t"
"s_addk_i32 m0, 2048\n\t"
"s_add_u32 s47, s47, s98\n\t"
"buffer_load_dwordx4 v13, s[4:7], s47 offen offset:0 lds\n\t"
"v_pk_add_f32 v[98:99], v[98:99], v[48:49] op_sel_hi:[1,0] neg_lo:[0,1] neg_hi:[0,1]\n\t"
"v_cvt_pk_bf16_f32 v110, v96, v97\n\t"
"v_cvt_pk_bf16_f32 v111, v98, v99\n\t"
"v_pk_add_f32 v[100:101], v[100:101], v[48:49] op_sel_hi:[1,0] neg_lo:[0,1] neg_hi:[0,1]\n\t"
"v_pk_add_f32 v[102:103], v[102:103], v[48:49] op_sel_hi:[1,0] neg_lo:[0,1] neg_hi:[0,1]\n\t"
"v_cvt_pk_bf16_f32 v112, v100, v101\n\t"
"buffer_load_dwordx4 v14, s[4:7], s47 offen offset:1024 lds\n\t"
"s_addk_i32 s40, 128\n\t"
"s_cmp_eq_u32 s40, s48\n\t"
"s_cselect_b32 s40, s76, s40\n\t"
"v_cvt_pk_bf16_f32 v113, v102, v103\n\t"
"v_pk_mul_f32 v[50:51], v[84:85], v[84:85]\n\t"
"v_pk_fma_f32 v[50:51], v[86:87], v[86:87], v[50:51]\n\t"
"v_pk_fma_f32 v[50:51], v[88:89], v[88:89], v[50:51]\n\t"
"v_pk_fma_f32 v[50:51], v[90:91], v[90:91], v[50:51]\n\t"
"v_pk_fma_f32 v[50:51], v[92:93], v[92:93], v[50:51]\n\t"
"v_pk_fma_f32 v[50:51], v[94:95], v[94:95], v[50:51]\n\t"
"v_pk_fma_f32 v[50:51], v[96:97], v[96:97], v[50:51]\n\t"
"v_pk_fma_f32 v[50:51], v[98:99], v[98:99], v[50:51]\n\t"
"v_pk_fma_f32 v[50:51], v[100:101], v[100:101], v[50:51]\n\t"
"v_pk_fma_f32 v[50:51], v[102:103], v[102:103], v[50:51]\n\t"
"s_waitcnt vmcnt(22)\n\t"
"v_dot2_f32_bf16 v52, v104, v74, 0\n\t"
"v_dot2_f32_bf16 v53, v105, v75, 0\n\t"
"v_dot2_f32_bf16 v52, v106, v76, v52\n\t"
"v_dot2_f32_bf16 v53, v107, v77, v53\n\t"
"v_dot2_f32_bf16 v52, v108, v78, v52\n\t"
"v_dot2_f32_bf16 v53, v109, v79, v53\n\t"
"v_dot2_f32_bf16 v52, v110, v80, v52\n\t"
"v_dot2_f32_bf16 v53, v111, v81, v53\n\t"
"v_dot2_f32_bf16 v52, v112, v82, v52\n\t"
"v_dot2_f32_bf16 v53, v113, v83, v53\n\t"
"v_add_f32 v50, v50, v51\n\t"
"v_sub_f32 v49, v58, v48\n\t"
"v_readfirstlane_b32 s39, v118\n\t"
"v_add_f32 v52, v52, v53\n\t"
"v_mul_f32 v49, v49, v49\n\t"
"s_add_i32 s39, s39, 7\n\t"
".Ljoin%=:\n"
"s_waitcnt vmcnt(17)\n\t"
"s_barrier\n\t"
"ds_read_b64_tr_b16 a[192:193], v5 offset:0\n\t"
"ds_read_b64_tr_b16 a[194:195], v5 offset:256\n\t"
"ds_read_b128 a[0:3], v15 offset:0\n\t"
"ds_read_b128 a[16:19], v16 offset:0\n\t"
"s_waitcnt vmcnt(15)\n\t"
"ds_read_b128 a[4:7], v15 offset:2048\n\t"
"ds_read_b128 a[20:23], v16 offset:2048\n\t"
"s_waitcnt vmcnt(13)\n\t"
"ds_read_b128 a[8:11], v15 offset:4096\n\t"
"ds_read_b128 a[24:27], v16 offset:4096\n\t"
"s_waitcnt vmcnt(11)\n\t"
"ds_read_b128 a[12:15], v15 offset:6144\n\t"
"ds_read_b128 a[28:31], v16 offset:6144\n\t"
"ds_read_b64_tr_b16 a[196:197], v5 offset:8\n\t"
"ds_read_b64_tr_b16 a[198:199], v5 offset:264\n\t"
"ds_read_b64_tr_b16 a[200:201], v5 offset:512\n\t"
"ds_read_b64_tr_b16 a[202:203], v5 offset:768\n\t"
"ds_read_b64_tr_b16 a[204:205], v5 offset:520\n\t"
"ds_read_b64_tr_b16 a[206:207], v5 offset:776\n\t"
"s_waitcnt lgkmcnt(6)\n\t"
"v_mfma_f32_16x16x32_bf16 v[64:67], a[192:195], a[0:3], 0\n\t"
"ds_read_b64_tr_b16 a[208:209], v5 offset:1024\n\t"
"v_mfma_f32_16x16x32_bf16 v[68:71], a[192:195], a[4:7], 0\n\t"
"s_add_u32 m0, s42, 24640\n\t"
"ds_read_b64_tr_b16 a[210:211], v5 offset:1280\n\t"
"buffer_load_dwordx4 v2, s[12:15], s41 offen offset:0 lds\n\t"
"v_mfma_f32_16x16x32_bf16 v[72:75], a[192:195], a[8:11], 0\n\t"
"ds_read_b64_tr_b16 a[248:249], v17 offset:0\n\t"
"v_add_f32_dpp v50, v50, v50 quad_perm:[1,0,3,2] row_mask:0xf bank_mask:0xf\n\t"
"v_add_f32_dpp v52, v52, v52 quad_perm:[1,0,3,2] row_mask:0xf bank_mask:0xf\n\t"
"v_mfma_f32_16x16x32_bf16 v[76:79], a[192:195], a[12:15], 0\n\t"
"s_waitcnt lgkmcnt(7)\n\t"
"ds_read_b64_tr_b16 a[250:251], v17 offset:256\n\t"
"v_add_f32_dpp v50, v50, v50 quad_perm:[2,3,0,1] row_mask:0xf bank_mask:0xf\n\t"
"v_add_f32_dpp v52, v52, v52 quad_perm:[2,3,0,1] row_mask:0xf bank_mask:0xf\n\t"
"v_mfma_f32_16x16x32_bf16 v[80:83], a[196:199], a[0:3], 0\n\t"
"ds_read_b64_tr_b16 a[212:213], v5 offset:1032\n\t"
"v_add_f32_dpp v50, v50, v50 row_half_mirror row_mask:0xf bank_mask:0xf\n\t"
"v_mfma_f32_16x16x32_bf16 v[84:87], a[196:199], a[4:7], 0\n\t"
"buffer_load_dwordx4 v8, s[12:15], s41 offen offset:1024 lds\n\t"
"v_mfma_f32_16x16x32_bf16 v[88:91], a[196:199], a[8:11], 0\n\t"
"ds_read_b64_tr_b16 a[214:215], v5 offset:1288\n\t"
"v_add_f32_dpp v52, v52, v52 row_half_mirror row_mask:0xf bank_mask:0xf\n\t"
"v_mfma_f32_16x16x32_bf16 v[92:95], a[196:199], a[12:15], 0\n\t"
"s_waitcnt lgkmcnt(8)\n\t"
"v_add_f32_dpp v50, v50, v50 row_mirror row_mask:0xf bank_mask:0xf\n\t"
"v_add_f32_dpp v52, v52, v52 row_mirror row_mask:0xf bank_mask:0xf\n\t"
"v_mfma_f32_16x16x32_bf16 v[96:99], a[200:203], a[0:3], 0\n\t"
"ds_read_b64_tr_b16 a[192:193], v5 offset:1536\n\t"
"v_mov_b32 v56, v50\n\t"
"v_mfma_f32_16x16x32_bf16 v[100:103], a[200:203], a[4:7], 0\n\t"
"buffer_load_dwordx4 v9, s[12:15], s41 offen offset:2048 lds\n\t"
"v_mfma_f32_16x16x32_bf16 v[104:107], a[200:203], a[8:11], 0\n\t"
"s_add_u32 s41, s41, s56\n\t"
"ds_read_b64_tr_b16 a[194:195], v5 offset:1792\n\t"
"v_mov_b32 v57, v52\n\t"
"v_mfma_f32_16x16x32_bf16 v[108:111], a[200:203], a[12:15], 0\n\t"
"s_waitcnt lgkmcnt(4)\n\t"
"v_permlane16_swap_b32 v50, v56\n\t"
"v_permlane16_swap_b32 v52, v57\n\t"
"v_mfma_f32_16x16x32_bf16 v[112:115], a[204:207], a[0:3], 0\n\t"
"s_addk_i32 m0, 12320\n\t"
"ds_read_b64_tr_b16 a[196:197], v5 offset:1544\n\t"
"buffer_load_dwordx4 v2, s[12:15], s41 offen offset:0 lds\n\t"
"v_mfma_f32_16x16x32_bf16 v[116:119], a[204:207], a[4:7], 0\n\t"
"ds_read_b64_tr_b16 a[198:199], v5 offset:1800\n\t"
"v_add_f32 v50, v50, v56\n\t"
"v_mfma_f32_16x16x32_bf16 v[120:123], a[204:207], a[8:11], 0\n\t"
"s_mov_b32 s80, 0x3acccccd\n\t"
"v_add_f32 v52, v52, v57\n\t"
"v_fma_f32 v50, v50, s80, -v49\n\t"
"v_mfma_f32_16x16x32_bf16 a[236:239], a[232:235], a[248:251], 0\n\t"
"ds_read_b64_tr_b16 a[252:253], v18 offset:0\n\t"
"v_cvt_pk_bf16_f32 v50, v50, v50\n\t"
"v_mfma_f32_16x16x32_bf16 v[124:127], a[204:207], a[12:15], 0\n\t"
"s_waitcnt lgkmcnt(5)\n\t"
"ds_read_b64_tr_b16 a[254:255], v18 offset:256\n\t"
"v_lshlrev_b32 v50, 16, v50\n\t"
"v_mfma_f32_16x16x32_bf16 v[128:131], a[208:211], a[0:3], 0\n\t"
"ds_read_b64_tr_b16 a[200:201], v5 offset:2048\n\t"
"buffer_load_dwordx4 v8, s[12:15], s41 offen offset:1024 lds\n\t"
"v_mfma_f32_16x16x32_bf16 v[144:147], a[212:215], a[0:3], 0\n\t"
"ds_read_b64_tr_b16 a[202:203], v5 offset:2304\n\t"
"v_add_f32 v50, 0x3727c5ac, v50\n\t"
"v_mfma_f32_16x16x32_bf16 v[148:151], a[212:215], a[4:7], 0\n\t"
"v_cvt_pk_bf16_f32 v50, v50, v50\n\t"
"v_lshlrev_b32 v50, 16, v50\n\t"
"v_mfma_f32_16x16x32_bf16 v[132:135], a[208:211], a[4:7], 0\n\t"
"v_sqrt_f32 v50, v50\n\t"
"v_mfma_f32_16x16x32_bf16 v[136:139], a[208:211], a[8:11], 0\n\t"
"ds_read_b64_tr_b16 a[204:205], v5 offset:2056\n\t"
"buffer_load_dwordx4 v9, s[12:15], s41 offen offset:2048 lds\n\t"
"v_mfma_f32_16x16x32_bf16 v[152:155], a[212:215], a[8:11], 0\n\t"
"s_add_u32 s41, s41, s56\n\t"
"ds_read_b64_tr_b16 a[206:207], v5 offset:2312\n\t"
"v_mov_b32 v55, s39\n\t"
"v_mfma_f32_16x16x32_bf16 v[156:159], a[212:215], a[12:15], 0\n\t"
"s_cmp_eq_u32 s41, s79\n\t"
"s_cselect_b32 s41, 0, s41\n\t"
"v_cvt_pk_bf16_f32 v50, v50, v50\n\t"
"v_mfma_f32_16x16x32_bf16 v[140:143], a[208:211], a[12:15], 0\n\t"
"s_waitcnt lgkmcnt(4)\n\t"
"v_lshlrev_b32 v51, 16, v50\n\t"
"v_bfe_u32 v34, %[vT], 3, 3\n\t"
"v_mfma_f32_16x16x32_bf16 v[172:175], a[192:195], a[12:15], 0\n\t"
"ds_read_b64_tr_b16 a[208:209], v5 offset:2560\n\t"
"v_mul_u32_u24 v32, 3072, v34\n\t"
"v_mfma_f32_16x16x32_bf16 v[188:191], a[196:199], a[12:15], 0\n\t"
"ds_read_b64_tr_b16 a[210:211], v5 offset:2816\n\t"
"v_bfe_u32 v34, %[vT], 3, 2\n\t"
"v_mfma_f32_16x16x32_bf16 v[184:187], a[196:199], a[8:11], 0\n\t"
"v_rcp_f32 v51, v51\n\t"
"v_mfma_f32_16x16x32_bf16 a[240:243], a[232:235], a[252:255], 0\n\t"
"ds_read_b64_tr_b16 a[224:225], v19 offset:0\n\t"
"v_lshrrev_b32 v54, 16, v50\n\t"
"v_mfma_f32_16x16x32_bf16 v[168:171], a[192:195], a[8:11], 0\n\t"
"ds_read_b64_tr_b16 a[226:227], v19 offset:256\n\t"
"v_mul_f32 v52, v52, v51\n\t"
"v_mfma_f32_16x16x32_bf16 v[164:167], a[192:195], a[4:7], 0\n\t"
"s_waitcnt vmcnt(14)\n\t"
"ds_read_b64_tr_b16 a[212:213], v5 offset:2568\n\t"
"s_barrier\n\t"
"v_mfma_f32_16x16x32_bf16 v[180:183], a[196:199], a[4:7], 0\n\t"
"ds_read_b64_tr_b16 a[214:215], v5 offset:2824\n\t"
"v_cvt_pk_bf16_f32 v52, v52, v52\n\t"
"v_mfma_f32_16x16x32_bf16 v[176:179], a[196:199], a[0:3], 0\n\t"
"v_ashrrev_i32 v53, 31, v52\n\t"
"v_or_b32 v53, 2147483648, v53\n\t"
"v_mfma_f32_16x16x32_bf16 v[160:163], a[192:195], a[0:3], 0\n\t"
"s_waitcnt lgkmcnt(6)\n\t"
"v_xor_b32 v52, v52, v53\n\t"
"v_or_b32 v54, v54, v48\n\t"
"v_mfma_f32_16x16x32_bf16 v[192:195], a[200:203], a[0:3], 0\n\t"
"ds_read_b64_tr_b16 a[192:193], v5 offset:12320\n\t"
"v_readlane_b32 s80, v52, 0\n\t"
"v_mfma_f32_16x16x32_bf16 v[208:211], a[204:207], a[0:3], 0\n\t"
"ds_read_b64_tr_b16 a[194:195], v5 offset:12576\n\t"
"v_readlane_b32 s81, v52, 32\n\t"
"v_mfma_f32_16x16x32_bf16 v[212:215], a[204:207], a[4:7], 0\n\t"
"s_pack_hh_b32_b16 s80, s80, s81\n\t"
"s_nop 0\n\t"
"v_writelane_b32 v54, s80, 1\n\t"
"v_mfma_f32_16x16x32_bf16 v[196:199], a[200:203], a[4:7], 0\n\t"
"buffer_store_dwordx2 v[54:55], v63, s[8:11], 0 offen offset:0 sc1\n\t"
"v_and_b32 v34, 2, v34\n\t"
"v_and_b32 v35, 7, %[vT]\n\t"
"v_mfma_f32_16x16x32_bf16 v[200:203], a[200:203], a[8:11], 0\n\t"
"ds_read_b64_tr_b16 a[196:197], v5 offset:12328\n\t"
"v_xor_b32 v34, v34, v35\n\t"
"v_mfma_f32_16x16x32_bf16 v[216:219], a[204:207], a[8:11], 0\n\t"
"ds_read_b64_tr_b16 a[198:199], v5 offset:12584\n\t"
"v_lshl_add_u32 v32, v34, 4, v32\n\t"
"v_mfma_f32_16x16x32_bf16 v[220:223], a[204:207], a[12:15], 0\n\t"
"s_cmp_lt_u32 s50, 96\n\t"
"s_cselect_b64 s[64:65], s[30:31], s[32:33]\n\t"
"s_cselect_b32 s86, 0, 96\n\t"
"v_mfma_f32_16x16x32_bf16 v[204:207], a[200:203], a[12:15], 0\n\t"
"s_mov_b32 s66, 1966080\n\t"
"s_sub_u32 s86, s50, s86\n\t"
"s_waitcnt lgkmcnt(4)\n\t"
"v_mfma_f32_16x16x32_bf16 v[236:239], a[208:211], a[12:15], 0\n\t"
"s_lshr_b32 s86, s86, 2\n\t"
"ds_read_b64_tr_b16 a[200:201], v5 offset:12832\n\t"
"s_lshl_b32 s86, s86, 7\n\t"
"v_mfma_f32_16x16x32_bf16 v[252:255], a[212:215], a[12:15], 0\n\t"
"s_and_b32 s87, s50, 3\n\t"
"ds_read_b64_tr_b16 a[202:203], v5 offset:13088\n\t"
"s_lshl_b32 s87, s87, 2\n\t"
"v_mfma_f32_16x16x32_bf16 v[248:251], a[212:215], a[8:11], 0\n\t"
"v_mfma_f32_16x16x32_bf16 a[244:247], a[232:235], a[224:227], 0\n\t"
"v_mfma_f32_16x16x32_bf16 v[232:235], a[208:211], a[8:11], 0\n\t"
"s_add_u32 s87, s87, s38\n\t"
"s_mul_i32 s87, s87, 122880\n\t"
"s_add_u32 s86, s86, s87\n\t"
"v_mfma_f32_16x16x32_bf16 v[228:231], a[208:211], a[4:7], 0\n\t"
"s_cmp_lt_u32 s50, 192\n\t"
"ds_read_b64_tr_b16 a[204:205], v5 offset:12840\n\t"
"s_cselect_b32 s86, s86, 1073741824\n\t"
"v_mfma_f32_16x16x32_bf16 v[244:247], a[212:215], a[4:7], 0\n\t"
"s_mul_i32 s87, s38, 5120\n\t"
"ds_read_b64_tr_b16 a[206:207], v5 offset:13096\n\t"
"s_add_u32 s87, s87, 143360\n\t"
"v_mfma_f32_16x16x32_bf16 v[240:243], a[212:215], a[0:3], 0\n\t"
"v_bfe_u32 v34, %[vT], 4, 2\n\t"
"v_bfe_u32 v35, %[vT], 2, 2\n\t"
"v_mfma_f32_16x16x32_bf16 v[224:227], a[208:211], a[0:3], 0\n\t"
"s_waitcnt lgkmcnt(4)\n\t"
"v_lshl_add_u32 v34, v34, 3, v35\n\t"
"v_lshlrev_b32 v33, 7, v34\n\t"
"v_mfma_f32_16x16x32_bf16 v[64:67], a[192:195], a[16:19], v[64:67]\n\t"
"ds_read_b64_tr_b16 a[208:209], v5 offset:13344\n\t"
"v_bfe_u32 v34, %[vT], 3, 1\n\t"
"v_mfma_f32_16x16x32_bf16 v[80:83], a[196:199], a[16:19], v[80:83]\n\t"
"ds_read_b64_tr_b16 a[210:211], v5 offset:13600\n\t"
"v_xor_b32 v34, s38, v34\n\t"
"v_mfma_f32_16x16x32_bf16 v[84:87], a[196:199], a[20:23], v[84:87]\n\t"
"ds_read_b64_tr_b16 a[248:249], v17 offset:12320\n\t"
"v_lshl_add_u32 v33, v34, 5, v33\n\t"
"v_mfma_f32_16x16x32_bf16 v[68:71], a[192:195], a[20:23], v[68:71]\n\t"
"ds_read_b64_tr_b16 a[250:251], v17 offset:12576\n\t"
"v_and_b32 v35, 3, %[vT]\n\t"
"v_mfma_f32_16x16x32_bf16 v[72:75], a[192:195], a[24:27], v[72:75]\n\t"
"ds_read_b64_tr_b16 a[212:213], v5 offset:13352\n\t"
"v_lshl_add_u32 v33, v35, 3, v33\n\t"
"v_mfma_f32_16x16x32_bf16 v[88:91], a[196:199], a[24:27], v[88:91]\n\t"
"ds_read_b64_tr_b16 a[214:215], v5 offset:13608\n\t"
"v_add_u32 v33, 143360, v33\n\t"
"v_mfma_f32_16x16x32_bf16 v[92:95], a[196:199], a[28:31], v[92:95]\n\t"
"v_and_b32 v35, 1, %[vT]\n\t"
"v_lshlrev_b32 v47, 6, v35\n\t"
"v_mfma_f32_16x16x32_bf16 v[76:79], a[192:195], a[28:31], v[76:79]\n\t"
"s_waitcnt lgkmcnt(6)\n\t"
"v_and_b32 v35, 48, %[vT]\n\t"
"v_add_u32 v47, v47, v35\n\t"
"v_mfma_f32_16x16x32_bf16 v[108:111], a[200:203], a[28:31], v[108:111]\n\t"
"ds_read_b64_tr_b16 a[192:193], v5 offset:13856\n\t"
"v_and_b32 v35, 14, %[vT]\n\t"
"v_mfma_f32_16x16x32_bf16 v[124:127], a[204:207], a[28:31], v[124:127]\n\t"
"ds_read_b64_tr_b16 a[194:195], v5 offset:14112\n\t"
"v_cmp_eq_u32 vcc, 0, v35\n\t"
"v_mfma_f32_16x16x32_bf16 v[120:123], a[204:207], a[24:27], v[120:123]\n\t"
"v_mov_b32 v35, 1073741824\n\t"
"v_subrev_u32 v10, 896, v3\n\t"
"v_mfma_f32_16x16x32_bf16 v[104:107], a[200:203], a[24:27], v[104:107]\n\t"
"v_cndmask_b32 v47, v35, v47, vcc\n\t"
"v_subrev_u32 v11, 1792, v3\n\t"
"v_mfma_f32_16x16x32_bf16 v[100:103], a[200:203], a[20:23], v[100:103]\n\t"
"ds_read_b64_tr_b16 a[196:197], v5 offset:13864\n\t"
"v_xor_b32 v12, 64, v6\n\t"
"v_mfma_f32_16x16x32_bf16 v[116:119], a[204:207], a[20:23], v[116:119]\n\t"
"s_mul_i32 s43, s38, 3072\n\t"
"ds_read_b64_tr_b16 a[198:199], v5 offset:14120\n\t"
"s_add_u32 s43, s43, 77824\n\t"
"v_mfma_f32_16x16x32_bf16 v[112:115], a[204:207], a[16:19], v[112:115]\n\t"
"s_lshl_b32 s44, s38, 10\n\t"
"ds_read_b64_tr_b16 a[252:253], v18 offset:12320\n\t"
"s_mul_i32 s52, s45, 192\n\t"
"v_mfma_f32_16x16x32_bf16 v[96:99], a[200:203], a[16:19], v[96:99]\n\t"
"s_mul_i32 s46, s78, 6\n\t"
"ds_read_b64_tr_b16 a[254:255], v18 offset:12576\n\t"
"s_waitcnt lgkmcnt(6)\n\t"
"v_mfma_f32_16x16x32_bf16 v[128:131], a[208:211], a[16:19], v[128:131]\n\t"
"s_add_u32 m0, s51, 0\n\t"
"ds_read_b64_tr_b16 a[200:201], v5 offset:14368\n\t"
"s_add_u32 s47, s40, 0\n\t"
"v_mfma_f32_16x16x32_bf16 v[144:147], a[212:215], a[16:19], v[144:147]\n\t"
"s_add_u32 s52, s52, s46\n\t"
"ds_read_b64_tr_b16 a[202:203], v5 offset:14624\n\t"
"s_lshl_b32 s52, s52, 12\n\t"
"v_mfma_f32_16x16x32_bf16 v[148:151], a[212:215], a[20:23], v[148:151]\n\t"
"s_waitcnt vmcnt(13)\n\t"
"ds_read_b128 a[32:35], v15 offset:32768\n\t"
"v_mfma_f32_16x16x32_bf16 a[236:239], a[232:235], a[248:251], a[236:239]\n\t"
"buffer_load_dwordx4 v13, s[4:7], s47 offen offset:0 lds\n\t"
"v_mfma_f32_16x16x32_bf16 v[132:135], a[208:211], a[20:23], v[132:135]\n\t"
"s_add_u32 s52, s52, 2650112\n\t"
"ds_read_b128 a[48:51], v16 offset:32768\n\t"
"s_lshr_b32 s46, s38, 1\n\t"
"v_mfma_f32_16x16x32_bf16 v[136:139], a[208:211], a[24:27], v[136:139]\n\t"
"ds_read_b64_tr_b16 a[204:205], v5 offset:14376\n\t"
"buffer_load_dwordx4 v14, s[4:7], s47 offen offset:1024 lds\n\t"
"v_mfma_f32_16x16x32_bf16 v[152:155], a[212:215], a[24:27], v[152:155]\n\t"
"s_mul_i32 s73, s46, 10528\n\t"
".Lpg1%=:\n"
"s_cbranch_execz .Lpg5%=\n\t"
"ds_read_b64_tr_b16 a[206:207], v5 offset:14632\n\t"
"s_and_b32 s75, s38, 1\n\t"
"v_mfma_f32_16x16x32_bf16 v[156:159], a[212:215], a[28:31], v[156:159]\n\t"
"s_mul_i32 s54, s75, 1056\n\t"
"s_add_u32 s73, s73, s54\n\t"
"s_mul_i32 s70, s75, 5120\n\t"
"v_mfma_f32_16x16x32_bf16 v[140:143], a[208:211], a[28:31], v[140:143]\n\t"
"s_addk_i32 m0, 2048\n\t"
"s_add_u32 s47, s40, s98\n\t"
"s_waitcnt lgkmcnt(6)\n\t"
"v_mfma_f32_16x16x32_bf16 v[172:175], a[192:195], a[28:31], v[172:175]\n\t"
"s_and_b32 s72, s78, 3\n\t"
"ds_read_b64_tr_b16 a[208:209], v5 offset:14880\n\t"
"s_lshl_b32 s75, s72, 8\n\t"
"v_mfma_f32_16x16x32_bf16 v[188:191], a[196:199], a[28:31], v[188:191]\n\t"
"s_add_u32 s70, s70, s75\n\t"
"ds_read_b64_tr_b16 a[210:211], v5 offset:15136\n\t"
"s_mul_i32 s53, s46, 40960\n\t"
"v_mfma_f32_16x16x32_bf16 v[184:187], a[196:199], a[24:27], v[184:187]\n\t"
"s_waitcnt vmcnt(13)\n\t"
"buffer_load_dwordx4 v13, s[4:7], s47 offen offset:0 lds\n\t"
"ds_read_b128 a[36:39], v15 offset:34816\n\t"
"v_mfma_f32_16x16x32_bf16 a[240:243], a[232:235], a[252:255], a[240:243]\n\t"
"ds_read_b64_tr_b16 a[224:225], v19 offset:12320\n\t"
"buffer_load_dwordx4 v14, s[4:7], s47 offen offset:1024 lds\n\t"
"v_mfma_f32_16x16x32_bf16 v[168:171], a[192:195], a[24:27], v[168:171]\n\t"
"s_addk_i32 m0, 2048\n\t"
"ds_read_b128 a[52:55], v16 offset:34816\n\t"
"ds_read_b64_tr_b16 a[226:227], v19 offset:12576\n\t"
"v_mfma_f32_16x16x32_bf16 v[164:167], a[192:195], a[20:23], v[164:167]\n\t"
"s_waitcnt vmcnt(8)\n\t"
"ds_read_b64_tr_b16 a[212:213], v5 offset:14888\n\t"
"s_barrier\n\t"
"v_mfma_f32_16x16x32_bf16 v[180:183], a[196:199], a[20:23], v[180:183]\n\t"
"s_add_u32 s53, s53, s70\n\t"
"ds_read_b64_tr_b16 a[214:215], v5 offset:15144\n\t"
"s_mul_i32 s75, s78, 245760\n\t"
"v_mfma_f32_16x16x32_bf16 v[176:179], a[196:199], a[16:19], v[176:179]\n\t"
"s_add_u32 s47, s40, s99\n\t"
"buffer_load_dwordx4 v13, s[4:7], s47 offen offset:0 lds\n\t"
"ds_read_b128 a[40:43], v15 offset:36864\n\t"
"v_mfma_f32_16x16x32_bf16 v[160:163], a[192:195], a[16:19], v[160:163]\n\t"
"s_add_u32 s60, s75, s53\n\t"
"ds_read_b128 a[56:59], v16 offset:36864\n\t"
"s_waitcnt lgkmcnt(10)\n\t"
"v_mfma_f32_16x16x32_bf16 v[192:195], a[200:203], a[16:19], v[192:195]\n\t"
"s_add_u32 s75, s46, 1\n\t"
"ds_read_b64_tr_b16 a[192:193], v5 offset:24640\n\t"
"s_xor_b32 s75, s75, s78\n\t"
"v_mfma_f32_16x16x32_bf16 v[208:211], a[204:207], a[16:19], v[208:211]\n\t"
"s_mul_i32 s75, s75, 245760\n\t"
"ds_read_b64_tr_b16 a[194:195], v5 offset:24896\n\t"
"s_add_u32 s61, s75, s70\n\t"
"v_mfma_f32_16x16x32_bf16 v[212:215], a[204:207], a[20:23], v[212:215]\n\t"
"buffer_load_dwordx4 v14, s[4:7], s47 offen offset:1024 lds\n\t"
"v_mfma_f32_16x16x32_bf16 v[196:199], a[200:203], a[20:23], v[196:199]\n\t"
"s_lshl_b32 s75, s46, 1\n\t"
"s_xor_b32 s75, s75, s78\n\t"
"s_xor_b32 s75, s75, 3\n\t"
"v_mfma_f32_16x16x32_bf16 v[200:203], a[200:203], a[24:27], v[200:203]\n\t"
"s_mul_i32 s75, s75, 245760\n\t"
"ds_read_b64_tr_b16 a[196:197], v5 offset:24648\n\t"
"s_add_u32 s62, s75, s53\n\t"
"v_mfma_f32_16x16x32_bf16 v[216:219], a[204:207], a[24:27], v[216:219]\n\t"
"s_addk_i32 m0, 2048\n\t"
"s_add_u32 s47, s47, s98\n\t"
"ds_read_b64_tr_b16 a[198:199], v5 offset:24904\n\t"
"buffer_load_dwordx4 v13, s[4:7], s47 offen offset:0 lds\n\t"
"v_mfma_f32_16x16x32_bf16 v[220:223], a[204:207], a[28:31], v[220:223]\n\t"
"ds_read_b128 a[44:47], v15 offset:38912\n\t"
"s_xor_b32 s75, s46, s78\n\t"
"v_mfma_f32_16x16x32_bf16 v[204:207], a[200:203], a[28:31], v[204:207]\n\t"
"s_xor_b32 s75, s75, 2\n\t"
"ds_read_b128 a[60:63], v16 offset:38912\n\t"
"s_waitcnt lgkmcnt(6)\n\t"
"v_mfma_f32_16x16x32_bf16 v[236:239], a[208:211], a[28:31], v[236:239]\n\t"
"s_mul_i32 s75, s75, 245760\n\t"
"ds_read_b64_tr_b16 a[200:201], v5 offset:25152\n\t"
"s_add_u32 s70, s70, 40960\n\t"
"v_mfma_f32_16x16x32_bf16 v[252:255], a[212:215], a[28:31], v[252:255]\n\t"
"buffer_load_dwordx4 v14, s[4:7], s47 offen offset:1024 lds\n\t"
"v_mfma_f32_16x16x32_bf16 v[248:251], a[212:215], a[24:27], v[248:251]\n\t"
"s_addk_i32 s40, 128\n\t"
"s_cmp_eq_u32 s40, s48\n\t"
"s_cselect_b32 s40, s76, s40\n\t"
"v_mfma_f32_16x16x32_bf16 a[244:247], a[232:235], a[224:227], a[244:247]\n\t"
"ds_read_b64_tr_b16 a[202:203], v5 offset:25408\n\t"
"v_mfma_f32_16x16x32_bf16 v[232:235], a[208:211], a[24:27], v[232:235]\n\t"
"s_add_u32 s63, s75, s70\n\t"
"s_and_b32 s46, s50, 252\n\t"
"s_lshl_b32 s46, s46, 10\n\t"
"v_mfma_f32_16x16x32_bf16 v[228:231], a[208:211], a[20:23], v[228:231]\n\t"
"s_add_u32 m0, s42, 49280\n\t"
"ds_read_b64_tr_b16 a[204:205], v5 offset:25160\n\t"
"buffer_load_dwordx4 v2, s[12:15], s41 offen offset:0 lds\n\t"
"v_mfma_f32_16x16x32_bf16 v[244:247], a[212:215], a[20:23], v[244:247]\n\t"
"ds_read_b64_tr_b16 a[206:207], v5 offset:25416\n\t"
"v_and_b32 v30, 3, %[vT]\n\t"
"v_mfma_f32_16x16x32_bf16 v[240:243], a[212:215], a[16:19], v[240:243]\n\t"
"s_add_u32 s46, s46, 28340224\n\t"
"v_lshl_add_u32 v29, v30, 10, s46\n\t"
"s_mul_i32 s53, s45, 786432\n\t"
"v_mfma_f32_16x16x32_bf16 v[224:227], a[208:211], a[16:19], v[224:227]\n\t"
"s_xor_b32 s46, s78, 1\n\t"
"s_mul_i32 s46, s46, 24576\n\t"
"s_waitcnt lgkmcnt(4)\n\t"
"v_mfma_f32_16x16x32_bf16 v[64:67], a[192:195], a[32:35], v[64:67]\n\t"
"s_add_u32 s53, s53, 2650112\n\t"
"ds_read_b64_tr_b16 a[208:209], v5 offset:25664\n\t"
"buffer_load_dwordx4 v8, s[12:15], s41 offen offset:1024 lds\n\t"
"v_mfma_f32_16x16x32_bf16 v[80:83], a[196:199], a[32:35], v[80:83]\n\t"
"s_add_u32 s57, s46, s53\n\t"
"ds_read_b64_tr_b16 a[210:211], v5 offset:25920\n\t"
"s_xor_b32 s46, s78, 2\n\t"
"v_mfma_f32_16x16x32_bf16 v[84:87], a[196:199], a[36:39], v[84:87]\n\t"
"s_mul_i32 s46, s46, 24576\n\t"
"ds_read_b64_tr_b16 a[248:249], v17 offset:24640\n\t"
"s_add_u32 s58, s46, s53\n\t"
"v_mfma_f32_16x16x32_bf16 v[68:71], a[192:195], a[36:39], v[68:71]\n\t"
"s_xor_b32 s46, s78, 3\n\t"
"ds_read_b64_tr_b16 a[250:251], v17 offset:24896\n\t"
"s_mul_i32 s46, s46, 24576\n\t"
"v_mfma_f32_16x16x32_bf16 v[72:75], a[192:195], a[40:43], v[72:75]\n\t"
"s_waitcnt lgkmcnt(4)\n\t"
"ds_read_b64_tr_b16 a[212:213], v5 offset:25672\n\t"
"buffer_load_dwordx4 v9, s[12:15], s41 offen offset:2048 lds\n\t"
"v_mfma_f32_16x16x32_bf16 v[88:91], a[196:199], a[40:43], v[88:91]\n\t"
"s_add_u32 s41, s41, s56\n\t"
"ds_read_b64_tr_b16 a[214:215], v5 offset:25928\n\t"
"s_add_u32 s59, s46, s53\n\t"
"v_mfma_f32_16x16x32_bf16 v[92:95], a[196:199], a[44:47], v[92:95]\n\t"
"s_lshl_b32 s71, s45, 2\n\t"
"s_and_b32 s46, %[sB], 3\n\t"
"s_add_u32 s71, s71, s46\n\t"
"v_mfma_f32_16x16x32_bf16 v[76:79], a[192:195], a[44:47], v[76:79]\n\t"
"s_addk_i32 m0, 12320\n\t"
"s_nop 0\n\t"
"buffer_load_dwordx4 v2, s[12:15], s41 offen offset:0 lds\n\t"
"v_mfma_f32_16x16x32_bf16 v[108:111], a[200:203], a[44:47], v[108:111]\n\t"
"s_lshl_b32 s71, s71, 4\n\t"
"ds_read_b64_tr_b16 a[192:193], v5 offset:26176\n\t"
"s_lshl_b32 s72, s72, 1\n\t"
"v_mfma_f32_16x16x32_bf16 v[124:127], a[204:207], a[44:47], v[124:127]\n\t"
"s_bfe_u32 s46, %[sB], 65538\n\t"
"ds_read_b64_tr_b16 a[194:195], v5 offset:26432\n\t"
"s_add_u32 s72, s72, s46\n\t"
"v_mfma_f32_16x16x32_bf16 v[120:123], a[204:207], a[40:43], v[120:123]\n\t"
"s_mul_i32 s53, s50, 205\n\t"
"s_lshr_b32 s53, s53, 13\n\t"
"s_mul_i32 s46, s53, 40\n\t"
"v_mfma_f32_16x16x32_bf16 v[104:107], a[200:203], a[40:43], v[104:107]\n\t"
"s_sub_u32 s54, s50, s46\n\t"
"s_lshl_b32 s46, s53, 2\n\t"
"s_add_u32 s46, s46, s38\n\t"
"v_mfma_f32_16x16x32_bf16 v[100:103], a[200:203], a[36:39], v[100:103]\n\t"
"ds_read_b64_tr_b16 a[196:197], v5 offset:26184\n\t"
"buffer_load_dwordx4 v8, s[12:15], s41 offen offset:1024 lds\n\t"
"v_mfma_f32_16x16x32_bf16 v[116:119], a[204:207], a[36:39], v[116:119]\n\t"
"s_mul_i32 s82, s46, 81920\n\t"
"ds_read_b64_tr_b16 a[198:199], v5 offset:26440\n\t"
"s_lshl_b32 s75, s54, 5\n\t"
"v_mfma_f32_16x16x32_bf16 v[112:115], a[204:207], a[32:35], v[112:115]\n\t"
"s_add_u32 s82, s82, s75\n\t"
"ds_read_b64_tr_b16 a[252:253], v18 offset:24640\n\t"
"s_lshl_b32 s83, s46, 8\n\t"
"v_mfma_f32_16x16x32_bf16 v[96:99], a[200:203], a[32:35], v[96:99]\n\t"
"s_mul_i32 s54, s54, 6\n\t"
"ds_read_b64_tr_b16 a[254:255], v18 offset:24896\n\t"
"s_waitcnt lgkmcnt(6)\n\t"
"v_mfma_f32_16x16x32_bf16 v[128:131], a[208:211], a[32:35], v[128:131]\n\t"
"s_add_u32 s54, s54, s53\n\t"
"ds_read_b64_tr_b16 a[200:201], v5 offset:26688\n\t"
"s_lshl_b32 s54, s54, 7\n\t"
"v_mfma_f32_16x16x32_bf16 v[144:147], a[212:215], a[32:35], v[144:147]\n\t"
"buffer_load_dwordx4 v9, s[12:15], s41 offen offset:2048 lds\n\t"
"v_mfma_f32_16x16x32_bf16 v[148:151], a[212:215], a[36:39], v[148:151]\n\t"
"s_add_u32 s41, s41, s56\n\t"
"s_cmp_eq_u32 s41, s79\n\t"
"s_cselect_b32 s41, 0, s41\n\t"
"v_mfma_f32_16x16x32_bf16 a[236:239], a[232:235], a[248:251], a[236:239]\n\t"
"ds_read_b64_tr_b16 a[202:203], v5 offset:26944\n\t"
"v_mfma_f32_16x16x32_bf16 v[132:135], a[208:211], a[36:39], v[132:135]\n\t"
"s_add_u32 s54, s54, 28700672\n\t"
"s_cmp_lt_u32 s50, 240\n\t"
"s_cselect_b32 s84, s54, 1073741824\n\t"
"v_mfma_f32_16x16x32_bf16 v[136:139], a[208:211], a[40:43], v[136:139]\n\t"
"s_add_u32 m0, s42, 0\n\t"
"ds_read_b64_tr_b16 a[204:205], v5 offset:26696\n\t"
"v_mfma_f32_16x16x32_bf16 v[152:155], a[212:215], a[40:43], v[152:155]\n\t"
"ds_read_b64_tr_b16 a[206:207], v5 offset:26952\n\t"
"v_mfma_f32_16x16x32_bf16 v[156:159], a[212:215], a[44:47], v[156:159]\n\t"
"buffer_load_dwordx4 v2, s[12:15], s41 offen offset:0 lds\n\t"
"v_mfma_f32_16x16x32_bf16 v[140:143], a[208:211], a[44:47], v[140:143]\n\t"
"s_waitcnt lgkmcnt(4)\n\t"
"v_mfma_f32_16x16x32_bf16 v[172:175], a[192:195], a[44:47], v[172:175]\n\t"
"ds_read_b64_tr_b16 a[208:209], v5 offset:27200\n\t"
"v_mfma_f32_16x16x32_bf16 v[188:191], a[196:199], a[44:47], v[188:191]\n\t"
"buffer_load_dwordx4 v8, s[12:15], s41 offen offset:1024 lds\n\t"
"v_mfma_f32_16x16x32_bf16 v[184:187], a[196:199], a[40:43], v[184:187]\n\t"
"ds_read_b64_tr_b16 a[210:211], v5 offset:27456\n\t"
"v_mfma_f32_16x16x32_bf16 a[240:243], a[232:235], a[252:255], a[240:243]\n\t"
"ds_read_b64_tr_b16 a[224:225], v19 offset:24640\n\t"
"v_mfma_f32_16x16x32_bf16 v[168:171], a[192:195], a[40:43], v[168:171]\n\t"
"ds_read_b64_tr_b16 a[226:227], v19 offset:24896\n\t"
"ds_read_b64_tr_b16 a[212:213], v5 offset:27208\n\t"
"v_mfma_f32_16x16x32_bf16 v[164:167], a[192:195], a[36:39], v[164:167]\n\t"
"s_waitcnt vmcnt(17)\n\t"
"s_barrier\n\t"
"v_mfma_f32_16x16x32_bf16 v[180:183], a[196:199], a[36:39], v[180:183]\n\t"
"ds_read_b64_tr_b16 a[214:215], v5 offset:27464\n\t"
"v_mfma_f32_16x16x32_bf16 v[176:179], a[196:199], a[32:35], v[176:179]\n\t"
"buffer_load_dwordx4 v9, s[12:15], s41 offen offset:2048 lds\n\t"
"v_mfma_f32_16x16x32_bf16 v[160:163], a[192:195], a[32:35], v[160:163]\n\t"
"s_add_u32 s41, s41, s56\n\t"
"s_waitcnt lgkmcnt(6)\n\t"
"v_mfma_f32_16x16x32_bf16 v[192:195], a[200:203], a[32:35], v[192:195]\n\t"
"ds_read_b64_tr_b16 a[192:193], v5 offset:36960\n\t"
"s_addk_i32 m0, 12320\n\t"
"v_mfma_f32_16x16x32_bf16 v[208:211], a[204:207], a[32:35], v[208:211]\n\t"
"buffer_load_dwordx4 v2, s[12:15], s41 offen offset:0 lds\n\t"
"v_mfma_f32_16x16x32_bf16 v[212:215], a[204:207], a[36:39], v[212:215]\n\t"
"ds_read_b64_tr_b16 a[194:195], v5 offset:37216\n\t"
"v_mfma_f32_16x16x32_bf16 v[196:199], a[200:203], a[36:39], v[196:199]\n\t"
"v_mfma_f32_16x16x32_bf16 v[200:203], a[200:203], a[40:43], v[200:203]\n\t"
"ds_read_b64_tr_b16 a[196:197], v5 offset:36968\n\t"
"v_mfma_f32_16x16x32_bf16 v[216:219], a[204:207], a[40:43], v[216:219]\n\t"
"ds_read_b64_tr_b16 a[198:199], v5 offset:37224\n\t"
"v_mfma_f32_16x16x32_bf16 v[220:223], a[204:207], a[44:47], v[220:223]\n\t"
"buffer_load_dwordx4 v8, s[12:15], s41 offen offset:1024 lds\n\t"
"v_mfma_f32_16x16x32_bf16 v[204:207], a[200:203], a[44:47], v[204:207]\n\t"
"s_waitcnt lgkmcnt(4)\n\t"
"v_mfma_f32_16x16x32_bf16 v[236:239], a[208:211], a[44:47], v[236:239]\n\t"
"ds_read_b64_tr_b16 a[200:201], v5 offset:37472\n\t"
"v_mfma_f32_16x16x32_bf16 v[252:255], a[212:215], a[44:47], v[252:255]\n\t"
"ds_read_b64_tr_b16 a[202:203], v5 offset:37728\n\t"
"v_mfma_f32_16x16x32_bf16 v[248:251], a[212:215], a[40:43], v[248:251]\n\t"
"v_mfma_f32_16x16x32_bf16 a[244:247], a[232:235], a[224:227], a[244:247]\n\t"
"v_mfma_f32_16x16x32_bf16 v[232:235], a[208:211], a[40:43], v[232:235]\n\t"
"buffer_load_dwordx4 v9, s[12:15], s41 offen offset:2048 lds\n\t"
"v_mfma_f32_16x16x32_bf16 v[228:231], a[208:211], a[36:39], v[228:231]\n\t"
"ds_read_b64_tr_b16 a[204:205], v5 offset:37480\n\t"
"s_add_u32 s41, s41, s56\n\t"
"v_mfma_f32_16x16x32_bf16 v[244:247], a[212:215], a[36:39], v[244:247]\n\t"
"s_cmp_eq_u32 s41, s79\n\t"
"ds_read_b64_tr_b16 a[206:207], v5 offset:37736\n\t"
"s_cselect_b32 s41, 0, s41\n\t"
"v_mfma_f32_16x16x32_bf16 v[240:243], a[212:215], a[32:35], v[240:243]\n\t"
"s_add_u32 m0, s51, 32768\n\t"
"s_add_u32 s47, s40, 0\n\t"
"v_mfma_f32_16x16x32_bf16 v[224:227], a[208:211], a[32:35], v[224:227]\n\t"
"s_waitcnt lgkmcnt(4)\n\t"
"buffer_load_dwordx4 v13, s[4:7], s47 offen offset:0 lds\n\t"
"v_mfma_f32_16x16x32_bf16 v[64:67], a[192:195], a[48:51], v[64:67]\n\t"
"ds_read_b64_tr_b16 a[208:209], v5 offset:37984\n\t"
"v_mfma_f32_16x16x32_bf16 v[80:83], a[196:199], a[48:51], v[80:83]\n\t"
"ds_read_b64_tr_b16 a[210:211], v5 offset:38240\n\t"
"v_mfma_f32_16x16x32_bf16 v[84:87], a[196:199], a[52:55], v[84:87]\n\t"
"ds_read_b64_tr_b16 a[248:249], v17 offset:36960\n\t"
"v_mfma_f32_16x16x32_bf16 v[68:71], a[192:195], a[52:55], v[68:71]\n\t"
"buffer_load_dwordx4 v14, s[4:7], s47 offen offset:1024 lds\n\t"
"ds_read_b64_tr_b16 a[250:251], v17 offset:37216\n\t"
"v_mfma_f32_16x16x32_bf16 v[72:75], a[192:195], a[56:59], v[72:75]\n\t"
"ds_read_b64_tr_b16 a[212:213], v5 offset:37992\n\t"
"v_mfma_f32_16x16x32_bf16 v[88:91], a[196:199], a[56:59], v[88:91]\n\t"
"ds_read_b64_tr_b16 a[214:215], v5 offset:38248\n\t"
"v_mfma_f32_16x16x32_bf16 v[92:95], a[196:199], a[60:63], v[92:95]\n\t"
"s_addk_i32 m0, 2048\n\t"
"s_add_u32 s47, s40, s98\n\t"
"v_mfma_f32_16x16x32_bf16 v[76:79], a[192:195], a[60:63], v[76:79]\n\t"
"s_waitcnt lgkmcnt(6)\n\t"
"buffer_load_dwordx4 v13, s[4:7], s47 offen offset:0 lds\n\t"
"v_mfma_f32_16x16x32_bf16 v[108:111], a[200:203], a[60:63], v[108:111]\n\t"
"ds_read_b64_tr_b16 a[192:193], v5 offset:38496\n\t"
"v_mfma_f32_16x16x32_bf16 v[124:127], a[204:207], a[60:63], v[124:127]\n\t"
"ds_read_b64_tr_b16 a[194:195], v5 offset:38752\n\t"
"v_mfma_f32_16x16x32_bf16 v[120:123], a[204:207], a[56:59], v[120:123]\n\t"
"s_waitcnt vmcnt(21)\n\t"
"ds_read_b128 a[64:67], v15 offset:0\n\t"
"v_mfma_f32_16x16x32_bf16 v[104:107], a[200:203], a[56:59], v[104:107]\n\t"
"buffer_load_dwordx4 v14, s[4:7], s47 offen offset:1024 lds\n\t"
"ds_read_b128 a[80:83], v16 offset:0\n\t"
"v_mfma_f32_16x16x32_bf16 v[100:103], a[200:203], a[52:55], v[100:103]\n\t"
"ds_read_b64_tr_b16 a[196:197], v5 offset:38504\n\t"
"v_mfma_f32_16x16x32_bf16 v[116:119], a[204:207], a[52:55], v[116:119]\n\t"
"ds_read_b64_tr_b16 a[198:199], v5 offset:38760\n\t"
"v_mfma_f32_16x16x32_bf16 v[112:115], a[204:207], a[48:51], v[112:115]\n\t"
"s_addk_i32 m0, 2048\n\t"
"s_add_u32 s47, s40, s99\n\t"
"ds_read_b64_tr_b16 a[252:253], v18 offset:36960\n\t"
"v_mfma_f32_16x16x32_bf16 v[96:99], a[200:203], a[48:51], v[96:99]\n\t"
"ds_read_b64_tr_b16 a[254:255], v18 offset:37216\n\t"
"s_waitcnt lgkmcnt(8)\n\t"
"v_mfma_f32_16x16x32_bf16 v[128:131], a[208:211], a[48:51], v[128:131]\n\t"
"ds_read_b64_tr_b16 a[200:201], v5 offset:39008\n\t"
"v_mfma_f32_16x16x32_bf16 v[144:147], a[212:215], a[48:51], v[144:147]\n\t"
"ds_read_b64_tr_b16 a[202:203], v5 offset:39264\n\t"
"v_mfma_f32_16x16x32_bf16 v[148:151], a[212:215], a[52:55], v[148:151]\n\t"
"s_waitcnt vmcnt(20)\n\t"
"ds_read_b128 a[68:71], v15 offset:2048\n\t"
"v_mfma_f32_16x16x32_bf16 a[236:239], a[232:235], a[248:251], a[236:239]\n\t"
"buffer_load_dwordx4 v13, s[4:7], s47 offen offset:0 lds\n\t"
"v_mfma_f32_16x16x32_bf16 v[132:135], a[208:211], a[52:55], v[132:135]\n\t"
"buffer_load_dwordx4 v14, s[4:7], s47 offen offset:1024 lds\n\t"
"ds_read_b128 a[84:87], v16 offset:2048\n\t"
"v_mfma_f32_16x16x32_bf16 v[136:139], a[208:211], a[56:59], v[136:139]\n\t"
"ds_read_b64_tr_b16 a[204:205], v5 offset:39016\n\t"
"v_mfma_f32_16x16x32_bf16 v[152:155], a[212:215], a[56:59], v[152:155]\n\t"
"ds_read_b64_tr_b16 a[206:207], v5 offset:39272\n\t"
"v_mfma_f32_16x16x32_bf16 v[156:159], a[212:215], a[60:63], v[156:159]\n\t"
"s_waitcnt vmcnt(20)\n\t"
"ds_read_b128 a[72:75], v15 offset:4096\n\t"
"buffer_load_dwordx2 v[36:37], v38, s[8:11], 0 offen offset:0 sc1\n\t"
"v_mfma_f32_16x16x32_bf16 v[140:143], a[208:211], a[60:63], v[140:143]\n\t"
"ds_read_b128 a[88:91], v16 offset:4096\n\t"
"s_waitcnt lgkmcnt(8)\n\t"
"v_mfma_f32_16x16x32_bf16 v[172:175], a[192:195], a[60:63], v[172:175]\n\t"
"ds_read_b64_tr_b16 a[208:209], v5 offset:39520\n\t"
"v_mfma_f32_16x16x32_bf16 v[188:191], a[196:199], a[60:63], v[188:191]\n\t"
"buffer_load_dwordx4 v[48:51], v39, s[8:11], 0 offen offset:0 sc1\n\t"
"v_mfma_f32_16x16x32_bf16 v[184:187], a[196:199], a[56:59], v[184:187]\n\t"
"ds_read_b64_tr_b16 a[210:211], v5 offset:39776\n\t"
"v_mfma_f32_16x16x32_bf16 a[240:243], a[232:235], a[252:255], a[240:243]\n\t"
"ds_read_b64_tr_b16 a[224:225], v19 offset:36960\n\t"
"v_mfma_f32_16x16x32_bf16 v[168:171], a[192:195], a[56:59], v[168:171]\n\t"
"ds_read_b64_tr_b16 a[226:227], v19 offset:37216\n\t"
"ds_read_b64_tr_b16 a[212:213], v5 offset:39528\n\t"
"v_mfma_f32_16x16x32_bf16 v[164:167], a[192:195], a[52:55], v[164:167]\n\t"
"s_waitcnt vmcnt(17)\n\t"
"s_barrier\n\t"
"v_mfma_f32_16x16x32_bf16 v[180:183], a[196:199], a[52:55], v[180:183]\n\t"
"s_add_u32 m0, s42, 24640\n\t"
"ds_read_b64_tr_b16 a[214:215], v5 offset:39784\n\t"
"buffer_load_dwordx4 v2, s[12:15], s41 offen offset:0 lds\n\t"
"v_mfma_f32_16x16x32_bf16 v[176:179], a[196:199], a[48:51], v[176:179]\n\t"
"ds_read_b128 a[76:79], v15 offset:6144\n\t"
"v_mfma_f32_16x16x32_bf16 v[160:163], a[192:195], a[48:51], v[160:163]\n\t"
"ds_read_b128 a[92:95], v16 offset:6144\n\t"
"s_waitcnt lgkmcnt(8)\n\t"
"v_mfma_f32_16x16x32_bf16 v[192:195], a[200:203], a[48:51], v[192:195]\n\t"
"ds_read_b64_tr_b16 a[192:193], v5 offset:49280\n\t"
"v_mfma_f32_16x16x32_bf16 v[208:211], a[204:207], a[48:51], v[208:211]\n\t"
"ds_read_b64_tr_b16 a[194:195], v5 offset:49536\n\t"
"v_mfma_f32_16x16x32_bf16 v[212:215], a[204:207], a[52:55], v[212:215]\n\t"
"buffer_load_dwordx4 v8, s[12:15], s41 offen offset:1024 lds\n\t"
"v_mfma_f32_16x16x32_bf16 v[196:199], a[200:203], a[52:55], v[196:199]\n\t"
"v_mfma_f32_16x16x32_bf16 v[200:203], a[200:203], a[56:59], v[200:203]\n\t"
"ds_read_b64_tr_b16 a[196:197], v5 offset:49288\n\t"
"v_lshrrev_b32 v17, 1, v1\n\t"
"v_mfma_f32_16x16x32_bf16 v[216:219], a[204:207], a[56:59], v[216:219]\n\t"
"ds_read_b64_tr_b16 a[198:199], v5 offset:49544\n\t"
"v_mov_b32 v19, 1073741824\n\t"
"v_mfma_f32_16x16x32_bf16 v[220:223], a[204:207], a[60:63], v[220:223]\n\t"
"v_accvgpr_read_b32 v18, a236\n\t"
"v_mfma_f32_16x16x32_bf16 v[204:207], a[200:203], a[60:63], v[204:207]\n\t"
"s_waitcnt lgkmcnt(4)\n\t"
"buffer_load_dwordx4 v9, s[12:15], s41 offen offset:2048 lds\n\t"
"v_mfma_f32_16x16x32_bf16 v[236:239], a[208:211], a[60:63], v[236:239]\n\t"
"s_add_u32 s41, s41, s56\n\t"
"ds_read_b64_tr_b16 a[200:201], v5 offset:49792\n\t"
"v_mov_b32_dpp v17, v19 quad_perm:[0,1,2,3] row_mask:0x8 bank_mask:0xf\n\t"
"v_mfma_f32_16x16x32_bf16 v[252:255], a[212:215], a[60:63], v[252:255]\n\t"
"ds_read_b64_tr_b16 a[202:203], v5 offset:50048\n\t"
"v_accvgpr_read_b32 v19, a240\n\t"
"v_mfma_f32_16x16x32_bf16 v[248:251], a[212:215], a[56:59], v[248:251]\n\t"
"s_add_u32 m0, s51, 38912\n\t"
"s_add_u32 s47, s47, s98\n\t"
"v_mfma_f32_16x16x32_bf16 a[244:247], a[232:235], a[224:227], a[244:247]\n\t"
"buffer_load_dwordx4 v13, s[4:7], s47 offen offset:0 lds\n\t"
"v_mfma_f32_16x16x32_bf16 v[232:235], a[208:211], a[56:59], v[232:235]\n\t"
"v_mov_b32_dpp v18, v19 quad_perm:[0,1,2,3] row_mask:0x2 bank_mask:0xf\n\t"
"v_mfma_f32_16x16x32_bf16 v[228:231], a[208:211], a[52:55], v[228:231]\n\t"
"ds_read_b64_tr_b16 a[204:205], v5 offset:49800\n\t"
"v_mfma_f32_16x16x32_bf16 v[244:247], a[212:215], a[52:55], v[244:247]\n\t"
"ds_read_b64_tr_b16 a[206:207], v5 offset:50056\n\t"
"v_mfma_f32_16x16x32_bf16 v[240:243], a[212:215], a[48:51], v[240:243]\n\t"
"v_mfma_f32_16x16x32_bf16 v[224:227], a[208:211], a[48:51], v[224:227]\n\t"
"s_waitcnt lgkmcnt(4)\n\t"
"buffer_load_dwordx4 v14, s[4:7], s47 offen offset:1024 lds\n\t"
"v_mfma_f32_16x16x32_bf16 v[64:67], a[192:195], a[64:67], v[64:67]\n\t"
"ds_read_b64_tr_b16 a[208:209], v5 offset:50304\n\t"
"s_addk_i32 s40, 128\n\t"
"v_mfma_f32_16x16x32_bf16 v[80:83], a[196:199], a[64:67], v[80:83]\n\t"
"s_cmp_eq_u32 s40, s48\n\t"
"ds_read_b64_tr_b16 a[210:211], v5 offset:50560\n\t"
"s_cselect_b32 s40, s76, s40\n\t"
"v_mfma_f32_16x16x32_bf16 v[84:87], a[196:199], a[68:71], v[84:87]\n\t"
"s_add_u32 m0, s51, 0\n\t"
"s_add_u32 s47, s40, 0\n\t"
"v_accvgpr_read_b32 v19, a244\n\t"
"v_mfma_f32_16x16x32_bf16 v[68:71], a[192:195], a[68:71], v[68:71]\n\t"
"buffer_load_dwordx4 v13, s[4:7], s47 offen offset:0 lds\n\t"
"v_mfma_f32_16x16x32_bf16 v[72:75], a[192:195], a[72:75], v[72:75]\n\t"
"ds_read_b64_tr_b16 a[212:213], v5 offset:50312\n\t"
"v_mfma_f32_16x16x32_bf16 v[88:91], a[196:199], a[72:75], v[88:91]\n\t"
"ds_read_b64_tr_b16 a[214:215], v5 offset:50568\n\t"
"v_mov_b32_dpp v18, v19 quad_perm:[0,1,2,3] row_mask:0x4 bank_mask:0xf\n\t"
"v_mfma_f32_16x16x32_bf16 v[92:95], a[196:199], a[76:79], v[92:95]\n\t"
"v_mov_b32 v19, s39\n\t"
"v_mfma_f32_16x16x32_bf16 v[76:79], a[192:195], a[76:79], v[76:79]\n\t"
"s_waitcnt lgkmcnt(4)\n\t"
"buffer_load_dwordx4 v14, s[4:7], s47 offen offset:1024 lds\n\t"
"v_mfma_f32_16x16x32_bf16 v[108:111], a[200:203], a[76:79], v[108:111]\n\t"
"ds_read_b64_tr_b16 a[192:193], v5 offset:50816\n\t"
"v_mfma_f32_16x16x32_bf16 v[124:127], a[204:207], a[76:79], v[124:127]\n\t"
"ds_read_b64_tr_b16 a[194:195], v5 offset:51072\n\t"
"v_mfma_f32_16x16x32_bf16 v[120:123], a[204:207], a[72:75], v[120:123]\n\t"
"s_addk_i32 m0, 2048\n\t"
"s_add_u32 s47, s40, s98\n\t"
"ds_read_b64_tr_b16 a[196:197], v5 offset:50824\n\t"
"v_mfma_f32_16x16x32_bf16 v[104:107], a[200:203], a[72:75], v[104:107]\n\t"
"buffer_load_dwordx4 v13, s[4:7], s47 offen offset:0 lds\n\t"
"v_mfma_f32_16x16x32_bf16 v[100:103], a[200:203], a[68:71], v[100:103]\n\t"
"buffer_store_dwordx2 v[18:19], v17, s[8:11], s74 offen offset:0 sc1\n\t"
"v_mfma_f32_16x16x32_bf16 v[116:119], a[204:207], a[68:71], v[116:119]\n\t"
"ds_read_b64_tr_b16 a[198:199], v5 offset:51080\n\t"
"v_mfma_f32_16x16x32_bf16 v[112:115], a[204:207], a[64:67], v[112:115]\n\t"
"v_mfma_f32_16x16x32_bf16 v[96:99], a[200:203], a[64:67], v[96:99]\n\t"
"s_waitcnt lgkmcnt(4)\n\t"
"buffer_load_dwordx4 v14, s[4:7], s47 offen offset:1024 lds\n\t"
"v_mfma_f32_16x16x32_bf16 v[128:131], a[208:211], a[64:67], v[128:131]\n\t"
"ds_read_b64_tr_b16 a[200:201], v5 offset:51328\n\t"
"v_mfma_f32_16x16x32_bf16 v[144:147], a[212:215], a[64:67], v[144:147]\n\t"
"ds_read_b64_tr_b16 a[202:203], v5 offset:51584\n\t"
"v_mfma_f32_16x16x32_bf16 v[148:151], a[212:215], a[68:71], v[148:151]\n\t"
"s_addk_i32 m0, 2048\n\t"
"s_add_u32 s47, s40, s99\n\t"
"v_mfma_f32_16x16x32_bf16 v[132:135], a[208:211], a[68:71], v[132:135]\n\t"
"buffer_load_dwordx4 v13, s[4:7], s47 offen offset:0 lds\n\t"
"v_mfma_f32_16x16x32_bf16 v[136:139], a[208:211], a[72:75], v[136:139]\n\t"
"ds_read_b64_tr_b16 a[204:205], v5 offset:51336\n\t"
"v_mfma_f32_16x16x32_bf16 v[152:155], a[212:215], a[72:75], v[152:155]\n\t"
"ds_read_b64_tr_b16 a[206:207], v5 offset:51592\n\t"
"v_mfma_f32_16x16x32_bf16 v[156:159], a[212:215], a[76:79], v[156:159]\n\t"
"v_mfma_f32_16x16x32_bf16 v[140:143], a[208:211], a[76:79], v[140:143]\n\t"
"s_waitcnt lgkmcnt(4)\n\t"
"buffer_load_dwordx4 v14, s[4:7], s47 offen offset:1024 lds\n\t"
"v_mfma_f32_16x16x32_bf16 v[172:175], a[192:195], a[76:79], v[172:175]\n\t"
"ds_read_b64_tr_b16 a[208:209], v5 offset:51840\n\t"
"v_mfma_f32_16x16x32_bf16 v[188:191], a[196:199], a[76:79], v[188:191]\n\t"
"ds_read_b64_tr_b16 a[210:211], v5 offset:52096\n\t"
"v_mfma_f32_16x16x32_bf16 v[184:187], a[196:199], a[72:75], v[184:187]\n\t"
"buffer_load_dwordx4 v[52:55], v39, s[8:11], 0 offen offset:1024 sc1\n\t"
"v_mfma_f32_16x16x32_bf16 v[168:171], a[192:195], a[72:75], v[168:171]\n\t"
"ds_read_b64_tr_b16 a[212:213], v5 offset:51848\n\t"
"s_add_u32 m0, s42, 36960\n\t"
"v_mfma_f32_16x16x32_bf16 v[164:167], a[192:195], a[68:71], v[164:167]\n\t"
"s_waitcnt vmcnt(27)\n\t"
"s_barrier\n\t"
"v_mfma_f32_16x16x32_bf16 v[180:183], a[196:199], a[68:71], v[180:183]\n\t"
"buffer_load_dwordx4 v2, s[12:15], s41 offen offset:0 lds\n\t"
"v_mfma_f32_16x16x32_bf16 v[176:179], a[196:199], a[64:67], v[176:179]\n\t"
"ds_read_b64_tr_b16 a[214:215], v5 offset:52104\n\t"
"v_mfma_f32_16x16x32_bf16 v[160:163], a[192:195], a[64:67], v[160:163]\n\t"
"s_waitcnt lgkmcnt(4)\n\t"
"v_mfma_f32_16x16x32_bf16 v[192:195], a[200:203], a[64:67], v[192:195]\n\t"
"ds_read_b64_tr_b16 a[192:193], v5 offset:61600\n\t"
"v_mfma_f32_16x16x32_bf16 v[208:211], a[204:207], a[64:67], v[208:211]\n\t"
"ds_read_b64_tr_b16 a[194:195], v5 offset:61856\n\t"
"v_mfma_f32_16x16x32_bf16 v[212:215], a[204:207], a[68:71], v[212:215]\n\t"
"buffer_load_dwordx4 v8, s[12:15], s41 offen offset:1024 lds\n\t"
"v_mfma_f32_16x16x32_bf16 v[196:199], a[200:203], a[68:71], v[196:199]\n\t"
"v_mfma_f32_16x16x32_bf16 v[200:203], a[200:203], a[72:75], v[200:203]\n\t"
"ds_read_b64_tr_b16 a[196:197], v5 offset:61608\n\t"
"v_mfma_f32_16x16x32_bf16 v[216:219], a[204:207], a[72:75], v[216:219]\n\t"
"ds_read_b64_tr_b16 a[198:199], v5 offset:61864\n\t"
"v_mfma_f32_16x16x32_bf16 v[220:223], a[204:207], a[76:79], v[220:223]\n\t"
"v_mfma_f32_16x16x32_bf16 v[204:207], a[200:203], a[76:79], v[204:207]\n\t"
"s_waitcnt lgkmcnt(4)\n\t"
"buffer_load_dwordx4 v9, s[12:15], s41 offen offset:2048 lds\n\t"
"v_mfma_f32_16x16x32_bf16 v[236:239], a[208:211], a[76:79], v[236:239]\n\t"
"ds_read_b64_tr_b16 a[200:201], v5 offset:62112\n\t"
"s_add_u32 s41, s41, s56\n\t"
"v_mfma_f32_16x16x32_bf16 v[252:255], a[212:215], a[76:79], v[252:255]\n\t"
"s_cmp_eq_u32 s41, s79\n\t"
"ds_read_b64_tr_b16 a[202:203], v5 offset:62368\n\t"
"s_cselect_b32 s41, 0, s41\n\t"
"v_mfma_f32_16x16x32_bf16 v[248:251], a[212:215], a[72:75], v[248:251]\n\t"
"s_add_u32 m0, s51, 6144\n\t"
"s_add_u32 s47, s47, s98\n\t"
"v_mfma_f32_16x16x32_bf16 v[232:235], a[208:211], a[72:75], v[232:235]\n\t"
"buffer_load_dwordx4 v13, s[4:7], s47 offen offset:0 lds\n\t"
"v_mfma_f32_16x16x32_bf16 v[228:231], a[208:211], a[68:71], v[228:231]\n\t"
"ds_read_b64_tr_b16 a[204:205], v5 offset:62120\n\t"
"v_mfma_f32_16x16x32_bf16 v[244:247], a[212:215], a[68:71], v[244:247]\n\t"
"ds_read_b64_tr_b16 a[206:207], v5 offset:62376\n\t"
"v_mfma_f32_16x16x32_bf16 v[240:243], a[212:215], a[64:67], v[240:243]\n\t"
"v_mfma_f32_16x16x32_bf16 v[224:227], a[208:211], a[64:67], v[224:227]\n\t"
"s_waitcnt lgkmcnt(4)\n\t"
"buffer_load_dwordx4 v14, s[4:7], s47 offen offset:1024 lds\n\t"
"v_mfma_f32_16x16x32_bf16 v[64:67], a[192:195], a[80:83], v[64:67]\n\t"
"ds_read_b64_tr_b16 a[208:209], v5 offset:62624\n\t"
"s_addk_i32 s40, 128\n\t"
"v_mfma_f32_16x16x32_bf16 v[80:83], a[196:199], a[80:83], v[80:83]\n\t"
"s_cmp_eq_u32 s40, s48\n\t"
"ds_read_b64_tr_b16 a[210:211], v5 offset:62880\n\t"
"s_cselect_b32 s40, s76, s40\n\t"
"v_mfma_f32_16x16x32_bf16 v[84:87], a[196:199], a[84:87], v[84:87]\n\t"
"s_waitcnt vmcnt(24)\n\t"
"ds_read_b128 a[0:3], v15 offset:32768\n\t"
"v_mfma_f32_16x16x32_bf16 v[68:71], a[192:195], a[84:87], v[68:71]\n\t"
"ds_read_b128 a[16:19], v16 offset:32768\n\t"
"v_mfma_f32_16x16x32_bf16 v[72:75], a[192:195], a[88:91], v[72:75]\n\t"
"ds_read_b64_tr_b16 a[212:213], v5 offset:62632\n\t"
"v_mfma_f32_16x16x32_bf16 v[88:91], a[196:199], a[88:91], v[88:91]\n\t"
"ds_read_b64_tr_b16 a[214:215], v5 offset:62888\n\t"
"v_mfma_f32_16x16x32_bf16 v[92:95], a[196:199], a[92:95], v[92:95]\n\t"
"s_barrier\n\t"
"s_waitcnt vmcnt(22)\n\t"
"ds_read_b128 a[4:7], v15 offset:34816\n\t"
"v_mfma_f32_16x16x32_bf16 v[76:79], a[192:195], a[92:95], v[76:79]\n\t"
"ds_read_b128 a[20:23], v16 offset:34816\n\t"
"s_waitcnt lgkmcnt(8)\n\t"
"v_mfma_f32_16x16x32_bf16 v[108:111], a[200:203], a[92:95], v[108:111]\n\t"
"s_add_u32 m0, s42, 49280\n\t"
"ds_read_b64_tr_b16 a[192:193], v5 offset:63136\n\t"
"v_mfma_f32_16x16x32_bf16 v[124:127], a[204:207], a[92:95], v[124:127]\n\t"
"ds_read_b64_tr_b16 a[194:195], v5 offset:63392\n\t"
"v_mfma_f32_16x16x32_bf16 v[120:123], a[204:207], a[88:91], v[120:123]\n\t"
"buffer_load_dwordx4 v2, s[12:15], s41 offen offset:0 lds\n\t"
"v_mfma_f32_16x16x32_bf16 v[104:107], a[200:203], a[88:91], v[104:107]\n\t"
"v_lshrrev_b32 v30, 4, %[vT]\n\t"
"v_xor_b32 v30, v30, %[vT]\n\t"
"v_mfma_f32_16x16x32_bf16 v[100:103], a[200:203], a[84:87], v[100:103]\n\t"
"ds_read_b64_tr_b16 a[196:197], v5 offset:63144\n\t"
"buffer_load_dwordx4 v8, s[12:15], s41 offen offset:1024 lds\n\t"
"v_mfma_f32_16x16x32_bf16 v[116:119], a[204:207], a[84:87], v[116:119]\n\t"
"ds_read_b64_tr_b16 a[198:199], v5 offset:63400\n\t"
"v_not_b32 v30, v30\n\t"
"v_mfma_f32_16x16x32_bf16 v[112:115], a[204:207], a[80:83], v[112:115]\n\t"
"s_waitcnt vmcnt(22)\n\t"
"ds_read_b128 a[8:11], v15 offset:36864\n\t"
"v_bfe_i32 v30, v30, 1, 1\n\t"
"v_mfma_f32_16x16x32_bf16 v[96:99], a[200:203], a[80:83], v[96:99]\n\t"
"s_waitcnt lgkmcnt(5)\n\t"
"ds_read_b128 a[24:27], v16 offset:36864\n\t"
"v_and_b32 v30, 1065369472, v30\n\t"
"v_mfma_f32_16x16x32_bf16 v[128:131], a[208:211], a[80:83], v[128:131]\n\t"
"ds_read_b64_tr_b16 a[200:201], v5 offset:63648\n\t"
"v_accvgpr_write_b32 a244, v30\n\t"
"v_mfma_f32_16x16x32_bf16 v[144:147], a[212:215], a[80:83], v[144:147]\n\t"
"ds_read_b64_tr_b16 a[202:203], v5 offset:63904\n\t"
"buffer_load_dwordx4 v9, s[12:15], s41 offen offset:2048 lds\n\t"
"v_mfma_f32_16x16x32_bf16 v[148:151], a[212:215], a[84:87], v[148:151]\n\t"
"s_add_u32 s41, s41, s56\n\t"
"s_waitcnt vmcnt(16)\n\t"
"ds_read_b128 a[12:15], v15 offset:38912\n\t"
"v_mfma_f32_16x16x32_bf16 v[132:135], a[208:211], a[84:87], v[132:135]\n\t"
"s_add_u32 m0, s51, 32768\n\t"
"ds_read_b128 a[28:31], v16 offset:38912\n\t"
"v_accvgpr_write_b32 a245, v30\n\t"
"v_mfma_f32_16x16x32_bf16 v[136:139], a[208:211], a[88:91], v[136:139]\n\t"
"s_add_u32 s47, s40, 0\n\t"
"ds_read_b64_tr_b16 a[204:205], v5 offset:63656\n\t"
"buffer_load_dwordx4 v13, s[4:7], s47 offen offset:0 lds\n\t"
"v_mfma_f32_16x16x32_bf16 v[152:155], a[212:215], a[88:91], v[152:155]\n\t"
"s_getpc_b64 %[karg]\n\t"
"ds_read_b64_tr_b16 a[206:207], v5 offset:63912\n\t"
"v_accvgpr_write_b32 a246, v30\n\t"
".Lpn%=:\n"
"v_mfma_f32_16x16x32_bf16 v[156:159], a[212:215], a[92:95], v[156:159]\n\t"
"s_lshr_b32 s85, %[sB], 3\n\t"
"v_accvgpr_write_b32 a247, v30\n\t"
"v_and_b32 v30, 1, %[vT]\n\t"
"v_mfma_f32_16x16x32_bf16 v[140:143], a[208:211], a[92:95], v[140:143]\n\t"
"s_waitcnt lgkmcnt(6)\n\t"
"v_mul_u32_u24 v30, 43690, v30\n\t"
"v_add_u32 v30, 17476, v30\n\t"
"v_mfma_f32_16x16x32_bf16 v[172:175], a[192:195], a[92:95], v[172:175]\n\t"
"s_lshl2_add_u32 s85, s85, s38\n\t"
"ds_read_b64_tr_b16 a[208:209], v5 offset:64160\n\t"
"s_lshl_b32 s85, s85, 7\n\t"
"v_mfma_f32_16x16x32_bf16 v[188:191], a[196:199], a[92:95], v[188:191]\n\t"
"buffer_load_dwordx4 v14, s[4:7], s47 offen offset:1024 lds\n\t"
"v_mfma_f32_16x16x32_bf16 v[184:187], a[196:199], a[88:91], v[184:187]\n\t"
"ds_read_b64_tr_b16 a[210:211], v5 offset:64416\n\t"
"v_mfma_f32_16x16x32_bf16 v[168:171], a[192:195], a[88:91], v[168:171]\n\t"
"v_mfma_f32_16x16x32_bf16 v[164:167], a[192:195], a[84:87], v[164:167]\n\t"
"s_addk_i32 m0, 2048\n\t"
"ds_read_b64_tr_b16 a[212:213], v5 offset:64168\n\t"
"s_add_u32 s47, s40, s98\n\t"
"v_mfma_f32_16x16x32_bf16 v[180:183], a[196:199], a[84:87], v[180:183]\n\t"
"ds_read_b64_tr_b16 a[214:215], v5 offset:64424\n\t"
"v_mfma_f32_16x16x32_bf16 v[176:179], a[196:199], a[80:83], v[176:179]\n\t"
"buffer_load_dwordx4 v13, s[4:7], s47 offen offset:0 lds\n\t"
".Lpg2%=:\n"
"s_cbranch_execz .Lpg6%=\n\t"
"v_mfma_f32_16x16x32_bf16 v[160:163], a[192:195], a[80:83], v[160:163]\n\t"
"s_waitcnt lgkmcnt(4)\n\t"
"v_mfma_f32_16x16x32_bf16 v[192:195], a[200:203], a[80:83], v[192:195]\n\t"
"ds_read_b64_tr_b16 a[192:193], v5 offset:0\n\t"
"v_mfma_f32_16x16x32_bf16 v[208:211], a[204:207], a[80:83], v[208:211]\n\t"
"buffer_load_dwordx4 v14, s[4:7], s47 offen offset:1024 lds\n\t"
"v_mfma_f32_16x16x32_bf16 v[212:215], a[204:207], a[84:87], v[212:215]\n\t"
"ds_read_b64_tr_b16 a[194:195], v5 offset:256\n\t"
"v_mfma_f32_16x16x32_bf16 v[196:199], a[200:203], a[84:87], v[196:199]\n\t"
"v_mfma_f32_16x16x32_bf16 v[200:203], a[200:203], a[88:91], v[200:203]\n\t"
"s_addk_i32 m0, 2048\n\t"
"ds_read_b64_tr_b16 a[196:197], v5 offset:8\n\t"
"s_add_u32 s47, s40, s99\n\t"
"v_mfma_f32_16x16x32_bf16 v[216:219], a[204:207], a[88:91], v[216:219]\n\t"
"ds_read_b64_tr_b16 a[198:199], v5 offset:264\n\t"
"v_mfma_f32_16x16x32_bf16 v[220:223], a[204:207], a[92:95], v[220:223]\n\t"
"buffer_load_dwordx4 v13, s[4:7], s47 offen offset:0 lds\n\t"
"v_mfma_f32_16x16x32_bf16 v[204:207], a[200:203], a[92:95], v[204:207]\n\t"
"s_waitcnt lgkmcnt(4)\n\t"
"v_mfma_f32_16x16x32_bf16 v[236:239], a[208:211], a[92:95], v[236:239]\n\t"
"ds_read_b64_tr_b16 a[200:201], v5 offset:512\n\t"
"v_mfma_f32_16x16x32_bf16 v[252:255], a[212:215], a[92:95], v[252:255]\n\t"
"buffer_load_dwordx4 v14, s[4:7], s47 offen offset:1024 lds\n\t"
"v_mfma_f32_16x16x32_bf16 v[248:251], a[212:215], a[88:91], v[248:251]\n\t"
"ds_read_b64_tr_b16 a[202:203], v5 offset:768\n\t"
"v_mfma_f32_16x16x32_bf16 v[232:235], a[208:211], a[88:91], v[232:235]\n\t"
"v_mfma_f32_16x16x32_bf16 v[228:231], a[208:211], a[84:87], v[228:231]\n\t"
"s_addk_i32 m0, 2048\n\t"
"ds_read_b64_tr_b16 a[204:205], v5 offset:520\n\t"
"s_add_u32 s47, s47, s98\n\t"
"v_mfma_f32_16x16x32_bf16 v[244:247], a[212:215], a[84:87], v[244:247]\n\t"
"ds_read_b64_tr_b16 a[206:207], v5 offset:776\n\t"
"v_mfma_f32_16x16x32_bf16 v[240:243], a[212:215], a[80:83], v[240:243]\n\t"
"buffer_load_dwordx4 v13, s[4:7], s47 offen offset:0 lds\n\t"
"v_mfma_f32_16x16x32_bf16 v[224:227], a[208:211], a[80:83], v[224:227]\n\t"
"s_waitcnt lgkmcnt(4)\n\t"
"v_mfma_f32_16x16x32_bf16 v[64:67], a[192:195], a[0:3], v[64:67]\n\t"
"ds_read_b64_tr_b16 a[208:209], v5 offset:1024\n\t"
"v_mfma_f32_16x16x32_bf16 v[80:83], a[196:199], a[0:3], v[80:83]\n\t"
"buffer_load_dwordx4 v14, s[4:7], s47 offen offset:1024 lds\n\t"
"v_mfma_f32_16x16x32_bf16 v[84:87], a[196:199], a[4:7], v[84:87]\n\t"
"s_addk_i32 s40, 128\n\t"
"s_cmp_eq_u32 s40, s48\n\t"
"s_cselect_b32 s40, s76, s40\n\t"
"v_mfma_f32_16x16x32_bf16 v[68:71], a[192:195], a[4:7], v[68:71]\n\t"
"ds_read_b64_tr_b16 a[210:211], v5 offset:1280\n\t"
"v_mfma_f32_16x16x32_bf16 v[72:75], a[192:195], a[8:11], v[72:75]\n\t"
"ds_read_b64_tr_b16 a[212:213], v5 offset:1032\n\t"
"buffer_load_dwordx4 a[224:227], v1, s[20:23], s79 offen offset:0\n\t"
"v_mfma_f32_16x16x32_bf16 v[88:91], a[196:199], a[8:11], v[88:91]\n\t"
"ds_read_b64_tr_b16 a[214:215], v5 offset:1288\n\t"
"v_mfma_f32_16x16x32_bf16 v[92:95], a[196:199], a[12:15], v[92:95]\n\t"
"s_barrier\n\t"
"v_mfma_f32_16x16x32_bf16 v[76:79], a[192:195], a[12:15], v[76:79]\n\t"
"s_add_u32 m0, s42, 61600\n\t"
"s_waitcnt lgkmcnt(4)\n\t"
"v_mfma_f32_16x16x32_bf16 v[108:111], a[200:203], a[12:15], v[108:111]\n\t"
"ds_read_b64_tr_b16 a[192:193], v5 offset:1536\n\t"
"v_mfma_f32_16x16x32_bf16 v[124:127], a[204:207], a[12:15], v[124:127]\n\t"
"ds_read_b64_tr_b16 a[194:195], v5 offset:1792\n\t"
"v_mfma_f32_16x16x32_bf16 v[120:123], a[204:207], a[8:11], v[120:123]\n\t"
"buffer_load_dwordx4 v2, s[12:15], s41 offen offset:0 lds\n\t"
"v_mfma_f32_16x16x32_bf16 v[104:107], a[200:203], a[8:11], v[104:107]\n\t"
"ds_read_b64_tr_b16 a[196:197], v5 offset:1544\n\t"
"v_mfma_f32_16x16x32_bf16 v[100:103], a[200:203], a[4:7], v[100:103]\n\t"
"buffer_load_dwordx4 v8, s[12:15], s41 offen offset:1024 lds\n\t"
"v_mfma_f32_16x16x32_bf16 v[116:119], a[204:207], a[4:7], v[116:119]\n\t"
"s_load_dword s3, %[karg], s85 offset:6808\n\t"
"ds_read_b64_tr_b16 a[198:199], v5 offset:1800\n\t"
"v_mfma_f32_16x16x32_bf16 v[112:115], a[204:207], a[0:3], v[112:115]\n\t"
"v_mfma_f32_16x16x32_bf16 v[96:99], a[200:203], a[0:3], v[96:99]\n\t"
"s_waitcnt lgkmcnt(4)\n\t"
"v_mfma_f32_16x16x32_bf16 v[128:131], a[208:211], a[0:3], v[128:131]\n\t"
"ds_read_b64_tr_b16 a[200:201], v5 offset:2048\n\t"
"v_mfma_f32_16x16x32_bf16 v[144:147], a[212:215], a[0:3], v[144:147]\n\t"
"buffer_load_dwordx4 v9, s[12:15], s41 offen offset:2048 lds\n\t"
"v_mfma_f32_16x16x32_bf16 v[148:151], a[212:215], a[4:7], v[148:151]\n\t"
"s_add_u32 s41, s41, s56\n\t"
"s_cmp_eq_u32 s41, s79\n\t"
"s_cselect_b32 s41, 0, s41\n\t"
"v_mfma_f32_16x16x32_bf16 v[132:135], a[208:211], a[4:7], v[132:135]\n\t"
"ds_read_b64_tr_b16 a[202:203], v5 offset:2304\n\t"
"v_mfma_f32_16x16x32_bf16 v[136:139], a[208:211], a[8:11], v[136:139]\n\t"
"ds_read_b64_tr_b16 a[204:205], v5 offset:2056\n\t"
"v_mfma_f32_16x16x32_bf16 v[152:155], a[212:215], a[8:11], v[152:155]\n\t"
"buffer_load_dwordx2 a[96:97], v42, s[8:11], 0 offen offset:0 sc1\n\t"
"v_mfma_f32_16x16x32_bf16 v[156:159], a[212:215], a[12:15], v[156:159]\n\t"
"ds_read_b64_tr_b16 a[206:207], v5 offset:2312\n\t"
"v_mfma_f32_16x16x32_bf16 v[140:143], a[208:211], a[12:15], v[140:143]\n\t"
"s_waitcnt lgkmcnt(4)\n\t"
"v_mfma_f32_16x16x32_bf16 v[172:175], a[192:195], a[12:15], v[172:175]\n\t"
"ds_read_b64_tr_b16 a[208:209], v5 offset:2560\n\t"
"v_mfma_f32_16x16x32_bf16 v[188:191], a[196:199], a[12:15], v[188:191]\n\t"
"buffer_load_dwordx2 a[98:99], v42, s[8:11], 0 offen offset:1536 sc1\n\t"
"v_mfma_f32_16x16x32_bf16 v[184:187], a[196:199], a[8:11], v[184:187]\n\t"
"ds_read_b64_tr_b16 a[210:211], v5 offset:2816\n\t"
"v_mfma_f32_16x16x32_bf16 v[168:171], a[192:195], a[8:11], v[168:171]\n\t"
"buffer_load_dwordx2 a[100:101], v42, s[8:11], 0 offen offset:3072 sc1\n\t"
"v_mfma_f32_16x16x32_bf16 v[164:167], a[192:195], a[4:7], v[164:167]\n\t"
"ds_read_b64_tr_b16 a[212:213], v5 offset:2568\n\t"
"v_mfma_f32_16x16x32_bf16 v[180:183], a[196:199], a[4:7], v[180:183]\n\t"
"ds_read_b64_tr_b16 a[214:215], v5 offset:2824\n\t"
"v_mfma_f32_16x16x32_bf16 v[176:179], a[196:199], a[0:3], v[176:179]\n\t"
"s_waitcnt vmcnt(23)\n\t"
"v_xor_b32 v49, s39, v49\n\t"
"v_xor_b32 v51, s39, v51\n\t"
"v_mfma_f32_16x16x32_bf16 v[160:163], a[192:195], a[0:3], v[160:163]\n\t"
"s_waitcnt lgkmcnt(4)\n\t"
"v_xor_b32 v53, s39, v53\n\t"
"v_xor_b32 v55, s39, v55\n\t"
"v_mfma_f32_16x16x32_bf16 v[192:195], a[200:203], a[0:3], v[192:195]\n\t"
"ds_read_b64_tr_b16 a[192:193], v5 offset:12320\n\t"
"v_xor_b32 v37, s39, v37\n\t"
"v_mfma_f32_16x16x32_bf16 v[208:211], a[204:207], a[0:3], v[208:211]\n\t"
"ds_read_b64_tr_b16 a[194:195], v5 offset:12576\n\t"
"buffer_load_dwordx2 a[102:103], v43, s[8:11], 0 offen offset:0 sc1\n\t"
"v_mfma_f32_16x16x32_bf16 v[212:215], a[204:207], a[4:7], v[212:215]\n\t"
"ds_read_b128 a[32:35], v15 offset:0\n\t"
"v_or3_b32 v49, v49, v51, v53\n\t"
"v_mfma_f32_16x16x32_bf16 v[196:199], a[200:203], a[4:7], v[196:199]\n\t"
"ds_read_b128 a[48:51], v16 offset:0\n\t"
"v_or3_b32 v49, v49, v55, v37\n\t"
"v_mfma_f32_16x16x32_bf16 v[200:203], a[200:203], a[8:11], v[200:203]\n\t"
"ds_read_b64_tr_b16 a[196:197], v5 offset:12328\n\t"
"v_cmp_ne_u32 vcc, 0, v49\n\t"
"s_cbranch_vccnz .LslK%=\n\t"
".LbkK%=:\n"
"v_mfma_f32_16x16x32_bf16 v[216:219], a[204:207], a[8:11], v[216:219]\n\t"
"ds_read_b64_tr_b16 a[198:199], v5 offset:12584\n\t"
"buffer_load_dwordx2 a[104:105], v43, s[8:11], 0 offen offset:1536 sc1\n\t"
"v_mfma_f32_16x16x32_bf16 v[220:223], a[204:207], a[12:15], v[220:223]\n\t"
"s_mov_b32 s80, 4294901760\n\t"
"v_and_or_b32 v49, v48, s80, 6\n\t"
"v_lshl_or_b32 v48, v48, 16, 7\n\t"
"v_mfma_f32_16x16x32_bf16 v[204:207], a[200:203], a[12:15], v[204:207]\n\t"
"s_waitcnt lgkmcnt(6)\n\t"
"v_and_or_b32 v51, v50, s80, 4\n\t"
"v_lshl_or_b32 v50, v50, 16, 5\n\t"
"v_mfma_f32_16x16x32_bf16 v[236:239], a[208:211], a[12:15], v[236:239]\n\t"
"ds_read_b64_tr_b16 a[200:201], v5 offset:12832\n\t"
"buffer_load_dwordx4 a[228:231], v1, s[20:23], s79 offen offset:0\n\t"
"v_and_or_b32 v53, v52, s80, 2\n\t"
"v_mfma_f32_16x16x32_bf16 v[252:255], a[212:215], a[12:15], v[252:255]\n\t"
"ds_read_b64_tr_b16 a[202:203], v5 offset:13088\n\t"
"v_lshl_or_b32 v52, v52, 16, 3\n\t"
"v_mfma_f32_16x16x32_bf16 v[248:251], a[212:215], a[8:11], v[248:251]\n\t"
"ds_read_b128 a[36:39], v15 offset:2048\n\t"
"v_and_or_b32 v55, v54, s80, 0\n\t"
"v_mfma_f32_16x16x32_bf16 v[232:235], a[208:211], a[8:11], v[232:235]\n\t"
"ds_read_b128 a[52:55], v16 offset:2048\n\t"
"v_lshl_or_b32 v54, v54, 16, 1\n\t"
"v_mfma_f32_16x16x32_bf16 v[228:231], a[208:211], a[4:7], v[228:231]\n\t"
"ds_read_b64_tr_b16 a[204:205], v5 offset:12840\n\t"
"v_max_u32 v56, v48, v49\n\t"
"v_mfma_f32_16x16x32_bf16 v[244:247], a[212:215], a[4:7], v[244:247]\n\t"
"ds_read_b64_tr_b16 a[206:207], v5 offset:13096\n\t"
"v_min_u32 v57, v48, v49\n\t"
"v_mfma_f32_16x16x32_bf16 v[240:243], a[212:215], a[0:3], v[240:243]\n\t"
"ds_read_b128 a[40:43], v15 offset:4096\n\t"
"v_med3_u32 v57, v56, v57, v50\n\t"
"v_mfma_f32_16x16x32_bf16 v[224:227], a[208:211], a[0:3], v[224:227]\n\t"
"s_waitcnt lgkmcnt(7)\n\t"
"ds_read_b128 a[56:59], v16 offset:4096\n\t"
"v_max_u32 v56, v56, v50\n\t"
"v_mfma_f32_16x16x32_bf16 v[64:67], a[192:195], a[16:19], v[64:67]\n\t"
"ds_read_b64_tr_b16 a[208:209], v5 offset:13344\n\t"
"v_med3_u32 v57, v56, v57, v51\n\t"
"v_mfma_f32_16x16x32_bf16 v[80:83], a[196:199], a[16:19], v[80:83]\n\t"
"ds_read_b64_tr_b16 a[210:211], v5 offset:13600\n\t"
"v_max_u32 v56, v56, v51\n\t"
"v_mfma_f32_16x16x32_bf16 v[84:87], a[196:199], a[20:23], v[84:87]\n\t"
"v_med3_u32 v57, v56, v57, v52\n\t"
"v_max_u32 v56, v56, v52\n\t"
"v_mfma_f32_16x16x32_bf16 v[68:71], a[192:195], a[20:23], v[68:71]\n\t"
"v_med3_u32 v57, v56, v57, v53\n\t"
"v_max_u32 v56, v56, v53\n\t"
"v_mfma_f32_16x16x32_bf16 v[72:75], a[192:195], a[24:27], v[72:75]\n\t"
"s_add_u32 m0, s42, 0\n\t"
"ds_read_b64_tr_b16 a[212:213], v5 offset:13352\n\t"
"v_med3_u32 v57, v56, v57, v54\n\t"
"v_mfma_f32_16x16x32_bf16 v[88:91], a[196:199], a[24:27], v[88:91]\n\t"
"ds_read_b64_tr_b16 a[214:215], v5 offset:13608\n\t"
"v_max_u32 v56, v56, v54\n\t"
"v_mfma_f32_16x16x32_bf16 v[92:95], a[196:199], a[28:31], v[92:95]\n\t"
"s_barrier\n\t"
"s_waitcnt vmcnt(21)\n\t"
"ds_read_b128 a[44:47], v15 offset:6144\n\t"
"v_mfma_f32_16x16x32_bf16 v[76:79], a[192:195], a[28:31], v[76:79]\n\t"
"buffer_load_dwordx4 v2, s[12:15], s41 offen offset:0 lds\n\t"
"ds_read_b128 a[60:63], v16 offset:6144\n\t"
"s_waitcnt lgkmcnt(6)\n\t"
"v_mfma_f32_16x16x32_bf16 v[108:111], a[200:203], a[28:31], v[108:111]\n\t"
"ds_read_b64_tr_b16 a[192:193], v5 offset:13856\n\t"
"v_med3_u32 v57, v56, v57, v55\n\t"
"v_mfma_f32_16x16x32_bf16 v[124:127], a[204:207], a[28:31], v[124:127]\n\t"
"ds_read_b64_tr_b16 a[194:195], v5 offset:14112\n\t"
"v_max_u32 v56, v56, v55\n\t"
"v_mfma_f32_16x16x32_bf16 v[120:123], a[204:207], a[24:27], v[120:123]\n\t"
"v_sub_u32 v58, 255, %[vT]\n\t"
"v_lshl_or_b32 v56, v58, 3, v56\n\t"
"v_mfma_f32_16x16x32_bf16 v[104:107], a[200:203], a[24:27], v[104:107]\n\t"
"v_lshl_or_b32 v57, v58, 3, v57\n\t"
"v_mov_b32 v58, v56\n\t"
"v_mfma_f32_16x16x32_bf16 v[100:103], a[200:203], a[20:23], v[100:103]\n\t"
"ds_read_b64_tr_b16 a[196:197], v5 offset:13864\n\t"
"buffer_load_dwordx4 v8, s[12:15], s41 offen offset:1024 lds\n\t"
"v_mfma_f32_16x16x32_bf16 v[116:119], a[204:207], a[20:23], v[116:119]\n\t"
"ds_read_b64_tr_b16 a[198:199], v5 offset:14120\n\t"
"v_max_u32_dpp v58, v58, v58 row_shr:1 row_mask:0xf bank_mask:0xf\n\t"
"v_mfma_f32_16x16x32_bf16 v[112:115], a[204:207], a[16:19], v[112:115]\n\t"
"v_and_b32 v46, 48, %[vT]\n\t"
"v_add_u32 v46, 74944, v46\n\t"
"v_mfma_f32_16x16x32_bf16 v[96:99], a[200:203], a[16:19], v[96:99]\n\t"
"v_max_u32_dpp v58, v58, v58 row_shr:2 row_mask:0xf bank_mask:0xf\n\t"
"s_waitcnt lgkmcnt(4)\n\t"
"v_mfma_f32_16x16x32_bf16 v[128:131], a[208:211], a[16:19], v[128:131]\n\t"
"ds_read_b64_tr_b16 a[200:201], v5 offset:14368\n\t"
"v_mfma_f32_16x16x32_bf16 v[144:147], a[212:215], a[16:19], v[144:147]\n\t"
"buffer_load_dwordx4 v9, s[12:15], s41 offen offset:2048 lds\n\t"
"v_mfma_f32_16x16x32_bf16 v[148:151], a[212:215], a[20:23], v[148:151]\n\t"
"s_add_u32 s41, s41, s56\n\t"
"ds_read_b64_tr_b16 a[202:203], v5 offset:14624\n\t"
"v_max_u32_dpp v58, v58, v58 row_shr:4 row_mask:0xf bank_mask:0xf\n\t"
"v_mfma_f32_16x16x32_bf16 v[132:135], a[208:211], a[20:23], v[132:135]\n\t"
"v_mfma_f32_16x16x32_bf16 v[136:139], a[208:211], a[24:27], v[136:139]\n\t"
"s_add_u32 m0, s51, 0\n\t"
"ds_read_b64_tr_b16 a[204:205], v5 offset:14376\n\t"
"s_add_u32 s47, s40, 0\n\t"
"v_mfma_f32_16x16x32_bf16 v[152:155], a[212:215], a[24:27], v[152:155]\n\t"
"ds_read_b64_tr_b16 a[206:207], v5 offset:14632\n\t"
"v_max_u32_dpp v58, v58, v58 row_shr:8 row_mask:0xf bank_mask:0xf\n\t"
"v_mfma_f32_16x16x32_bf16 v[156:159], a[212:215], a[28:31], v[156:159]\n\t"
"buffer_load_dwordx4 v13, s[4:7], s47 offen offset:0 lds\n\t"
"v_mfma_f32_16x16x32_bf16 v[140:143], a[208:211], a[28:31], v[140:143]\n\t"
"v_max_u32_dpp v58, v58, v58 row_bcast:15 row_mask:0xa bank_mask:0xf\n\t"
"s_waitcnt lgkmcnt(4)\n\t"
"v_mfma_f32_16x16x32_bf16 v[172:175], a[192:195], a[28:31], v[172:175]\n\t"
"ds_read_b64_tr_b16 a[208:209], v5 offset:14880\n\t"
"v_mfma_f32_16x16x32_bf16 v[188:191], a[196:199], a[28:31], v[188:191]\n\t"
"buffer_load_dwordx4 v14, s[4:7], s47 offen offset:1024 lds\n\t"
"v_mfma_f32_16x16x32_bf16 v[184:187], a[196:199], a[24:27], v[184:187]\n\t"
"ds_read_b64_tr_b16 a[210:211], v5 offset:15136\n\t"
"v_max_u32_dpp v58, v58, v58 row_bcast:31 row_mask:0xc bank_mask:0xf\n\t"
"v_mfma_f32_16x16x32_bf16 v[168:171], a[192:195], a[24:27], v[168:171]\n\t"
"v_readlane_b32 s80, v58, 63\n\t"
"v_mfma_f32_16x16x32_bf16 v[164:167], a[192:195], a[20:23], v[164:167]\n\t"
"s_addk_i32 m0, 2048\n\t"
"ds_read_b64_tr_b16 a[212:213], v5 offset:14888\n\t"
"s_add_u32 s47, s40, s98\n\t"
"v_mfma_f32_16x16x32_bf16 v[180:183], a[196:199], a[20:23], v[180:183]\n\t"
"ds_read_b64_tr_b16 a[214:215], v5 offset:15144\n\t"
"v_cmp_eq_u32 vcc, s80, v56\n\t"
"v_mfma_f32_16x16x32_bf16 v[176:179], a[196:199], a[16:19], v[176:179]\n\t"
"s_lshl_b32 s81, s38, 3\n\t"
"v_mov_b32 v58, s80\n\t"
"v_cndmask_b32 v56, v56, v57, vcc\n\t"
"v_mfma_f32_16x16x32_bf16 v[160:163], a[192:195], a[16:19], v[160:163]\n\t"
"s_waitcnt lgkmcnt(4)\n\t"
"buffer_load_dwordx4 v13, s[4:7], s47 offen offset:0 lds\n\t"
"v_mfma_f32_16x16x32_bf16 v[192:195], a[200:203], a[16:19], v[192:195]\n\t"
"ds_read_b64_tr_b16 a[192:193], v5 offset:24640\n\t"
"v_max_u32_dpp v56, v56, v56 row_shr:1 row_mask:0xf bank_mask:0xf\n\t"
"v_mfma_f32_16x16x32_bf16 v[208:211], a[204:207], a[16:19], v[208:211]\n\t"
"buffer_load_dwordx4 v14, s[4:7], s47 offen offset:1024 lds\n\t"
"v_mfma_f32_16x16x32_bf16 v[212:215], a[204:207], a[20:23], v[212:215]\n\t"
"ds_read_b64_tr_b16 a[194:195], v5 offset:24896\n\t"
"v_max_u32_dpp v56, v56, v56 row_shr:2 row_mask:0xf bank_mask:0xf\n\t"
"v_mfma_f32_16x16x32_bf16 v[196:199], a[200:203], a[20:23], v[196:199]\n\t"
"v_mfma_f32_16x16x32_bf16 v[200:203], a[200:203], a[24:27], v[200:203]\n\t"
"s_addk_i32 m0, 2048\n\t"
"ds_read_b64_tr_b16 a[196:197], v5 offset:24648\n\t"
"s_add_u32 s47, s40, s99\n\t"
"v_mfma_f32_16x16x32_bf16 v[216:219], a[204:207], a[24:27], v[216:219]\n\t"
"ds_read_b64_tr_b16 a[198:199], v5 offset:24904\n\t"
"v_max_u32_dpp v56, v56, v56 row_shr:4 row_mask:0xf bank_mask:0xf\n\t"
"v_mfma_f32_16x16x32_bf16 v[220:223], a[204:207], a[28:31], v[220:223]\n\t"
"buffer_load_dwordx4 v13, s[4:7], s47 offen offset:0 lds\n\t"
"v_mfma_f32_16x16x32_bf16 v[204:207], a[200:203], a[28:31], v[204:207]\n\t"
"v_max_u32_dpp v56, v56, v56 row_shr:8 row_mask:0xf bank_mask:0xf\n\t"
"s_waitcnt lgkmcnt(4)\n\t"
"v_mfma_f32_16x16x32_bf16 v[236:239], a[208:211], a[28:31], v[236:239]\n\t"
"ds_read_b64_tr_b16 a[200:201], v5 offset:25152\n\t"
"v_mfma_f32_16x16x32_bf16 v[252:255], a[212:215], a[28:31], v[252:255]\n\t"
"buffer_load_dwordx4 v14, s[4:7], s47 offen offset:1024 lds\n\t"
"v_mfma_f32_16x16x32_bf16 v[248:251], a[212:215], a[24:27], v[248:251]\n\t"
"ds_read_b64_tr_b16 a[202:203], v5 offset:25408\n\t"
"v_max_u32_dpp v56, v56, v56 row_bcast:15 row_mask:0xa bank_mask:0xf\n\t"
"v_mfma_f32_16x16x32_bf16 v[232:235], a[208:211], a[24:27], v[232:235]\n\t"
"s_addk_i32 m0, 2048\n\t"
"v_mfma_f32_16x16x32_bf16 v[228:231], a[208:211], a[20:23], v[228:231]\n\t"
"s_add_u32 s47, s47, s98\n\t"
"ds_read_b64_tr_b16 a[204:205], v5 offset:25160\n\t"
"buffer_load_dwordx4 v13, s[4:7], s47 offen offset:0 lds\n\t"
"v_mfma_f32_16x16x32_bf16 v[244:247], a[212:215], a[20:23], v[244:247]\n\t"
"ds_read_b64_tr_b16 a[206:207], v5 offset:25416\n\t"
"v_max_u32_dpp v56, v56, v56 row_bcast:31 row_mask:0xc bank_mask:0xf\n\t"
"v_mfma_f32_16x16x32_bf16 v[240:243], a[212:215], a[16:19], v[240:243]\n\t"
"s_add_u32 s81, s81, 73920\n\t"
"v_readlane_b32 s80, v56, 63\n\t"
"v_mov_b32 v60, s81\n\t"
"v_mfma_f32_16x16x32_bf16 v[224:227], a[208:211], a[16:19], v[224:227]\n\t"
"v_mov_b32 v59, s80\n\t"
"ds_write_b64 v60, v[58:59] offset:0\n\t"
"s_waitcnt lgkmcnt(5)\n\t"
"v_mfma_f32_16x16x32_bf16 v[64:67], a[192:195], a[32:35], v[64:67]\n\t"
"ds_read_b64_tr_b16 a[208:209], v5 offset:25664\n\t"
"v_mov_b32 v60, 73920\n\t"
"v_mfma_f32_16x16x32_bf16 v[80:83], a[196:199], a[32:35], v[80:83]\n\t"
"buffer_load_dwordx4 v14, s[4:7], s47 offen offset:1024 lds\n\t"
"v_mfma_f32_16x16x32_bf16 v[84:87], a[196:199], a[36:39], v[84:87]\n\t"
"s_addk_i32 s40, 128\n\t"
"s_cmp_eq_u32 s40, s48\n\t"
"s_cselect_b32 s40, s76, s40\n\t"
"v_mfma_f32_16x16x32_bf16 v[68:71], a[192:195], a[36:39], v[68:71]\n\t"
"ds_read_b64_tr_b16 a[210:211], v5 offset:25920\n\t"
"v_mfma_f32_16x16x32_bf16 v[72:75], a[192:195], a[40:43], v[72:75]\n\t"
"ds_read_b64_tr_b16 a[212:213], v5 offset:25672\n\t"
"v_mfma_f32_16x16x32_bf16 v[88:91], a[196:199], a[40:43], v[88:91]\n\t"
"ds_read_b64_tr_b16 a[214:215], v5 offset:25928\n\t"
"s_add_u32 m0, s42, 12320\n\t"
"v_mfma_f32_16x16x32_bf16 v[92:95], a[196:199], a[44:47], v[92:95]\n\t"
"s_waitcnt vmcnt(34) lgkmcnt(4)\n\t"
"s_barrier\n\t"
"v_mfma_f32_16x16x32_bf16 v[76:79], a[192:195], a[44:47], v[76:79]\n\t"
"buffer_load_dwordx4 v2, s[12:15], s41 offen offset:0 lds\n\t"
"v_mfma_f32_16x16x32_bf16 v[108:111], a[200:203], a[44:47], v[108:111]\n\t"
"ds_read_b64_tr_b16 a[192:193], v5 offset:26176\n\t"
"ds_read_b128 v[48:51], v60 offset:0\n\t"
"v_mfma_f32_16x16x32_bf16 v[124:127], a[204:207], a[44:47], v[124:127]\n\t"
"ds_read_b64_tr_b16 a[194:195], v5 offset:26432\n\t"
"v_mfma_f32_16x16x32_bf16 v[120:123], a[204:207], a[40:43], v[120:123]\n\t"
"v_and_b32 v61, 1, %[vT]\n\t"
"v_cmp_eq_u32 s[68:69], 1, v61\n\t"
"v_mfma_f32_16x16x32_bf16 v[104:107], a[200:203], a[40:43], v[104:107]\n\t"
"ds_read_b128 v[52:55], v60 offset:16\n\t"
"v_mfma_f32_16x16x32_bf16 v[100:103], a[200:203], a[36:39], v[100:103]\n\t"
"ds_read_b64_tr_b16 a[196:197], v5 offset:26184\n\t"
"buffer_load_dwordx4 v8, s[12:15], s41 offen offset:1024 lds\n\t"
"v_mfma_f32_16x16x32_bf16 v[116:119], a[204:207], a[36:39], v[116:119]\n\t"
"ds_read_b64_tr_b16 a[198:199], v5 offset:26440\n\t"
"v_mfma_f32_16x16x32_bf16 v[112:115], a[204:207], a[32:35], v[112:115]\n\t"
"s_waitcnt lgkmcnt(2)\n\t"
"v_max_u32 v56, v48, v49\n\t"
"v_min_u32 v57, v48, v49\n\t"
"v_mfma_f32_16x16x32_bf16 v[96:99], a[200:203], a[32:35], v[96:99]\n\t"
"v_med3_u32 v57, v56, v57, v50\n\t"
"v_max_u32 v56, v56, v50\n\t"
"v_mfma_f32_16x16x32_bf16 v[128:131], a[208:211], a[32:35], v[128:131]\n\t"
"ds_read_b64_tr_b16 a[200:201], v5 offset:26688\n\t"
"v_med3_u32 v57, v56, v57, v51\n\t"
"v_mfma_f32_16x16x32_bf16 v[144:147], a[212:215], a[32:35], v[144:147]\n\t"
"s_waitcnt vmcnt(29)\n\t"
"ds_read_b64_tr_b16 a[202:203], v5 offset:26944\n\t"
"buffer_load_dwordx4 v9, s[12:15], s41 offen offset:2048 lds\n\t"
"v_mfma_f32_16x16x32_bf16 v[148:151], a[212:215], a[36:39], v[148:151]\n\t"
"s_add_u32 s41, s41, s56\n\t"
"s_cmp_eq_u32 s41, s79\n\t"
"ds_read_b128 a[64:67], v15 offset:32768\n\t"
"v_mfma_f32_16x16x32_bf16 v[132:135], a[208:211], a[36:39], v[132:135]\n\t"
"s_cselect_b32 s41, 0, s41\n\t"
"ds_read_b128 a[80:83], v16 offset:32768\n\t"
"v_max_u32 v56, v56, v51\n\t"
"v_mfma_f32_16x16x32_bf16 v[136:139], a[208:211], a[40:43], v[136:139]\n\t"
"ds_read_b64_tr_b16 a[204:205], v5 offset:26696\n\t"
"v_med3_u32 v57, v56, v57, v52\n\t"
"v_mfma_f32_16x16x32_bf16 v[152:155], a[212:215], a[40:43], v[152:155]\n\t"
"ds_read_b64_tr_b16 a[206:207], v5 offset:26952\n\t"
"v_max_u32 v56, v56, v52\n\t"
"v_mfma_f32_16x16x32_bf16 v[156:159], a[212:215], a[44:47], v[156:159]\n\t"
"v_med3_u32 v57, v56, v57, v53\n\t"
"v_max_u32 v56, v56, v53\n\t"
"v_mfma_f32_16x16x32_bf16 v[140:143], a[208:211], a[44:47], v[140:143]\n\t"
"s_waitcnt lgkmcnt(6)\n\t"
"v_med3_u32 v57, v56, v57, v54\n\t"
"v_max_u32 v56, v56, v54\n\t"
"v_mfma_f32_16x16x32_bf16 v[172:175], a[192:195], a[44:47], v[172:175]\n\t"
"ds_read_b64_tr_b16 a[208:209], v5 offset:27200\n\t"
"v_med3_u32 v57, v56, v57, v55\n\t"
"v_mfma_f32_16x16x32_bf16 v[188:191], a[196:199], a[44:47], v[188:191]\n\t"
"ds_read_b64_tr_b16 a[210:211], v5 offset:27456\n\t"
"v_max_u32 v56, v56, v55\n\t"
"v_mfma_f32_16x16x32_bf16 v[184:187], a[196:199], a[40:43], v[184:187]\n\t"
"s_waitcnt vmcnt(28)\n\t"
"ds_read_b128 a[68:71], v15 offset:34816\n\t"
"v_cndmask_b32 v61, v56, v57, s[68:69]\n\t"
"v_mfma_f32_16x16x32_bf16 v[168:171], a[192:195], a[40:43], v[168:171]\n\t"
"ds_read_b128 a[84:87], v16 offset:34816\n\t"
"v_readfirstlane_b32 s28, v56\n\t"
"v_mfma_f32_16x16x32_bf16 v[164:167], a[192:195], a[36:39], v[164:167]\n\t"
"ds_read_b64_tr_b16 a[212:213], v5 offset:27208\n\t"
"v_readfirstlane_b32 s29, v57\n\t"
"v_mfma_f32_16x16x32_bf16 v[180:183], a[196:199], a[36:39], v[180:183]\n\t"
"s_add_u32 m0, s51, 32768\n\t"
"ds_read_b64_tr_b16 a[214:215], v5 offset:27464\n\t"
"v_not_b32 v61, v61\n\t"
"v_mfma_f32_16x16x32_bf16 v[176:179], a[196:199], a[32:35], v[176:179]\n\t"
"s_waitcnt vmcnt(26)\n\t"
"ds_read_b128 a[72:75], v15 offset:36864\n\t"
"v_and_b32 v61, 2047, v61\n\t"
"v_mfma_f32_16x16x32_bf16 v[160:163], a[192:195], a[32:35], v[160:163]\n\t"
"s_and_b32 s80, s50, 3\n\t"
"ds_read_b128 a[88:91], v16 offset:36864\n\t"
"s_waitcnt lgkmcnt(8)\n\t"
"v_mfma_f32_16x16x32_bf16 v[192:195], a[200:203], a[32:35], v[192:195]\n\t"
"s_add_u32 s47, s40, 0\n\t"
"ds_read_b64_tr_b16 a[192:193], v5 offset:36960\n\t"
"buffer_load_dwordx4 v13, s[4:7], s47 offen offset:0 lds\n\t"
"v_mfma_f32_16x16x32_bf16 v[208:211], a[204:207], a[32:35], v[208:211]\n\t"
"s_mul_i32 s80, s80, 320\n\t"
"ds_read_b64_tr_b16 a[194:195], v5 offset:37216\n\t"
"v_mul_u32_u24 v59, 1280, v61\n\t"
"v_mfma_f32_16x16x32_bf16 v[212:215], a[204:207], a[36:39], v[212:215]\n\t"
"v_and_b32 v62, 48, %[vT]\n\t"
"v_add3_u32 v59, v59, v62, s80\n\t"
"v_mfma_f32_16x16x32_bf16 v[196:199], a[200:203], a[36:39], v[196:199]\n\t"
"v_and_b32 v62, 2147479552, v47\n\t"
"v_or_b32 v59, v59, v62\n\t"
"v_mfma_f32_16x16x32_bf16 v[200:203], a[200:203], a[40:43], v[200:203]\n\t"
"ds_read_b64_tr_b16 a[196:197], v5 offset:36968\n\t"
"v_lshlrev_b32 v34, 3, v61\n\t"
"v_mfma_f32_16x16x32_bf16 v[216:219], a[204:207], a[40:43], v[216:219]\n\t"
"ds_read_b64_tr_b16 a[198:199], v5 offset:37224\n\t"
"buffer_load_dwordx4 v14, s[4:7], s47 offen offset:1024 lds\n\t"
"v_mfma_f32_16x16x32_bf16 v[220:223], a[204:207], a[44:47], v[220:223]\n\t"
"s_waitcnt vmcnt(26)\n\t"
"ds_read_b128 a[76:79], v15 offset:38912\n\t"
"v_add_u32 v34, 2621440, v34\n\t"
"v_mfma_f32_16x16x32_bf16 v[204:207], a[200:203], a[44:47], v[204:207]\n\t"
"ds_read_b128 a[92:95], v16 offset:38912\n\t"
"s_waitcnt lgkmcnt(6)\n\t"
"v_mfma_f32_16x16x32_bf16 v[236:239], a[208:211], a[44:47], v[236:239]\n\t"
"s_addk_i32 m0, 2048\n\t"
"ds_read_b64_tr_b16 a[200:201], v5 offset:37472\n\t"
"s_add_u32 s47, s40, s98\n\t"
"v_mfma_f32_16x16x32_bf16 v[252:255], a[212:215], a[44:47], v[252:255]\n\t"
"s_cmp_eq_u32 s50, 0\n\t"
"ds_read_b64_tr_b16 a[202:203], v5 offset:37728\n\t"
"s_cselect_b32 s80, 2646016, 1073741824\n\t"
"v_mfma_f32_16x16x32_bf16 v[248:251], a[212:215], a[40:43], v[248:251]\n\t"
"v_subrev_u32 v62, 28340224, v28\n\t"
"v_add_u32_e64 v63, s39, 1\n\t"
"v_mfma_f32_16x16x32_bf16 v[232:235], a[208:211], a[40:43], v[232:235]\n\t"
"buffer_load_dwordx4 v13, s[4:7], s47 offen offset:0 lds\n\t"
"v_mfma_f32_16x16x32_bf16 v[228:231], a[208:211], a[36:39], v[228:231]\n\t"
"ds_read_b64_tr_b16 a[204:205], v5 offset:37480\n\t"
"v_mfma_f32_16x16x32_bf16 v[244:247], a[212:215], a[36:39], v[244:247]\n\t"
"buffer_load_dwordx4 v14, s[4:7], s47 offen offset:1024 lds\n\t"
"v_mfma_f32_16x16x32_bf16 v[240:243], a[212:215], a[32:35], v[240:243]\n\t"
"ds_read_b64_tr_b16 a[206:207], v5 offset:37736\n\t"
"v_mfma_f32_16x16x32_bf16 v[224:227], a[208:211], a[32:35], v[224:227]\n\t"
"s_waitcnt lgkmcnt(4)\n\t"
"ds_read_b64_tr_b16 a[208:209], v5 offset:37984\n\t"
"v_mfma_f32_16x16x32_bf16 v[64:67], a[192:195], a[48:51], v[64:67]\n\t"
"buffer_store_dword v63, v62, s[8:11], s80 offen offset:0 sc1\n\t"
"v_mfma_f32_16x16x32_bf16 v[80:83], a[196:199], a[48:51], v[80:83]\n\t"
"ds_read_b64_tr_b16 a[210:211], v5 offset:38240\n\t"
"v_and_b32 v17, 4294901760, v36\n\t"
"v_mfma_f32_16x16x32_bf16 v[84:87], a[196:199], a[52:55], v[84:87]\n\t"
"v_lshlrev_b32 v36, 16, v36\n\t"
"v_sub_f32 v17, 0, v17\n\t"
"v_mfma_f32_16x16x32_bf16 v[68:71], a[192:195], a[52:55], v[68:71]\n\t"
"v_rcp_f32 v36, v36\n\t"
"v_mfma_f32_16x16x32_bf16 v[72:75], a[192:195], a[56:59], v[72:75]\n\t"
"ds_read_b64_tr_b16 a[212:213], v5 offset:37992\n\t"
"v_mov_b32 v19, v17\n\t"
"v_mfma_f32_16x16x32_bf16 v[88:91], a[196:199], a[56:59], v[88:91]\n\t"
"ds_read_b64_tr_b16 a[214:215], v5 offset:38248\n\t"
"s_add_u32 m0, s42, 24640\n\t"
"v_mfma_f32_16x16x32_bf16 v[92:95], a[196:199], a[60:63], v[92:95]\n\t"
"s_barrier\n\t"
"v_mfma_f32_16x16x32_bf16 v[76:79], a[192:195], a[60:63], v[76:79]\n\t"
"buffer_load_dwordx4 v2, s[12:15], s41 offen offset:0 lds\n\t"
"s_waitcnt lgkmcnt(4)\n\t"
"v_mfma_f32_16x16x32_bf16 v[108:111], a[200:203], a[60:63], v[108:111]\n\t"
"ds_read_b64_tr_b16 a[192:193], v5 offset:38496\n\t"
"v_permlane32_swap_b32 v17, v19\n\t"
"v_mfma_f32_16x16x32_bf16 v[124:127], a[204:207], a[60:63], v[124:127]\n\t"
"ds_read_b64_tr_b16 a[194:195], v5 offset:38752\n\t"
"v_mov_b32 v18, v17\n\t"
"v_mfma_f32_16x16x32_bf16 v[120:123], a[204:207], a[56:59], v[120:123]\n\t"
"v_mov_b32 v35, v19\n\t"
"v_mfma_f32_16x16x32_bf16 v[104:107], a[200:203], a[56:59], v[104:107]\n\t"
"ds_read_b64_tr_b16 a[196:197], v5 offset:38504\n\t"
"v_mfma_f32_16x16x32_bf16 v[100:103], a[200:203], a[52:55], v[100:103]\n\t"
"buffer_load_dwordx4 v8, s[12:15], s41 offen offset:1024 lds\n\t"
"v_mfma_f32_16x16x32_bf16 v[116:119], a[204:207], a[52:55], v[116:119]\n\t"
"ds_read_b64_tr_b16 a[198:199], v5 offset:38760\n\t"
"v_permlane16_swap_b32 v17, v18\n\t"
"v_mfma_f32_16x16x32_bf16 v[112:115], a[204:207], a[48:51], v[112:115]\n\t"
"v_permlane16_swap_b32 v19, v35\n\t"
"v_mul_f32 v20, 0x3dd53b95, v36\n\t"
"v_mfma_f32_16x16x32_bf16 v[96:99], a[200:203], a[48:51], v[96:99]\n\t"
"v_mov_b32 v22, v20\n\t"
"s_waitcnt lgkmcnt(4)\n\t"
"v_mfma_f32_16x16x32_bf16 v[128:131], a[208:211], a[48:51], v[128:131]\n\t"
"ds_read_b64_tr_b16 a[200:201], v5 offset:39008\n\t"
"v_mfma_f32_16x16x32_bf16 v[144:147], a[212:215], a[48:51], v[144:147]\n\t"
"buffer_load_dwordx4 v9, s[12:15], s41 offen offset:2048 lds\n\t"
"v_mfma_f32_16x16x32_bf16 v[148:151], a[212:215], a[52:55], v[148:151]\n\t"
"s_add_u32 s41, s41, s56\n\t"
"v_permlane32_swap_b32 v20, v22\n\t"
"v_mov_b32 v21, v20\n\t"
"v_mfma_f32_16x16x32_bf16 v[132:135], a[208:211], a[52:55], v[132:135]\n\t"
"s_add_u32 m0, s51, 36864\n\t"
"ds_read_b64_tr_b16 a[202:203], v5 offset:39264\n\t"
"v_mov_b32 v23, v22\n\t"
"v_mfma_f32_16x16x32_bf16 v[136:139], a[208:211], a[56:59], v[136:139]\n\t"
"s_add_u32 s47, s40, s99\n\t"
"ds_read_b64_tr_b16 a[204:205], v5 offset:39016\n\t"
"buffer_load_dwordx4 v13, s[4:7], s47 offen offset:0 lds\n\t"
"v_mfma_f32_16x16x32_bf16 v[152:155], a[212:215], a[56:59], v[152:155]\n\t"
"ds_read_b64_tr_b16 a[206:207], v5 offset:39272\n\t"
"v_permlane16_swap_b32 v20, v21\n\t"
"v_mfma_f32_16x16x32_bf16 v[156:159], a[212:215], a[60:63], v[156:159]\n\t"
"v_permlane16_swap_b32 v22, v23\n\t"
"v_mfma_f32_16x16x32_bf16 v[140:143], a[208:211], a[60:63], v[140:143]\n\t"
"s_waitcnt vmcnt(24)\n\t"
"v_accvgpr_read_b32 v48, a96\n\t"
"s_waitcnt lgkmcnt(4)\n\t"
"v_mfma_f32_16x16x32_bf16 v[172:175], a[192:195], a[60:63], v[172:175]\n\t"
"ds_read_b64_tr_b16 a[208:209], v5 offset:39520\n\t"
"v_accvgpr_read_b32 v49, a97\n\t"
"v_mfma_f32_16x16x32_bf16 v[188:191], a[196:199], a[60:63], v[188:191]\n\t"
"ds_read_b64_tr_b16 a[210:211], v5 offset:39776\n\t"
"buffer_load_dwordx4 v14, s[4:7], s47 offen offset:1024 lds\n\t"
"v_mfma_f32_16x16x32_bf16 v[184:187], a[196:199], a[56:59], v[184:187]\n\t"
"v_accvgpr_read_b32 v50, a98\n\t"
"v_accvgpr_read_b32 v51, a99\n\t"
"v_mfma_f32_16x16x32_bf16 v[168:171], a[192:195], a[56:59], v[168:171]\n\t"
"s_addk_i32 m0, 2048\n\t"
"v_accvgpr_read_b32 v52, a100\n\t"
"v_accvgpr_read_b32 v53, a101\n\t"
"v_mfma_f32_16x16x32_bf16 v[164:167], a[192:195], a[52:55], v[164:167]\n\t"
"s_add_u32 s47, s47, s98\n\t"
"ds_read_b64_tr_b16 a[212:213], v5 offset:39528\n\t"
"buffer_load_dwordx4 v13, s[4:7], s47 offen offset:0 lds\n\t"
"v_mfma_f32_16x16x32_bf16 v[180:183], a[196:199], a[52:55], v[180:183]\n\t"
"ds_read_b64_tr_b16 a[214:215], v5 offset:39784\n\t"
"v_accvgpr_read_b32 v54, a102\n\t"
"v_mfma_f32_16x16x32_bf16 v[176:179], a[196:199], a[48:51], v[176:179]\n\t"
"v_accvgpr_read_b32 v55, a103\n\t"
"v_accvgpr_read_b32 v56, a104\n\t"
"v_mfma_f32_16x16x32_bf16 v[160:163], a[192:195], a[48:51], v[160:163]\n\t"
"s_waitcnt lgkmcnt(4)\n\t"
"v_accvgpr_read_b32 v57, a105\n\t"
"v_xor_b32 v49, s39, v49\n\t"
"v_mfma_f32_16x16x32_bf16 v[192:195], a[200:203], a[48:51], v[192:195]\n\t"
"ds_read_b64_tr_b16 a[192:193], v5 offset:49280\n\t"
"v_xor_b32 v51, s39, v51\n\t"
"v_mfma_f32_16x16x32_bf16 v[208:211], a[204:207], a[48:51], v[208:211]\n\t"
"ds_read_b64_tr_b16 a[194:195], v5 offset:49536\n\t"
"buffer_load_dwordx4 v14, s[4:7], s47 offen offset:1024 lds\n\t"
"v_mfma_f32_16x16x32_bf16 v[212:215], a[204:207], a[52:55], v[212:215]\n\t"
"s_addk_i32 s40, 128\n\t"
"s_cmp_eq_u32 s40, s48\n\t"
"s_cselect_b32 s40, s76, s40\n\t"
"v_mfma_f32_16x16x32_bf16 v[196:199], a[200:203], a[52:55], v[196:199]\n\t"
"v_xor_b32 v53, s39, v53\n\t"
"v_xor_b32 v55, s39, v55\n\t"
"v_mfma_f32_16x16x32_bf16 v[200:203], a[200:203], a[56:59], v[200:203]\n\t"
"ds_read_b64_tr_b16 a[196:197], v5 offset:49288\n\t"
"v_xor_b32 v57, s39, v57\n\t"
"v_mfma_f32_16x16x32_bf16 v[216:219], a[204:207], a[56:59], v[216:219]\n\t"
"ds_read_b64_tr_b16 a[198:199], v5 offset:49544\n\t"
"v_or3_b32 v49, v49, v51, v53\n\t"
"v_mfma_f32_16x16x32_bf16 v[220:223], a[204:207], a[60:63], v[220:223]\n\t"
"v_or3_b32 v49, v49, v55, v57\n\t"
"v_cmp_ne_u32 vcc, 0, v49\n\t"
"s_cbranch_vccnz .LslG%=\n\t"
".LbkG%=:\n"
"v_mfma_f32_16x16x32_bf16 v[204:207], a[200:203], a[60:63], v[204:207]\n\t"
"s_waitcnt lgkmcnt(4)\n\t"
"v_add_f32 v48, v48, v50\n\t"
"v_add_f32 v52, v52, v54\n\t"
"v_mfma_f32_16x16x32_bf16 v[236:239], a[208:211], a[60:63], v[236:239]\n\t"
"ds_read_b64_tr_b16 a[200:201], v5 offset:49792\n\t"
"v_add_f32 v48, v48, v56\n\t"
"v_mfma_f32_16x16x32_bf16 v[252:255], a[212:215], a[60:63], v[252:255]\n\t"
"ds_read_b64_tr_b16 a[202:203], v5 offset:50048\n\t"
"v_add_f32 v48, v48, v52\n\t"
"v_mfma_f32_16x16x32_bf16 v[248:251], a[212:215], a[56:59], v[248:251]\n\t"
"ds_write_b32 v41, v48 offset:0\n\t"
"v_mfma_f32_16x16x32_bf16 v[232:235], a[208:211], a[56:59], v[232:235]\n\t"
".Lpg3%=:\n"
"s_cbranch_execz .Lpg7%=\n\t"
"v_mfma_f32_16x16x32_bf16 v[228:231], a[208:211], a[52:55], v[228:231]\n\t"
"ds_read_b64_tr_b16 a[204:205], v5 offset:49800\n\t"
"v_mfma_f32_16x16x32_bf16 v[244:247], a[212:215], a[52:55], v[244:247]\n\t"
"ds_read_b64_tr_b16 a[206:207], v5 offset:50056\n\t"
"v_mfma_f32_16x16x32_bf16 v[240:243], a[212:215], a[48:51], v[240:243]\n\t"
"v_mfma_f32_16x16x32_bf16 v[224:227], a[208:211], a[48:51], v[224:227]\n\t"
"s_waitcnt lgkmcnt(5)\n\t"
"v_mfma_f32_16x16x32_bf16 v[64:67], a[192:195], a[64:67], v[64:67]\n\t"
"ds_read_b64_tr_b16 a[208:209], v5 offset:50304\n\t"
"v_mfma_f32_16x16x32_bf16 v[80:83], a[196:199], a[64:67], v[80:83]\n\t"
"ds_read_b64_tr_b16 a[210:211], v5 offset:50560\n\t"
"v_mfma_f32_16x16x32_bf16 v[84:87], a[196:199], a[68:71], v[84:87]\n\t"
"s_waitcnt vmcnt(21)\n\t"
"ds_read_b128 a[0:3], v15 offset:0\n\t"
"v_mfma_f32_16x16x32_bf16 v[68:71], a[192:195], a[68:71], v[68:71]\n\t"
"ds_read_b128 a[16:19], v16 offset:0\n\t"
"v_mfma_f32_16x16x32_bf16 v[72:75], a[192:195], a[72:75], v[72:75]\n\t"
"ds_read_b64_tr_b16 a[212:213], v5 offset:50312\n\t"
"v_mfma_f32_16x16x32_bf16 v[88:91], a[196:199], a[72:75], v[88:91]\n\t"
"ds_read_b64_tr_b16 a[214:215], v5 offset:50568\n\t"
"s_waitcnt vmcnt(32) lgkmcnt(8)\n\t"
"v_mfma_f32_16x16x32_bf16 v[92:95], a[196:199], a[76:79], v[92:95]\n\t"
"s_barrier\n\t"
"s_waitcnt vmcnt(19)\n\t"
"ds_read_b128 a[4:7], v15 offset:2048\n\t"
"v_mfma_f32_16x16x32_bf16 v[76:79], a[192:195], a[76:79], v[76:79]\n\t"
"ds_read_b128 a[20:23], v16 offset:2048\n\t"
"s_waitcnt lgkmcnt(8)\n\t"
"v_mfma_f32_16x16x32_bf16 v[108:111], a[200:203], a[76:79], v[108:111]\n\t"
"s_add_u32 m0, s42, 36960\n\t"
"ds_read_b64_tr_b16 a[192:193], v5 offset:50816\n\t"
"ds_read_b128 v[36:39], v46 offset:0\n\t"
"v_mfma_f32_16x16x32_bf16 v[124:127], a[204:207], a[76:79], v[124:127]\n\t"
"ds_read_b64_tr_b16 a[194:195], v5 offset:51072\n\t"
"v_mfma_f32_16x16x32_bf16 v[120:123], a[204:207], a[72:75], v[120:123]\n\t"
"buffer_load_dwordx4 v2, s[12:15], s41 offen offset:0 lds\n\t"
"v_mfma_f32_16x16x32_bf16 v[104:107], a[200:203], a[72:75], v[104:107]\n\t"
"ds_read_b128 v[40:43], v46 offset:64\n\t"
"v_mfma_f32_16x16x32_bf16 v[100:103], a[200:203], a[68:71], v[100:103]\n\t"
"ds_read_b64_tr_b16 a[196:197], v5 offset:50824\n\t"
"buffer_load_dwordx4 v8, s[12:15], s41 offen offset:1024 lds\n\t"
"v_mfma_f32_16x16x32_bf16 v[116:119], a[204:207], a[68:71], v[116:119]\n\t"
"ds_read_b64_tr_b16 a[198:199], v5 offset:51080\n\t"
"v_mfma_f32_16x16x32_bf16 v[112:115], a[204:207], a[64:67], v[112:115]\n\t"
"s_waitcnt vmcnt(19)\n\t"
"ds_read_b128 a[8:11], v15 offset:4096\n\t"
"v_mfma_f32_16x16x32_bf16 v[96:99], a[200:203], a[64:67], v[96:99]\n\t"
"ds_read_b128 a[24:27], v16 offset:4096\n\t"
"s_waitcnt lgkmcnt(4)\n\t"
"v_mfma_f32_16x16x32_bf16 v[128:131], a[208:211], a[64:67], v[128:131]\n\t"
"ds_read_b64_tr_b16 a[200:201], v5 offset:51328\n\t"
"v_fmac_f32 v64, v17, v36\n\t"
"v_mfma_f32_16x16x32_bf16 v[144:147], a[212:215], a[64:67], v[144:147]\n\t"
"s_waitcnt vmcnt(17)\n\t"
"ds_read_b64_tr_b16 a[202:203], v5 offset:51584\n\t"
"buffer_load_dwordx4 v9, s[12:15], s41 offen offset:2048 lds\n\t"
"v_mfma_f32_16x16x32_bf16 v[148:151], a[212:215], a[68:71], v[148:151]\n\t"
"s_add_u32 s41, s41, s56\n\t"
"s_cmp_eq_u32 s41, s79\n\t"
"ds_read_b128 a[12:15], v15 offset:6144\n\t"
"v_mfma_f32_16x16x32_bf16 v[132:135], a[208:211], a[68:71], v[132:135]\n\t"
"s_cselect_b32 s41, 0, s41\n\t"
"ds_read_b128 a[28:31], v16 offset:6144\n\t"
"v_fmac_f32 v65, v17, v37\n\t"
"v_mfma_f32_16x16x32_bf16 v[136:139], a[208:211], a[72:75], v[136:139]\n\t"
"s_add_u32 m0, s51, 0\n\t"
"s_add_u32 s47, s40, 0\n\t"
"ds_read_b64_tr_b16 a[204:205], v5 offset:51336\n\t"
"buffer_load_dwordx4 v13, s[4:7], s47 offen offset:0 lds\n\t"
"v_mfma_f32_16x16x32_bf16 v[152:155], a[212:215], a[72:75], v[152:155]\n\t"
"ds_read_b64_tr_b16 a[206:207], v5 offset:51592\n\t"
"v_fmac_f32 v66, v17, v38\n\t"
"v_mfma_f32_16x16x32_bf16 v[156:159], a[212:215], a[76:79], v[156:159]\n\t"
"v_fmac_f32 v67, v17, v39\n\t"
"v_fmac_f32 v68, v18, v36\n\t"
"v_mfma_f32_16x16x32_bf16 v[140:143], a[208:211], a[76:79], v[140:143]\n\t"
"s_waitcnt lgkmcnt(6)\n\t"
"v_fmac_f32 v69, v18, v37\n\t"
"v_fmac_f32 v70, v18, v38\n\t"
"v_mfma_f32_16x16x32_bf16 v[172:175], a[192:195], a[76:79], v[172:175]\n\t"
"ds_read_b64_tr_b16 a[208:209], v5 offset:51840\n\t"
"v_fmac_f32 v71, v18, v39\n\t"
"v_mfma_f32_16x16x32_bf16 v[188:191], a[196:199], a[76:79], v[188:191]\n\t"
"ds_read_b64_tr_b16 a[210:211], v5 offset:52096\n\t"
"buffer_load_dwordx4 v14, s[4:7], s47 offen offset:1024 lds\n\t"
"v_mfma_f32_16x16x32_bf16 v[184:187], a[196:199], a[72:75], v[184:187]\n\t"
"v_fmac_f32 v72, v19, v36\n\t"
"v_fmac_f32 v73, v19, v37\n\t"
"v_mfma_f32_16x16x32_bf16 v[168:171], a[192:195], a[72:75], v[168:171]\n\t"
"s_addk_i32 m0, 2048\n\t"
"v_fmac_f32 v74, v19, v38\n\t"
"v_fmac_f32 v75, v19, v39\n\t"
"v_mfma_f32_16x16x32_bf16 v[164:167], a[192:195], a[68:71], v[164:167]\n\t"
"s_add_u32 s47, s40, s98\n\t"
"ds_read_b64_tr_b16 a[212:213], v5 offset:51848\n\t"
"buffer_load_dwordx4 v13, s[4:7], s47 offen offset:0 lds\n\t"
"v_mfma_f32_16x16x32_bf16 v[180:183], a[196:199], a[68:71], v[180:183]\n\t"
"ds_read_b64_tr_b16 a[214:215], v5 offset:52104\n\t"
"v_fmac_f32 v76, v35, v36\n\t"
"v_mfma_f32_16x16x32_bf16 v[176:179], a[196:199], a[64:67], v[176:179]\n\t"
"v_fmac_f32 v77, v35, v37\n\t"
"v_fmac_f32 v78, v35, v38\n\t"
"v_mfma_f32_16x16x32_bf16 v[160:163], a[192:195], a[64:67], v[160:163]\n\t"
"s_waitcnt lgkmcnt(4)\n\t"
"v_fmac_f32 v79, v35, v39\n\t"
"v_fmac_f32 v80, v17, v40\n\t"
"v_mfma_f32_16x16x32_bf16 v[192:195], a[200:203], a[64:67], v[192:195]\n\t"
"ds_read_b64_tr_b16 a[192:193], v5 offset:61600\n\t"
"ds_read_b128 v[36:39], v46 offset:128\n\t"
"v_mfma_f32_16x16x32_bf16 v[208:211], a[204:207], a[64:67], v[208:211]\n\t"
"ds_read_b64_tr_b16 a[194:195], v5 offset:61856\n\t"
"buffer_load_dwordx4 v14, s[4:7], s47 offen offset:1024 lds\n\t"
"v_mfma_f32_16x16x32_bf16 v[212:215], a[204:207], a[68:71], v[212:215]\n\t"
"v_fmac_f32 v81, v17, v41\n\t"
"v_fmac_f32 v82, v17, v42\n\t"
"v_mfma_f32_16x16x32_bf16 v[196:199], a[200:203], a[68:71], v[196:199]\n\t"
"s_addk_i32 m0, 2048\n\t"
"v_fmac_f32 v83, v17, v43\n\t"
"v_fmac_f32 v84, v18, v40\n\t"
"v_mfma_f32_16x16x32_bf16 v[200:203], a[200:203], a[72:75], v[200:203]\n\t"
"s_add_u32 s47, s40, s99\n\t"
"ds_read_b64_tr_b16 a[196:197], v5 offset:61608\n\t"
"buffer_load_dwordx4 v13, s[4:7], s47 offen offset:0 lds\n\t"
"v_mfma_f32_16x16x32_bf16 v[216:219], a[204:207], a[72:75], v[216:219]\n\t"
"ds_read_b64_tr_b16 a[198:199], v5 offset:61864\n\t"
"v_fmac_f32 v85, v18, v41\n\t"
"v_mfma_f32_16x16x32_bf16 v[220:223], a[204:207], a[76:79], v[220:223]\n\t"
"v_fmac_f32 v86, v18, v42\n\t"
"v_fmac_f32 v87, v18, v43\n\t"
"v_mfma_f32_16x16x32_bf16 v[204:207], a[200:203], a[76:79], v[204:207]\n\t"
"s_waitcnt lgkmcnt(3)\n\t"
"v_fmac_f32 v88, v19, v40\n\t"
"v_fmac_f32 v89, v19, v41\n\t"
"v_mfma_f32_16x16x32_bf16 v[236:239], a[208:211], a[76:79], v[236:239]\n\t"
"ds_read_b64_tr_b16 a[200:201], v5 offset:62112\n\t"
"v_fmac_f32 v90, v19, v42\n\t"
"v_mfma_f32_16x16x32_bf16 v[252:255], a[212:215], a[76:79], v[252:255]\n\t"
"ds_read_b64_tr_b16 a[202:203], v5 offset:62368\n\t"
"buffer_load_dwordx4 v14, s[4:7], s47 offen offset:1024 lds\n\t"
"v_mfma_f32_16x16x32_bf16 v[248:251], a[212:215], a[72:75], v[248:251]\n\t"
"v_fmac_f32 v91, v19, v43\n\t"
"v_fmac_f32 v92, v35, v40\n\t"
"v_mfma_f32_16x16x32_bf16 v[232:235], a[208:211], a[72:75], v[232:235]\n\t"
"s_addk_i32 m0, 2048\n\t"
"v_fmac_f32 v93, v35, v41\n\t"
"v_fmac_f32 v94, v35, v42\n\t"
"v_mfma_f32_16x16x32_bf16 v[228:231], a[208:211], a[68:71], v[228:231]\n\t"
"s_add_u32 s47, s47, s98\n\t"
"ds_read_b64_tr_b16 a[204:205], v5 offset:62120\n\t"
"buffer_load_dwordx4 v13, s[4:7], s47 offen offset:0 lds\n\t"
"v_mfma_f32_16x16x32_bf16 v[244:247], a[212:215], a[68:71], v[244:247]\n\t"
"ds_read_b64_tr_b16 a[206:207], v5 offset:62376\n\t"
"v_fmac_f32 v95, v35, v43\n\t"
"v_mfma_f32_16x16x32_bf16 v[240:243], a[212:215], a[64:67], v[240:243]\n\t"
"ds_read_b128 v[40:43], v46 offset:192\n\t"
"v_fmac_f32 v96, v17, v36\n\t"
"v_mfma_f32_16x16x32_bf16 v[224:227], a[208:211], a[64:67], v[224:227]\n\t"
"s_waitcnt lgkmcnt(5)\n\t"
"v_fmac_f32 v97, v17, v37\n\t"
"v_fmac_f32 v98, v17, v38\n\t"
"v_mfma_f32_16x16x32_bf16 v[64:67], a[192:195], a[80:83], v[64:67]\n\t"
"ds_read_b64_tr_b16 a[208:209], v5 offset:62624\n\t"
"v_fmac_f32 v99, v17, v39\n\t"
"v_mfma_f32_16x16x32_bf16 v[80:83], a[196:199], a[80:83], v[80:83]\n\t"
"ds_read_b64_tr_b16 a[210:211], v5 offset:62880\n\t"
"buffer_load_dwordx4 v14, s[4:7], s47 offen offset:1024 lds\n\t"
"v_mfma_f32_16x16x32_bf16 v[84:87], a[196:199], a[84:87], v[84:87]\n\t"
"s_addk_i32 s40, 128\n\t"
"s_cmp_eq_u32 s40, s48\n\t"
"s_cselect_b32 s40, s76, s40\n\t"
"v_mfma_f32_16x16x32_bf16 v[68:71], a[192:195], a[84:87], v[68:71]\n\t"
"v_fmac_f32 v100, v18, v36\n\t"
"v_fmac_f32 v101, v18, v37\n\t"
"v_mfma_f32_16x16x32_bf16 v[72:75], a[192:195], a[88:91], v[72:75]\n\t"
"ds_read_b64_tr_b16 a[212:213], v5 offset:62632\n\t"
"v_fmac_f32 v102, v18, v38\n\t"
"v_mfma_f32_16x16x32_bf16 v[88:91], a[196:199], a[88:91], v[88:91]\n\t"
"ds_read_b64_tr_b16 a[214:215], v5 offset:62888\n\t"
"v_fmac_f32 v103, v18, v39\n\t"
"v_mfma_f32_16x16x32_bf16 v[92:95], a[196:199], a[92:95], v[92:95]\n\t"
"s_barrier\n\t"
"v_mfma_f32_16x16x32_bf16 v[76:79], a[192:195], a[92:95], v[76:79]\n\t"
"s_add_u32 m0, s42, 49280\n\t"
"s_waitcnt lgkmcnt(4)\n\t"
"v_mfma_f32_16x16x32_bf16 v[108:111], a[200:203], a[92:95], v[108:111]\n\t"
"ds_read_b64_tr_b16 a[192:193], v5 offset:63136\n\t"
"v_fmac_f32 v104, v19, v36\n\t"
"v_mfma_f32_16x16x32_bf16 v[124:127], a[204:207], a[92:95], v[124:127]\n\t"
"ds_read_b64_tr_b16 a[194:195], v5 offset:63392\n\t"
"v_mfma_f32_16x16x32_bf16 v[120:123], a[204:207], a[88:91], v[120:123]\n\t"
"buffer_load_dwordx4 v2, s[12:15], s41 offen offset:0 lds\n\t"
"v_mfma_f32_16x16x32_bf16 v[104:107], a[200:203], a[88:91], v[104:107]\n\t"
"ds_read_b64_tr_b16 a[196:197], v5 offset:63144\n\t"
"v_mfma_f32_16x16x32_bf16 v[100:103], a[200:203], a[84:87], v[100:103]\n\t"
"buffer_load_dwordx4 v8, s[12:15], s41 offen offset:1024 lds\n\t"
"v_mfma_f32_16x16x32_bf16 v[116:119], a[204:207], a[84:87], v[116:119]\n\t"
"ds_read_b64_tr_b16 a[198:199], v5 offset:63400\n\t"
"v_mfma_f32_16x16x32_bf16 v[112:115], a[204:207], a[80:83], v[112:115]\n\t"
"v_mfma_f32_16x16x32_bf16 v[96:99], a[200:203], a[80:83], v[96:99]\n\t"
"s_waitcnt lgkmcnt(4)\n\t"
"buffer_load_dwordx4 v9, s[12:15], s41 offen offset:2048 lds\n\t"
"v_mfma_f32_16x16x32_bf16 v[128:131], a[208:211], a[80:83], v[128:131]\n\t"
"ds_read_b64_tr_b16 a[200:201], v5 offset:63648\n\t"
"v_fmac_f32 v108, v35, v36\n\t"
"v_mfma_f32_16x16x32_bf16 v[144:147], a[212:215], a[80:83], v[144:147]\n\t"
"ds_read_b64_tr_b16 a[202:203], v5 offset:63904\n\t"
"v_mfma_f32_16x16x32_bf16 v[148:151], a[212:215], a[84:87], v[148:151]\n\t"
"s_add_u32 s41, s41, s56\n\t"
"v_fmac_f32 v109, v35, v37\n\t"
"v_fmac_f32 v110, v35, v38\n\t"
"v_mfma_f32_16x16x32_bf16 v[132:135], a[208:211], a[84:87], v[132:135]\n\t"
"v_fmac_f32 v105, v19, v37\n\t"
"v_fmac_f32 v106, v19, v38\n\t"
"v_mfma_f32_16x16x32_bf16 v[136:139], a[208:211], a[88:91], v[136:139]\n\t"
"ds_read_b64_tr_b16 a[204:205], v5 offset:63656\n\t"
"v_fmac_f32 v107, v19, v39\n\t"
"v_mfma_f32_16x16x32_bf16 v[152:155], a[212:215], a[88:91], v[152:155]\n\t"
"ds_read_b64_tr_b16 a[206:207], v5 offset:63912\n\t"
"v_fmac_f32 v111, v35, v39\n\t"
"v_mfma_f32_16x16x32_bf16 v[156:159], a[212:215], a[92:95], v[156:159]\n\t"
"ds_read_b128 v[36:39], v46 offset:256\n\t"
"v_fmac_f32 v112, v17, v40\n\t"
"v_mfma_f32_16x16x32_bf16 v[140:143], a[208:211], a[92:95], v[140:143]\n\t"
"s_waitcnt lgkmcnt(5)\n\t"
"v_fmac_f32 v113, v17, v41\n\t"
"v_fmac_f32 v114, v17, v42\n\t"
"v_mfma_f32_16x16x32_bf16 v[172:175], a[192:195], a[92:95], v[172:175]\n\t"
"ds_read_b64_tr_b16 a[208:209], v5 offset:64160\n\t"
"v_fmac_f32 v115, v17, v43\n\t"
"v_mfma_f32_16x16x32_bf16 v[188:191], a[196:199], a[92:95], v[188:191]\n\t"
"ds_read_b64_tr_b16 a[210:211], v5 offset:64416\n\t"
"v_fmac_f32 v116, v18, v40\n\t"
"v_mfma_f32_16x16x32_bf16 v[184:187], a[196:199], a[88:91], v[184:187]\n\t"
"v_fmac_f32 v117, v18, v41\n\t"
"v_fmac_f32 v118, v18, v42\n\t"
"v_mfma_f32_16x16x32_bf16 v[168:171], a[192:195], a[88:91], v[168:171]\n\t"
"v_fmac_f32 v119, v18, v43\n\t"
"v_fmac_f32 v120, v19, v40\n\t"
"v_mfma_f32_16x16x32_bf16 v[164:167], a[192:195], a[84:87], v[164:167]\n\t"
"ds_read_b64_tr_b16 a[212:213], v5 offset:64168\n\t"
"v_fmac_f32 v121, v19, v41\n\t"
"v_mfma_f32_16x16x32_bf16 v[180:183], a[196:199], a[84:87], v[180:183]\n\t"
"ds_read_b64_tr_b16 a[214:215], v5 offset:64424\n\t"
"v_fmac_f32 v122, v19, v42\n\t"
"v_mfma_f32_16x16x32_bf16 v[176:179], a[196:199], a[80:83], v[176:179]\n\t"
"s_waitcnt vmcnt(24)\n\t"
"ds_read_b128 a[32:35], v15 offset:32768\n\t"
"v_fmac_f32 v123, v19, v43\n\t"
"v_mfma_f32_16x16x32_bf16 v[160:163], a[192:195], a[80:83], v[160:163]\n\t"
"s_waitcnt lgkmcnt(5)\n\t"
"ds_read_b128 a[48:51], v16 offset:32768\n\t"
"v_fmac_f32 v124, v35, v40\n\t"
"v_mfma_f32_16x16x32_bf16 v[192:195], a[200:203], a[80:83], v[192:195]\n\t"
"ds_read_b64_tr_b16 a[192:193], v5 offset:0\n\t"
"v_fmac_f32 v125, v35, v41\n\t"
"v_mfma_f32_16x16x32_bf16 v[208:211], a[204:207], a[80:83], v[208:211]\n\t"
"ds_read_b64_tr_b16 a[194:195], v5 offset:256\n\t"
"v_fmac_f32 v126, v35, v42\n\t"
"v_mfma_f32_16x16x32_bf16 v[212:215], a[204:207], a[84:87], v[212:215]\n\t"
"s_waitcnt vmcnt(22)\n\t"
"ds_read_b128 a[36:39], v15 offset:34816\n\t"
"v_fmac_f32 v127, v35, v43\n\t"
"v_mfma_f32_16x16x32_bf16 v[196:199], a[200:203], a[84:87], v[196:199]\n\t"
"ds_read_b128 a[52:55], v16 offset:34816\n\t"
"ds_read_b128 v[40:43], v46 offset:320\n\t"
"v_mfma_f32_16x16x32_bf16 v[200:203], a[200:203], a[88:91], v[200:203]\n\t"
"ds_read_b64_tr_b16 a[196:197], v5 offset:8\n\t"
"v_fmac_f32 v128, v17, v36\n\t"
"v_mfma_f32_16x16x32_bf16 v[216:219], a[204:207], a[88:91], v[216:219]\n\t"
"ds_read_b64_tr_b16 a[198:199], v5 offset:264\n\t"
"v_fmac_f32 v129, v17, v37\n\t"
"v_mfma_f32_16x16x32_bf16 v[220:223], a[204:207], a[92:95], v[220:223]\n\t"
"s_add_u32 m0, s51, 32768\n\t"
"v_fmac_f32 v130, v17, v38\n\t"
"v_fmac_f32 v131, v17, v39\n\t"
"v_mfma_f32_16x16x32_bf16 v[204:207], a[200:203], a[92:95], v[204:207]\n\t"
"s_waitcnt lgkmcnt(7)\n\t"
"v_fmac_f32 v132, v18, v36\n\t"
"v_fmac_f32 v133, v18, v37\n\t"
"v_mfma_f32_16x16x32_bf16 v[236:239], a[208:211], a[92:95], v[236:239]\n\t"
"s_add_u32 s47, s40, 0\n\t"
"ds_read_b64_tr_b16 a[200:201], v5 offset:512\n\t"
"buffer_load_dwordx4 v13, s[4:7], s47 offen offset:0 lds\n\t"
"v_mfma_f32_16x16x32_bf16 v[252:255], a[212:215], a[92:95], v[252:255]\n\t"
"ds_read_b64_tr_b16 a[202:203], v5 offset:768\n\t"
"v_fmac_f32 v134, v18, v38\n\t"
"v_mfma_f32_16x16x32_bf16 v[248:251], a[212:215], a[88:91], v[248:251]\n\t"
"s_waitcnt vmcnt(17)\n\t"
"ds_read_b128 a[40:43], v15 offset:36864\n\t"
"v_fmac_f32 v135, v18, v39\n\t"
"v_mfma_f32_16x16x32_bf16 v[232:235], a[208:211], a[88:91], v[232:235]\n\t"
"ds_read_b128 a[56:59], v16 offset:36864\n\t"
"v_fmac_f32 v136, v19, v36\n\t"
"v_mfma_f32_16x16x32_bf16 v[228:231], a[208:211], a[84:87], v[228:231]\n\t"
"ds_read_b64_tr_b16 a[204:205], v5 offset:520\n\t"
"v_fmac_f32 v137, v19, v37\n\t"
"v_mfma_f32_16x16x32_bf16 v[244:247], a[212:215], a[84:87], v[244:247]\n\t"
"ds_read_b64_tr_b16 a[206:207], v5 offset:776\n\t"
"buffer_load_dwordx4 v14, s[4:7], s47 offen offset:1024 lds\n\t"
"v_mfma_f32_16x16x32_bf16 v[240:243], a[212:215], a[80:83], v[240:243]\n\t"
"s_waitcnt vmcnt(16)\n\t"
"ds_read_b128 a[44:47], v15 offset:38912\n\t"
"v_fmac_f32 v138, v19, v38\n\t"
"v_mfma_f32_16x16x32_bf16 v[224:227], a[208:211], a[80:83], v[224:227]\n\t"
"s_waitcnt lgkmcnt(7)\n\t"
"ds_read_b128 a[60:63], v16 offset:38912\n\t"
"v_fmac_f32 v139, v19, v39\n\t"
"v_mfma_f32_16x16x32_bf16 v[64:67], a[192:195], a[0:3], v[64:67]\n\t"
"ds_read_b64_tr_b16 a[208:209], v5 offset:1024\n\t"
"v_fmac_f32 v140, v35, v36\n\t"
"v_mfma_f32_16x16x32_bf16 v[80:83], a[196:199], a[0:3], v[80:83]\n\t"
"ds_read_b64_tr_b16 a[210:211], v5 offset:1280\n\t"
"v_fmac_f32 v141, v35, v37\n\t"
"v_mfma_f32_16x16x32_bf16 v[84:87], a[196:199], a[4:7], v[84:87]\n\t"
"v_fmac_f32 v142, v35, v38\n\t"
"v_fmac_f32 v143, v35, v39\n\t"
"v_mfma_f32_16x16x32_bf16 v[68:71], a[192:195], a[4:7], v[68:71]\n\t"
"ds_read_b128 v[36:39], v46 offset:384\n\t"
"v_fmac_f32 v144, v17, v40\n\t"
"v_mfma_f32_16x16x32_bf16 v[72:75], a[192:195], a[8:11], v[72:75]\n\t"
"ds_read_b64_tr_b16 a[212:213], v5 offset:1032\n\t"
"v_fmac_f32 v145, v17, v41\n\t"
"v_mfma_f32_16x16x32_bf16 v[88:91], a[196:199], a[8:11], v[88:91]\n\t"
"s_add_u32 m0, s42, 61600\n\t"
"ds_read_b64_tr_b16 a[214:215], v5 offset:1288\n\t"
"v_fmac_f32 v146, v17, v42\n\t"
"v_mfma_f32_16x16x32_bf16 v[92:95], a[196:199], a[12:15], v[92:95]\n\t"
"s_barrier\n\t"
"v_mfma_f32_16x16x32_bf16 v[76:79], a[192:195], a[12:15], v[76:79]\n\t"
"buffer_load_dwordx4 v2, s[12:15], s41 offen offset:0 lds\n\t"
"s_waitcnt lgkmcnt(5)\n\t"
"v_mfma_f32_16x16x32_bf16 v[108:111], a[200:203], a[12:15], v[108:111]\n\t"
"ds_read_b64_tr_b16 a[192:193], v5 offset:1536\n\t"
"v_fmac_f32 v147, v17, v43\n\t"
"v_mfma_f32_16x16x32_bf16 v[124:127], a[204:207], a[12:15], v[124:127]\n\t"
"ds_read_b64_tr_b16 a[194:195], v5 offset:1792\n\t"
"v_fmac_f32 v148, v18, v40\n\t"
"v_mfma_f32_16x16x32_bf16 v[120:123], a[204:207], a[8:11], v[120:123]\n\t"
"v_fmac_f32 v149, v18, v41\n\t"
"v_fmac_f32 v150, v18, v42\n\t"
"v_mfma_f32_16x16x32_bf16 v[104:107], a[200:203], a[8:11], v[104:107]\n\t"
"v_fmac_f32 v151, v18, v43\n\t"
"v_fmac_f32 v152, v19, v40\n\t"
"v_mfma_f32_16x16x32_bf16 v[100:103], a[200:203], a[4:7], v[100:103]\n\t"
"ds_read_b64_tr_b16 a[196:197], v5 offset:1544\n\t"
"buffer_load_dwordx4 v8, s[12:15], s41 offen offset:1024 lds\n\t"
"v_mfma_f32_16x16x32_bf16 v[116:119], a[204:207], a[4:7], v[116:119]\n\t"
"ds_read_b64_tr_b16 a[198:199], v5 offset:1800\n\t"
"v_fmac_f32 v153, v19, v41\n\t"
"v_mfma_f32_16x16x32_bf16 v[112:115], a[204:207], a[0:3], v[112:115]\n\t"
"v_fmac_f32 v154, v19, v42\n\t"
"v_fmac_f32 v155, v19, v43\n\t"
"v_mfma_f32_16x16x32_bf16 v[96:99], a[200:203], a[0:3], v[96:99]\n\t"
"s_waitcnt lgkmcnt(4)\n\t"
"v_fmac_f32 v156, v35, v40\n\t"
"v_fmac_f32 v157, v35, v41\n\t"
"v_mfma_f32_16x16x32_bf16 v[128:131], a[208:211], a[0:3], v[128:131]\n\t"
"ds_read_b64_tr_b16 a[200:201], v5 offset:2048\n\t"
"v_fmac_f32 v158, v35, v42\n\t"
"v_mfma_f32_16x16x32_bf16 v[144:147], a[212:215], a[0:3], v[144:147]\n\t"
"ds_read_b64_tr_b16 a[202:203], v5 offset:2304\n\t"
"buffer_load_dwordx4 v9, s[12:15], s41 offen offset:2048 lds\n\t"
"v_mfma_f32_16x16x32_bf16 v[148:151], a[212:215], a[4:7], v[148:151]\n\t"
"s_add_u32 s41, s41, s56\n\t"
"s_cmp_eq_u32 s41, s79\n\t"
"s_cselect_b32 s41, 0, s41\n\t"
"v_mfma_f32_16x16x32_bf16 v[132:135], a[208:211], a[4:7], v[132:135]\n\t"
"s_add_u32 m0, s51, 34816\n\t"
"v_fmac_f32 v159, v35, v43\n\t"
"v_fmac_f32 v160, v17, v36\n\t"
"v_mfma_f32_16x16x32_bf16 v[136:139], a[208:211], a[8:11], v[136:139]\n\t"
"s_add_u32 s47, s40, s98\n\t"
"ds_read_b64_tr_b16 a[204:205], v5 offset:2056\n\t"
"buffer_load_dwordx4 v13, s[4:7], s47 offen offset:0 lds\n\t"
"v_mfma_f32_16x16x32_bf16 v[152:155], a[212:215], a[8:11], v[152:155]\n\t"
"ds_read_b64_tr_b16 a[206:207], v5 offset:2312\n\t"
"ds_read_b128 v[40:43], v46 offset:448\n\t"
"v_mfma_f32_16x16x32_bf16 v[156:159], a[212:215], a[12:15], v[156:159]\n\t"
"v_fmac_f32 v161, v17, v37\n\t"
"v_fmac_f32 v162, v17, v38\n\t"
"v_mfma_f32_16x16x32_bf16 v[140:143], a[208:211], a[12:15], v[140:143]\n\t"
"s_waitcnt lgkmcnt(5)\n\t"
"v_fmac_f32 v163, v17, v39\n\t"
"v_fmac_f32 v164, v18, v36\n\t"
"v_mfma_f32_16x16x32_bf16 v[172:175], a[192:195], a[12:15], v[172:175]\n\t"
"ds_read_b64_tr_b16 a[208:209], v5 offset:2560\n\t"
"v_fmac_f32 v165, v18, v37\n\t"
"v_mfma_f32_16x16x32_bf16 v[188:191], a[196:199], a[12:15], v[188:191]\n\t"
"buffer_load_dwordx4 v14, s[4:7], s47 offen offset:1024 lds\n\t"
"v_mfma_f32_16x16x32_bf16 v[184:187], a[196:199], a[8:11], v[184:187]\n\t"
"ds_read_b64_tr_b16 a[210:211], v5 offset:2816\n\t"
"v_mfma_f32_16x16x32_bf16 v[168:171], a[192:195], a[8:11], v[168:171]\n\t"
"v_mfma_f32_16x16x32_bf16 v[164:167], a[192:195], a[4:7], v[164:167]\n\t"
"s_addk_i32 m0, 2048\n\t"
"ds_read_b64_tr_b16 a[212:213], v5 offset:2568\n\t"
"s_add_u32 s47, s40, s99\n\t"
"v_mfma_f32_16x16x32_bf16 v[180:183], a[196:199], a[4:7], v[180:183]\n\t"
"ds_read_b64_tr_b16 a[214:215], v5 offset:2824\n\t"
"v_mfma_f32_16x16x32_bf16 v[176:179], a[196:199], a[0:3], v[176:179]\n\t"
"buffer_load_dwordx4 v13, s[4:7], s47 offen offset:0 lds\n\t"
"v_mfma_f32_16x16x32_bf16 v[160:163], a[192:195], a[0:3], v[160:163]\n\t"
"s_waitcnt lgkmcnt(4)\n\t"
"buffer_load_dwordx4 v14, s[4:7], s47 offen offset:1024 lds\n\t"
"v_mfma_f32_16x16x32_bf16 v[192:195], a[200:203], a[0:3], v[192:195]\n\t"
"ds_read_b64_tr_b16 a[192:193], v5 offset:12320\n\t"
"v_fmac_f32 v172, v35, v36\n\t"
"v_mfma_f32_16x16x32_bf16 v[208:211], a[204:207], a[0:3], v[208:211]\n\t"
"ds_read_b64_tr_b16 a[194:195], v5 offset:12576\n\t"
"v_mfma_f32_16x16x32_bf16 v[212:215], a[204:207], a[4:7], v[212:215]\n\t"
"v_fmac_f32 v173, v35, v37\n\t"
"v_fmac_f32 v174, v35, v38\n\t"
"v_mfma_f32_16x16x32_bf16 v[196:199], a[200:203], a[4:7], v[196:199]\n\t"
"s_addk_i32 m0, 2048\n\t"
"v_fmac_f32 v168, v19, v36\n\t"
"v_fmac_f32 v169, v19, v37\n\t"
"v_mfma_f32_16x16x32_bf16 v[200:203], a[200:203], a[8:11], v[200:203]\n\t"
"s_add_u32 s47, s47, s98\n\t"
"ds_read_b64_tr_b16 a[196:197], v5 offset:12328\n\t"
"buffer_load_dwordx4 v13, s[4:7], s47 offen offset:0 lds\n\t"
"v_mfma_f32_16x16x32_bf16 v[216:219], a[204:207], a[8:11], v[216:219]\n\t"
"ds_read_b64_tr_b16 a[198:199], v5 offset:12584\n\t"
"v_fmac_f32 v166, v18, v38\n\t"
"v_mfma_f32_16x16x32_bf16 v[220:223], a[204:207], a[12:15], v[220:223]\n\t"
"v_fmac_f32 v167, v18, v39\n\t"
"v_fmac_f32 v170, v19, v38\n\t"
"v_mfma_f32_16x16x32_bf16 v[204:207], a[200:203], a[12:15], v[204:207]\n\t"
"s_waitcnt lgkmcnt(4)\n\t"
"v_fmac_f32 v171, v19, v39\n\t"
"v_fmac_f32 v175, v35, v39\n\t"
"v_mfma_f32_16x16x32_bf16 v[236:239], a[208:211], a[12:15], v[236:239]\n\t"
"ds_read_b64_tr_b16 a[200:201], v5 offset:12832\n\t"
"ds_read_b128 v[36:39], v46 offset:512\n\t"
"v_mfma_f32_16x16x32_bf16 v[252:255], a[212:215], a[12:15], v[252:255]\n\t"
"ds_read_b64_tr_b16 a[202:203], v5 offset:13088\n\t"
"buffer_load_dwordx4 v14, s[4:7], s47 offen offset:1024 lds\n\t"
"v_mfma_f32_16x16x32_bf16 v[248:251], a[212:215], a[8:11], v[248:251]\n\t"
"v_fmac_f32 v176, v17, v40\n\t"
"v_fmac_f32 v177, v17, v41\n\t"
"v_mfma_f32_16x16x32_bf16 v[232:235], a[208:211], a[8:11], v[232:235]\n\t"
"v_fmac_f32 v178, v17, v42\n\t"
"v_fmac_f32 v179, v17, v43\n\t"
"v_mfma_f32_16x16x32_bf16 v[228:231], a[208:211], a[4:7], v[228:231]\n\t"
"ds_read_b64_tr_b16 a[204:205], v5 offset:12840\n\t"
"v_fmac_f32 v180, v18, v40\n\t"
"v_mfma_f32_16x16x32_bf16 v[244:247], a[212:215], a[4:7], v[244:247]\n\t"
"ds_read_b64_tr_b16 a[206:207], v5 offset:13096\n\t"
"v_fmac_f32 v181, v18, v41\n\t"
"v_mfma_f32_16x16x32_bf16 v[240:243], a[212:215], a[0:3], v[240:243]\n\t"
"v_fmac_f32 v182, v18, v42\n\t"
"v_fmac_f32 v183, v18, v43\n\t"
"v_mfma_f32_16x16x32_bf16 v[224:227], a[208:211], a[0:3], v[224:227]\n\t"
"s_waitcnt lgkmcnt(3)\n\t"
"v_fmac_f32 v184, v19, v40\n\t"
"v_fmac_f32 v185, v19, v41\n\t"
"v_mfma_f32_16x16x32_bf16 v[64:67], a[192:195], a[16:19], v[64:67]\n\t"
"ds_read_b64_tr_b16 a[208:209], v5 offset:13344\n\t"
"v_fmac_f32 v186, v19, v42\n\t"
"v_mfma_f32_16x16x32_bf16 v[80:83], a[196:199], a[16:19], v[80:83]\n\t"
"ds_read_b64_tr_b16 a[210:211], v5 offset:13600\n\t"
"v_fmac_f32 v187, v19, v43\n\t"
"v_mfma_f32_16x16x32_bf16 v[84:87], a[196:199], a[20:23], v[84:87]\n\t"
"v_fmac_f32 v188, v35, v40\n\t"
"v_fmac_f32 v189, v35, v41\n\t"
"v_mfma_f32_16x16x32_bf16 v[68:71], a[192:195], a[20:23], v[68:71]\n\t"
"v_fmac_f32 v190, v35, v42\n\t"
"v_fmac_f32 v191, v35, v43\n\t"
"v_mfma_f32_16x16x32_bf16 v[72:75], a[192:195], a[24:27], v[72:75]\n\t"
"s_add_u32 m0, s42, 0\n\t"
"ds_read_b64_tr_b16 a[212:213], v5 offset:13352\n\t"
"ds_read_b128 v[40:43], v46 offset:576\n\t"
"v_mfma_f32_16x16x32_bf16 v[88:91], a[196:199], a[24:27], v[88:91]\n\t"
"ds_read_b64_tr_b16 a[214:215], v5 offset:13608\n\t"
"v_fmac_f32 v192, v17, v36\n\t"
"v_mfma_f32_16x16x32_bf16 v[92:95], a[196:199], a[28:31], v[92:95]\n\t"
"s_barrier\n\t"
"s_waitcnt vmcnt(20)\n\t"
"ds_read_b128 a[64:67], v15 offset:0\n\t"
"v_mfma_f32_16x16x32_bf16 v[76:79], a[192:195], a[28:31], v[76:79]\n\t"
"buffer_load_dwordx4 v2, s[12:15], s41 offen offset:0 lds\n\t"
"ds_read_b128 a[80:83], v16 offset:0\n\t"
"s_waitcnt lgkmcnt(7)\n\t"
"v_mfma_f32_16x16x32_bf16 v[108:111], a[200:203], a[28:31], v[108:111]\n\t"
"ds_read_b64_tr_b16 a[192:193], v5 offset:13856\n\t"
"v_fmac_f32 v193, v17, v37\n\t"
"v_mfma_f32_16x16x32_bf16 v[124:127], a[204:207], a[28:31], v[124:127]\n\t"
"ds_read_b64_tr_b16 a[194:195], v5 offset:14112\n\t"
"v_fmac_f32 v194, v17, v38\n\t"
"v_mfma_f32_16x16x32_bf16 v[120:123], a[204:207], a[24:27], v[120:123]\n\t"
"s_waitcnt vmcnt(19)\n\t"
"ds_read_b128 a[68:71], v15 offset:2048\n\t"
"v_fmac_f32 v195, v17, v39\n\t"
"v_mfma_f32_16x16x32_bf16 v[104:107], a[200:203], a[24:27], v[104:107]\n\t"
"ds_read_b128 a[84:87], v16 offset:2048\n\t"
"v_fmac_f32 v196, v18, v36\n\t"
"v_mfma_f32_16x16x32_bf16 v[100:103], a[200:203], a[20:23], v[100:103]\n\t"
"ds_read_b64_tr_b16 a[196:197], v5 offset:13864\n\t"
"buffer_load_dwordx4 v8, s[12:15], s41 offen offset:1024 lds\n\t"
"v_mfma_f32_16x16x32_bf16 v[116:119], a[204:207], a[20:23], v[116:119]\n\t"
"ds_read_b64_tr_b16 a[198:199], v5 offset:14120\n\t"
"v_fmac_f32 v197, v18, v37\n\t"
"v_mfma_f32_16x16x32_bf16 v[112:115], a[204:207], a[16:19], v[112:115]\n\t"
"v_fmac_f32 v198, v18, v38\n\t"
"v_fmac_f32 v199, v18, v39\n\t"
"v_mfma_f32_16x16x32_bf16 v[96:99], a[200:203], a[16:19], v[96:99]\n\t"
"s_waitcnt lgkmcnt(6)\n\t"
"v_fmac_f32 v200, v19, v36\n\t"
"v_fmac_f32 v201, v19, v37\n\t"
"v_mfma_f32_16x16x32_bf16 v[128:131], a[208:211], a[16:19], v[128:131]\n\t"
"ds_read_b64_tr_b16 a[200:201], v5 offset:14368\n\t"
"v_fmac_f32 v202, v19, v38\n\t"
"v_mfma_f32_16x16x32_bf16 v[144:147], a[212:215], a[16:19], v[144:147]\n\t"
"ds_read_b64_tr_b16 a[202:203], v5 offset:14624\n\t"
"buffer_load_dwordx4 v9, s[12:15], s41 offen offset:2048 lds\n\t"
"v_mfma_f32_16x16x32_bf16 v[148:151], a[212:215], a[20:23], v[148:151]\n\t"
"s_add_u32 s41, s41, s56\n\t"
"s_waitcnt vmcnt(19)\n\t"
"ds_read_b128 a[72:75], v15 offset:4096\n\t"
"v_mfma_f32_16x16x32_bf16 v[132:135], a[208:211], a[20:23], v[132:135]\n\t"
"ds_read_b128 a[88:91], v16 offset:4096\n\t"
"v_fmac_f32 v203, v19, v39\n\t"
"v_mfma_f32_16x16x32_bf16 v[136:139], a[208:211], a[24:27], v[136:139]\n\t"
"ds_read_b64_tr_b16 a[204:205], v5 offset:14376\n\t"
"v_fmac_f32 v204, v35, v36\n\t"
"v_mfma_f32_16x16x32_bf16 v[152:155], a[212:215], a[24:27], v[152:155]\n\t"
"ds_read_b64_tr_b16 a[206:207], v5 offset:14632\n\t"
"buffer_load_dwordx4 a[176:179], v59, s[4:7], 0 offen offset:0\n\t"
"v_mfma_f32_16x16x32_bf16 v[156:159], a[212:215], a[28:31], v[156:159]\n\t"
"s_waitcnt vmcnt(18)\n\t"
"ds_read_b128 a[76:79], v15 offset:6144\n\t"
"v_fmac_f32 v205, v35, v37\n\t"
"v_mfma_f32_16x16x32_bf16 v[140:143], a[208:211], a[28:31], v[140:143]\n\t"
"s_waitcnt lgkmcnt(7)\n\t"
"ds_read_b128 a[92:95], v16 offset:6144\n\t"
"v_fmac_f32 v206, v35, v38\n\t"
"v_mfma_f32_16x16x32_bf16 v[172:175], a[192:195], a[28:31], v[172:175]\n\t"
"ds_read_b64_tr_b16 a[208:209], v5 offset:14880\n\t"
"v_fmac_f32 v207, v35, v39\n\t"
"v_mfma_f32_16x16x32_bf16 v[188:191], a[196:199], a[28:31], v[188:191]\n\t"
"ds_read_b64_tr_b16 a[210:211], v5 offset:15136\n\t"
"buffer_load_dwordx4 a[180:183], v59, s[4:7], 0 offen offset:64\n\t"
"v_mfma_f32_16x16x32_bf16 v[184:187], a[196:199], a[24:27], v[184:187]\n\t"
"ds_read_b128 v[36:39], v46 offset:640\n\t"
"v_fmac_f32 v208, v17, v40\n\t"
"v_mfma_f32_16x16x32_bf16 v[168:171], a[192:195], a[24:27], v[168:171]\n\t"
"v_fmac_f32 v209, v17, v41\n\t"
"v_fmac_f32 v210, v17, v42\n\t"
"v_mfma_f32_16x16x32_bf16 v[164:167], a[192:195], a[20:23], v[164:167]\n\t"
"ds_read_b64_tr_b16 a[212:213], v5 offset:14888\n\t"
"v_fmac_f32 v211, v17, v43\n\t"
"v_mfma_f32_16x16x32_bf16 v[180:183], a[196:199], a[20:23], v[180:183]\n\t"
"ds_read_b64_tr_b16 a[214:215], v5 offset:15144\n\t"
"buffer_load_dwordx4 a[184:187], v59, s[4:7], 0 offen offset:128\n\t"
"v_mfma_f32_16x16x32_bf16 v[176:179], a[196:199], a[16:19], v[176:179]\n\t"
"v_fmac_f32 v212, v18, v40\n\t"
"v_fmac_f32 v213, v18, v41\n\t"
"v_mfma_f32_16x16x32_bf16 v[160:163], a[192:195], a[16:19], v[160:163]\n\t"
"s_waitcnt lgkmcnt(5)\n\t"
"v_fmac_f32 v214, v18, v42\n\t"
"v_fmac_f32 v215, v18, v43\n\t"
"v_mfma_f32_16x16x32_bf16 v[192:195], a[200:203], a[16:19], v[192:195]\n\t"
"ds_read_b64_tr_b16 a[192:193], v5 offset:24640\n\t"
"v_fmac_f32 v216, v19, v40\n\t"
"v_mfma_f32_16x16x32_bf16 v[208:211], a[204:207], a[16:19], v[208:211]\n\t"
"ds_read_b64_tr_b16 a[194:195], v5 offset:24896\n\t"
"buffer_load_dwordx4 a[188:191], v59, s[4:7], 0 offen offset:192\n\t"
"v_mfma_f32_16x16x32_bf16 v[212:215], a[204:207], a[20:23], v[212:215]\n\t"
"v_fmac_f32 v217, v19, v41\n\t"
"v_fmac_f32 v218, v19, v42\n\t"
"v_mfma_f32_16x16x32_bf16 v[196:199], a[200:203], a[20:23], v[196:199]\n\t"
"v_fmac_f32 v220, v35, v40\n\t"
"v_fmac_f32 v221, v35, v41\n\t"
"v_mfma_f32_16x16x32_bf16 v[200:203], a[200:203], a[24:27], v[200:203]\n\t"
"ds_read_b64_tr_b16 a[196:197], v5 offset:24648\n\t"
"v_mfma_f32_16x16x32_bf16 v[216:219], a[204:207], a[24:27], v[216:219]\n\t"
"buffer_load_dwordx4 a[220:223], v59, s[4:7], 0 offen offset:256\n\t"
"v_mfma_f32_16x16x32_bf16 v[220:223], a[204:207], a[28:31], v[220:223]\n\t"
"ds_read_b64_tr_b16 a[198:199], v5 offset:24904\n\t"
"v_mfma_f32_16x16x32_bf16 v[204:207], a[200:203], a[28:31], v[204:207]\n\t"
"s_waitcnt lgkmcnt(4)\n\t"
"v_mfma_f32_16x16x32_bf16 v[236:239], a[208:211], a[28:31], v[236:239]\n\t"
"ds_read_b64_tr_b16 a[200:201], v5 offset:25152\n\t"
"v_mfma_f32_16x16x32_bf16 v[252:255], a[212:215], a[28:31], v[252:255]\n\t"
"buffer_load_dwordx2 v[44:45], v34, s[8:11], 0 offen offset:0 sc1\n\t"
"v_mfma_f32_16x16x32_bf16 v[248:251], a[212:215], a[24:27], v[248:251]\n\t"
"ds_read_b64_tr_b16 a[202:203], v5 offset:25408\n\t"
"v_mfma_f32_16x16x32_bf16 v[232:235], a[208:211], a[24:27], v[232:235]\n\t"
"v_mfma_f32_16x16x32_bf16 v[228:231], a[208:211], a[20:23], v[228:231]\n\t"
"ds_read_b64_tr_b16 a[204:205], v5 offset:25160\n\t"
"v_mfma_f32_16x16x32_bf16 v[244:247], a[212:215], a[20:23], v[244:247]\n\t"
"ds_read_b64_tr_b16 a[206:207], v5 offset:25416\n\t"
"v_fmac_f32 v219, v19, v43\n\t"
"v_mfma_f32_16x16x32_bf16 v[240:243], a[212:215], a[16:19], v[240:243]\n\t"
"v_fmac_f32 v222, v35, v42\n\t"
"v_fmac_f32 v223, v35, v43\n\t"
"v_mfma_f32_16x16x32_bf16 v[224:227], a[208:211], a[16:19], v[224:227]\n\t"
"ds_read_b128 v[40:43], v46 offset:704\n\t"
"s_waitcnt lgkmcnt(5)\n\t"
"v_mfma_f32_16x16x32_bf16 v[64:67], a[192:195], a[32:35], v[64:67]\n\t"
"ds_read_b64_tr_b16 a[208:209], v5 offset:25664\n\t"
"v_fmac_f32 v236, v35, v36\n\t"
"v_mfma_f32_16x16x32_bf16 v[80:83], a[196:199], a[32:35], v[80:83]\n\t"
"ds_read_b64_tr_b16 a[210:211], v5 offset:25920\n\t"
"v_fmac_f32 v237, v35, v37\n\t"
"v_mfma_f32_16x16x32_bf16 v[84:87], a[196:199], a[36:39], v[84:87]\n\t"
"v_fmac_f32 v238, v35, v38\n\t"
"v_fmac_f32 v239, v35, v39\n\t"
"v_mfma_f32_16x16x32_bf16 v[68:71], a[192:195], a[36:39], v[68:71]\n\t"
"v_fmac_f32 v232, v19, v36\n\t"
"v_fmac_f32 v233, v19, v37\n\t"
"v_mfma_f32_16x16x32_bf16 v[72:75], a[192:195], a[40:43], v[72:75]\n\t"
"ds_read_b64_tr_b16 a[212:213], v5 offset:25672\n\t"
"v_fmac_f32 v228, v18, v36\n\t"
"v_mfma_f32_16x16x32_bf16 v[88:91], a[196:199], a[40:43], v[88:91]\n\t"
"s_addk_i32 m0, 12320\n\t"
"ds_read_b64_tr_b16 a[214:215], v5 offset:25928\n\t"
"v_fmac_f32 v229, v18, v37\n\t"
"v_mfma_f32_16x16x32_bf16 v[92:95], a[196:199], a[44:47], v[92:95]\n\t"
"s_waitcnt vmcnt(20)\n\t"
"s_barrier\n\t"
"v_mfma_f32_16x16x32_bf16 v[76:79], a[192:195], a[44:47], v[76:79]\n\t"
"buffer_load_dwordx4 v2, s[12:15], s41 offen offset:0 lds\n\t"
"s_waitcnt lgkmcnt(4)\n\t"
"v_mfma_f32_16x16x32_bf16 v[108:111], a[200:203], a[44:47], v[108:111]\n\t"
"ds_read_b64_tr_b16 a[192:193], v5 offset:26176\n\t"
"v_fmac_f32 v224, v17, v36\n\t"
"v_mfma_f32_16x16x32_bf16 v[124:127], a[204:207], a[44:47], v[124:127]\n\t"
"ds_read_b64_tr_b16 a[194:195], v5 offset:26432\n\t"
"v_fmac_f32 v225, v17, v37\n\t"
"v_mfma_f32_16x16x32_bf16 v[120:123], a[204:207], a[40:43], v[120:123]\n\t"
"v_fmac_f32 v226, v17, v38\n\t"
"v_fmac_f32 v227, v17, v39\n\t"
"v_mfma_f32_16x16x32_bf16 v[104:107], a[200:203], a[40:43], v[104:107]\n\t"
"v_fmac_f32 v230, v18, v38\n\t"
"v_fmac_f32 v231, v18, v39\n\t"
"v_mfma_f32_16x16x32_bf16 v[100:103], a[200:203], a[36:39], v[100:103]\n\t"
".Lpg4%=:\n"
"s_cbranch_execz .Lpg8%=\n\t"
"ds_read_b64_tr_b16 a[196:197], v5 offset:26184\n\t"
"buffer_load_dwordx4 v8, s[12:15], s41 offen offset:1024 lds\n\t"
"v_mfma_f32_16x16x32_bf16 v[116:119], a[204:207], a[36:39], v[116:119]\n\t"
"ds_read_b64_tr_b16 a[198:199], v5 offset:26440\n\t"
"v_fmac_f32 v234, v19, v38\n\t"
"v_mfma_f32_16x16x32_bf16 v[112:115], a[204:207], a[32:35], v[112:115]\n\t"
"s_waitcnt vmcnt(20)\n\t"
"ds_read_b128 a[0:3], v15 offset:32768\n\t"
"v_fmac_f32 v235, v19, v39\n\t"
"v_mfma_f32_16x16x32_bf16 v[96:99], a[200:203], a[32:35], v[96:99]\n\t"
"s_waitcnt lgkmcnt(5)\n\t"
"ds_read_b128 a[16:19], v16 offset:32768\n\t"
"v_fmac_f32 v240, v17, v40\n\t"
"v_mfma_f32_16x16x32_bf16 v[128:131], a[208:211], a[32:35], v[128:131]\n\t"
"ds_read_b64_tr_b16 a[200:201], v5 offset:26688\n\t"
"v_fmac_f32 v241, v17, v41\n\t"
"v_mfma_f32_16x16x32_bf16 v[144:147], a[212:215], a[32:35], v[144:147]\n\t"
"ds_read_b64_tr_b16 a[202:203], v5 offset:26944\n\t"
"buffer_load_dwordx4 v9, s[12:15], s41 offen offset:2048 lds\n\t"
"v_mfma_f32_16x16x32_bf16 v[148:151], a[212:215], a[36:39], v[148:151]\n\t"
"s_waitcnt vmcnt(16)\n\t"
"ds_read_b128 a[4:7], v15 offset:34816\n\t"
"v_fmac_f32 v242, v17, v42\n\t"
"v_mfma_f32_16x16x32_bf16 v[132:135], a[208:211], a[36:39], v[132:135]\n\t"
"ds_read_b128 a[20:23], v16 offset:34816\n\t"
"v_fmac_f32 v243, v17, v43\n\t"
"v_mfma_f32_16x16x32_bf16 v[136:139], a[208:211], a[40:43], v[136:139]\n\t"
"ds_read_b64_tr_b16 a[204:205], v5 offset:26696\n\t"
"v_fmac_f32 v244, v18, v40\n\t"
"v_mfma_f32_16x16x32_bf16 v[152:155], a[212:215], a[40:43], v[152:155]\n\t"
"s_add_u32 m0, s87, 0\n\t"
"ds_read_b64_tr_b16 a[206:207], v5 offset:26952\n\t"
"buffer_load_dwordx4 v32, s[64:67], s86 offen offset:0 lds\n\t"
"v_mfma_f32_16x16x32_bf16 v[156:159], a[212:215], a[44:47], v[156:159]\n\t"
"s_waitcnt vmcnt(15)\n\t"
"ds_read_b128 a[8:11], v15 offset:36864\n\t"
"s_addk_i32 s86, 24576\n\t"
"v_mfma_f32_16x16x32_bf16 v[140:143], a[208:211], a[44:47], v[140:143]\n\t"
"s_waitcnt lgkmcnt(7)\n\t"
"ds_read_b128 a[24:27], v16 offset:36864\n\t"
"v_fmac_f32 v245, v18, v41\n\t"
"v_mfma_f32_16x16x32_bf16 v[172:175], a[192:195], a[44:47], v[172:175]\n\t"
"ds_read_b64_tr_b16 a[208:209], v5 offset:27200\n\t"
"v_fmac_f32 v246, v18, v42\n\t"
"v_mfma_f32_16x16x32_bf16 v[188:191], a[196:199], a[44:47], v[188:191]\n\t"
"s_addk_i32 m0, 1024\n\t"
"ds_read_b64_tr_b16 a[210:211], v5 offset:27456\n\t"
"buffer_load_dwordx4 v32, s[64:67], s86 offen offset:0 lds\n\t"
"v_mfma_f32_16x16x32_bf16 v[184:187], a[196:199], a[40:43], v[184:187]\n\t"
"s_waitcnt vmcnt(14)\n\t"
"ds_read_b128 a[12:15], v15 offset:38912\n\t"
"s_addk_i32 s86, 24576\n\t"
"v_mfma_f32_16x16x32_bf16 v[168:171], a[192:195], a[40:43], v[168:171]\n\t"
"ds_read_b128 a[28:31], v16 offset:38912\n\t"
"v_fmac_f32 v247, v18, v43\n\t"
"v_mfma_f32_16x16x32_bf16 v[164:167], a[192:195], a[36:39], v[164:167]\n\t"
"ds_read_b64_tr_b16 a[212:213], v5 offset:27208\n\t"
"v_fmac_f32 v248, v19, v40\n\t"
"v_mfma_f32_16x16x32_bf16 v[180:183], a[196:199], a[36:39], v[180:183]\n\t"
"s_addk_i32 m0, 1024\n\t"
"ds_read_b64_tr_b16 a[214:215], v5 offset:27464\n\t"
"buffer_load_dwordx4 v32, s[64:67], s86 offen offset:0 lds\n\t"
"v_mfma_f32_16x16x32_bf16 v[176:179], a[196:199], a[32:35], v[176:179]\n\t"
"s_addk_i32 s86, 24576\n\t"
"v_fmac_f32 v249, v19, v41\n\t"
"v_fmac_f32 v250, v19, v42\n\t"
"v_mfma_f32_16x16x32_bf16 v[160:163], a[192:195], a[32:35], v[160:163]\n\t"
"s_waitcnt lgkmcnt(6)\n\t"
"v_fmac_f32 v251, v19, v43\n\t"
"v_fmac_f32 v252, v35, v40\n\t"
"v_mfma_f32_16x16x32_bf16 v[192:195], a[200:203], a[32:35], v[192:195]\n\t"
"ds_read_b64_tr_b16 a[192:193], v5 offset:36960\n\t"
"v_fmac_f32 v253, v35, v41\n\t"
"v_mfma_f32_16x16x32_bf16 v[208:211], a[204:207], a[32:35], v[208:211]\n\t"
"s_addk_i32 m0, 1024\n\t"
"ds_read_b64_tr_b16 a[194:195], v5 offset:37216\n\t"
"buffer_load_dwordx4 v32, s[64:67], s86 offen offset:0 lds\n\t"
"v_mfma_f32_16x16x32_bf16 v[212:215], a[204:207], a[36:39], v[212:215]\n\t"
"s_addk_i32 s86, 24576\n\t"
"v_fmac_f32 v254, v35, v42\n\t"
"v_fmac_f32 v255, v35, v43\n\t"
"v_mfma_f32_16x16x32_bf16 v[196:199], a[200:203], a[36:39], v[196:199]\n\t"
"v_xor_b32 v13, 32, v7\n\t"
"v_xor_b32 v14, 64, v7\n\t"
"v_mfma_f32_16x16x32_bf16 v[200:203], a[200:203], a[40:43], v[200:203]\n\t"
"ds_read_b64_tr_b16 a[196:197], v5 offset:36968\n\t"
"v_xor_b32 v15, 96, v7\n\t"
"v_mfma_f32_16x16x32_bf16 v[216:219], a[204:207], a[40:43], v[216:219]\n\t"
"s_addk_i32 m0, 1024\n\t"
"ds_read_b64_tr_b16 a[198:199], v5 offset:37224\n\t"
"buffer_load_dwordx4 v32, s[64:67], s86 offen offset:0 lds\n\t"
"v_mfma_f32_16x16x32_bf16 v[220:223], a[204:207], a[44:47], v[220:223]\n\t"
"v_add_u32 v16, 65536, v7\n\t"
"v_add_u32 v17, 65536, v13\n\t"
"v_mfma_f32_16x16x32_bf16 v[204:207], a[200:203], a[44:47], v[204:207]\n\t"
"s_waitcnt lgkmcnt(4)\n\t"
"v_add_u32 v18, 65536, v14\n\t"
"v_add_u32 v19, 65536, v15\n\t"
"v_mfma_f32_16x16x32_bf16 v[236:239], a[208:211], a[44:47], v[236:239]\n\t"
"ds_read_b64_tr_b16 a[200:201], v5 offset:49280\n\t"
"v_mfma_f32_16x16x32_bf16 v[252:255], a[212:215], a[44:47], v[252:255]\n\t"
"ds_read_b64_tr_b16 a[202:203], v5 offset:49536\n\t"
"v_mfma_f32_16x16x32_bf16 v[248:251], a[212:215], a[40:43], v[248:251]\n\t"
"v_mfma_f32_16x16x32_bf16 v[232:235], a[208:211], a[40:43], v[232:235]\n\t"
"v_mfma_f32_16x16x32_bf16 v[228:231], a[208:211], a[36:39], v[228:231]\n\t"
"s_add_u32 m0, s43, 0\n\t"
"ds_read_b64_tr_b16 a[204:205], v5 offset:49288\n\t"
"s_mov_b32 s47, 0\n\t"
"v_mfma_f32_16x16x32_bf16 v[244:247], a[212:215], a[36:39], v[244:247]\n\t"
"ds_read_b64_tr_b16 a[206:207], v5 offset:49544\n\t"
"v_mfma_f32_16x16x32_bf16 v[240:243], a[212:215], a[32:35], v[240:243]\n\t"
"buffer_load_dwordx4 v3, s[16:19], s47 offen offset:0 lds\n\t"
"v_mfma_f32_16x16x32_bf16 v[224:227], a[208:211], a[32:35], v[224:227]\n\t"
"s_barrier\n\t"
"s_waitcnt lgkmcnt(4)\n\t"
"v_mfma_f32_16x16x32_bf16 v[64:67], a[192:195], a[48:51], v[64:67]\n\t"
"ds_read_b64_tr_b16 a[208:209], v5 offset:61600\n\t"
"v_mfma_f32_16x16x32_bf16 v[80:83], a[196:199], a[48:51], v[80:83]\n\t"
"ds_read_b64_tr_b16 a[210:211], v5 offset:61856\n\t"
"v_mfma_f32_16x16x32_bf16 v[84:87], a[196:199], a[52:55], v[84:87]\n\t"
"v_mfma_f32_16x16x32_bf16 v[68:71], a[192:195], a[52:55], v[68:71]\n\t"
"buffer_load_dwordx4 v10, s[16:19], s47 offen offset:1024 lds\n\t"
"v_mfma_f32_16x16x32_bf16 v[72:75], a[192:195], a[56:59], v[72:75]\n\t"
"ds_read_b64_tr_b16 a[212:213], v5 offset:61608\n\t"
"s_waitcnt vmcnt(16)\n\t"
"v_mfma_f32_16x16x32_bf16 v[88:91], a[196:199], a[56:59], v[88:91]\n\t"
"s_and_b32 s53, s78, 3\n\t"
"ds_read_b64_tr_b16 a[214:215], v5 offset:61864\n\t"
"s_mul_i32 s54, s53, -192\n\t"
"v_mfma_f32_16x16x32_bf16 v[92:95], a[196:199], a[60:63], v[92:95]\n\t"
"s_and_b32 s53, s78, 1\n\t"
"s_lshl_b32 s53, s53, 5\n\t"
"s_sub_u32 s54, s54, s53\n\t"
"v_mfma_f32_16x16x32_bf16 v[76:79], a[192:195], a[60:63], v[76:79]\n\t"
"s_barrier\n\t"
"s_waitcnt lgkmcnt(4)\n\t"
"v_mfma_f32_16x16x32_bf16 v[64:67], a[200:203], a[64:67], v[64:67]\n\t"
"s_addk_i32 s54, 960\n\t"
"ds_read_b64_tr_b16 a[192:193], v5 offset:0\n\t"
"s_andn2_b32 s53, 4, %[sB]\n\t"
"v_mfma_f32_16x16x32_bf16 v[80:83], a[204:207], a[64:67], v[80:83]\n\t"
"s_lshl_b32 s53, s53, 4\n\t"
"ds_read_b64_tr_b16 a[194:195], v5 offset:256\n\t"
"v_bfe_u32 v2, %[vT], 4, 2\n\t"
"v_mfma_f32_16x16x32_bf16 v[84:87], a[204:207], a[68:71], v[84:87]\n\t"
"buffer_load_dwordx4 v11, s[16:19], s47 offen offset:2048 lds\n\t"
"v_mfma_f32_16x16x32_bf16 v[68:71], a[200:203], a[68:71], v[68:71]\n\t"
"v_mul_u32_u24 v2, 10240, v2\n\t"
"v_bfe_u32 v9, %[vT], 2, 2\n\t"
"v_mfma_f32_16x16x32_bf16 v[72:75], a[200:203], a[72:75], v[72:75]\n\t"
"ds_read_b64_tr_b16 a[196:197], v5 offset:8\n\t"
"v_mul_u32_u24 v9, 1280, v9\n\t"
"v_mfma_f32_16x16x32_bf16 v[88:91], a[204:207], a[72:75], v[88:91]\n\t"
"ds_read_b64_tr_b16 a[198:199], v5 offset:264\n\t"
"v_and_b32 v48, 32, v1\n\t"
"v_mfma_f32_16x16x32_bf16 v[92:95], a[204:207], a[76:79], v[92:95]\n\t"
"s_waitcnt vmcnt(8)\n\t"
"v_lshl_add_u32 v9, v48, 1, v9\n\t"
"v_xor_b32 v9, s53, v9\n\t"
"v_mfma_f32_16x16x32_bf16 v[76:79], a[200:203], a[76:79], v[76:79]\n\t"
"s_barrier\n\t"
"s_waitcnt lgkmcnt(4)\n\t"
"v_mfma_f32_16x16x32_bf16 v[64:67], a[208:211], a[80:83], v[64:67]\n\t"
"ds_read_b64_tr_b16 a[200:201], v5 offset:12320\n\t"
"v_and_b32 v48, 16, v1\n\t"
"v_mfma_f32_16x16x32_bf16 v[80:83], a[212:215], a[80:83], v[80:83]\n\t"
"ds_read_b64_tr_b16 a[202:203], v5 offset:12576\n\t"
"v_add3_u32 v9, v9, v48, v2\n\t"
"v_mfma_f32_16x16x32_bf16 v[84:87], a[212:215], a[84:87], v[84:87]\n\t"
"v_add_u32 v9, s54, v9\n\t"
"s_add_u32 s54, s54, s53\n\t"
"v_mfma_f32_16x16x32_bf16 v[68:71], a[208:211], a[84:87], v[68:71]\n\t"
"s_lshl_b32 s53, s53, 1\n\t"
"v_and_b32 v48, 240, v1\n\t"
"v_add_u32 v2, v2, v48\n\t"
"v_mfma_f32_16x16x32_bf16 v[72:75], a[208:211], a[88:91], v[72:75]\n\t"
"ds_read_b64_tr_b16 a[204:205], v5 offset:12328\n\t"
"v_xor_b32 v2, s53, v2\n\t"
"v_mfma_f32_16x16x32_bf16 v[88:91], a[212:215], a[88:91], v[88:91]\n\t"
"ds_read_b64_tr_b16 a[206:207], v5 offset:12584\n\t"
"s_pack_ll_b32_b16 s72, s72, s54\n\t"
"v_mfma_f32_16x16x32_bf16 v[92:95], a[212:215], a[92:95], v[92:95]\n\t"
"v_mfma_f32_16x16x32_bf16 v[76:79], a[208:211], a[92:95], v[76:79]\n\t"
"s_waitcnt lgkmcnt(4)\n\t"
"v_mfma_f32_16x16x32_bf16 v[64:67], a[192:195], a[0:3], v[64:67]\n\t"
"ds_read_b64_tr_b16 a[208:209], v5 offset:37472\n\t"
"v_mfma_f32_16x16x32_bf16 v[80:83], a[196:199], a[0:3], v[80:83]\n\t"
"ds_read_b64_tr_b16 a[210:211], v5 offset:37728\n\t"
"v_mfma_f32_16x16x32_bf16 v[84:87], a[196:199], a[4:7], v[84:87]\n\t"
"v_mfma_f32_16x16x32_bf16 v[68:71], a[192:195], a[4:7], v[68:71]\n\t"
"v_mfma_f32_16x16x32_bf16 v[72:75], a[192:195], a[8:11], v[72:75]\n\t"
"ds_read_b64_tr_b16 a[212:213], v5 offset:37480\n\t"
"v_mfma_f32_16x16x32_bf16 v[88:91], a[196:199], a[8:11], v[88:91]\n\t"
"ds_read_b64_tr_b16 a[214:215], v5 offset:37736\n\t"
"v_mfma_f32_16x16x32_bf16 v[92:95], a[196:199], a[12:15], v[92:95]\n\t"
"v_mfma_f32_16x16x32_bf16 v[76:79], a[192:195], a[12:15], v[76:79]\n\t"
"s_waitcnt lgkmcnt(4)\n\t"
"v_mfma_f32_16x16x32_bf16 v[64:67], a[200:203], a[16:19], v[64:67]\n\t"
"ds_read_b64_tr_b16 a[192:193], v5 offset:49792\n\t"
"v_mfma_f32_16x16x32_bf16 v[80:83], a[204:207], a[16:19], v[80:83]\n\t"
"ds_read_b64_tr_b16 a[194:195], v5 offset:50048\n\t"
"v_mfma_f32_16x16x32_bf16 v[84:87], a[204:207], a[20:23], v[84:87]\n\t"
"v_mfma_f32_16x16x32_bf16 v[68:71], a[200:203], a[20:23], v[68:71]\n\t"
"v_mfma_f32_16x16x32_bf16 v[72:75], a[200:203], a[24:27], v[72:75]\n\t"
"ds_read_b64_tr_b16 a[196:197], v5 offset:49800\n\t"
"v_mfma_f32_16x16x32_bf16 v[88:91], a[204:207], a[24:27], v[88:91]\n\t"
"ds_read_b64_tr_b16 a[198:199], v5 offset:50056\n\t"
"v_mfma_f32_16x16x32_bf16 v[92:95], a[204:207], a[28:31], v[92:95]\n\t"
"v_mfma_f32_16x16x32_bf16 v[76:79], a[200:203], a[28:31], v[76:79]\n\t"
"s_waitcnt lgkmcnt(4)\n\t"
"v_mul_f32 v64, v20, v64\n\t"
"v_mul_f32 v65, v20, v65\n\t"
"v_mfma_f32_16x16x32_bf16 v[96:99], a[208:211], a[48:51], v[96:99]\n\t"
"ds_read_b64_tr_b16 a[200:201], v5 offset:62112\n\t"
"v_mul_f32 v66, v20, v66\n\t"
"v_mfma_f32_16x16x32_bf16 v[112:115], a[212:215], a[48:51], v[112:115]\n\t"
"ds_read_b64_tr_b16 a[202:203], v5 offset:62368\n\t"
"v_mul_f32 v67, v20, v67\n\t"
"v_mfma_f32_16x16x32_bf16 v[116:119], a[212:215], a[52:55], v[116:119]\n\t"
"v_mul_f32 v80, v20, v80\n\t"
"v_mul_f32 v81, v20, v81\n\t"
"v_mfma_f32_16x16x32_bf16 v[100:103], a[208:211], a[52:55], v[100:103]\n\t"
"v_mul_f32 v82, v20, v82\n\t"
"v_mul_f32 v83, v20, v83\n\t"
"v_cvt_pk_bf16_f32 v64, v64, v65\n\t"
"v_mfma_f32_16x16x32_bf16 v[104:107], a[208:211], a[56:59], v[104:107]\n\t"
"ds_read_b64_tr_b16 a[204:205], v5 offset:62120\n\t"
"v_cvt_pk_bf16_f32 v66, v66, v67\n\t"
"v_mfma_f32_16x16x32_bf16 v[120:123], a[212:215], a[56:59], v[120:123]\n\t"
"ds_read_b64_tr_b16 a[206:207], v5 offset:62376\n\t"
"v_cvt_pk_bf16_f32 v80, v80, v81\n\t"
"v_mfma_f32_16x16x32_bf16 v[124:127], a[212:215], a[60:63], v[124:127]\n\t"
"v_cvt_pk_bf16_f32 v82, v82, v83\n\t"
"v_accvgpr_write_b32 a96, v64\n\t"
"v_mfma_f32_16x16x32_bf16 v[108:111], a[208:211], a[60:63], v[108:111]\n\t"
"s_waitcnt lgkmcnt(4)\n\t"
"v_accvgpr_write_b32 a97, v66\n\t"
"v_accvgpr_write_b32 a98, v80\n\t"
"v_mfma_f32_16x16x32_bf16 v[96:99], a[192:195], a[64:67], v[96:99]\n\t"
"ds_read_b64_tr_b16 a[208:209], v5 offset:512\n\t"
"v_accvgpr_write_b32 a99, v82\n\t"
"v_mfma_f32_16x16x32_bf16 v[112:115], a[196:199], a[64:67], v[112:115]\n\t"
"ds_read_b64_tr_b16 a[210:211], v5 offset:768\n\t"
"v_mul_f32 v68, v21, v68\n\t"
"v_mfma_f32_16x16x32_bf16 v[116:119], a[196:199], a[68:71], v[116:119]\n\t"
"v_mul_f32 v69, v21, v69\n\t"
"v_mul_f32 v70, v21, v70\n\t"
"v_mfma_f32_16x16x32_bf16 v[100:103], a[192:195], a[68:71], v[100:103]\n\t"
"v_mul_f32 v71, v21, v71\n\t"
"v_mul_f32 v84, v21, v84\n\t"
"v_mfma_f32_16x16x32_bf16 v[104:107], a[192:195], a[72:75], v[104:107]\n\t"
"ds_read_b64_tr_b16 a[212:213], v5 offset:520\n\t"
"v_mul_f32 v85, v21, v85\n\t"
"v_mfma_f32_16x16x32_bf16 v[120:123], a[196:199], a[72:75], v[120:123]\n\t"
"ds_read_b64_tr_b16 a[214:215], v5 offset:776\n\t"
"v_mul_f32 v86, v21, v86\n\t"
"v_mul_f32 v87, v21, v87\n\t"
"v_mfma_f32_16x16x32_bf16 v[124:127], a[196:199], a[76:79], v[124:127]\n\t"
"v_cvt_pk_bf16_f32 v68, v68, v69\n\t"
"v_cvt_pk_bf16_f32 v70, v70, v71\n\t"
"v_mfma_f32_16x16x32_bf16 v[108:111], a[192:195], a[76:79], v[108:111]\n\t"
"s_waitcnt lgkmcnt(4)\n\t"
"v_cvt_pk_bf16_f32 v84, v84, v85\n\t"
"v_cvt_pk_bf16_f32 v86, v86, v87\n\t"
"v_mfma_f32_16x16x32_bf16 v[96:99], a[200:203], a[80:83], v[96:99]\n\t"
"ds_read_b64_tr_b16 a[192:193], v5 offset:12832\n\t"
"v_accvgpr_write_b32 a100, v68\n\t"
"v_mfma_f32_16x16x32_bf16 v[112:115], a[204:207], a[80:83], v[112:115]\n\t"
"ds_read_b64_tr_b16 a[194:195], v5 offset:13088\n\t"
"v_accvgpr_write_b32 a101, v70\n\t"
"v_mfma_f32_16x16x32_bf16 v[116:119], a[204:207], a[84:87], v[116:119]\n\t"
"v_accvgpr_write_b32 a102, v84\n\t"
"v_accvgpr_write_b32 a103, v86\n\t"
"v_mfma_f32_16x16x32_bf16 v[100:103], a[200:203], a[84:87], v[100:103]\n\t"
"v_mul_f32 v72, v22, v72\n\t"
"v_mul_f32 v73, v22, v73\n\t"
"v_mfma_f32_16x16x32_bf16 v[104:107], a[200:203], a[88:91], v[104:107]\n\t"
"ds_read_b64_tr_b16 a[196:197], v5 offset:12840\n\t"
"v_mul_f32 v74, v22, v74\n\t"
"v_mfma_f32_16x16x32_bf16 v[120:123], a[204:207], a[88:91], v[120:123]\n\t"
"ds_read_b64_tr_b16 a[198:199], v5 offset:13096\n\t"
"v_mul_f32 v75, v22, v75\n\t"
"v_mfma_f32_16x16x32_bf16 v[124:127], a[204:207], a[92:95], v[124:127]\n\t"
"v_mul_f32 v88, v22, v88\n\t"
"v_mul_f32 v89, v22, v89\n\t"
"v_mfma_f32_16x16x32_bf16 v[108:111], a[200:203], a[92:95], v[108:111]\n\t"
"s_waitcnt lgkmcnt(4)\n\t"
"v_mul_f32 v90, v22, v90\n\t"
"v_mul_f32 v91, v22, v91\n\t"
"v_cvt_pk_bf16_f32 v72, v72, v73\n\t"
"v_mfma_f32_16x16x32_bf16 v[96:99], a[208:211], a[0:3], v[96:99]\n\t"
"ds_read_b64_tr_b16 a[200:201], v5 offset:37984\n\t"
"v_cvt_pk_bf16_f32 v74, v74, v75\n\t"
"v_mfma_f32_16x16x32_bf16 v[112:115], a[212:215], a[0:3], v[112:115]\n\t"
"ds_read_b64_tr_b16 a[202:203], v5 offset:38240\n\t"
"v_cvt_pk_bf16_f32 v88, v88, v89\n\t"
"v_mfma_f32_16x16x32_bf16 v[116:119], a[212:215], a[4:7], v[116:119]\n\t"
"v_cvt_pk_bf16_f32 v90, v90, v91\n\t"
"v_accvgpr_write_b32 a104, v72\n\t"
"v_mfma_f32_16x16x32_bf16 v[100:103], a[208:211], a[4:7], v[100:103]\n\t"
"v_accvgpr_write_b32 a105, v74\n\t"
"v_accvgpr_write_b32 a106, v88\n\t"
"v_mfma_f32_16x16x32_bf16 v[104:107], a[208:211], a[8:11], v[104:107]\n\t"
"ds_read_b64_tr_b16 a[204:205], v5 offset:37992\n\t"
"v_accvgpr_write_b32 a107, v90\n\t"
"v_mfma_f32_16x16x32_bf16 v[120:123], a[212:215], a[8:11], v[120:123]\n\t"
"ds_read_b64_tr_b16 a[206:207], v5 offset:38248\n\t"
"v_mul_f32 v76, v23, v76\n\t"
"v_mfma_f32_16x16x32_bf16 v[124:127], a[212:215], a[12:15], v[124:127]\n\t"
"v_mul_f32 v77, v23, v77\n\t"
"v_mul_f32 v78, v23, v78\n\t"
"v_mfma_f32_16x16x32_bf16 v[108:111], a[208:211], a[12:15], v[108:111]\n\t"
"s_waitcnt lgkmcnt(4)\n\t"
"v_mul_f32 v79, v23, v79\n\t"
"v_mul_f32 v92, v23, v92\n\t"
"v_mfma_f32_16x16x32_bf16 v[96:99], a[192:195], a[16:19], v[96:99]\n\t"
"ds_read_b64_tr_b16 a[208:209], v5 offset:50304\n\t"
"v_mul_f32 v93, v23, v93\n\t"
"v_mfma_f32_16x16x32_bf16 v[112:115], a[196:199], a[16:19], v[112:115]\n\t"
"ds_read_b64_tr_b16 a[210:211], v5 offset:50560\n\t"
"v_mul_f32 v94, v23, v94\n\t"
"v_mul_f32 v95, v23, v95\n\t"
"v_mfma_f32_16x16x32_bf16 v[116:119], a[196:199], a[20:23], v[116:119]\n\t"
"v_cvt_pk_bf16_f32 v76, v76, v77\n\t"
"v_cvt_pk_bf16_f32 v78, v78, v79\n\t"
"v_mfma_f32_16x16x32_bf16 v[100:103], a[192:195], a[20:23], v[100:103]\n\t"
"v_cvt_pk_bf16_f32 v92, v92, v93\n\t"
"v_cvt_pk_bf16_f32 v94, v94, v95\n\t"
"v_mfma_f32_16x16x32_bf16 v[104:107], a[192:195], a[24:27], v[104:107]\n\t"
"ds_read_b64_tr_b16 a[212:213], v5 offset:50312\n\t"
"v_accvgpr_write_b32 a108, v76\n\t"
"v_mfma_f32_16x16x32_bf16 v[120:123], a[196:199], a[24:27], v[120:123]\n\t"
"ds_read_b64_tr_b16 a[214:215], v5 offset:50568\n\t"
"v_accvgpr_write_b32 a109, v78\n\t"
"v_mfma_f32_16x16x32_bf16 v[124:127], a[196:199], a[28:31], v[124:127]\n\t"
"s_addk_i32 s47, 12288\n\t"
"v_accvgpr_write_b32 a110, v92\n\t"
"v_accvgpr_write_b32 a111, v94\n\t"
"v_mfma_f32_16x16x32_bf16 v[108:111], a[192:195], a[28:31], v[108:111]\n\t"
"s_waitcnt lgkmcnt(4)\n\t"
"v_mul_f32 v96, v20, v96\n\t"
"v_mul_f32 v97, v20, v97\n\t"
"v_mul_f32 v98, v20, v98\n\t"
"v_mfma_f32_16x16x32_bf16 v[128:131], a[200:203], a[48:51], v[128:131]\n\t"
"s_addk_i32 m0, 12288\n\t"
"ds_read_b64_tr_b16 a[192:193], v5 offset:62624\n\t"
"v_mul_f32 v99, v20, v99\n\t"
"v_mfma_f32_16x16x32_bf16 v[144:147], a[204:207], a[48:51], v[144:147]\n\t"
"s_load_dword s3, %[karg], s85 offset:16864\n\t"
"ds_read_b64_tr_b16 a[194:195], v5 offset:62880\n\t"
"v_mul_f32 v112, v20, v112\n\t"
"v_mfma_f32_16x16x32_bf16 v[148:151], a[204:207], a[52:55], v[148:151]\n\t"
"buffer_load_dwordx4 v3, s[16:19], s47 offen offset:0 lds\n\t"
"v_mul_f32 v113, v20, v113\n\t"
"v_mfma_f32_16x16x32_bf16 v[132:135], a[200:203], a[52:55], v[132:135]\n\t"
"v_mul_f32 v114, v20, v114\n\t"
"v_mul_f32 v115, v20, v115\n\t"
"v_cvt_pk_bf16_f32 v96, v96, v97\n\t"
"v_mfma_f32_16x16x32_bf16 v[136:139], a[200:203], a[56:59], v[136:139]\n\t"
"ds_read_b64_tr_b16 a[196:197], v5 offset:62632\n\t"
"v_cvt_pk_bf16_f32 v98, v98, v99\n\t"
"v_cvt_pk_bf16_f32 v112, v112, v113\n\t"
"v_mfma_f32_16x16x32_bf16 v[152:155], a[204:207], a[56:59], v[152:155]\n\t"
"ds_read_b64_tr_b16 a[198:199], v5 offset:62888\n\t"
"v_cvt_pk_bf16_f32 v114, v114, v115\n\t"
"v_mfma_f32_16x16x32_bf16 v[156:159], a[204:207], a[60:63], v[156:159]\n\t"
"v_accvgpr_write_b32 a112, v96\n\t"
"v_accvgpr_write_b32 a113, v98\n\t"
"v_accvgpr_write_b32 a114, v112\n\t"
"v_mfma_f32_16x16x32_bf16 v[140:143], a[200:203], a[60:63], v[140:143]\n\t"
"s_waitcnt lgkmcnt(4)\n\t"
"buffer_load_dwordx4 v10, s[16:19], s47 offen offset:1024 lds\n\t"
"v_accvgpr_write_b32 a115, v114\n\t"
"v_mfma_f32_16x16x32_bf16 v[128:131], a[208:211], a[64:67], v[128:131]\n\t"
"ds_read_b64_tr_b16 a[200:201], v5 offset:1024\n\t"
"v_mul_f32 v100, v21, v100\n\t"
"v_mul_f32 v101, v21, v101\n\t"
"v_mfma_f32_16x16x32_bf16 v[144:147], a[212:215], a[64:67], v[144:147]\n\t"
"ds_read_b64_tr_b16 a[202:203], v5 offset:1280\n\t"
"v_mul_f32 v102, v21, v102\n\t"
"v_mfma_f32_16x16x32_bf16 v[148:151], a[212:215], a[68:71], v[148:151]\n\t"
"v_mul_f32 v103, v21, v103\n\t"
"v_mul_f32 v116, v21, v116\n\t"
"v_mul_f32 v117, v21, v117\n\t"
"v_mfma_f32_16x16x32_bf16 v[132:135], a[208:211], a[68:71], v[132:135]\n\t"
"v_mul_f32 v118, v21, v118\n\t"
"v_mul_f32 v119, v21, v119\n\t"
"v_cvt_pk_bf16_f32 v100, v100, v101\n\t"
"v_mfma_f32_16x16x32_bf16 v[136:139], a[208:211], a[72:75], v[136:139]\n\t"
"ds_read_b64_tr_b16 a[204:205], v5 offset:1032\n\t"
"v_cvt_pk_bf16_f32 v102, v102, v103\n\t"
"v_cvt_pk_bf16_f32 v116, v116, v117\n\t"
"v_mfma_f32_16x16x32_bf16 v[152:155], a[212:215], a[72:75], v[152:155]\n\t"
"ds_read_b64_tr_b16 a[206:207], v5 offset:1288\n\t"
"v_cvt_pk_bf16_f32 v118, v118, v119\n\t"
"v_mfma_f32_16x16x32_bf16 v[156:159], a[212:215], a[76:79], v[156:159]\n\t"
"buffer_load_dwordx4 v11, s[16:19], s47 offen offset:2048 lds\n\t"
"v_accvgpr_write_b32 a116, v100\n\t"
"v_mfma_f32_16x16x32_bf16 v[140:143], a[208:211], a[76:79], v[140:143]\n\t"
"s_waitcnt lgkmcnt(4)\n\t"
"v_accvgpr_write_b32 a117, v102\n\t"
"v_accvgpr_write_b32 a118, v116\n\t"
"v_accvgpr_write_b32 a119, v118\n\t"
"v_mfma_f32_16x16x32_bf16 v[128:131], a[192:195], a[80:83], v[128:131]\n\t"
"ds_read_b64_tr_b16 a[208:209], v5 offset:13344\n\t"
"v_mul_f32 v104, v22, v104\n\t"
"v_mul_f32 v105, v22, v105\n\t"
"v_mfma_f32_16x16x32_bf16 v[144:147], a[196:199], a[80:83], v[144:147]\n\t"
"ds_read_b64_tr_b16 a[210:211], v5 offset:13600\n\t"
"v_mul_f32 v106, v22, v106\n\t"
"v_mfma_f32_16x16x32_bf16 v[148:151], a[196:199], a[84:87], v[148:151]\n\t"
"v_mul_f32 v107, v22, v107\n\t"
"v_mul_f32 v120, v22, v120\n\t"
"v_mul_f32 v121, v22, v121\n\t"
"v_mfma_f32_16x16x32_bf16 v[132:135], a[192:195], a[84:87], v[132:135]\n\t"
"s_addk_i32 s47, 12288\n\t"
"v_mul_f32 v122, v22, v122\n\t"
"v_mul_f32 v123, v22, v123\n\t"
"v_cvt_pk_bf16_f32 v104, v104, v105\n\t"
"v_mfma_f32_16x16x32_bf16 v[136:139], a[192:195], a[88:91], v[136:139]\n\t"
"s_addk_i32 m0, 12288\n\t"
"ds_read_b64_tr_b16 a[212:213], v5 offset:13352\n\t"
"v_cvt_pk_bf16_f32 v106, v106, v107\n\t"
"v_mfma_f32_16x16x32_bf16 v[152:155], a[196:199], a[88:91], v[152:155]\n\t"
"ds_read_b64_tr_b16 a[214:215], v5 offset:13608\n\t"
"v_cvt_pk_bf16_f32 v120, v120, v121\n\t"
"v_mfma_f32_16x16x32_bf16 v[156:159], a[196:199], a[92:95], v[156:159]\n\t"
"buffer_load_dwordx4 v3, s[16:19], s47 offen offset:0 lds\n\t"
"v_cvt_pk_bf16_f32 v122, v122, v123\n\t"
"v_mfma_f32_16x16x32_bf16 v[140:143], a[192:195], a[92:95], v[140:143]\n\t"
"s_waitcnt lgkmcnt(4)\n\t"
"v_accvgpr_write_b32 a120, v104\n\t"
"v_accvgpr_write_b32 a121, v106\n\t"
"v_accvgpr_write_b32 a122, v120\n\t"
"v_mfma_f32_16x16x32_bf16 v[128:131], a[200:203], a[0:3], v[128:131]\n\t"
"ds_read_b64_tr_b16 a[192:193], v5 offset:38496\n\t"
"v_accvgpr_write_b32 a123, v122\n\t"
"v_mul_f32 v108, v23, v108\n\t"
"v_mfma_f32_16x16x32_bf16 v[144:147], a[204:207], a[0:3], v[144:147]\n\t"
"ds_read_b64_tr_b16 a[194:195], v5 offset:38752\n\t"
"v_mul_f32 v109, v23, v109\n\t"
"v_mfma_f32_16x16x32_bf16 v[148:151], a[204:207], a[4:7], v[148:151]\n\t"
"v_mul_f32 v110, v23, v110\n\t"
"v_mul_f32 v111, v23, v111\n\t"
"v_mul_f32 v124, v23, v124\n\t"
"v_mfma_f32_16x16x32_bf16 v[132:135], a[200:203], a[4:7], v[132:135]\n\t"
"buffer_load_dwordx4 v10, s[16:19], s47 offen offset:1024 lds\n\t"
"v_mul_f32 v125, v23, v125\n\t"
"v_mfma_f32_16x16x32_bf16 v[136:139], a[200:203], a[8:11], v[136:139]\n\t"
"ds_read_b64_tr_b16 a[196:197], v5 offset:38504\n\t"
"v_mul_f32 v126, v23, v126\n\t"
"v_mul_f32 v127, v23, v127\n\t"
"v_mfma_f32_16x16x32_bf16 v[152:155], a[204:207], a[8:11], v[152:155]\n\t"
"s_waitcnt vmcnt(8)\n\t"
"ds_read_b64_tr_b16 a[198:199], v5 offset:38760\n\t"
"v_cvt_pk_bf16_f32 v108, v108, v109\n\t"
"v_mfma_f32_16x16x32_bf16 v[156:159], a[204:207], a[12:15], v[156:159]\n\t"
"s_barrier\n\t"
"v_cvt_pk_bf16_f32 v110, v110, v111\n\t"
"v_mfma_f32_16x16x32_bf16 v[140:143], a[200:203], a[12:15], v[140:143]\n\t"
"s_waitcnt lgkmcnt(4)\n\t"
"ds_read_b64_tr_b16 a[160:161], v33 offset:0\n\t"
"ds_read_b64_tr_b16 a[162:163], v33 offset:512\n\t"
"v_cvt_pk_bf16_f32 v124, v124, v125\n\t"
"v_mfma_f32_16x16x32_bf16 v[128:131], a[208:211], a[16:19], v[128:131]\n\t"
"ds_read_b64_tr_b16 a[200:201], v5 offset:50816\n\t"
"ds_read_b64_tr_b16 a[164:165], v33 offset:4096\n\t"
"v_cvt_pk_bf16_f32 v126, v126, v127\n\t"
"v_mfma_f32_16x16x32_bf16 v[144:147], a[212:215], a[16:19], v[144:147]\n\t"
"ds_read_b64_tr_b16 a[202:203], v5 offset:51072\n\t"
"ds_read_b64_tr_b16 a[166:167], v33 offset:4608\n\t"
"v_mfma_f32_16x16x32_bf16 v[148:151], a[212:215], a[20:23], v[148:151]\n\t"
"buffer_load_dwordx4 v11, s[16:19], s47 offen offset:2048 lds\n\t"
"ds_read_b64_tr_b16 a[168:169], v33 offset:8192\n\t"
"v_accvgpr_write_b32 a124, v108\n\t"
"v_mfma_f32_16x16x32_bf16 v[132:135], a[208:211], a[20:23], v[132:135]\n\t"
"ds_read_b64_tr_b16 a[170:171], v33 offset:8704\n\t"
"v_accvgpr_write_b32 a125, v110\n\t"
"v_mfma_f32_16x16x32_bf16 v[136:139], a[208:211], a[24:27], v[136:139]\n\t"
"ds_read_b64_tr_b16 a[204:205], v5 offset:50824\n\t"
"ds_read_b64_tr_b16 a[172:173], v33 offset:12288\n\t"
"v_accvgpr_write_b32 a126, v124\n\t"
"v_mfma_f32_16x16x32_bf16 v[152:155], a[212:215], a[24:27], v[152:155]\n\t"
"ds_read_b64_tr_b16 a[206:207], v5 offset:51080\n\t"
"ds_read_b64_tr_b16 a[174:175], v33 offset:12800\n\t"
"v_mfma_f32_16x16x32_bf16 v[156:159], a[212:215], a[28:31], v[156:159]\n\t"
"ds_read_b64_tr_b16 a[216:217], v33 offset:16384\n\t"
"v_accvgpr_write_b32 a127, v126\n\t"
"v_mfma_f32_16x16x32_bf16 v[140:143], a[208:211], a[28:31], v[140:143]\n\t"
"ds_read_b64_tr_b16 a[218:219], v33 offset:16896\n\t"
"v_cmp_ne_u32 vcc, s39, v45\n\t"
"s_cbranch_vccnz .LslS%=\n\t"
".LbkS%=:\n"
"v_mul_f32 v128, v20, v128\n\t"
"s_waitcnt lgkmcnt(14)\n\t"
"v_mfma_f32_16x16x32_bf16 v[160:163], a[192:195], a[48:51], v[160:163]\n\t"
"s_addk_i32 m0, 12288\n\t"
"ds_read_b64_tr_b16 a[208:209], v5 offset:63136\n\t"
"v_mul_f32 v129, v20, v129\n\t"
"v_mfma_f32_16x16x32_bf16 v[176:179], a[196:199], a[48:51], v[176:179]\n\t"
"s_addk_i32 s47, 12288\n\t"
"ds_read_b64_tr_b16 a[210:211], v5 offset:63392\n\t"
"v_and_b32 v45, 4294901760, v44\n\t"
"v_mul_f32 v130, v20, v130\n\t"
"v_mul_f32 v131, v20, v131\n\t"
"v_mfma_f32_16x16x32_bf16 v[180:183], a[196:199], a[52:55], v[180:183]\n\t"
"buffer_load_dwordx4 v3, s[16:19], s47 offen offset:0 lds\n\t"
"v_mul_f32 v144, v20, v144\n\t"
"v_mul_f32 v145, v20, v145\n\t"
"v_mfma_f32_16x16x32_bf16 v[164:167], a[192:195], a[52:55], v[164:167]\n\t"
"v_lshlrev_b32 v44, 16, v44\n\t"
"v_mul_f32 v146, v20, v146\n\t"
"v_mul_f32 v147, v20, v147\n\t"
"v_cvt_pk_bf16_f32 v128, v128, v129\n\t"
"v_mfma_f32_16x16x32_bf16 v[168:171], a[192:195], a[56:59], v[168:171]\n\t"
"ds_read_b64_tr_b16 a[212:213], v5 offset:63144\n\t"
"v_cvt_pk_bf16_f32 v130, v130, v131\n\t"
"v_cvt_pk_bf16_f32 v144, v144, v145\n\t"
"v_mfma_f32_16x16x32_bf16 v[184:187], a[196:199], a[56:59], v[184:187]\n\t"
"ds_read_b64_tr_b16 a[214:215], v5 offset:63400\n\t"
"v_cvt_pk_bf16_f32 v146, v146, v147\n\t"
"v_accvgpr_write_b32 a128, v128\n\t"
"v_accvgpr_write_b32 a129, v130\n\t"
"v_mfma_f32_16x16x32_bf16 v[188:191], a[196:199], a[60:63], v[188:191]\n\t"
"v_rcp_f32 v44, v44\n\t"
"v_accvgpr_write_b32 a130, v144\n\t"
"v_accvgpr_write_b32 a131, v146\n\t"
"v_mfma_f32_16x16x32_bf16 v[172:175], a[192:195], a[60:63], v[172:175]\n\t"
"s_waitcnt lgkmcnt(7)\n\t"
"v_mul_f32 v132, v21, v132\n\t"
"v_mul_f32 v133, v21, v133\n\t"
"v_mfma_f32_16x16x32_bf16 v[160:163], a[200:203], a[64:67], v[160:163]\n\t"
"ds_read_b64_tr_b16 a[192:193], v5 offset:1536\n\t"
"v_mul_f32 v135, v21, v135\n\t"
"v_mfma_f32_16x16x32_bf16 v[176:179], a[204:207], a[64:67], v[176:179]\n\t"
"s_waitcnt lgkmcnt(5)\n\t"
"v_mul_f32 v149, v21, v149\n\t"
"v_mul_f32 v150, v21, v150\n\t"
"v_mfma_f32_16x16x32_bf16 v[180:183], a[204:207], a[68:71], v[180:183]\n\t"
"buffer_load_dwordx4 v10, s[16:19], s47 offen offset:1024 lds\n\t"
"v_mfma_f32_16x16x32_bf16 v[48:51], a[160:163], a[176:179], 0\n\t"
"ds_read_b64_tr_b16 a[194:195], v5 offset:1792\n\t"
"v_mul_f32 v134, v21, v134\n\t"
"v_mfma_f32_16x16x32_bf16 v[164:167], a[200:203], a[68:71], v[164:167]\n\t"
"v_mul_f32 v148, v21, v148\n\t"
"v_mfma_f32_16x16x32_bf16 v[52:55], a[160:163], a[232:235], 0\n\t"
"ds_read_b64_tr_b16 a[196:197], v5 offset:1544\n\t"
"v_cvt_pk_bf16_f32 v134, v134, v135\n\t"
"v_mfma_f32_16x16x32_bf16 v[168:171], a[200:203], a[72:75], v[168:171]\n\t"
"v_mul_f32 v151, v21, v151\n\t"
"v_cvt_pk_bf16_f32 v132, v132, v133\n\t"
"v_mfma_f32_16x16x32_bf16 v[184:187], a[204:207], a[72:75], v[184:187]\n\t"
"v_cvt_pk_bf16_f32 v148, v148, v149\n\t"
"v_cvt_pk_bf16_f32 v150, v150, v151\n\t"
"v_mfma_f32_16x16x32_bf16 v[188:191], a[204:207], a[76:79], v[188:191]\n\t"
"v_accvgpr_write_b32 a132, v132\n\t"
"v_accvgpr_write_b32 a133, v134\n\t"
"v_mfma_f32_16x16x32_bf16 v[172:175], a[200:203], a[76:79], v[172:175]\n\t"
"ds_read_b64_tr_b16 a[198:199], v5 offset:1800\n\t"
"v_accvgpr_write_b32 a134, v148\n\t"
"v_mfma_f32_16x16x32_bf16 v[48:51], a[164:167], a[180:183], v[48:51]\n\t"
"s_waitcnt lgkmcnt(4)\n\t"
"buffer_load_dwordx4 v11, s[16:19], s47 offen offset:2048 lds\n\t"
"v_mfma_f32_16x16x32_bf16 v[160:163], a[208:211], a[80:83], v[160:163]\n\t"
"ds_read_b64_tr_b16 a[200:201], v5 offset:13856\n\t"
"v_accvgpr_write_b32 a135, v150\n\t"
"v_mfma_f32_16x16x32_bf16 v[176:179], a[212:215], a[80:83], v[176:179]\n\t"
"v_mul_f32 v137, v22, v137\n\t"
"v_mul_f32 v138, v22, v138\n\t"
"v_mfma_f32_16x16x32_bf16 v[180:183], a[212:215], a[84:87], v[180:183]\n\t"
"ds_read_b64_tr_b16 a[202:203], v5 offset:14112\n\t"
"v_mul_f32 v136, v22, v136\n\t"
"v_mfma_f32_16x16x32_bf16 v[52:55], a[164:167], a[232:235], v[52:55]\n\t"
"v_mfma_f32_16x16x32_bf16 v[164:167], a[208:211], a[84:87], v[164:167]\n\t"
"v_mfma_f32_16x16x32_bf16 v[48:51], a[168:171], a[184:187], v[48:51]\n\t"
"ds_read_b64_tr_b16 a[204:205], v5 offset:13864\n\t"
"v_mul_f32 v152, v22, v152\n\t"
"v_mfma_f32_16x16x32_bf16 v[168:171], a[208:211], a[88:91], v[168:171]\n\t"
"s_addk_i32 m0, 12288\n\t"
"s_addk_i32 s47, 12288\n\t"
"v_mul_f32 v139, v22, v139\n\t"
"v_mfma_f32_16x16x32_bf16 v[184:187], a[212:215], a[88:91], v[184:187]\n\t"
"v_mul_f32 v153, v22, v153\n\t"
"v_mul_f32 v154, v22, v154\n\t"
"v_mfma_f32_16x16x32_bf16 v[188:191], a[212:215], a[92:95], v[188:191]\n\t"
"v_mul_f32 v155, v22, v155\n\t"
"v_cvt_pk_bf16_f32 v138, v138, v139\n\t"
"v_mfma_f32_16x16x32_bf16 v[172:175], a[208:211], a[92:95], v[172:175]\n\t"
"ds_read_b64_tr_b16 a[206:207], v5 offset:14120\n\t"
"v_cvt_pk_bf16_f32 v136, v136, v137\n\t"
"v_mfma_f32_16x16x32_bf16 v[52:55], a[168:171], a[232:235], v[52:55]\n\t"
"s_waitcnt lgkmcnt(4)\n\t"
"buffer_load_dwordx4 v3, s[16:19], s47 offen offset:0 lds\n\t"
"v_mfma_f32_16x16x32_bf16 v[160:163], a[192:195], a[0:3], v[160:163]\n\t"
"ds_read_b64_tr_b16 a[208:209], v5 offset:39008\n\t"
"v_cvt_pk_bf16_f32 v152, v152, v153\n\t"
"v_mfma_f32_16x16x32_bf16 v[176:179], a[196:199], a[0:3], v[176:179]\n\t"
"v_accvgpr_write_b32 a136, v136\n\t"
"v_accvgpr_write_b32 a137, v138\n\t"
"v_mfma_f32_16x16x32_bf16 v[180:183], a[196:199], a[4:7], v[180:183]\n\t"
"ds_read_b64_tr_b16 a[210:211], v5 offset:39264\n\t"
"v_cvt_pk_bf16_f32 v154, v154, v155\n\t"
"v_mfma_f32_16x16x32_bf16 v[48:51], a[172:175], a[188:191], v[48:51]\n\t"
".Lpg5%=:\n"
"s_cbranch_execz .Lpg9%=\n\t"
"buffer_load_dwordx4 v10, s[16:19], s47 offen offset:1024 lds\n\t"
"v_mfma_f32_16x16x32_bf16 v[164:167], a[192:195], a[4:7], v[164:167]\n\t"
"ds_read_b64_tr_b16 a[212:213], v5 offset:39016\n\t"
"v_accvgpr_write_b32 a138, v152\n\t"
"v_mfma_f32_16x16x32_bf16 v[168:171], a[192:195], a[8:11], v[168:171]\n\t"
"v_mul_f32 v140, v23, v140\n\t"
"v_mul_f32 v141, v23, v141\n\t"
"v_mfma_f32_16x16x32_bf16 v[184:187], a[196:199], a[8:11], v[184:187]\n\t"
"v_mul_f32 v142, v23, v142\n\t"
"v_mul_f32 v143, v23, v143\n\t"
"v_mfma_f32_16x16x32_bf16 v[188:191], a[196:199], a[12:15], v[188:191]\n\t"
"ds_read_b64_tr_b16 a[214:215], v5 offset:39272\n\t"
"v_accvgpr_write_b32 a139, v154\n\t"
"v_mfma_f32_16x16x32_bf16 v[52:55], a[172:175], a[232:235], v[52:55]\n\t"
"v_mfma_f32_16x16x32_bf16 v[172:175], a[192:195], a[12:15], v[172:175]\n\t"
"v_mfma_f32_16x16x32_bf16 v[48:51], a[216:219], a[220:223], v[48:51]\n\t"
"s_waitcnt lgkmcnt(4)\n\t"
"ds_read_b64_tr_b16 a[192:193], v5 offset:51328\n\t"
"v_mul_f32 v156, v23, v156\n\t"
"v_mfma_f32_16x16x32_bf16 v[160:163], a[200:203], a[16:19], v[160:163]\n\t"
"v_mul_f32 v157, v23, v157\n\t"
"v_mul_f32 v158, v23, v158\n\t"
"v_mfma_f32_16x16x32_bf16 v[176:179], a[204:207], a[16:19], v[176:179]\n\t"
"v_mul_f32 v159, v23, v159\n\t"
"v_cvt_pk_bf16_f32 v140, v140, v141\n\t"
"v_mfma_f32_16x16x32_bf16 v[180:183], a[204:207], a[20:23], v[180:183]\n\t"
"v_cvt_pk_bf16_f32 v142, v142, v143\n\t"
"v_cvt_pk_bf16_f32 v156, v156, v157\n\t"
"v_mfma_f32_16x16x32_bf16 v[164:167], a[200:203], a[20:23], v[164:167]\n\t"
"ds_read_b64_tr_b16 a[194:195], v5 offset:51584\n\t"
"v_cvt_pk_bf16_f32 v158, v158, v159\n\t"
"v_mfma_f32_16x16x32_bf16 v[52:55], a[216:219], a[232:235], v[52:55]\n\t"
"buffer_load_dwordx4 v11, s[16:19], s47 offen offset:2048 lds\n\t"
"v_mfma_f32_16x16x32_bf16 v[168:171], a[200:203], a[24:27], v[168:171]\n\t"
"s_cmp_lt_u32 s50, 96\n\t"
"ds_read_b64_tr_b16 a[196:197], v5 offset:51336\n\t"
"v_accvgpr_write_b32 a140, v140\n\t"
"v_mfma_f32_16x16x32_bf16 v[184:187], a[204:207], a[24:27], v[184:187]\n\t"
"s_cselect_b32 s80, 0, -96256\n\t"
"ds_read_b64_tr_b16 a[198:199], v5 offset:51592\n\t"
"s_lshr_b32 s81, s50, 2\n\t"
"s_lshl_b32 s81, s81, 12\n\t"
"v_accvgpr_write_b32 a141, v142\n\t"
"v_mfma_f32_16x16x32_bf16 v[188:191], a[204:207], a[28:31], v[188:191]\n\t"
"s_add_u32 s80, s80, s81\n\t"
"s_and_b32 s81, s50, 3\n\t"
"s_lshl_b32 s81, s81, 2\n\t"
"v_accvgpr_write_b32 a142, v156\n\t"
"v_accvgpr_write_b32 a143, v158\n\t"
"v_mfma_f32_16x16x32_bf16 v[172:175], a[200:203], a[28:31], v[172:175]\n\t"
"s_add_u32 s81, s81, s38\n\t"
"v_mul_f32 v160, v20, v160\n\t"
"s_waitcnt lgkmcnt(4)\n\t"
"v_mfma_f32_16x16x32_bf16 v[192:195], a[208:211], a[48:51], v[192:195]\n\t"
"s_lshl_b32 s81, s81, 7\n\t"
"ds_read_b64_tr_b16 a[200:201], v5 offset:63648\n\t"
"s_add_u32 s80, s80, s81\n\t"
"v_mfma_f32_16x16x32_bf16 v[208:211], a[212:215], a[48:51], v[208:211]\n\t"
"s_add_u32 s80, s80, 28602368\n\t"
"ds_read_b64_tr_b16 a[202:203], v5 offset:63904\n\t"
"s_cmp_lt_u32 s50, 192\n\t"
"v_mul_f32 v161, v20, v161\n\t"
"v_mfma_f32_16x16x32_bf16 v[212:215], a[212:215], a[52:55], v[212:215]\n\t"
"s_cselect_b32 s80, s80, 1073741824\n\t"
"v_mul_f32 v162, v20, v162\n\t"
"v_mul_f32 v163, v20, v163\n\t"
"v_mfma_f32_16x16x32_bf16 v[196:199], a[208:211], a[52:55], v[196:199]\n\t"
"v_mul_f32 v176, v20, v176\n\t"
"v_mul_f32 v177, v20, v177\n\t"
"v_mul_f32 v178, v20, v178\n\t"
"v_mfma_f32_16x16x32_bf16 v[200:203], a[208:211], a[56:59], v[200:203]\n\t"
"ds_read_b64_tr_b16 a[204:205], v5 offset:63656\n\t"
"v_mul_f32 v179, v20, v179\n\t"
"v_cvt_pk_bf16_f32 v160, v160, v161\n\t"
"v_mfma_f32_16x16x32_bf16 v[216:219], a[212:215], a[56:59], v[216:219]\n\t"
"ds_read_b64_tr_b16 a[206:207], v5 offset:63912\n\t"
"v_fma_f32 v48, -v45, v52, v48\n\t"
"v_mfma_f32_16x16x32_bf16 v[220:223], a[212:215], a[60:63], v[220:223]\n\t"
"v_fma_f32 v49, -v45, v53, v49\n\t"
"v_fma_f32 v50, -v45, v54, v50\n\t"
"v_cvt_pk_bf16_f32 v162, v162, v163\n\t"
"v_mfma_f32_16x16x32_bf16 v[204:207], a[208:211], a[60:63], v[204:207]\n\t"
"s_waitcnt lgkmcnt(4)\n\t"
"v_fma_f32 v51, -v45, v55, v51\n\t"
"v_mul_f32 v48, v48, v44\n\t"
"v_mfma_f32_16x16x32_bf16 v[192:195], a[192:195], a[64:67], v[192:195]\n\t"
"ds_read_b64_tr_b16 a[208:209], v5 offset:2048\n\t"
"v_mul_f32 v49, v49, v44\n\t"
"v_cvt_pk_bf16_f32 v176, v176, v177\n\t"
"v_mfma_f32_16x16x32_bf16 v[208:211], a[196:199], a[64:67], v[208:211]\n\t"
"ds_read_b64_tr_b16 a[210:211], v5 offset:2304\n\t"
"v_mul_f32 v50, v50, v44\n\t"
"v_cvt_pk_bf16_f32 v178, v178, v179\n\t"
"v_mul_f32 v51, v51, v44\n\t"
"v_mfma_f32_16x16x32_bf16 v[212:215], a[196:199], a[68:71], v[212:215]\n\t"
"buffer_store_dwordx4 v[48:51], v47, s[8:11], s80 offen offset:0 sc1\n\t"
"v_mfma_f32_16x16x32_bf16 v[196:199], a[192:195], a[68:71], v[196:199]\n\t"
"s_mov_b64 s[64:65], s[34:35]\n\t"
"s_mov_b32 s66, 1966080\n\t"
"v_bfe_u32 v56, %[vT], 1, 5\n\t"
"v_mfma_f32_16x16x32_bf16 v[200:203], a[192:195], a[72:75], v[200:203]\n\t"
"ds_read_b64_tr_b16 a[212:213], v5 offset:2056\n\t"
"v_mul_u32_u24 v56, 1280, v56\n\t"
"v_mfma_f32_16x16x32_bf16 v[216:219], a[196:199], a[72:75], v[216:219]\n\t"
"s_waitcnt lgkmcnt(3)\n\t"
"ds_read_b64_tr_b16 a[214:215], v5 offset:2312\n\t"
"v_and_b32 v57, 1, %[vT]\n\t"
"v_accvgpr_write_b32 a144, v160\n\t"
"v_mfma_f32_16x16x32_bf16 v[220:223], a[196:199], a[76:79], v[220:223]\n\t"
"s_lshl_b32 s87, s38, 11\n\t"
"v_lshl_add_u32 v56, v57, 4, v56\n\t"
"v_accvgpr_write_b32 a145, v162\n\t"
"v_mfma_f32_16x16x32_bf16 v[204:207], a[192:195], a[76:79], v[204:207]\n\t"
"s_add_u32 s87, s87, 147456\n\t"
"v_accvgpr_write_b32 a146, v176\n\t"
"v_accvgpr_write_b32 a147, v178\n\t"
"v_mfma_f32_16x16x32_bf16 v[192:195], a[200:203], a[80:83], v[192:195]\n\t"
"ds_read_b64_tr_b16 a[192:193], v5 offset:14368\n\t"
"v_mul_f32 v164, v21, v164\n\t"
"v_mul_f32 v165, v21, v165\n\t"
"v_mfma_f32_16x16x32_bf16 v[208:211], a[204:207], a[80:83], v[208:211]\n\t"
"ds_read_b64_tr_b16 a[194:195], v5 offset:14624\n\t"
"v_mul_f32 v166, v21, v166\n\t"
"v_mfma_f32_16x16x32_bf16 v[212:215], a[204:207], a[84:87], v[212:215]\n\t"
"v_mul_f32 v167, v21, v167\n\t"
"v_mul_f32 v180, v21, v180\n\t"
"v_mul_f32 v181, v21, v181\n\t"
"v_mfma_f32_16x16x32_bf16 v[196:199], a[200:203], a[84:87], v[196:199]\n\t"
"v_mul_f32 v182, v21, v182\n\t"
"v_mul_f32 v183, v21, v183\n\t"
"v_cvt_pk_bf16_f32 v164, v164, v165\n\t"
"v_mfma_f32_16x16x32_bf16 v[200:203], a[200:203], a[88:91], v[200:203]\n\t"
"ds_read_b64_tr_b16 a[196:197], v5 offset:14376\n\t"
"v_cvt_pk_bf16_f32 v166, v166, v167\n\t"
"v_mfma_f32_16x16x32_bf16 v[216:219], a[204:207], a[88:91], v[216:219]\n\t"
"ds_read_b64_tr_b16 a[198:199], v5 offset:14632\n\t"
"v_cvt_pk_bf16_f32 v180, v180, v181\n\t"
"v_cvt_pk_bf16_f32 v182, v182, v183\n\t"
"v_mfma_f32_16x16x32_bf16 v[220:223], a[204:207], a[92:95], v[220:223]\n\t"
"v_accvgpr_write_b32 a148, v164\n\t"
"v_accvgpr_write_b32 a149, v166\n\t"
"v_mfma_f32_16x16x32_bf16 v[204:207], a[200:203], a[92:95], v[204:207]\n\t"
"s_waitcnt lgkmcnt(4)\n\t"
"v_accvgpr_write_b32 a150, v180\n\t"
"v_accvgpr_write_b32 a151, v182\n\t"
"v_mul_f32 v168, v22, v168\n\t"
"v_mfma_f32_16x16x32_bf16 v[192:195], a[208:211], a[0:3], v[192:195]\n\t"
"ds_read_b64_tr_b16 a[200:201], v5 offset:39520\n\t"
"v_mul_f32 v169, v22, v169\n\t"
"v_mul_f32 v170, v22, v170\n\t"
"v_mfma_f32_16x16x32_bf16 v[208:211], a[212:215], a[0:3], v[208:211]\n\t"
"ds_read_b64_tr_b16 a[202:203], v5 offset:39776\n\t"
"v_mul_f32 v171, v22, v171\n\t"
"v_mfma_f32_16x16x32_bf16 v[212:215], a[212:215], a[4:7], v[212:215]\n\t"
"v_mul_f32 v184, v22, v184\n\t"
"v_mul_f32 v185, v22, v185\n\t"
"v_mul_f32 v186, v22, v186\n\t"
"v_mfma_f32_16x16x32_bf16 v[196:199], a[208:211], a[4:7], v[196:199]\n\t"
"v_mul_f32 v187, v22, v187\n\t"
"v_cvt_pk_bf16_f32 v168, v168, v169\n\t"
"v_mfma_f32_16x16x32_bf16 v[200:203], a[208:211], a[8:11], v[200:203]\n\t"
"ds_read_b64_tr_b16 a[204:205], v5 offset:39528\n\t"
"v_cvt_pk_bf16_f32 v170, v170, v171\n\t"
"v_cvt_pk_bf16_f32 v184, v184, v185\n\t"
"v_mfma_f32_16x16x32_bf16 v[216:219], a[212:215], a[8:11], v[216:219]\n\t"
"ds_read_b64_tr_b16 a[206:207], v5 offset:39784\n\t"
"v_cvt_pk_bf16_f32 v186, v186, v187\n\t"
"v_accvgpr_write_b32 a152, v168\n\t"
"v_mfma_f32_16x16x32_bf16 v[220:223], a[212:215], a[12:15], v[220:223]\n\t"
"v_accvgpr_write_b32 a153, v170\n\t"
"v_accvgpr_write_b32 a154, v184\n\t"
"v_mfma_f32_16x16x32_bf16 v[204:207], a[208:211], a[12:15], v[204:207]\n\t"
"s_waitcnt lgkmcnt(4)\n\t"
"v_accvgpr_write_b32 a155, v186\n\t"
"v_mul_f32 v172, v23, v172\n\t"
"v_mul_f32 v173, v23, v173\n\t"
"v_mfma_f32_16x16x32_bf16 v[192:195], a[192:195], a[16:19], v[192:195]\n\t"
"ds_read_b64_tr_b16 a[208:209], v5 offset:51840\n\t"
"v_mul_f32 v174, v23, v174\n\t"
"v_mfma_f32_16x16x32_bf16 v[208:211], a[196:199], a[16:19], v[208:211]\n\t"
"ds_read_b64_tr_b16 a[210:211], v5 offset:52096\n\t"
"v_mul_f32 v175, v23, v175\n\t"
"v_mul_f32 v188, v23, v188\n\t"
"v_mfma_f32_16x16x32_bf16 v[212:215], a[196:199], a[20:23], v[212:215]\n\t"
"v_mul_f32 v189, v23, v189\n\t"
"v_mul_f32 v190, v23, v190\n\t"
"v_mul_f32 v191, v23, v191\n\t"
"v_mfma_f32_16x16x32_bf16 v[196:199], a[192:195], a[20:23], v[196:199]\n\t"
"v_cvt_pk_bf16_f32 v172, v172, v173\n\t"
"v_cvt_pk_bf16_f32 v174, v174, v175\n\t"
"v_mfma_f32_16x16x32_bf16 v[200:203], a[192:195], a[24:27], v[200:203]\n\t"
"ds_read_b64_tr_b16 a[212:213], v5 offset:51848\n\t"
"v_cvt_pk_bf16_f32 v188, v188, v189\n\t"
"v_cvt_pk_bf16_f32 v190, v190, v191\n\t"
"v_mfma_f32_16x16x32_bf16 v[216:219], a[196:199], a[24:27], v[216:219]\n\t"
"ds_read_b64_tr_b16 a[214:215], v5 offset:52104\n\t"
"v_accvgpr_write_b32 a156, v172\n\t"
"v_mfma_f32_16x16x32_bf16 v[220:223], a[196:199], a[28:31], v[220:223]\n\t"
"v_accvgpr_write_b32 a157, v174\n\t"
"v_accvgpr_write_b32 a158, v188\n\t"
"v_accvgpr_write_b32 a159, v190\n\t"
"v_mfma_f32_16x16x32_bf16 v[204:207], a[192:195], a[28:31], v[204:207]\n\t"
"s_waitcnt lgkmcnt(4)\n\t"
"v_mul_f32 v192, v20, v192\n\t"
"v_mul_f32 v193, v20, v193\n\t"
"v_mfma_f32_16x16x32_bf16 v[224:227], a[200:203], a[48:51], v[224:227]\n\t"
"ds_read_b64_tr_b16 a[192:193], v5 offset:64160\n\t"
"v_mul_f32 v194, v20, v194\n\t"
"v_mfma_f32_16x16x32_bf16 v[240:243], a[204:207], a[48:51], v[240:243]\n\t"
"ds_read_b64_tr_b16 a[194:195], v5 offset:64416\n\t"
"v_mul_f32 v195, v20, v195\n\t"
"v_mfma_f32_16x16x32_bf16 v[244:247], a[204:207], a[52:55], v[244:247]\n\t"
"v_mul_f32 v208, v20, v208\n\t"
"v_mul_f32 v209, v20, v209\n\t"
"v_mfma_f32_16x16x32_bf16 v[228:231], a[200:203], a[52:55], v[228:231]\n\t"
"v_mul_f32 v210, v20, v210\n\t"
"v_mul_f32 v211, v20, v211\n\t"
"v_mfma_f32_16x16x32_bf16 v[232:235], a[200:203], a[56:59], v[232:235]\n\t"
"ds_read_b64_tr_b16 a[196:197], v5 offset:64168\n\t"
"v_cvt_pk_bf16_f32 v192, v192, v193\n\t"
"v_mfma_f32_16x16x32_bf16 v[248:251], a[204:207], a[56:59], v[248:251]\n\t"
"ds_read_b64_tr_b16 a[198:199], v5 offset:64424\n\t"
"v_cvt_pk_bf16_f32 v194, v194, v195\n\t"
"v_cvt_pk_bf16_f32 v208, v208, v209\n\t"
"v_mfma_f32_16x16x32_bf16 v[252:255], a[204:207], a[60:63], v[252:255]\n\t"
"v_cvt_pk_bf16_f32 v210, v210, v211\n\t"
"v_accvgpr_write_b32 a160, v192\n\t"
"v_mfma_f32_16x16x32_bf16 v[236:239], a[200:203], a[60:63], v[236:239]\n\t"
"s_waitcnt lgkmcnt(4)\n\t"
"v_accvgpr_write_b32 a161, v194\n\t"
"v_accvgpr_write_b32 a162, v208\n\t"
"v_mfma_f32_16x16x32_bf16 v[224:227], a[208:211], a[64:67], v[224:227]\n\t"
"ds_read_b64_tr_b16 a[200:201], v5 offset:2560\n\t"
"v_accvgpr_write_b32 a163, v210\n\t"
"v_mfma_f32_16x16x32_bf16 v[240:243], a[212:215], a[64:67], v[240:243]\n\t"
"ds_read_b64_tr_b16 a[202:203], v5 offset:2816\n\t"
"v_mul_f32 v196, v21, v196\n\t"
"v_mfma_f32_16x16x32_bf16 v[244:247], a[212:215], a[68:71], v[244:247]\n\t"
"v_mul_f32 v197, v21, v197\n\t"
"v_mul_f32 v198, v21, v198\n\t"
"v_mfma_f32_16x16x32_bf16 v[228:231], a[208:211], a[68:71], v[228:231]\n\t"
"v_mul_f32 v199, v21, v199\n\t"
"v_mul_f32 v212, v21, v212\n\t"
"v_mfma_f32_16x16x32_bf16 v[232:235], a[208:211], a[72:75], v[232:235]\n\t"
"ds_read_b64_tr_b16 a[204:205], v5 offset:2568\n\t"
"v_mul_f32 v213, v21, v213\n\t"
"v_mfma_f32_16x16x32_bf16 v[248:251], a[212:215], a[72:75], v[248:251]\n\t"
"ds_read_b64_tr_b16 a[206:207], v5 offset:2824\n\t"
"v_mul_f32 v214, v21, v214\n\t"
"v_mfma_f32_16x16x32_bf16 v[252:255], a[212:215], a[76:79], v[252:255]\n\t"
"v_mul_f32 v215, v21, v215\n\t"
"v_cvt_pk_bf16_f32 v196, v196, v197\n\t"
"v_mfma_f32_16x16x32_bf16 v[236:239], a[208:211], a[76:79], v[236:239]\n\t"
"s_waitcnt lgkmcnt(4)\n\t"
"v_cvt_pk_bf16_f32 v198, v198, v199\n\t"
"v_cvt_pk_bf16_f32 v212, v212, v213\n\t"
"v_mfma_f32_16x16x32_bf16 v[224:227], a[192:195], a[80:83], v[224:227]\n\t"
"ds_read_b64_tr_b16 a[208:209], v5 offset:14880\n\t"
"v_cvt_pk_bf16_f32 v214, v214, v215\n\t"
"v_mfma_f32_16x16x32_bf16 v[240:243], a[196:199], a[80:83], v[240:243]\n\t"
"ds_read_b64_tr_b16 a[210:211], v5 offset:15136\n\t"
"v_accvgpr_write_b32 a164, v196\n\t"
"v_mfma_f32_16x16x32_bf16 v[244:247], a[196:199], a[84:87], v[244:247]\n\t"
"v_accvgpr_write_b32 a165, v198\n\t"
"v_accvgpr_write_b32 a166, v212\n\t"
"v_accvgpr_write_b32 a167, v214\n\t"
"v_mfma_f32_16x16x32_bf16 v[228:231], a[192:195], a[84:87], v[228:231]\n\t"
"v_mul_f32 v200, v22, v200\n\t"
"v_mul_f32 v201, v22, v201\n\t"
"v_mfma_f32_16x16x32_bf16 v[232:235], a[192:195], a[88:91], v[232:235]\n\t"
"ds_read_b64_tr_b16 a[212:213], v5 offset:14888\n\t"
"v_mul_f32 v202, v22, v202\n\t"
"v_mfma_f32_16x16x32_bf16 v[248:251], a[196:199], a[88:91], v[248:251]\n\t"
"ds_read_b64_tr_b16 a[214:215], v5 offset:15144\n\t"
"v_mul_f32 v203, v22, v203\n\t"
"v_mfma_f32_16x16x32_bf16 v[252:255], a[196:199], a[92:95], v[252:255]\n\t"
"s_waitcnt vmcnt(13)\n\t"
"v_mul_f32 v216, v22, v216\n\t"
"v_mul_f32 v217, v22, v217\n\t"
"v_mfma_f32_16x16x32_bf16 v[236:239], a[192:195], a[92:95], v[236:239]\n\t"
"s_barrier\n\t"
"s_waitcnt lgkmcnt(4)\n\t"
"v_mfma_f32_16x16x32_bf16 v[224:227], a[200:203], a[0:3], v[224:227]\n\t"
"ds_read_b128 a[192:195], v6 offset:0\n\t"
"v_mfma_f32_16x16x32_bf16 v[240:243], a[204:207], a[0:3], v[240:243]\n\t"
"v_mul_f32 v218, v22, v218\n\t"
"v_mul_f32 v219, v22, v219\n\t"
"v_mfma_f32_16x16x32_bf16 v[244:247], a[204:207], a[4:7], v[244:247]\n\t"
"v_cvt_pk_bf16_f32 v200, v200, v201\n\t"
"v_cvt_pk_bf16_f32 v202, v202, v203\n\t"
"v_mfma_f32_16x16x32_bf16 v[228:231], a[200:203], a[4:7], v[228:231]\n\t"
"v_cvt_pk_bf16_f32 v216, v216, v217\n\t"
"v_cvt_pk_bf16_f32 v218, v218, v219\n\t"
"v_mfma_f32_16x16x32_bf16 v[232:235], a[200:203], a[8:11], v[232:235]\n\t"
"ds_read_b128 a[196:199], v12 offset:0\n\t"
"v_accvgpr_write_b32 a168, v200\n\t"
"v_mfma_f32_16x16x32_bf16 v[248:251], a[204:207], a[8:11], v[248:251]\n\t"
"v_accvgpr_write_b32 a169, v202\n\t"
"v_accvgpr_write_b32 a170, v216\n\t"
"v_mfma_f32_16x16x32_bf16 v[252:255], a[204:207], a[12:15], v[252:255]\n\t"
"v_accvgpr_write_b32 a171, v218\n\t"
"v_mul_f32 v204, v23, v204\n\t"
"v_mfma_f32_16x16x32_bf16 v[236:239], a[200:203], a[12:15], v[236:239]\n\t"
"s_waitcnt lgkmcnt(2)\n\t"
"v_mul_f32 v205, v23, v205\n\t"
"v_mul_f32 v206, v23, v206\n\t"
"v_mfma_f32_16x16x32_bf16 v[224:227], a[208:211], a[16:19], v[224:227]\n\t"
"ds_read_b128 a[200:203], v6 offset:1024\n\t"
"v_mul_f32 v207, v23, v207\n\t"
"v_mul_f32 v220, v23, v220\n\t"
"v_mfma_f32_16x16x32_bf16 v[240:243], a[212:215], a[16:19], v[240:243]\n\t"
"v_mul_f32 v221, v23, v221\n\t"
"v_mul_f32 v222, v23, v222\n\t"
"v_mfma_f32_16x16x32_bf16 v[244:247], a[212:215], a[20:23], v[244:247]\n\t"
"v_mul_f32 v223, v23, v223\n\t"
"v_cvt_pk_bf16_f32 v204, v204, v205\n\t"
"v_mfma_f32_16x16x32_bf16 v[228:231], a[208:211], a[20:23], v[228:231]\n\t"
"v_cvt_pk_bf16_f32 v206, v206, v207\n\t"
"v_cvt_pk_bf16_f32 v220, v220, v221\n\t"
"v_mfma_f32_16x16x32_bf16 v[232:235], a[208:211], a[24:27], v[232:235]\n\t"
"ds_read_b128 a[204:207], v12 offset:1024\n\t"
"v_cvt_pk_bf16_f32 v222, v222, v223\n\t"
"v_mfma_f32_16x16x32_bf16 v[248:251], a[212:215], a[24:27], v[248:251]\n\t"
"v_accvgpr_write_b32 a172, v204\n\t"
"v_accvgpr_write_b32 a173, v206\n\t"
"v_mfma_f32_16x16x32_bf16 v[252:255], a[212:215], a[28:31], v[252:255]\n\t"
"v_accvgpr_write_b32 a174, v220\n\t"
"v_accvgpr_write_b32 a175, v222\n\t"
"v_mfma_f32_16x16x32_bf16 v[236:239], a[208:211], a[28:31], v[236:239]\n\t"
"s_waitcnt lgkmcnt(2)\n\t"
"v_mul_f32 v224, v20, v224\n\t"
"v_mul_f32 v225, v20, v225\n\t"
"v_mul_f32 v226, v20, v226\n\t"
"v_mfma_f32_16x16x32_bf16 v[64:67], a[192:195], a[96:99], 0\n\t"
"ds_read_b128 a[208:211], v6 offset:2048\n\t"
"v_mul_f32 v227, v20, v227\n\t"
"v_mul_f32 v240, v20, v240\n\t"
"v_mfma_f32_16x16x32_bf16 v[68:71], a[192:195], a[100:103], 0\n\t"
"v_mul_f32 v241, v20, v241\n\t"
"v_mul_f32 v242, v20, v242\n\t"
"v_mul_f32 v243, v20, v243\n\t"
"v_mfma_f32_16x16x32_bf16 v[72:75], a[192:195], a[104:107], 0\n\t"
"v_cvt_pk_bf16_f32 v224, v224, v225\n\t"
"v_cvt_pk_bf16_f32 v226, v226, v227\n\t"
"v_cvt_pk_bf16_f32 v240, v240, v241\n\t"
"v_mfma_f32_16x16x32_bf16 v[76:79], a[192:195], a[108:111], 0\n\t"
"v_cvt_pk_bf16_f32 v242, v242, v243\n\t"
"v_accvgpr_write_b32 a176, v224\n\t"
"v_accvgpr_write_b32 a177, v226\n\t"
"v_mfma_f32_16x16x32_bf16 v[64:67], a[196:199], a[112:115], v[64:67]\n\t"
"ds_read_b128 a[212:215], v6 offset:6144\n\t"
"v_accvgpr_write_b32 a178, v240\n\t"
"v_mfma_f32_16x16x32_bf16 v[68:71], a[196:199], a[116:119], v[68:71]\n\t"
"v_accvgpr_write_b32 a179, v242\n\t"
"v_mul_f32 v228, v21, v228\n\t"
"v_mul_f32 v229, v21, v229\n\t"
"v_mfma_f32_16x16x32_bf16 v[72:75], a[196:199], a[120:123], v[72:75]\n\t"
"v_mul_f32 v230, v21, v230\n\t"
"v_mul_f32 v231, v21, v231\n\t"
"v_mul_f32 v244, v21, v244\n\t"
"v_mfma_f32_16x16x32_bf16 v[76:79], a[196:199], a[124:127], v[76:79]\n\t"
"s_waitcnt lgkmcnt(2)\n\t"
"v_mul_f32 v245, v21, v245\n\t"
"v_mul_f32 v246, v21, v246\n\t"
"v_mul_f32 v247, v21, v247\n\t"
"v_mfma_f32_16x16x32_bf16 v[64:67], a[200:203], a[128:131], v[64:67]\n\t"
"ds_read_b128 a[192:195], v12 offset:2048\n\t"
"v_cvt_pk_bf16_f32 v228, v228, v229\n\t"
"v_cvt_pk_bf16_f32 v230, v230, v231\n\t"
"v_mfma_f32_16x16x32_bf16 v[68:71], a[200:203], a[132:135], v[68:71]\n\t"
"v_cvt_pk_bf16_f32 v244, v244, v245\n\t"
"v_cvt_pk_bf16_f32 v246, v246, v247\n\t"
"v_accvgpr_write_b32 a180, v228\n\t"
"v_mfma_f32_16x16x32_bf16 v[72:75], a[200:203], a[136:139], v[72:75]\n\t"
"v_accvgpr_write_b32 a181, v230\n\t"
"v_accvgpr_write_b32 a182, v244\n\t"
"v_accvgpr_write_b32 a183, v246\n\t"
"v_mfma_f32_16x16x32_bf16 v[76:79], a[200:203], a[140:143], v[76:79]\n\t"
"v_mul_f32 v232, v22, v232\n\t"
"v_mul_f32 v233, v22, v233\n\t"
"v_mul_f32 v234, v22, v234\n\t"
"v_mfma_f32_16x16x32_bf16 v[64:67], a[204:207], a[144:147], v[64:67]\n\t"
"ds_read_b128 a[196:199], v12 offset:6144\n\t"
"v_mul_f32 v235, v22, v235\n\t"
"v_mul_f32 v248, v22, v248\n\t"
"v_mfma_f32_16x16x32_bf16 v[68:71], a[204:207], a[148:151], v[68:71]\n\t"
"v_mul_f32 v249, v22, v249\n\t"
"v_mul_f32 v250, v22, v250\n\t"
"v_mul_f32 v251, v22, v251\n\t"
"v_mfma_f32_16x16x32_bf16 v[72:75], a[204:207], a[152:155], v[72:75]\n\t"
"v_cvt_pk_bf16_f32 v232, v232, v233\n\t"
"v_cvt_pk_bf16_f32 v234, v234, v235\n\t"
"v_cvt_pk_bf16_f32 v248, v248, v249\n\t"
"v_mfma_f32_16x16x32_bf16 v[76:79], a[204:207], a[156:159], v[76:79]\n\t"
"s_waitcnt lgkmcnt(2)\n\t"
"v_cvt_pk_bf16_f32 v250, v250, v251\n\t"
"v_accvgpr_write_b32 a184, v232\n\t"
"v_accvgpr_write_b32 a185, v234\n\t"
"v_mfma_f32_16x16x32_bf16 v[64:67], a[208:211], a[160:163], v[64:67]\n\t"
"ds_read_b128 a[200:203], v6 offset:7168\n\t"
"v_accvgpr_write_b32 a186, v248\n\t"
"v_mfma_f32_16x16x32_bf16 v[68:71], a[208:211], a[164:167], v[68:71]\n\t"
"v_accvgpr_write_b32 a187, v250\n\t"
"v_mul_f32 v236, v23, v236\n\t"
"v_mul_f32 v237, v23, v237\n\t"
"v_mfma_f32_16x16x32_bf16 v[72:75], a[208:211], a[168:171], v[72:75]\n\t"
"v_mul_f32 v238, v23, v238\n\t"
"v_mul_f32 v239, v23, v239\n\t"
"v_mul_f32 v252, v23, v252\n\t"
"v_mfma_f32_16x16x32_bf16 v[76:79], a[208:211], a[172:175], v[76:79]\n\t"
"v_mul_f32 v253, v23, v253\n\t"
"v_mul_f32 v254, v23, v254\n\t"
"v_mul_f32 v255, v23, v255\n\t"
"v_mfma_f32_16x16x32_bf16 v[80:83], a[212:215], a[96:99], 0\n\t"
"v_cvt_pk_bf16_f32 v236, v236, v237\n\t"
"v_cvt_pk_bf16_f32 v238, v238, v239\n\t"
"v_mfma_f32_16x16x32_bf16 v[84:87], a[212:215], a[100:103], 0\n\t"
"v_cvt_pk_bf16_f32 v252, v252, v253\n\t"
"v_cvt_pk_bf16_f32 v254, v254, v255\n\t"
"v_mfma_f32_16x16x32_bf16 v[88:91], a[212:215], a[104:107], 0\n\t"
"v_accvgpr_write_b32 a190, v252\n\t"
"v_accvgpr_write_b32 a191, v254\n\t"
"v_mfma_f32_16x16x32_bf16 v[92:95], a[212:215], a[108:111], 0\n\t"
"ds_read_b128 a[204:207], v12 offset:7168\n\t"
"s_waitcnt lgkmcnt(2)\n\t"
"v_mfma_f32_16x16x32_bf16 v[64:67], a[192:195], a[176:179], v[64:67]\n\t"
"ds_read_b128 a[208:211], v6 offset:8192\n\t"
"v_accvgpr_write_b32 a188, v236\n\t"
"v_mfma_f32_16x16x32_bf16 v[68:71], a[192:195], a[180:183], v[68:71]\n\t"
"s_cmp_eq_u32 s49, 1\n\t"
"v_accvgpr_write_b32 a189, v238\n\t"
"s_cbranch_scc1 .Lexact%=\n\t"
"v_mfma_f32_16x16x32_bf16 v[72:75], a[192:195], a[184:187], v[72:75]\n\t"
"v_mfma_f32_16x16x32_bf16 v[76:79], a[192:195], a[188:191], v[76:79]\n\t"
"v_mfma_f32_16x16x32_bf16 v[80:83], a[196:199], a[112:115], v[80:83]\n\t"
"ds_read_b128 a[212:215], v12 offset:8192\n\t"
"v_mfma_f32_16x16x32_bf16 v[84:87], a[196:199], a[116:119], v[84:87]\n\t"
"v_mfma_f32_16x16x32_bf16 v[88:91], a[196:199], a[120:123], v[88:91]\n\t"
"s_waitcnt vmcnt(10)\n\t"
"v_max3_f32 v48, v64, v65, v66\n\t"
"v_max3_f32 v49, v68, v69, v70\n\t"
"v_mfma_f32_16x16x32_bf16 v[92:95], a[196:199], a[124:127], v[92:95]\n\t"
"s_barrier\n\t"
"s_waitcnt lgkmcnt(2)\n\t"
"v_mfma_f32_16x16x32_bf16 v[80:83], a[200:203], a[128:131], v[80:83]\n\t"
"ds_read_b128 a[192:195], v6 offset:12288\n\t"
"v_max3_f32 v50, v72, v73, v74\n\t"
"v_mfma_f32_16x16x32_bf16 v[84:87], a[200:203], a[132:135], v[84:87]\n\t"
"v_max3_f32 v51, v76, v77, v78\n\t"
"v_max3_f32 v48, v48, v67, v24\n\t"
"v_max3_f32 v49, v49, v71, v25\n\t"
"v_mfma_f32_16x16x32_bf16 v[88:91], a[200:203], a[136:139], v[88:91]\n\t"
"v_max3_f32 v50, v50, v75, v26\n\t"
"v_max3_f32 v51, v51, v79, v27\n\t"
"v_mov_b32 v52, v48\n\t"
"v_mfma_f32_16x16x32_bf16 v[92:95], a[200:203], a[140:143], v[92:95]\n\t"
"v_mov_b32 v53, v49\n\t"
"v_mov_b32 v54, v50\n\t"
"v_mov_b32 v55, v51\n\t"
"v_mfma_f32_16x16x32_bf16 v[80:83], a[204:207], a[144:147], v[80:83]\n\t"
"ds_read_b128 a[196:199], v6 offset:18432\n\t"
"v_permlane16_swap_b32 v48, v52\n\t"
"v_permlane16_swap_b32 v49, v53\n\t"
"v_permlane16_swap_b32 v50, v54\n\t"
"v_mfma_f32_16x16x32_bf16 v[84:87], a[204:207], a[148:151], v[84:87]\n\t"
"v_permlane16_swap_b32 v51, v55\n\t"
"v_max_f32 v48, v48, v52\n\t"
"v_max_f32 v49, v49, v53\n\t"
"v_mfma_f32_16x16x32_bf16 v[88:91], a[204:207], a[152:155], v[88:91]\n\t"
"v_max_f32 v50, v50, v54\n\t"
"v_max_f32 v51, v51, v55\n\t"
"v_mov_b32 v52, v48\n\t"
"v_mfma_f32_16x16x32_bf16 v[92:95], a[204:207], a[156:159], v[92:95]\n\t"
"s_waitcnt lgkmcnt(2)\n\t"
"v_mov_b32 v53, v49\n\t"
"v_mov_b32 v54, v50\n\t"
"v_mov_b32 v55, v51\n\t"
"v_permlane32_swap_b32 v48, v52\n\t"
"v_mfma_f32_16x16x32_bf16 v[80:83], a[208:211], a[160:163], v[80:83]\n\t"
"ds_read_b128 a[200:203], v12 offset:12288\n\t"
"v_permlane32_swap_b32 v49, v53\n\t"
"v_permlane32_swap_b32 v50, v54\n\t"
"v_mfma_f32_16x16x32_bf16 v[84:87], a[208:211], a[164:167], v[84:87]\n\t"
"v_permlane32_swap_b32 v51, v55\n\t"
"v_max_f32 v48, v48, v52\n\t"
"v_max_f32 v49, v49, v53\n\t"
"v_mfma_f32_16x16x32_bf16 v[88:91], a[208:211], a[168:171], v[88:91]\n\t"
"v_max_f32 v50, v50, v54\n\t"
"v_max_f32 v51, v51, v55\n\t"
"v_sub_f32 v32, 0, v48\n\t"
"v_sub_f32 v33, 0, v48\n\t"
"v_mfma_f32_16x16x32_bf16 v[92:95], a[208:211], a[172:175], v[92:95]\n\t"
"v_sub_f32 v34, 0, v48\n\t"
"v_sub_f32 v35, 0, v48\n\t"
"v_sub_f32 v36, 0, v49\n\t"
"v_mfma_f32_16x16x32_bf16 v[80:83], a[212:215], a[176:179], v[80:83]\n\t"
"ds_read_b128 a[204:207], v12 offset:18432\n\t"
"v_sub_f32 v37, 0, v49\n\t"
"v_sub_f32 v38, 0, v49\n\t"
"v_mfma_f32_16x16x32_bf16 v[84:87], a[212:215], a[180:183], v[84:87]\n\t"
"v_sub_f32 v39, 0, v49\n\t"
"v_sub_f32 v40, 0, v50\n\t"
"v_sub_f32 v41, 0, v50\n\t"
"v_mfma_f32_16x16x32_bf16 v[88:91], a[212:215], a[184:187], v[88:91]\n\t"
"s_waitcnt lgkmcnt(2)\n\t"
"v_sub_f32 v42, 0, v50\n\t"
"v_sub_f32 v43, 0, v50\n\t"
"v_sub_f32 v44, 0, v51\n\t"
"v_sub_f32 v45, 0, v51\n\t"
"v_mfma_f32_16x16x32_bf16 v[92:95], a[212:215], a[188:191], v[92:95]\n\t"
"s_nop 0\n\t"
"v_sub_f32 v46, 0, v51\n\t"
"v_sub_f32 v47, 0, v51\n\t"
"v_mfma_f32_16x16x32_bf16 v[96:99], a[192:195], a[96:99], v[32:35]\n\t"
"ds_read_b128 a[208:211], v6 offset:13312\n\t"
"v_add_f32 v64, v64, v32\n\t"
"v_mfma_f32_16x16x32_bf16 v[112:115], a[196:199], a[96:99], v[32:35]\n\t"
"v_exp_f32 v64, v64\n\t"
"v_add_f32 v65, v65, v32\n\t"
"v_mfma_f32_16x16x32_bf16 v[116:119], a[196:199], a[100:103], v[36:39]\n\t"
"v_exp_f32 v65, v65\n\t"
"v_mfma_f32_16x16x32_bf16 v[100:103], a[192:195], a[100:103], v[36:39]\n\t"
"v_add_f32 v66, v66, v32\n\t"
"v_add_f32 v67, v67, v32\n\t"
"v_mfma_f32_16x16x32_bf16 v[104:107], a[192:195], a[104:107], v[40:43]\n\t"
"ds_read_b128 a[212:215], v6 offset:19456\n\t"
"v_add_f32 v80, v80, v32\n\t"
"v_add_f32 v81, v81, v32\n\t"
"v_mfma_f32_16x16x32_bf16 v[120:123], a[196:199], a[104:107], v[40:43]\n\t"
"v_add_f32 v82, v82, v32\n\t"
"v_add_f32 v83, v83, v32\n\t"
"v_add_f32 v68, v68, v36\n\t"
"v_mfma_f32_16x16x32_bf16 v[124:127], a[196:199], a[108:111], v[44:47]\n\t"
"s_waitcnt vmcnt(7)\n\t"
"s_barrier\n\t"
"v_mfma_f32_16x16x32_bf16 v[108:111], a[192:195], a[108:111], v[44:47]\n\t"
"s_waitcnt lgkmcnt(2)\n\t"
"v_add_f32 v69, v69, v36\n\t"
"v_add_f32 v70, v70, v36\n\t"
"v_add_f32 v71, v71, v36\n\t"
"v_mfma_f32_16x16x32_bf16 v[96:99], a[200:203], a[112:115], v[96:99]\n\t"
"ds_read_b128 a[192:195], v12 offset:13312\n\t"
"v_add_f32 v84, v84, v36\n\t"
"v_add_f32 v85, v85, v36\n\t"
"v_mfma_f32_16x16x32_bf16 v[112:115], a[204:207], a[112:115], v[112:115]\n\t"
"s_add_u32 m0, s44, 36864\n\t"
"v_exp_f32 v66, v66\n\t"
"v_mfma_f32_16x16x32_bf16 v[116:119], a[204:207], a[116:119], v[116:119]\n\t"
"s_mov_b32 s48, 0\n\t"
"v_exp_f32 v67, v67\n\t"
"v_add_f32 v86, v86, v36\n\t"
"v_mfma_f32_16x16x32_bf16 v[100:103], a[200:203], a[116:119], v[100:103]\n\t"
"buffer_load_dwordx4 v4, s[20:23], s48 offen offset:0 lds\n\t"
"v_mfma_f32_16x16x32_bf16 v[104:107], a[200:203], a[120:123], v[104:107]\n\t"
"ds_read_b128 a[196:199], v12 offset:19456\n\t"
"v_add_f32 v87, v87, v36\n\t"
"v_cvt_pk_bf16_f32 v128, v64, v65\n\t"
"v_mfma_f32_16x16x32_bf16 v[120:123], a[204:207], a[120:123], v[120:123]\n\t"
"v_exp_f32 v80, v80\n\t"
"v_mfma_f32_16x16x32_bf16 v[124:127], a[204:207], a[124:127], v[124:127]\n\t"
"v_exp_f32 v81, v81\n\t"
"v_mfma_f32_16x16x32_bf16 v[108:111], a[200:203], a[124:127], v[108:111]\n\t"
"s_waitcnt lgkmcnt(2)\n\t"
"v_exp_f32 v82, v82\n\t"
"v_cvt_pk_bf16_f32 v129, v66, v67\n\t"
"v_mfma_f32_16x16x32_bf16 v[96:99], a[208:211], a[128:131], v[96:99]\n\t"
"ds_read_b128 a[200:203], v6 offset:14336\n\t"
"v_cvt_pk_bf16_f32 v130, v80, v81\n\t"
"v_mfma_f32_16x16x32_bf16 v[112:115], a[212:215], a[128:131], v[112:115]\n\t"
"v_exp_f32 v83, v83\n\t"
"v_exp_f32 v68, v68\n\t"
"v_mfma_f32_16x16x32_bf16 v[116:119], a[212:215], a[132:135], v[116:119]\n\t"
"v_cvt_pk_bf16_f32 v131, v82, v83\n\t"
"v_add_f32 v72, v72, v40\n\t"
"v_add_f32 v73, v73, v40\n\t"
"v_mfma_f32_16x16x32_bf16 v[100:103], a[208:211], a[132:135], v[100:103]\n\t"
"s_add_u32 m0, s43, 0\n\t"
"v_add_f32 v74, v74, v40\n\t"
"v_add_f32 v75, v75, v40\n\t"
"v_mfma_f32_16x16x32_bf16 v[104:107], a[208:211], a[136:139], v[104:107]\n\t"
"s_addk_i32 s47, 12288\n\t"
"ds_read_b128 a[204:207], v6 offset:20480\n\t"
"v_add_f32 v88, v88, v40\n\t"
"v_add_f32 v89, v89, v40\n\t"
"v_mfma_f32_16x16x32_bf16 v[120:123], a[212:215], a[136:139], v[120:123]\n\t"
"buffer_load_dwordx4 v3, s[16:19], s47 offen offset:0 lds\n\t"
"v_mfma_f32_16x16x32_bf16 v[124:127], a[212:215], a[140:143], v[124:127]\n\t"
"v_exp_f32 v69, v69\n\t"
"v_add_f32 v90, v90, v40\n\t"
"v_mfma_f32_16x16x32_bf16 v[108:111], a[208:211], a[140:143], v[108:111]\n\t"
"s_waitcnt lgkmcnt(2)\n\t"
"v_exp_f32 v70, v70\n\t"
"v_add_f32 v91, v91, v40\n\t"
"v_mfma_f32_16x16x32_bf16 v[96:99], a[192:195], a[144:147], v[96:99]\n\t"
"ds_read_b128 a[208:211], v12 offset:14336\n\t"
"v_cvt_pk_bf16_f32 v132, v68, v69\n\t"
"v_mfma_f32_16x16x32_bf16 v[112:115], a[196:199], a[144:147], v[112:115]\n\t"
"v_exp_f32 v71, v71\n\t"
"v_mfma_f32_16x16x32_bf16 v[116:119], a[196:199], a[148:151], v[116:119]\n\t"
"v_exp_f32 v84, v84\n\t"
"v_mfma_f32_16x16x32_bf16 v[100:103], a[192:195], a[148:151], v[100:103]\n\t"
"v_exp_f32 v85, v85\n\t"
"v_exp_f32 v86, v86\n\t"
"v_mfma_f32_16x16x32_bf16 v[104:107], a[192:195], a[152:155], v[104:107]\n\t"
"ds_read_b128 a[212:215], v12 offset:20480\n\t"
"v_cvt_pk_bf16_f32 v133, v70, v71\n\t"
"v_mfma_f32_16x16x32_bf16 v[120:123], a[196:199], a[152:155], v[120:123]\n\t"
"v_exp_f32 v87, v87\n\t"
"v_mfma_f32_16x16x32_bf16 v[124:127], a[196:199], a[156:159], v[124:127]\n\t"
"v_cvt_pk_bf16_f32 v134, v84, v85\n\t"
"v_cvt_pk_bf16_f32 v135, v86, v87\n\t"
"v_add_f32 v76, v76, v44\n\t"
"v_add_f32 v77, v77, v44\n\t"
"v_mfma_f32_16x16x32_bf16 v[108:111], a[192:195], a[156:159], v[108:111]\n\t"
"s_waitcnt lgkmcnt(2)\n\t"
"buffer_load_dwordx4 v10, s[16:19], s47 offen offset:1024 lds\n\t"
"v_mfma_f32_16x16x32_bf16 v[96:99], a[200:203], a[160:163], v[96:99]\n\t"
"ds_read_b128 a[192:195], v6 offset:24576\n\t"
"v_add_f32 v78, v78, v44\n\t"
"v_add_f32 v79, v79, v44\n\t"
"v_mfma_f32_16x16x32_bf16 v[112:115], a[204:207], a[160:163], v[112:115]\n\t"
"v_add_f32 v92, v92, v44\n\t"
"v_add_f32 v93, v93, v44\n\t"
"v_mfma_f32_16x16x32_bf16 v[116:119], a[204:207], a[164:167], v[116:119]\n\t"
"v_exp_f32 v72, v72\n\t"
"v_add_f32 v94, v94, v44\n\t"
"v_mfma_f32_16x16x32_bf16 v[100:103], a[200:203], a[164:167], v[100:103]\n\t"
"v_exp_f32 v73, v73\n\t"
"v_mfma_f32_16x16x32_bf16 v[104:107], a[200:203], a[168:171], v[104:107]\n\t"
"ds_read_b128 a[196:199], v6 offset:30720\n\t"
"v_add_f32 v95, v95, v44\n\t"
"v_mfma_f32_16x16x32_bf16 v[120:123], a[204:207], a[168:171], v[120:123]\n\t"
"v_exp_f32 v74, v74\n\t"
"v_exp_f32 v75, v75\n\t"
"v_mfma_f32_16x16x32_bf16 v[124:127], a[204:207], a[172:175], v[124:127]\n\t"
".Lpg6%=:\n"
"s_cbranch_execz .Lpg10%=\n\t"
"v_exp_f32 v88, v88\n\t"
"v_mfma_f32_16x16x32_bf16 v[108:111], a[200:203], a[172:175], v[108:111]\n\t"
"s_waitcnt lgkmcnt(2)\n\t"
"v_exp_f32 v89, v89\n\t"
"v_cvt_pk_bf16_f32 v136, v72, v73\n\t"
"v_mfma_f32_16x16x32_bf16 v[96:99], a[208:211], a[176:179], v[96:99]\n\t"
"ds_read_b128 a[200:203], v12 offset:24576\n\t"
"v_cvt_pk_bf16_f32 v137, v74, v75\n\t"
"v_mfma_f32_16x16x32_bf16 v[112:115], a[212:215], a[176:179], v[112:115]\n\t"
"buffer_load_dwordx4 v11, s[16:19], s47 offen offset:2048 lds\n\t"
"v_mfma_f32_16x16x32_bf16 v[116:119], a[212:215], a[180:183], v[116:119]\n\t"
"v_exp_f32 v90, v90\n\t"
"v_exp_f32 v91, v91\n\t"
"v_mfma_f32_16x16x32_bf16 v[100:103], a[208:211], a[180:183], v[100:103]\n\t"
"v_exp_f32 v76, v76\n\t"
"v_mfma_f32_16x16x32_bf16 v[104:107], a[208:211], a[184:187], v[104:107]\n\t"
"ds_read_b128 a[204:207], v12 offset:30720\n\t"
"v_cvt_pk_bf16_f32 v138, v88, v89\n\t"
"v_mfma_f32_16x16x32_bf16 v[120:123], a[212:215], a[184:187], v[120:123]\n\t"
"v_exp_f32 v77, v77\n\t"
"v_mfma_f32_16x16x32_bf16 v[124:127], a[212:215], a[188:191], v[124:127]\n\t"
"v_exp_f32 v78, v78\n\t"
"v_cvt_pk_bf16_f32 v139, v90, v91\n\t"
"v_mfma_f32_16x16x32_bf16 v[108:111], a[208:211], a[188:191], v[108:111]\n\t"
"s_waitcnt lgkmcnt(2)\n\t"
"v_exp_f32 v79, v79\n\t"
"v_exp_f32 v92, v92\n\t"
"v_mfma_f32_16x16x32_bf16 v[64:67], a[192:195], a[96:99], v[32:35]\n\t"
"ds_read_b128 a[208:211], v6 offset:25600\n\t"
"v_cvt_pk_bf16_f32 v140, v76, v77\n\t"
"v_mfma_f32_16x16x32_bf16 v[80:83], a[196:199], a[96:99], v[32:35]\n\t"
"v_exp_f32 v93, v93\n\t"
"v_mfma_f32_16x16x32_bf16 v[84:87], a[196:199], a[100:103], v[36:39]\n\t"
"v_exp_f32 v94, v94\n\t"
"v_mfma_f32_16x16x32_bf16 v[68:71], a[192:195], a[100:103], v[36:39]\n\t"
"v_exp_f32 v95, v95\n\t"
"v_mfma_f32_16x16x32_bf16 v[72:75], a[192:195], a[104:107], v[40:43]\n\t"
"ds_read_b128 a[212:215], v6 offset:31744\n\t"
"v_cvt_pk_bf16_f32 v141, v78, v79\n\t"
"v_mfma_f32_16x16x32_bf16 v[88:91], a[196:199], a[104:107], v[40:43]\n\t"
"v_cvt_pk_bf16_f32 v142, v92, v93\n\t"
"v_cvt_pk_bf16_f32 v143, v94, v95\n\t"
"v_mfma_f32_16x16x32_bf16 v[92:95], a[196:199], a[108:111], v[44:47]\n\t"
"s_waitcnt vmcnt(8)\n\t"
"s_barrier\n\t"
"v_mfma_f32_16x16x32_bf16 v[76:79], a[192:195], a[108:111], v[44:47]\n\t"
"s_waitcnt lgkmcnt(2)\n\t"
"v_exp_f32 v96, v96\n\t"
"v_mfma_f32_16x16x32_bf16 v[64:67], a[200:203], a[112:115], v[64:67]\n\t"
"ds_read_b128 a[192:195], v12 offset:25600\n\t"
"v_exp_f32 v97, v97\n\t"
"v_mfma_f32_16x16x32_bf16 v[80:83], a[204:207], a[112:115], v[80:83]\n\t"
"v_exp_f32 v98, v98\n\t"
"v_mfma_f32_16x16x32_bf16 v[84:87], a[204:207], a[116:119], v[84:87]\n\t"
"v_exp_f32 v99, v99\n\t"
"v_mfma_f32_16x16x32_bf16 v[68:71], a[200:203], a[116:119], v[68:71]\n\t"
"s_add_u32 m0, s44, 32768\n\t"
"v_exp_f32 v112, v112\n\t"
"v_mfma_f32_16x16x32_bf16 v[72:75], a[200:203], a[120:123], v[72:75]\n\t"
"s_addk_i32 s48, 12288\n\t"
"ds_read_b128 a[196:199], v12 offset:31744\n\t"
"v_cvt_pk_bf16_f32 v144, v96, v97\n\t"
"v_mfma_f32_16x16x32_bf16 v[88:91], a[204:207], a[120:123], v[88:91]\n\t"
"buffer_load_dwordx4 v4, s[20:23], s48 offen offset:0 lds\n\t"
"v_mfma_f32_16x16x32_bf16 v[92:95], a[204:207], a[124:127], v[92:95]\n\t"
"v_exp_f32 v113, v113\n\t"
"v_mfma_f32_16x16x32_bf16 v[76:79], a[200:203], a[124:127], v[76:79]\n\t"
"s_waitcnt lgkmcnt(2)\n\t"
"v_exp_f32 v114, v114\n\t"
"v_mfma_f32_16x16x32_bf16 v[64:67], a[208:211], a[128:131], v[64:67]\n\t"
"ds_read_b128 a[200:203], v6 offset:26624\n\t"
"v_cvt_pk_bf16_f32 v145, v98, v99\n\t"
"v_mfma_f32_16x16x32_bf16 v[80:83], a[212:215], a[128:131], v[80:83]\n\t"
"v_exp_f32 v115, v115\n\t"
"v_mfma_f32_16x16x32_bf16 v[84:87], a[212:215], a[132:135], v[84:87]\n\t"
"v_exp_f32 v100, v100\n\t"
"v_cvt_pk_bf16_f32 v146, v112, v113\n\t"
"v_mfma_f32_16x16x32_bf16 v[68:71], a[208:211], a[132:135], v[68:71]\n\t"
"v_exp_f32 v101, v101\n\t"
"v_mfma_f32_16x16x32_bf16 v[72:75], a[208:211], a[136:139], v[72:75]\n\t"
"s_add_u32 m0, s43, 12288\n\t"
"ds_read_b128 a[204:207], v6 offset:32768\n\t"
"v_cvt_pk_bf16_f32 v147, v114, v115\n\t"
"v_mfma_f32_16x16x32_bf16 v[88:91], a[212:215], a[136:139], v[88:91]\n\t"
"s_addk_i32 s47, 12288\n\t"
"v_exp_f32 v102, v102\n\t"
"v_mfma_f32_16x16x32_bf16 v[92:95], a[212:215], a[140:143], v[92:95]\n\t"
"buffer_load_dwordx4 v3, s[16:19], s47 offen offset:0 lds\n\t"
"v_mfma_f32_16x16x32_bf16 v[76:79], a[208:211], a[140:143], v[76:79]\n\t"
"s_waitcnt lgkmcnt(2)\n\t"
"v_exp_f32 v103, v103\n\t"
"v_mfma_f32_16x16x32_bf16 v[64:67], a[192:195], a[144:147], v[64:67]\n\t"
"ds_read_b128 a[208:211], v12 offset:26624\n\t"
"v_cvt_pk_bf16_f32 v148, v100, v101\n\t"
"v_mfma_f32_16x16x32_bf16 v[80:83], a[196:199], a[144:147], v[80:83]\n\t"
"v_exp_f32 v116, v116\n\t"
"v_mfma_f32_16x16x32_bf16 v[84:87], a[196:199], a[148:151], v[84:87]\n\t"
"v_exp_f32 v117, v117\n\t"
"v_mfma_f32_16x16x32_bf16 v[68:71], a[192:195], a[148:151], v[68:71]\n\t"
"v_exp_f32 v118, v118\n\t"
"v_cvt_pk_bf16_f32 v149, v102, v103\n\t"
"v_mfma_f32_16x16x32_bf16 v[72:75], a[192:195], a[152:155], v[72:75]\n\t"
"ds_read_b128 a[212:215], v12 offset:32768\n\t"
"v_cvt_pk_bf16_f32 v150, v116, v117\n\t"
"v_mfma_f32_16x16x32_bf16 v[88:91], a[196:199], a[152:155], v[88:91]\n\t"
"v_exp_f32 v119, v119\n\t"
"v_mfma_f32_16x16x32_bf16 v[92:95], a[196:199], a[156:159], v[92:95]\n\t"
"v_exp_f32 v104, v104\n\t"
"v_mfma_f32_16x16x32_bf16 v[76:79], a[192:195], a[156:159], v[76:79]\n\t"
"s_waitcnt lgkmcnt(2)\n\t"
"v_exp_f32 v105, v105\n\t"
"v_mfma_f32_16x16x32_bf16 v[64:67], a[200:203], a[160:163], v[64:67]\n\t"
"ds_read_b128 a[192:195], v6 offset:36864\n\t"
"v_cvt_pk_bf16_f32 v151, v118, v119\n\t"
"v_mfma_f32_16x16x32_bf16 v[80:83], a[204:207], a[160:163], v[80:83]\n\t"
"buffer_load_dwordx4 v10, s[16:19], s47 offen offset:1024 lds\n\t"
"v_mfma_f32_16x16x32_bf16 v[84:87], a[204:207], a[164:167], v[84:87]\n\t"
"v_exp_f32 v106, v106\n\t"
"v_mfma_f32_16x16x32_bf16 v[68:71], a[200:203], a[164:167], v[68:71]\n\t"
"v_exp_f32 v107, v107\n\t"
"v_mfma_f32_16x16x32_bf16 v[72:75], a[200:203], a[168:171], v[72:75]\n\t"
"ds_read_b128 a[196:199], v6 offset:43008\n\t"
"v_cvt_pk_bf16_f32 v152, v104, v105\n\t"
"v_mfma_f32_16x16x32_bf16 v[88:91], a[204:207], a[168:171], v[88:91]\n\t"
"v_exp_f32 v120, v120\n\t"
"v_mfma_f32_16x16x32_bf16 v[92:95], a[204:207], a[172:175], v[92:95]\n\t"
"v_exp_f32 v121, v121\n\t"
"v_mfma_f32_16x16x32_bf16 v[76:79], a[200:203], a[172:175], v[76:79]\n\t"
"s_waitcnt lgkmcnt(2)\n\t"
"v_exp_f32 v122, v122\n\t"
"v_cvt_pk_bf16_f32 v153, v106, v107\n\t"
"v_mfma_f32_16x16x32_bf16 v[64:67], a[208:211], a[176:179], v[64:67]\n\t"
"ds_read_b128 a[200:203], v12 offset:36864\n\t"
"v_cvt_pk_bf16_f32 v154, v120, v121\n\t"
"v_mfma_f32_16x16x32_bf16 v[80:83], a[212:215], a[176:179], v[80:83]\n\t"
"v_exp_f32 v123, v123\n\t"
"v_mfma_f32_16x16x32_bf16 v[84:87], a[212:215], a[180:183], v[84:87]\n\t"
"buffer_load_dwordx4 v11, s[16:19], s47 offen offset:2048 lds\n\t"
"v_mfma_f32_16x16x32_bf16 v[68:71], a[208:211], a[180:183], v[68:71]\n\t"
"v_exp_f32 v108, v108\n\t"
"v_mfma_f32_16x16x32_bf16 v[72:75], a[208:211], a[184:187], v[72:75]\n\t"
"ds_read_b128 a[204:207], v12 offset:43008\n\t"
"v_cvt_pk_bf16_f32 v155, v122, v123\n\t"
"v_mfma_f32_16x16x32_bf16 v[88:91], a[212:215], a[184:187], v[88:91]\n\t"
"v_exp_f32 v109, v109\n\t"
"v_mfma_f32_16x16x32_bf16 v[92:95], a[212:215], a[188:191], v[92:95]\n\t"
"v_exp_f32 v110, v110\n\t"
"v_mfma_f32_16x16x32_bf16 v[76:79], a[208:211], a[188:191], v[76:79]\n\t"
"v_exp_f32 v111, v111\n\t"
"v_smfmac_f32_16x16x64_bf16 a[224:227], a[244:247], v[128:135], v30\n\t"
"v_cvt_pk_bf16_f32 v156, v108, v109\n\t"
"v_smfmac_f32_16x16x64_bf16 a[228:231], a[244:247], v[136:143], v30\n\t"
"s_waitcnt lgkmcnt(2)\n\t"
"v_exp_f32 v124, v124\n\t"
"v_mfma_f32_16x16x32_bf16 v[96:99], a[192:195], a[96:99], v[32:35]\n\t"
"ds_read_b128 a[208:211], v6 offset:37888\n\t"
"v_cvt_pk_bf16_f32 v157, v110, v111\n\t"
"v_mfma_f32_16x16x32_bf16 v[112:115], a[196:199], a[96:99], v[32:35]\n\t"
"v_exp_f32 v125, v125\n\t"
"v_mfma_f32_16x16x32_bf16 v[116:119], a[196:199], a[100:103], v[36:39]\n\t"
"v_exp_f32 v126, v126\n\t"
"v_mfma_f32_16x16x32_bf16 v[100:103], a[192:195], a[100:103], v[36:39]\n\t"
"v_exp_f32 v127, v127\n\t"
"v_mfma_f32_16x16x32_bf16 v[104:107], a[192:195], a[104:107], v[40:43]\n\t"
"ds_read_b128 a[212:215], v6 offset:44032\n\t"
"v_cvt_pk_bf16_f32 v158, v124, v125\n\t"
"v_mfma_f32_16x16x32_bf16 v[120:123], a[196:199], a[104:107], v[40:43]\n\t"
"v_cvt_pk_bf16_f32 v159, v126, v127\n\t"
"v_exp_f32 v64, v64\n\t"
"v_mfma_f32_16x16x32_bf16 v[124:127], a[196:199], a[108:111], v[44:47]\n\t"
"s_waitcnt vmcnt(9)\n\t"
"s_barrier\n\t"
"v_mfma_f32_16x16x32_bf16 v[108:111], a[192:195], a[108:111], v[44:47]\n\t"
"s_waitcnt lgkmcnt(2)\n\t"
"v_exp_f32 v65, v65\n\t"
"v_mfma_f32_16x16x32_bf16 v[96:99], a[200:203], a[112:115], v[96:99]\n\t"
"s_add_u32 m0, s44, 28672\n\t"
"ds_read_b128 a[192:195], v12 offset:37888\n\t"
"v_cvt_pk_bf16_f32 v160, v64, v65\n\t"
"v_mfma_f32_16x16x32_bf16 v[112:115], a[204:207], a[112:115], v[112:115]\n\t"
"s_addk_i32 s48, 12288\n\t"
"v_exp_f32 v66, v66\n\t"
"v_mfma_f32_16x16x32_bf16 v[116:119], a[204:207], a[116:119], v[116:119]\n\t"
"buffer_load_dwordx4 v4, s[20:23], s48 offen offset:0 lds\n\t"
"v_mfma_f32_16x16x32_bf16 v[100:103], a[200:203], a[116:119], v[100:103]\n\t"
"v_exp_f32 v67, v67\n\t"
"v_mfma_f32_16x16x32_bf16 v[104:107], a[200:203], a[120:123], v[104:107]\n\t"
"ds_read_b128 a[196:199], v12 offset:44032\n\t"
"v_cvt_pk_bf16_f32 v161, v66, v67\n\t"
"v_mfma_f32_16x16x32_bf16 v[120:123], a[204:207], a[120:123], v[120:123]\n\t"
"v_exp_f32 v80, v80\n\t"
"v_mfma_f32_16x16x32_bf16 v[124:127], a[204:207], a[124:127], v[124:127]\n\t"
"s_add_u32 m0, s43, 24576\n\t"
"v_exp_f32 v81, v81\n\t"
"v_mfma_f32_16x16x32_bf16 v[108:111], a[200:203], a[124:127], v[108:111]\n\t"
"s_waitcnt lgkmcnt(2)\n\t"
"v_exp_f32 v82, v82\n\t"
"v_mfma_f32_16x16x32_bf16 v[96:99], a[208:211], a[128:131], v[96:99]\n\t"
"s_addk_i32 s47, 12288\n\t"
"ds_read_b128 a[200:203], v6 offset:38912\n\t"
"v_cvt_pk_bf16_f32 v162, v80, v81\n\t"
"v_exp_f32 v83, v83\n\t"
"v_mfma_f32_16x16x32_bf16 v[112:115], a[212:215], a[128:131], v[112:115]\n\t"
"buffer_load_dwordx4 v3, s[16:19], s47 offen offset:0 lds\n\t"
"v_mfma_f32_16x16x32_bf16 v[116:119], a[212:215], a[132:135], v[116:119]\n\t"
"v_exp_f32 v68, v68\n\t"
"v_mfma_f32_16x16x32_bf16 v[100:103], a[208:211], a[132:135], v[100:103]\n\t"
"v_exp_f32 v69, v69\n\t"
"v_mfma_f32_16x16x32_bf16 v[104:107], a[208:211], a[136:139], v[104:107]\n\t"
"ds_read_b128 a[204:207], v6 offset:45056\n\t"
"v_cvt_pk_bf16_f32 v163, v82, v83\n\t"
"v_mfma_f32_16x16x32_bf16 v[120:123], a[212:215], a[136:139], v[120:123]\n\t"
"v_exp_f32 v70, v70\n\t"
"v_mfma_f32_16x16x32_bf16 v[124:127], a[212:215], a[140:143], v[124:127]\n\t"
"v_exp_f32 v71, v71\n\t"
"v_mfma_f32_16x16x32_bf16 v[108:111], a[208:211], a[140:143], v[108:111]\n\t"
"s_waitcnt lgkmcnt(2)\n\t"
"v_exp_f32 v84, v84\n\t"
"v_cvt_pk_bf16_f32 v164, v68, v69\n\t"
"v_mfma_f32_16x16x32_bf16 v[96:99], a[192:195], a[144:147], v[96:99]\n\t"
"ds_read_b128 a[208:211], v12 offset:38912\n\t"
"v_cvt_pk_bf16_f32 v165, v70, v71\n\t"
"v_mfma_f32_16x16x32_bf16 v[112:115], a[196:199], a[144:147], v[112:115]\n\t"
"buffer_load_dwordx4 v10, s[16:19], s47 offen offset:1024 lds\n\t"
"v_mfma_f32_16x16x32_bf16 v[116:119], a[196:199], a[148:151], v[116:119]\n\t"
"v_exp_f32 v85, v85\n\t"
"v_mfma_f32_16x16x32_bf16 v[100:103], a[192:195], a[148:151], v[100:103]\n\t"
"v_exp_f32 v86, v86\n\t"
"v_mfma_f32_16x16x32_bf16 v[104:107], a[192:195], a[152:155], v[104:107]\n\t"
"ds_read_b128 a[212:215], v12 offset:45056\n\t"
"v_cvt_pk_bf16_f32 v166, v84, v85\n\t"
"v_mfma_f32_16x16x32_bf16 v[120:123], a[196:199], a[152:155], v[120:123]\n\t"
"v_exp_f32 v87, v87\n\t"
"v_mfma_f32_16x16x32_bf16 v[124:127], a[196:199], a[156:159], v[124:127]\n\t"
"buffer_load_dwordx4 v11, s[16:19], s47 offen offset:2048 lds\n\t"
"v_mfma_f32_16x16x32_bf16 v[108:111], a[192:195], a[156:159], v[108:111]\n\t"
"s_waitcnt lgkmcnt(2)\n\t"
"v_exp_f32 v72, v72\n\t"
"v_mfma_f32_16x16x32_bf16 v[96:99], a[200:203], a[160:163], v[96:99]\n\t"
"ds_read_b128 a[192:195], v6 offset:49152\n\t"
"v_cvt_pk_bf16_f32 v167, v86, v87\n\t"
"v_mfma_f32_16x16x32_bf16 v[112:115], a[204:207], a[160:163], v[112:115]\n\t"
"v_exp_f32 v73, v73\n\t"
"v_exp_f32 v74, v74\n\t"
"v_mfma_f32_16x16x32_bf16 v[116:119], a[204:207], a[164:167], v[116:119]\n\t"
"v_exp_f32 v75, v75\n\t"
"v_mfma_f32_16x16x32_bf16 v[100:103], a[200:203], a[164:167], v[100:103]\n\t"
"v_exp_f32 v88, v88\n\t"
"v_mfma_f32_16x16x32_bf16 v[104:107], a[200:203], a[168:171], v[104:107]\n\t"
"s_add_u32 m0, s87, 0\n\t"
"ds_read_b128 a[196:199], v6 offset:55296\n\t"
"v_cvt_pk_bf16_f32 v168, v72, v73\n\t"
"v_mfma_f32_16x16x32_bf16 v[120:123], a[204:207], a[168:171], v[120:123]\n\t"
"buffer_load_dwordx4 v56, s[64:67], s82 offen offset:0 lds\n\t"
"v_mfma_f32_16x16x32_bf16 v[124:127], a[204:207], a[172:175], v[124:127]\n\t"
"v_exp_f32 v89, v89\n\t"
"v_mfma_f32_16x16x32_bf16 v[108:111], a[200:203], a[172:175], v[108:111]\n\t"
"s_waitcnt lgkmcnt(2)\n\t"
"v_exp_f32 v90, v90\n\t"
"v_cvt_pk_bf16_f32 v169, v74, v75\n\t"
"v_mfma_f32_16x16x32_bf16 v[96:99], a[208:211], a[176:179], v[96:99]\n\t"
"ds_read_b128 a[200:203], v12 offset:49152\n\t"
"v_cvt_pk_bf16_f32 v170, v88, v89\n\t"
"v_mfma_f32_16x16x32_bf16 v[112:115], a[212:215], a[176:179], v[112:115]\n\t"
"v_exp_f32 v91, v91\n\t"
"v_mfma_f32_16x16x32_bf16 v[116:119], a[212:215], a[180:183], v[116:119]\n\t"
"v_exp_f32 v76, v76\n\t"
"v_mfma_f32_16x16x32_bf16 v[100:103], a[208:211], a[180:183], v[100:103]\n\t"
"s_addk_i32 m0, 1024\n\t"
"v_exp_f32 v77, v77\n\t"
"v_mfma_f32_16x16x32_bf16 v[104:107], a[208:211], a[184:187], v[104:107]\n\t"
"s_add_u32 s46, s82, 40960\n\t"
"ds_read_b128 a[204:207], v12 offset:55296\n\t"
"v_cvt_pk_bf16_f32 v171, v90, v91\n\t"
"v_mfma_f32_16x16x32_bf16 v[120:123], a[212:215], a[184:187], v[120:123]\n\t"
"buffer_load_dwordx4 v56, s[64:67], s46 offen offset:0 lds\n\t"
"v_mfma_f32_16x16x32_bf16 v[124:127], a[212:215], a[188:191], v[124:127]\n\t"
"v_exp_f32 v78, v78\n\t"
"v_mfma_f32_16x16x32_bf16 v[108:111], a[208:211], a[188:191], v[108:111]\n\t"
"v_exp_f32 v79, v79\n\t"
"v_smfmac_f32_16x16x64_bf16 a[224:227], a[244:247], v[144:151], v30\n\t"
"v_cvt_pk_bf16_f32 v172, v76, v77\n\t"
"v_smfmac_f32_16x16x64_bf16 a[228:231], a[244:247], v[152:159], v30\n\t"
"s_waitcnt lgkmcnt(2)\n\t"
"v_exp_f32 v92, v92\n\t"
"v_mfma_f32_16x16x32_bf16 v[64:67], a[192:195], a[96:99], v[32:35]\n\t"
"ds_read_b128 a[208:211], v6 offset:50176\n\t"
"v_cvt_pk_bf16_f32 v173, v78, v79\n\t"
"v_mfma_f32_16x16x32_bf16 v[80:83], a[196:199], a[96:99], v[32:35]\n\t"
"v_exp_f32 v93, v93\n\t"
"v_mfma_f32_16x16x32_bf16 v[84:87], a[196:199], a[100:103], v[36:39]\n\t"
"v_exp_f32 v94, v94\n\t"
"v_mfma_f32_16x16x32_bf16 v[68:71], a[192:195], a[100:103], v[36:39]\n\t"
"v_exp_f32 v95, v95\n\t"
"v_mfma_f32_16x16x32_bf16 v[72:75], a[192:195], a[104:107], v[40:43]\n\t"
"ds_read_b128 a[212:215], v6 offset:56320\n\t"
"v_cvt_pk_bf16_f32 v174, v92, v93\n\t"
"v_mfma_f32_16x16x32_bf16 v[88:91], a[196:199], a[104:107], v[40:43]\n\t"
"v_cvt_pk_bf16_f32 v175, v94, v95\n\t"
"v_mfma_f32_16x16x32_bf16 v[92:95], a[196:199], a[108:111], v[44:47]\n\t"
"s_waitcnt vmcnt(10)\n\t"
"s_barrier\n\t"
"v_mfma_f32_16x16x32_bf16 v[76:79], a[192:195], a[108:111], v[44:47]\n\t"
"s_waitcnt lgkmcnt(2)\n\t"
"v_exp_f32 v96, v96\n\t"
"v_mfma_f32_16x16x32_bf16 v[64:67], a[200:203], a[112:115], v[64:67]\n\t"
"ds_read_b128 a[192:195], v12 offset:50176\n\t"
"v_mfma_f32_16x16x32_bf16 v[80:83], a[204:207], a[112:115], v[80:83]\n\t"
"v_exp_f32 v97, v97\n\t"
"v_mfma_f32_16x16x32_bf16 v[84:87], a[204:207], a[116:119], v[84:87]\n\t"
"v_exp_f32 v98, v98\n\t"
"v_mfma_f32_16x16x32_bf16 v[68:71], a[200:203], a[116:119], v[68:71]\n\t"
"s_add_u32 m0, s44, 24576\n\t"
"v_exp_f32 v99, v99\n\t"
"v_mfma_f32_16x16x32_bf16 v[72:75], a[200:203], a[120:123], v[72:75]\n\t"
"s_addk_i32 s48, 12288\n\t"
"ds_read_b128 a[196:199], v12 offset:56320\n\t"
"v_cvt_pk_bf16_f32 v176, v96, v97\n\t"
"v_mfma_f32_16x16x32_bf16 v[88:91], a[204:207], a[120:123], v[88:91]\n\t"
"buffer_load_dwordx4 v4, s[20:23], s48 offen offset:0 lds\n\t"
"v_mfma_f32_16x16x32_bf16 v[92:95], a[204:207], a[124:127], v[92:95]\n\t"
"v_exp_f32 v112, v112\n\t"
"v_mfma_f32_16x16x32_bf16 v[76:79], a[200:203], a[124:127], v[76:79]\n\t"
"s_waitcnt lgkmcnt(2)\n\t"
"v_exp_f32 v113, v113\n\t"
"v_mfma_f32_16x16x32_bf16 v[64:67], a[208:211], a[128:131], v[64:67]\n\t"
"ds_read_b128 a[200:203], v6 offset:51200\n\t"
"v_cvt_pk_bf16_f32 v177, v98, v99\n\t"
"v_mfma_f32_16x16x32_bf16 v[80:83], a[212:215], a[128:131], v[80:83]\n\t"
"v_exp_f32 v114, v114\n\t"
"v_mfma_f32_16x16x32_bf16 v[84:87], a[212:215], a[132:135], v[84:87]\n\t"
"v_exp_f32 v115, v115\n\t"
"v_mfma_f32_16x16x32_bf16 v[68:71], a[208:211], a[132:135], v[68:71]\n\t"
"v_exp_f32 v100, v100\n\t"
"v_mfma_f32_16x16x32_bf16 v[72:75], a[208:211], a[136:139], v[72:75]\n\t"
"ds_read_b128 a[204:207], v6 offset:57344\n\t"
"v_cvt_pk_bf16_f32 v178, v112, v113\n\t"
"v_mfma_f32_16x16x32_bf16 v[88:91], a[212:215], a[136:139], v[88:91]\n\t"
"v_exp_f32 v101, v101\n\t"
"v_mfma_f32_16x16x32_bf16 v[92:95], a[212:215], a[140:143], v[92:95]\n\t"
"s_add_u32 m0, s43, 36864\n\t"
"v_exp_f32 v102, v102\n\t"
"v_mfma_f32_16x16x32_bf16 v[76:79], a[208:211], a[140:143], v[76:79]\n\t"
"s_waitcnt lgkmcnt(2)\n\t"
"v_exp_f32 v103, v103\n\t"
"v_mfma_f32_16x16x32_bf16 v[64:67], a[192:195], a[144:147], v[64:67]\n\t"
"s_addk_i32 s47, 12288\n\t"
"ds_read_b128 a[208:211], v12 offset:51200\n\t"
"v_cvt_pk_bf16_f32 v179, v114, v115\n\t"
"v_mfma_f32_16x16x32_bf16 v[80:83], a[196:199], a[144:147], v[80:83]\n\t"
"buffer_load_dwordx4 v3, s[16:19], s47 offen offset:0 lds\n\t"
"v_mfma_f32_16x16x32_bf16 v[84:87], a[196:199], a[148:151], v[84:87]\n\t"
"v_exp_f32 v116, v116\n\t"
"v_mfma_f32_16x16x32_bf16 v[68:71], a[192:195], a[148:151], v[68:71]\n\t"
"v_exp_f32 v117, v117\n\t"
"v_mfma_f32_16x16x32_bf16 v[72:75], a[192:195], a[152:155], v[72:75]\n\t"
"ds_read_b128 a[212:215], v12 offset:57344\n\t"
"v_cvt_pk_bf16_f32 v180, v100, v101\n\t"
"v_mfma_f32_16x16x32_bf16 v[88:91], a[196:199], a[152:155], v[88:91]\n\t"
"v_exp_f32 v118, v118\n\t"
"v_mfma_f32_16x16x32_bf16 v[92:95], a[196:199], a[156:159], v[92:95]\n\t"
"v_exp_f32 v119, v119\n\t"
"v_mfma_f32_16x16x32_bf16 v[76:79], a[192:195], a[156:159], v[76:79]\n\t"
"s_waitcnt lgkmcnt(2)\n\t"
"v_exp_f32 v104, v104\n\t"
"v_mfma_f32_16x16x32_bf16 v[64:67], a[200:203], a[160:163], v[64:67]\n\t"
"ds_read_b64_tr_b16 a[192:193], v7 offset:36864\n\t"
"v_cvt_pk_bf16_f32 v181, v102, v103\n\t"
"v_mfma_f32_16x16x32_bf16 v[80:83], a[204:207], a[160:163], v[80:83]\n\t"
"ds_read_b64_tr_b16 a[194:195], v7 offset:38912\n\t"
"v_cvt_pk_bf16_f32 v182, v116, v117\n\t"
"v_mfma_f32_16x16x32_bf16 v[84:87], a[204:207], a[164:167], v[84:87]\n\t"
"v_exp_f32 v105, v105\n\t"
"v_mfma_f32_16x16x32_bf16 v[68:71], a[200:203], a[164:167], v[68:71]\n\t"
"v_exp_f32 v106, v106\n\t"
"v_mfma_f32_16x16x32_bf16 v[72:75], a[200:203], a[168:171], v[72:75]\n\t"
"ds_read_b64_tr_b16 a[196:197], v13 offset:36864\n\t"
"v_cvt_pk_bf16_f32 v183, v118, v119\n\t"
"v_mfma_f32_16x16x32_bf16 v[88:91], a[204:207], a[168:171], v[88:91]\n\t"
"ds_read_b64_tr_b16 a[198:199], v13 offset:38912\n\t"
"v_cvt_pk_bf16_f32 v104, v104, v105\n\t"
"v_mfma_f32_16x16x32_bf16 v[92:95], a[204:207], a[172:175], v[92:95]\n\t"
"buffer_load_dwordx4 v10, s[16:19], s47 offen offset:1024 lds\n\t"
"v_mfma_f32_16x16x32_bf16 v[76:79], a[200:203], a[172:175], v[76:79]\n\t"
"s_waitcnt lgkmcnt(4)\n\t"
"v_exp_f32 v107, v107\n\t"
"v_mfma_f32_16x16x32_bf16 v[64:67], a[208:211], a[176:179], v[64:67]\n\t"
"ds_read_b128 a[200:203], v6 offset:0\n\t"
"v_accvgpr_write_b32 a0, v104\n\t"
"v_mfma_f32_16x16x32_bf16 v[80:83], a[212:215], a[176:179], v[80:83]\n\t"
"v_exp_f32 v120, v120\n\t"
"v_mfma_f32_16x16x32_bf16 v[84:87], a[212:215], a[180:183], v[84:87]\n\t"
"v_exp_f32 v121, v121\n\t"
"v_mfma_f32_16x16x32_bf16 v[68:71], a[208:211], a[180:183], v[68:71]\n\t"
"v_cvt_pk_bf16_f32 v106, v106, v107\n\t"
"v_accvgpr_write_b32 a1, v106\n\t"
"v_mfma_f32_16x16x32_bf16 v[72:75], a[208:211], a[184:187], v[72:75]\n\t"
"ds_read_b128 a[204:207], v6 offset:6144\n\t"
"v_cvt_pk_bf16_f32 v120, v120, v121\n\t"
"v_mfma_f32_16x16x32_bf16 v[88:91], a[212:215], a[184:187], v[88:91]\n\t"
"v_exp_f32 v122, v122\n\t"
"v_mfma_f32_16x16x32_bf16 v[92:95], a[212:215], a[188:191], v[92:95]\n\t"
"v_exp_f32 v123, v123\n\t"
"v_mfma_f32_16x16x32_bf16 v[76:79], a[208:211], a[188:191], v[76:79]\n\t"
"v_exp_f32 v108, v108\n\t"
"v_smfmac_f32_16x16x64_bf16 a[224:227], a[244:247], v[160:167], v30\n\t"
"v_accvgpr_write_b32 a2, v120\n\t"
"v_smfmac_f32_16x16x64_bf16 a[228:231], a[244:247], v[168:175], v30\n\t"
"s_waitcnt lgkmcnt(2)\n\t"
"v_cvt_pk_bf16_f32 v122, v122, v123\n\t"
"v_mfma_f32_16x16x32_bf16 a[216:219], a[192:195], v[128:131], 0\n\t"
"ds_read_b128 a[208:211], v12 offset:0\n\t"
"v_accvgpr_write_b32 a3, v122\n\t"
"v_mfma_f32_16x16x32_bf16 v[48:51], a[196:199], v[128:131], 0\n\t"
"buffer_load_dwordx4 v11, s[16:19], s47 offen offset:2048 lds\n\t"
"v_mfma_f32_16x16x32_bf16 v[52:55], a[196:199], v[132:135], 0\n\t"
"v_exp_f32 v109, v109\n\t"
"v_mfma_f32_16x16x32_bf16 a[220:223], a[192:195], v[132:135], 0\n\t"
"v_exp_f32 v110, v110\n\t"
"v_mfma_f32_16x16x32_bf16 a[232:235], a[192:195], v[136:139], 0\n\t"
"ds_read_b128 a[212:215], v12 offset:6144\n\t"
"v_cvt_pk_bf16_f32 v108, v108, v109\n\t"
"v_mfma_f32_16x16x32_bf16 v[56:59], a[196:199], v[136:139], 0\n\t"
"v_exp_f32 v111, v111\n\t"
"v_mfma_f32_16x16x32_bf16 v[60:63], a[196:199], v[140:143], 0\n\t"
"v_exp_f32 v124, v124\n\t"
"v_mfma_f32_16x16x32_bf16 a[248:251], a[192:195], v[140:143], 0\n\t"
"s_waitcnt lgkmcnt(2)\n\t"
"v_exp_f32 v125, v125\n\t"
"v_accvgpr_write_b32 a4, v108\n\t"
"v_mfma_f32_16x16x32_bf16 v[96:99], a[200:203], a[96:99], v[32:35]\n\t"
"ds_read_b128 a[192:195], v6 offset:1024\n\t"
"v_cvt_pk_bf16_f32 v110, v110, v111\n\t"
"v_mfma_f32_16x16x32_bf16 v[112:115], a[204:207], a[96:99], v[32:35]\n\t"
"v_exp_f32 v126, v126\n\t"
"v_mfma_f32_16x16x32_bf16 v[116:119], a[204:207], a[100:103], v[36:39]\n\t"
"v_exp_f32 v127, v127\n\t"
"v_mfma_f32_16x16x32_bf16 v[100:103], a[200:203], a[100:103], v[36:39]\n\t"
"v_accvgpr_write_b32 a5, v110\n\t"
"v_cvt_pk_bf16_f32 v124, v124, v125\n\t"
"v_mfma_f32_16x16x32_bf16 v[104:107], a[200:203], a[104:107], v[40:43]\n\t"
"ds_read_b128 a[196:199], v6 offset:7168\n\t"
"v_accvgpr_write_b32 a6, v124\n\t"
"v_mfma_f32_16x16x32_bf16 v[120:123], a[204:207], a[104:107], v[40:43]\n\t"
"v_cvt_pk_bf16_f32 v126, v126, v127\n\t"
"v_accvgpr_write_b32 a7, v126\n\t"
"v_mfma_f32_16x16x32_bf16 v[124:127], a[204:207], a[108:111], v[44:47]\n\t"
"s_waitcnt vmcnt(10)\n\t"
"s_barrier\n\t"
"v_mfma_f32_16x16x32_bf16 v[108:111], a[200:203], a[108:111], v[44:47]\n\t"
"s_waitcnt lgkmcnt(2)\n\t"
"v_exp_f32 v64, v64\n\t"
"v_mfma_f32_16x16x32_bf16 v[96:99], a[208:211], a[112:115], v[96:99]\n\t"
"ds_read_b128 a[200:203], v12 offset:1024\n\t"
"v_mfma_f32_16x16x32_bf16 v[112:115], a[212:215], a[112:115], v[112:115]\n\t"
"v_exp_f32 v65, v65\n\t"
"v_mfma_f32_16x16x32_bf16 v[116:119], a[212:215], a[116:119], v[116:119]\n\t"
"v_exp_f32 v66, v66\n\t"
"v_mfma_f32_16x16x32_bf16 v[100:103], a[208:211], a[116:119], v[100:103]\n\t"
"s_add_u32 m0, s44, 20480\n\t"
"v_exp_f32 v67, v67\n\t"
"v_mfma_f32_16x16x32_bf16 v[104:107], a[208:211], a[120:123], v[104:107]\n\t"
"s_addk_i32 s48, 12288\n\t"
"ds_read_b128 a[204:207], v12 offset:7168\n\t"
"v_cvt_pk_bf16_f32 v184, v64, v65\n\t"
"v_mfma_f32_16x16x32_bf16 v[120:123], a[212:215], a[120:123], v[120:123]\n\t"
"buffer_load_dwordx4 v4, s[20:23], s48 offen offset:0 lds\n\t"
"v_mfma_f32_16x16x32_bf16 v[124:127], a[212:215], a[124:127], v[124:127]\n\t"
"v_exp_f32 v80, v80\n\t"
"v_mfma_f32_16x16x32_bf16 v[108:111], a[208:211], a[124:127], v[108:111]\n\t"
"s_waitcnt lgkmcnt(2)\n\t"
"v_exp_f32 v81, v81\n\t"
"v_mfma_f32_16x16x32_bf16 v[96:99], a[192:195], a[128:131], v[96:99]\n\t"
"ds_read_b128 a[208:211], v6 offset:2048\n\t"
"v_cvt_pk_bf16_f32 v185, v66, v67\n\t"
"v_mfma_f32_16x16x32_bf16 v[112:115], a[196:199], a[128:131], v[112:115]\n\t"
"v_exp_f32 v82, v82\n\t"
"v_mfma_f32_16x16x32_bf16 v[116:119], a[196:199], a[132:135], v[116:119]\n\t"
"v_exp_f32 v83, v83\n\t"
"v_mfma_f32_16x16x32_bf16 v[100:103], a[192:195], a[132:135], v[100:103]\n\t"
"v_exp_f32 v68, v68\n\t"
"v_mfma_f32_16x16x32_bf16 v[104:107], a[192:195], a[136:139], v[104:107]\n\t"
"ds_read_b128 a[212:215], v6 offset:8192\n\t"
"v_cvt_pk_bf16_f32 v186, v80, v81\n\t"
"v_mfma_f32_16x16x32_bf16 v[120:123], a[196:199], a[136:139], v[120:123]\n\t"
"v_exp_f32 v69, v69\n\t"
"v_mfma_f32_16x16x32_bf16 v[124:127], a[196:199], a[140:143], v[124:127]\n\t"
"s_add_u32 m0, s43, 49152\n\t"
"v_exp_f32 v70, v70\n\t"
"v_mfma_f32_16x16x32_bf16 v[108:111], a[192:195], a[140:143], v[108:111]\n\t"
"s_waitcnt lgkmcnt(2)\n\t"
"v_exp_f32 v71, v71\n\t"
"v_mfma_f32_16x16x32_bf16 v[96:99], a[200:203], a[144:147], v[96:99]\n\t"
"s_addk_i32 s47, 12288\n\t"
"ds_read_b128 a[192:195], v12 offset:2048\n\t"
"v_cvt_pk_bf16_f32 v187, v82, v83\n\t"
"v_mfma_f32_16x16x32_bf16 v[112:115], a[204:207], a[144:147], v[112:115]\n\t"
"buffer_load_dwordx4 v3, s[16:19], s47 offen offset:0 lds\n\t"
"v_mfma_f32_16x16x32_bf16 v[116:119], a[204:207], a[148:151], v[116:119]\n\t"
"v_exp_f32 v84, v84\n\t"
"v_mfma_f32_16x16x32_bf16 v[100:103], a[200:203], a[148:151], v[100:103]\n\t"
"v_exp_f32 v85, v85\n\t"
"v_mfma_f32_16x16x32_bf16 v[104:107], a[200:203], a[152:155], v[104:107]\n\t"
"ds_read_b128 a[196:199], v12 offset:8192\n\t"
"v_cvt_pk_bf16_f32 v188, v68, v69\n\t"
"v_mfma_f32_16x16x32_bf16 v[120:123], a[204:207], a[152:155], v[120:123]\n\t"
"v_exp_f32 v86, v86\n\t"
"v_mfma_f32_16x16x32_bf16 v[124:127], a[204:207], a[156:159], v[124:127]\n\t"
"v_exp_f32 v87, v87\n\t"
"v_mfma_f32_16x16x32_bf16 v[108:111], a[200:203], a[156:159], v[108:111]\n\t"
"s_waitcnt lgkmcnt(2)\n\t"
"v_exp_f32 v72, v72\n\t"
"v_mfma_f32_16x16x32_bf16 v[96:99], a[208:211], a[160:163], v[96:99]\n\t"
"ds_read_b64_tr_b16 a[200:201], v7 offset:32768\n\t"
"v_cvt_pk_bf16_f32 v189, v70, v71\n\t"
"v_mfma_f32_16x16x32_bf16 v[112:115], a[212:215], a[160:163], v[112:115]\n\t"
"ds_read_b64_tr_b16 a[202:203], v7 offset:34816\n\t"
"v_cvt_pk_bf16_f32 v190, v84, v85\n\t"
"v_mfma_f32_16x16x32_bf16 v[116:119], a[212:215], a[164:167], v[116:119]\n\t"
"v_exp_f32 v73, v73\n\t"
"v_mfma_f32_16x16x32_bf16 v[100:103], a[208:211], a[164:167], v[100:103]\n\t"
"v_exp_f32 v74, v74\n\t"
"v_mfma_f32_16x16x32_bf16 v[104:107], a[208:211], a[168:171], v[104:107]\n\t"
"ds_read_b64_tr_b16 a[204:205], v13 offset:32768\n\t"
"v_cvt_pk_bf16_f32 v191, v86, v87\n\t"
"v_mfma_f32_16x16x32_bf16 v[120:123], a[212:215], a[168:171], v[120:123]\n\t"
"ds_read_b64_tr_b16 a[206:207], v13 offset:34816\n\t"
"v_cvt_pk_bf16_f32 v72, v72, v73\n\t"
"v_mfma_f32_16x16x32_bf16 v[124:127], a[212:215], a[172:175], v[124:127]\n\t"
"buffer_load_dwordx4 v10, s[16:19], s47 offen offset:1024 lds\n\t"
"v_mfma_f32_16x16x32_bf16 v[108:111], a[208:211], a[172:175], v[108:111]\n\t"
"s_waitcnt lgkmcnt(4)\n\t"
"v_exp_f32 v75, v75\n\t"
"v_mfma_f32_16x16x32_bf16 v[96:99], a[192:195], a[176:179], v[96:99]\n\t"
"ds_read_b128 a[208:211], v6 offset:12288\n\t"
"v_accvgpr_write_b32 a8, v72\n\t"
"v_mfma_f32_16x16x32_bf16 v[112:115], a[196:199], a[176:179], v[112:115]\n\t"
"v_exp_f32 v88, v88\n\t"
"v_mfma_f32_16x16x32_bf16 v[116:119], a[196:199], a[180:183], v[116:119]\n\t"
"v_exp_f32 v89, v89\n\t"
"v_mfma_f32_16x16x32_bf16 v[100:103], a[192:195], a[180:183], v[100:103]\n\t"
"v_cvt_pk_bf16_f32 v74, v74, v75\n\t"
"v_accvgpr_write_b32 a9, v74\n\t"
"v_mfma_f32_16x16x32_bf16 v[104:107], a[192:195], a[184:187], v[104:107]\n\t"
"ds_read_b128 a[212:215], v6 offset:18432\n\t"
"v_cvt_pk_bf16_f32 v88, v88, v89\n\t"
"v_mfma_f32_16x16x32_bf16 v[120:123], a[196:199], a[184:187], v[120:123]\n\t"
"v_exp_f32 v90, v90\n\t"
"v_mfma_f32_16x16x32_bf16 v[124:127], a[196:199], a[188:191], v[124:127]\n\t"
"v_exp_f32 v91, v91\n\t"
"v_mfma_f32_16x16x32_bf16 v[108:111], a[192:195], a[188:191], v[108:111]\n\t"
"v_exp_f32 v76, v76\n\t"
"v_smfmac_f32_16x16x64_bf16 a[224:227], a[244:247], v[176:183], v30\n\t"
"v_accvgpr_write_b32 a10, v88\n\t"
"v_smfmac_f32_16x16x64_bf16 a[228:231], a[244:247], a[0:7], v30\n\t"
"s_waitcnt lgkmcnt(2)\n\t"
"v_cvt_pk_bf16_f32 v90, v90, v91\n\t"
"v_mfma_f32_16x16x32_bf16 a[216:219], a[200:203], v[144:147], a[216:219]\n\t"
"ds_read_b128 a[192:195], v12 offset:12288\n\t"
"v_accvgpr_write_b32 a11, v90\n\t"
"v_mfma_f32_16x16x32_bf16 v[48:51], a[204:207], v[144:147], v[48:51]\n\t"
"buffer_load_dwordx4 v11, s[16:19], s47 offen offset:2048 lds\n\t"
"v_mfma_f32_16x16x32_bf16 v[52:55], a[204:207], v[148:151], v[52:55]\n\t"
"v_exp_f32 v77, v77\n\t"
"v_mfma_f32_16x16x32_bf16 a[220:223], a[200:203], v[148:151], a[220:223]\n\t"
"v_exp_f32 v78, v78\n\t"
"v_mfma_f32_16x16x32_bf16 a[232:235], a[200:203], v[152:155], a[232:235]\n\t"
"ds_read_b128 a[196:199], v12 offset:18432\n\t"
"v_cvt_pk_bf16_f32 v76, v76, v77\n\t"
"v_mfma_f32_16x16x32_bf16 v[56:59], a[204:207], v[152:155], v[56:59]\n\t"
"v_exp_f32 v79, v79\n\t"
"v_mfma_f32_16x16x32_bf16 v[60:63], a[204:207], v[156:159], v[60:63]\n\t"
"v_exp_f32 v92, v92\n\t"
"v_mfma_f32_16x16x32_bf16 a[248:251], a[200:203], v[156:159], a[248:251]\n\t"
"s_waitcnt lgkmcnt(2)\n\t"
"v_exp_f32 v93, v93\n\t"
"v_accvgpr_write_b32 a12, v76\n\t"
"v_mfma_f32_16x16x32_bf16 v[64:67], a[208:211], a[96:99], v[32:35]\n\t"
"ds_read_b128 a[200:203], v6 offset:13312\n\t"
"v_cvt_pk_bf16_f32 v78, v78, v79\n\t"
"v_mfma_f32_16x16x32_bf16 v[80:83], a[212:215], a[96:99], v[32:35]\n\t"
"v_exp_f32 v94, v94\n\t"
"v_mfma_f32_16x16x32_bf16 v[84:87], a[212:215], a[100:103], v[36:39]\n\t"
"v_exp_f32 v95, v95\n\t"
"v_mfma_f32_16x16x32_bf16 v[68:71], a[208:211], a[100:103], v[36:39]\n\t"
"v_accvgpr_write_b32 a13, v78\n\t"
"v_cvt_pk_bf16_f32 v92, v92, v93\n\t"
"v_mfma_f32_16x16x32_bf16 v[72:75], a[208:211], a[104:107], v[40:43]\n\t"
"ds_read_b128 a[204:207], v6 offset:19456\n\t"
"v_accvgpr_write_b32 a14, v92\n\t"
"v_mfma_f32_16x16x32_bf16 v[88:91], a[212:215], a[104:107], v[40:43]\n\t"
"v_cvt_pk_bf16_f32 v94, v94, v95\n\t"
"v_accvgpr_write_b32 a15, v94\n\t"
"v_mfma_f32_16x16x32_bf16 v[92:95], a[212:215], a[108:111], v[44:47]\n\t"
"s_waitcnt vmcnt(10)\n\t"
"s_barrier\n\t"
"v_mfma_f32_16x16x32_bf16 v[76:79], a[208:211], a[108:111], v[44:47]\n\t"
"s_waitcnt lgkmcnt(2)\n\t"
"v_exp_f32 v96, v96\n\t"
"v_mfma_f32_16x16x32_bf16 v[64:67], a[192:195], a[112:115], v[64:67]\n\t"
"ds_read_b128 a[208:211], v12 offset:13312\n\t"
"v_mfma_f32_16x16x32_bf16 v[80:83], a[196:199], a[112:115], v[80:83]\n\t"
"v_exp_f32 v97, v97\n\t"
"v_mfma_f32_16x16x32_bf16 v[84:87], a[196:199], a[116:119], v[84:87]\n\t"
"v_exp_f32 v98, v98\n\t"
"v_mfma_f32_16x16x32_bf16 v[68:71], a[192:195], a[116:119], v[68:71]\n\t"
"s_add_u32 m0, s44, 16384\n\t"
".Lpg7%=:\n"
"s_cbranch_execz .Lpg11%=\n\t"
"v_exp_f32 v99, v99\n\t"
"v_mfma_f32_16x16x32_bf16 v[72:75], a[192:195], a[120:123], v[72:75]\n\t"
"s_addk_i32 s48, 12288\n\t"
"ds_read_b128 a[212:215], v12 offset:19456\n\t"
"v_cvt_pk_bf16_f32 v192, v96, v97\n\t"
"v_mfma_f32_16x16x32_bf16 v[88:91], a[196:199], a[120:123], v[88:91]\n\t"
"buffer_load_dwordx4 v4, s[20:23], s48 offen offset:0 lds\n\t"
"v_mfma_f32_16x16x32_bf16 v[92:95], a[196:199], a[124:127], v[92:95]\n\t"
"v_exp_f32 v112, v112\n\t"
"v_mfma_f32_16x16x32_bf16 v[76:79], a[192:195], a[124:127], v[76:79]\n\t"
"s_waitcnt lgkmcnt(2)\n\t"
"v_exp_f32 v113, v113\n\t"
"v_mfma_f32_16x16x32_bf16 v[64:67], a[200:203], a[128:131], v[64:67]\n\t"
"ds_read_b128 a[192:195], v6 offset:14336\n\t"
"v_cvt_pk_bf16_f32 v193, v98, v99\n\t"
"v_mfma_f32_16x16x32_bf16 v[80:83], a[204:207], a[128:131], v[80:83]\n\t"
"v_exp_f32 v114, v114\n\t"
"v_mfma_f32_16x16x32_bf16 v[84:87], a[204:207], a[132:135], v[84:87]\n\t"
"v_exp_f32 v115, v115\n\t"
"v_mfma_f32_16x16x32_bf16 v[68:71], a[200:203], a[132:135], v[68:71]\n\t"
"v_exp_f32 v100, v100\n\t"
"v_mfma_f32_16x16x32_bf16 v[72:75], a[200:203], a[136:139], v[72:75]\n\t"
"ds_read_b128 a[196:199], v6 offset:20480\n\t"
"v_cvt_pk_bf16_f32 v194, v112, v113\n\t"
"v_mfma_f32_16x16x32_bf16 v[88:91], a[204:207], a[136:139], v[88:91]\n\t"
"v_exp_f32 v101, v101\n\t"
"v_mfma_f32_16x16x32_bf16 v[92:95], a[204:207], a[140:143], v[92:95]\n\t"
"s_add_u32 m0, s43, 0\n\t"
"v_exp_f32 v102, v102\n\t"
"v_mfma_f32_16x16x32_bf16 v[76:79], a[200:203], a[140:143], v[76:79]\n\t"
"s_waitcnt lgkmcnt(2)\n\t"
"v_exp_f32 v103, v103\n\t"
"v_mfma_f32_16x16x32_bf16 v[64:67], a[208:211], a[144:147], v[64:67]\n\t"
"s_addk_i32 s47, 12288\n\t"
"ds_read_b128 a[200:203], v12 offset:14336\n\t"
"v_cvt_pk_bf16_f32 v195, v114, v115\n\t"
"v_mfma_f32_16x16x32_bf16 v[80:83], a[212:215], a[144:147], v[80:83]\n\t"
"buffer_load_dwordx4 v3, s[16:19], s47 offen offset:0 lds\n\t"
"v_mfma_f32_16x16x32_bf16 v[84:87], a[212:215], a[148:151], v[84:87]\n\t"
"v_exp_f32 v116, v116\n\t"
"v_mfma_f32_16x16x32_bf16 v[68:71], a[208:211], a[148:151], v[68:71]\n\t"
"v_exp_f32 v117, v117\n\t"
"v_mfma_f32_16x16x32_bf16 v[72:75], a[208:211], a[152:155], v[72:75]\n\t"
"s_load_dword s3, %[karg], s85 offset:27080\n\t"
"ds_read_b128 a[204:207], v12 offset:20480\n\t"
"v_cvt_pk_bf16_f32 v196, v100, v101\n\t"
"v_mfma_f32_16x16x32_bf16 v[88:91], a[212:215], a[152:155], v[88:91]\n\t"
"v_exp_f32 v118, v118\n\t"
"v_mfma_f32_16x16x32_bf16 v[92:95], a[212:215], a[156:159], v[92:95]\n\t"
"v_exp_f32 v119, v119\n\t"
"v_mfma_f32_16x16x32_bf16 v[76:79], a[208:211], a[156:159], v[76:79]\n\t"
"s_waitcnt lgkmcnt(2)\n\t"
"v_exp_f32 v104, v104\n\t"
"v_mfma_f32_16x16x32_bf16 v[64:67], a[192:195], a[160:163], v[64:67]\n\t"
"ds_read_b64_tr_b16 a[208:209], v7 offset:28672\n\t"
"v_cvt_pk_bf16_f32 v197, v102, v103\n\t"
"v_mfma_f32_16x16x32_bf16 v[80:83], a[196:199], a[160:163], v[80:83]\n\t"
"ds_read_b64_tr_b16 a[210:211], v7 offset:30720\n\t"
"v_cvt_pk_bf16_f32 v198, v116, v117\n\t"
"v_mfma_f32_16x16x32_bf16 v[84:87], a[196:199], a[164:167], v[84:87]\n\t"
"v_exp_f32 v105, v105\n\t"
"v_mfma_f32_16x16x32_bf16 v[68:71], a[192:195], a[164:167], v[68:71]\n\t"
"v_exp_f32 v106, v106\n\t"
"v_mfma_f32_16x16x32_bf16 v[72:75], a[192:195], a[168:171], v[72:75]\n\t"
"ds_read_b64_tr_b16 a[212:213], v13 offset:28672\n\t"
"v_cvt_pk_bf16_f32 v199, v118, v119\n\t"
"v_mfma_f32_16x16x32_bf16 v[88:91], a[196:199], a[168:171], v[88:91]\n\t"
"ds_read_b64_tr_b16 a[214:215], v13 offset:30720\n\t"
"v_cvt_pk_bf16_f32 v104, v104, v105\n\t"
"v_mfma_f32_16x16x32_bf16 v[92:95], a[196:199], a[172:175], v[92:95]\n\t"
"buffer_load_dwordx4 v10, s[16:19], s47 offen offset:1024 lds\n\t"
"v_mfma_f32_16x16x32_bf16 v[76:79], a[192:195], a[172:175], v[76:79]\n\t"
"s_waitcnt lgkmcnt(4)\n\t"
"v_exp_f32 v107, v107\n\t"
"v_mfma_f32_16x16x32_bf16 v[64:67], a[200:203], a[176:179], v[64:67]\n\t"
"ds_read_b128 a[192:195], v6 offset:24576\n\t"
"v_accvgpr_write_b32 a16, v104\n\t"
"v_mfma_f32_16x16x32_bf16 v[80:83], a[204:207], a[176:179], v[80:83]\n\t"
"v_exp_f32 v120, v120\n\t"
"v_mfma_f32_16x16x32_bf16 v[84:87], a[204:207], a[180:183], v[84:87]\n\t"
"v_exp_f32 v121, v121\n\t"
"v_mfma_f32_16x16x32_bf16 v[68:71], a[200:203], a[180:183], v[68:71]\n\t"
"v_cvt_pk_bf16_f32 v106, v106, v107\n\t"
"v_accvgpr_write_b32 a17, v106\n\t"
"v_mfma_f32_16x16x32_bf16 v[72:75], a[200:203], a[184:187], v[72:75]\n\t"
"ds_read_b128 a[196:199], v6 offset:30720\n\t"
"v_cvt_pk_bf16_f32 v120, v120, v121\n\t"
"v_mfma_f32_16x16x32_bf16 v[88:91], a[204:207], a[184:187], v[88:91]\n\t"
"v_exp_f32 v122, v122\n\t"
"v_mfma_f32_16x16x32_bf16 v[92:95], a[204:207], a[188:191], v[92:95]\n\t"
"v_exp_f32 v123, v123\n\t"
"v_mfma_f32_16x16x32_bf16 v[76:79], a[200:203], a[188:191], v[76:79]\n\t"
"v_exp_f32 v108, v108\n\t"
"v_smfmac_f32_16x16x64_bf16 a[224:227], a[244:247], v[184:191], v30\n\t"
"v_accvgpr_write_b32 a18, v120\n\t"
"v_smfmac_f32_16x16x64_bf16 a[228:231], a[244:247], a[8:15], v30\n\t"
"s_waitcnt lgkmcnt(2)\n\t"
"v_cvt_pk_bf16_f32 v122, v122, v123\n\t"
"v_mfma_f32_16x16x32_bf16 a[216:219], a[208:211], v[160:163], a[216:219]\n\t"
"ds_read_b128 a[200:203], v12 offset:24576\n\t"
"v_accvgpr_write_b32 a19, v122\n\t"
"v_mfma_f32_16x16x32_bf16 v[48:51], a[212:215], v[160:163], v[48:51]\n\t"
"buffer_load_dwordx4 v11, s[16:19], s47 offen offset:2048 lds\n\t"
"v_mfma_f32_16x16x32_bf16 v[52:55], a[212:215], v[164:167], v[52:55]\n\t"
"v_exp_f32 v109, v109\n\t"
"v_mfma_f32_16x16x32_bf16 a[220:223], a[208:211], v[164:167], a[220:223]\n\t"
"v_exp_f32 v110, v110\n\t"
"v_mfma_f32_16x16x32_bf16 a[232:235], a[208:211], v[168:171], a[232:235]\n\t"
"ds_read_b128 a[204:207], v12 offset:30720\n\t"
"v_cvt_pk_bf16_f32 v108, v108, v109\n\t"
"v_mfma_f32_16x16x32_bf16 v[56:59], a[212:215], v[168:171], v[56:59]\n\t"
"v_exp_f32 v111, v111\n\t"
"v_mfma_f32_16x16x32_bf16 v[60:63], a[212:215], v[172:175], v[60:63]\n\t"
"v_exp_f32 v124, v124\n\t"
"v_mfma_f32_16x16x32_bf16 a[248:251], a[208:211], v[172:175], a[248:251]\n\t"
"s_waitcnt lgkmcnt(2)\n\t"
"v_exp_f32 v125, v125\n\t"
"v_accvgpr_write_b32 a20, v108\n\t"
"v_mfma_f32_16x16x32_bf16 v[96:99], a[192:195], a[96:99], v[32:35]\n\t"
"ds_read_b128 a[208:211], v6 offset:25600\n\t"
"v_cvt_pk_bf16_f32 v110, v110, v111\n\t"
"v_mfma_f32_16x16x32_bf16 v[112:115], a[196:199], a[96:99], v[32:35]\n\t"
"v_exp_f32 v126, v126\n\t"
"v_mfma_f32_16x16x32_bf16 v[116:119], a[196:199], a[100:103], v[36:39]\n\t"
"v_exp_f32 v127, v127\n\t"
"v_mfma_f32_16x16x32_bf16 v[100:103], a[192:195], a[100:103], v[36:39]\n\t"
"v_accvgpr_write_b32 a21, v110\n\t"
"v_cvt_pk_bf16_f32 v124, v124, v125\n\t"
"v_mfma_f32_16x16x32_bf16 v[104:107], a[192:195], a[104:107], v[40:43]\n\t"
"ds_read_b128 a[212:215], v6 offset:31744\n\t"
"v_accvgpr_write_b32 a22, v124\n\t"
"v_mfma_f32_16x16x32_bf16 v[120:123], a[196:199], a[104:107], v[40:43]\n\t"
"v_cvt_pk_bf16_f32 v126, v126, v127\n\t"
"v_accvgpr_write_b32 a23, v126\n\t"
"v_mfma_f32_16x16x32_bf16 v[124:127], a[196:199], a[108:111], v[44:47]\n\t"
"s_waitcnt vmcnt(8)\n\t"
"s_barrier\n\t"
"v_mfma_f32_16x16x32_bf16 v[108:111], a[192:195], a[108:111], v[44:47]\n\t"
"s_waitcnt lgkmcnt(2)\n\t"
"v_exp_f32 v64, v64\n\t"
"v_mfma_f32_16x16x32_bf16 v[96:99], a[200:203], a[112:115], v[96:99]\n\t"
"ds_read_b128 a[192:195], v12 offset:25600\n\t"
"v_exp_f32 v65, v65\n\t"
"v_mfma_f32_16x16x32_bf16 v[112:115], a[204:207], a[112:115], v[112:115]\n\t"
"s_add_u32 m0, s44, 12288\n\t"
"v_exp_f32 v66, v66\n\t"
"v_mfma_f32_16x16x32_bf16 v[116:119], a[204:207], a[116:119], v[116:119]\n\t"
"s_addk_i32 s48, 12288\n\t"
"v_exp_f32 v67, v67\n\t"
"v_mfma_f32_16x16x32_bf16 v[100:103], a[200:203], a[116:119], v[100:103]\n\t"
"buffer_load_dwordx4 v4, s[20:23], s48 offen offset:0 lds\n\t"
"v_mfma_f32_16x16x32_bf16 v[104:107], a[200:203], a[120:123], v[104:107]\n\t"
"ds_read_b128 a[196:199], v12 offset:31744\n\t"
"v_cvt_pk_bf16_f32 v200, v64, v65\n\t"
"v_mfma_f32_16x16x32_bf16 v[120:123], a[204:207], a[120:123], v[120:123]\n\t"
"v_exp_f32 v80, v80\n\t"
"v_mfma_f32_16x16x32_bf16 v[124:127], a[204:207], a[124:127], v[124:127]\n\t"
"v_exp_f32 v81, v81\n\t"
"v_mfma_f32_16x16x32_bf16 v[108:111], a[200:203], a[124:127], v[108:111]\n\t"
"s_waitcnt lgkmcnt(2)\n\t"
"v_exp_f32 v82, v82\n\t"
"v_mfma_f32_16x16x32_bf16 v[96:99], a[208:211], a[128:131], v[96:99]\n\t"
"ds_read_b128 a[200:203], v6 offset:26624\n\t"
"v_cvt_pk_bf16_f32 v201, v66, v67\n\t"
"v_mfma_f32_16x16x32_bf16 v[112:115], a[212:215], a[128:131], v[112:115]\n\t"
"v_exp_f32 v83, v83\n\t"
"v_mfma_f32_16x16x32_bf16 v[116:119], a[212:215], a[132:135], v[116:119]\n\t"
"v_exp_f32 v68, v68\n\t"
"v_cvt_pk_bf16_f32 v202, v80, v81\n\t"
"v_mfma_f32_16x16x32_bf16 v[100:103], a[208:211], a[132:135], v[100:103]\n\t"
"s_add_u32 m0, s43, 12288\n\t"
"v_exp_f32 v69, v69\n\t"
"v_mfma_f32_16x16x32_bf16 v[104:107], a[208:211], a[136:139], v[104:107]\n\t"
"s_addk_i32 s47, 12288\n\t"
"ds_read_b128 a[204:207], v6 offset:32768\n\t"
"v_cvt_pk_bf16_f32 v203, v82, v83\n\t"
"v_mfma_f32_16x16x32_bf16 v[120:123], a[212:215], a[136:139], v[120:123]\n\t"
"buffer_load_dwordx4 v3, s[16:19], s47 offen offset:0 lds\n\t"
"v_mfma_f32_16x16x32_bf16 v[124:127], a[212:215], a[140:143], v[124:127]\n\t"
"v_exp_f32 v70, v70\n\t"
"v_mfma_f32_16x16x32_bf16 v[108:111], a[208:211], a[140:143], v[108:111]\n\t"
"s_waitcnt lgkmcnt(2)\n\t"
"v_exp_f32 v71, v71\n\t"
"v_mfma_f32_16x16x32_bf16 v[96:99], a[192:195], a[144:147], v[96:99]\n\t"
"ds_read_b128 a[208:211], v12 offset:26624\n\t"
"v_cvt_pk_bf16_f32 v204, v68, v69\n\t"
"v_mfma_f32_16x16x32_bf16 v[112:115], a[196:199], a[144:147], v[112:115]\n\t"
"v_exp_f32 v84, v84\n\t"
"v_mfma_f32_16x16x32_bf16 v[116:119], a[196:199], a[148:151], v[116:119]\n\t"
"v_exp_f32 v85, v85\n\t"
"v_mfma_f32_16x16x32_bf16 v[100:103], a[192:195], a[148:151], v[100:103]\n\t"
"v_exp_f32 v86, v86\n\t"
"v_cvt_pk_bf16_f32 v205, v70, v71\n\t"
"v_mfma_f32_16x16x32_bf16 v[104:107], a[192:195], a[152:155], v[104:107]\n\t"
"ds_read_b128 a[212:215], v12 offset:32768\n\t"
"v_cvt_pk_bf16_f32 v206, v84, v85\n\t"
"v_mfma_f32_16x16x32_bf16 v[120:123], a[196:199], a[152:155], v[120:123]\n\t"
"v_exp_f32 v87, v87\n\t"
"v_mfma_f32_16x16x32_bf16 v[124:127], a[196:199], a[156:159], v[124:127]\n\t"
"v_exp_f32 v72, v72\n\t"
"v_mfma_f32_16x16x32_bf16 v[108:111], a[192:195], a[156:159], v[108:111]\n\t"
"s_waitcnt lgkmcnt(2)\n\t"
"buffer_load_dwordx4 v10, s[16:19], s47 offen offset:1024 lds\n\t"
"v_mfma_f32_16x16x32_bf16 v[96:99], a[200:203], a[160:163], v[96:99]\n\t"
"ds_read_b64_tr_b16 a[192:193], v7 offset:24576\n\t"
"v_cvt_pk_bf16_f32 v207, v86, v87\n\t"
"v_mfma_f32_16x16x32_bf16 v[112:115], a[204:207], a[160:163], v[112:115]\n\t"
"ds_read_b64_tr_b16 a[194:195], v7 offset:26624\n\t"
"v_exp_f32 v73, v73\n\t"
"v_mfma_f32_16x16x32_bf16 v[116:119], a[204:207], a[164:167], v[116:119]\n\t"
"v_exp_f32 v74, v74\n\t"
"v_mfma_f32_16x16x32_bf16 v[100:103], a[200:203], a[164:167], v[100:103]\n\t"
"v_exp_f32 v75, v75\n\t"
"v_mfma_f32_16x16x32_bf16 v[104:107], a[200:203], a[168:171], v[104:107]\n\t"
"ds_read_b64_tr_b16 a[196:197], v13 offset:24576\n\t"
"v_cvt_pk_bf16_f32 v72, v72, v73\n\t"
"v_mfma_f32_16x16x32_bf16 v[120:123], a[204:207], a[168:171], v[120:123]\n\t"
"ds_read_b64_tr_b16 a[198:199], v13 offset:26624\n\t"
"v_accvgpr_write_b32 a24, v72\n\t"
"v_mfma_f32_16x16x32_bf16 v[124:127], a[204:207], a[172:175], v[124:127]\n\t"
"v_exp_f32 v88, v88\n\t"
"v_mfma_f32_16x16x32_bf16 v[108:111], a[200:203], a[172:175], v[108:111]\n\t"
"s_waitcnt lgkmcnt(4)\n\t"
"v_exp_f32 v89, v89\n\t"
"v_mfma_f32_16x16x32_bf16 v[96:99], a[208:211], a[176:179], v[96:99]\n\t"
"ds_read_b128 a[200:203], v6 offset:36864\n\t"
"v_cvt_pk_bf16_f32 v74, v74, v75\n\t"
"v_mfma_f32_16x16x32_bf16 v[112:115], a[212:215], a[176:179], v[112:115]\n\t"
"buffer_load_dwordx4 v11, s[16:19], s47 offen offset:2048 lds\n\t"
"v_mfma_f32_16x16x32_bf16 v[116:119], a[212:215], a[180:183], v[116:119]\n\t"
"v_exp_f32 v90, v90\n\t"
"v_accvgpr_write_b32 a25, v74\n\t"
"v_mfma_f32_16x16x32_bf16 v[100:103], a[208:211], a[180:183], v[100:103]\n\t"
"v_exp_f32 v91, v91\n\t"
"v_mfma_f32_16x16x32_bf16 v[104:107], a[208:211], a[184:187], v[104:107]\n\t"
"ds_read_b128 a[204:207], v6 offset:43008\n\t"
"v_mul_u32_u24 v20, 8, v1\n\t"
"v_mfma_f32_16x16x32_bf16 v[120:123], a[212:215], a[184:187], v[120:123]\n\t"
"v_cvt_pk_bf16_f32 v88, v88, v89\n\t"
"v_accvgpr_write_b32 a26, v88\n\t"
"v_mfma_f32_16x16x32_bf16 v[124:127], a[212:215], a[188:191], v[124:127]\n\t"
"v_exp_f32 v76, v76\n\t"
"v_mfma_f32_16x16x32_bf16 v[108:111], a[208:211], a[188:191], v[108:111]\n\t"
"v_exp_f32 v77, v77\n\t"
"v_smfmac_f32_16x16x64_bf16 a[224:227], a[244:247], v[192:199], v30\n\t"
"v_cvt_pk_bf16_f32 v90, v90, v91\n\t"
"v_smfmac_f32_16x16x64_bf16 a[228:231], a[244:247], a[16:23], v30\n\t"
"s_waitcnt lgkmcnt(2)\n\t"
"v_accvgpr_write_b32 a27, v90\n\t"
"v_cvt_pk_bf16_f32 v76, v76, v77\n\t"
"v_mfma_f32_16x16x32_bf16 a[216:219], a[192:195], v[176:179], a[216:219]\n\t"
"ds_read_b128 a[208:211], v12 offset:36864\n\t"
"v_cmp_gt_u32 vcc, 320, v1\n\t"
"v_mfma_f32_16x16x32_bf16 v[48:51], a[196:199], v[176:179], v[48:51]\n\t"
"v_exp_f32 v78, v78\n\t"
"v_mfma_f32_16x16x32_bf16 v[52:55], a[196:199], v[180:183], v[52:55]\n\t"
"v_exp_f32 v79, v79\n\t"
"v_mfma_f32_16x16x32_bf16 a[220:223], a[192:195], v[180:183], a[220:223]\n\t"
"v_exp_f32 v92, v92\n\t"
"v_mfma_f32_16x16x32_bf16 a[232:235], a[192:195], a[0:3], a[232:235]\n\t"
"s_mul_i32 s80, s45, 2560\n\t"
"ds_read_b128 a[212:215], v12 offset:43008\n\t"
"v_mov_b32 v21, 1073741824\n\t"
"v_mfma_f32_16x16x32_bf16 v[56:59], a[196:199], a[0:3], v[56:59]\n\t"
"s_mul_i32 s46, s78, 245760\n\t"
"v_exp_f32 v93, v93\n\t"
"v_mfma_f32_16x16x32_bf16 v[60:63], a[196:199], a[4:7], v[60:63]\n\t"
"s_add_u32 s46, s46, s80\n\t"
"v_accvgpr_write_b32 a28, v76\n\t"
"v_cvt_pk_bf16_f32 v78, v78, v79\n\t"
"v_mfma_f32_16x16x32_bf16 a[248:251], a[192:195], a[4:7], a[248:251]\n\t"
"s_waitcnt lgkmcnt(2)\n\t"
"v_exp_f32 v94, v94\n\t"
"v_accvgpr_write_b32 a29, v78\n\t"
"v_mfma_f32_16x16x32_bf16 v[64:67], a[200:203], a[96:99], v[32:35]\n\t"
"ds_read_b128 a[192:195], v6 offset:37888\n\t"
"v_cndmask_b32 v20, v21, v20, vcc\n\t"
"v_mfma_f32_16x16x32_bf16 v[80:83], a[204:207], a[96:99], v[32:35]\n\t"
"buffer_load_dword a252, v20, s[24:27], s46 offen offset:0\n\t"
"v_mfma_f32_16x16x32_bf16 v[84:87], a[204:207], a[100:103], v[36:39]\n\t"
"s_add_u32 s80, s46, 81920\n\t"
"v_exp_f32 v95, v95\n\t"
"v_mfma_f32_16x16x32_bf16 v[68:71], a[200:203], a[100:103], v[36:39]\n\t"
"v_cvt_pk_bf16_f32 v92, v92, v93\n\t"
"v_accvgpr_write_b32 a30, v92\n\t"
"v_mfma_f32_16x16x32_bf16 v[72:75], a[200:203], a[104:107], v[40:43]\n\t"
"ds_read_b128 a[196:199], v6 offset:44032\n\t"
"v_cvt_pk_bf16_f32 v94, v94, v95\n\t"
"v_mfma_f32_16x16x32_bf16 v[88:91], a[204:207], a[104:107], v[40:43]\n\t"
"v_accvgpr_write_b32 a31, v94\n\t"
"v_mfma_f32_16x16x32_bf16 v[92:95], a[204:207], a[108:111], v[44:47]\n\t"
"s_waitcnt vmcnt(9)\n\t"
"s_barrier\n\t"
"v_mfma_f32_16x16x32_bf16 v[76:79], a[200:203], a[108:111], v[44:47]\n\t"
"s_waitcnt lgkmcnt(2)\n\t"
"v_exp_f32 v96, v96\n\t"
"v_mfma_f32_16x16x32_bf16 v[64:67], a[208:211], a[112:115], v[64:67]\n\t"
"ds_read_b128 a[200:203], v12 offset:37888\n\t"
"v_mfma_f32_16x16x32_bf16 v[80:83], a[212:215], a[112:115], v[80:83]\n\t"
"v_exp_f32 v97, v97\n\t"
"v_mfma_f32_16x16x32_bf16 v[84:87], a[212:215], a[116:119], v[84:87]\n\t"
"v_exp_f32 v98, v98\n\t"
"v_mfma_f32_16x16x32_bf16 v[68:71], a[208:211], a[116:119], v[68:71]\n\t"
"s_add_u32 m0, s44, 8192\n\t"
"v_exp_f32 v99, v99\n\t"
"v_mfma_f32_16x16x32_bf16 v[72:75], a[208:211], a[120:123], v[72:75]\n\t"
"s_addk_i32 s48, 12288\n\t"
"ds_read_b128 a[204:207], v12 offset:44032\n\t"
"v_cvt_pk_bf16_f32 v208, v96, v97\n\t"
"v_mfma_f32_16x16x32_bf16 v[88:91], a[212:215], a[120:123], v[88:91]\n\t"
"buffer_load_dwordx4 v4, s[20:23], s48 offen offset:0 lds\n\t"
"v_mfma_f32_16x16x32_bf16 v[92:95], a[212:215], a[124:127], v[92:95]\n\t"
"v_exp_f32 v112, v112\n\t"
"v_mfma_f32_16x16x32_bf16 v[76:79], a[208:211], a[124:127], v[76:79]\n\t"
"s_waitcnt lgkmcnt(2)\n\t"
"v_exp_f32 v113, v113\n\t"
"v_mfma_f32_16x16x32_bf16 v[64:67], a[192:195], a[128:131], v[64:67]\n\t"
"ds_read_b128 a[208:211], v6 offset:38912\n\t"
"v_cvt_pk_bf16_f32 v209, v98, v99\n\t"
"v_mfma_f32_16x16x32_bf16 v[80:83], a[196:199], a[128:131], v[80:83]\n\t"
"v_exp_f32 v114, v114\n\t"
"v_mfma_f32_16x16x32_bf16 v[84:87], a[196:199], a[132:135], v[84:87]\n\t"
"v_exp_f32 v115, v115\n\t"
"v_mfma_f32_16x16x32_bf16 v[68:71], a[192:195], a[132:135], v[68:71]\n\t"
"v_exp_f32 v100, v100\n\t"
"v_mfma_f32_16x16x32_bf16 v[72:75], a[192:195], a[136:139], v[72:75]\n\t"
"ds_read_b128 a[212:215], v6 offset:45056\n\t"
"v_cvt_pk_bf16_f32 v210, v112, v113\n\t"
"v_mfma_f32_16x16x32_bf16 v[88:91], a[196:199], a[136:139], v[88:91]\n\t"
"v_exp_f32 v101, v101\n\t"
"v_mfma_f32_16x16x32_bf16 v[92:95], a[196:199], a[140:143], v[92:95]\n\t"
"s_add_u32 m0, s43, 24576\n\t"
"v_exp_f32 v102, v102\n\t"
"v_mfma_f32_16x16x32_bf16 v[76:79], a[192:195], a[140:143], v[76:79]\n\t"
"s_waitcnt lgkmcnt(2)\n\t"
"v_exp_f32 v103, v103\n\t"
"v_mfma_f32_16x16x32_bf16 v[64:67], a[200:203], a[144:147], v[64:67]\n\t"
"s_addk_i32 s47, 12288\n\t"
"ds_read_b128 a[192:195], v12 offset:38912\n\t"
"v_cvt_pk_bf16_f32 v211, v114, v115\n\t"
"v_mfma_f32_16x16x32_bf16 v[80:83], a[204:207], a[144:147], v[80:83]\n\t"
"buffer_load_dwordx4 v3, s[16:19], s47 offen offset:0 lds\n\t"
"v_mfma_f32_16x16x32_bf16 v[84:87], a[204:207], a[148:151], v[84:87]\n\t"
"v_exp_f32 v116, v116\n\t"
"v_mfma_f32_16x16x32_bf16 v[68:71], a[200:203], a[148:151], v[68:71]\n\t"
"v_exp_f32 v117, v117\n\t"
"v_mfma_f32_16x16x32_bf16 v[72:75], a[200:203], a[152:155], v[72:75]\n\t"
"ds_read_b128 a[196:199], v12 offset:45056\n\t"
"v_cvt_pk_bf16_f32 v212, v100, v101\n\t"
"v_mfma_f32_16x16x32_bf16 v[88:91], a[204:207], a[152:155], v[88:91]\n\t"
"v_exp_f32 v118, v118\n\t"
"v_mfma_f32_16x16x32_bf16 v[92:95], a[204:207], a[156:159], v[92:95]\n\t"
"v_exp_f32 v119, v119\n\t"
"v_mfma_f32_16x16x32_bf16 v[76:79], a[200:203], a[156:159], v[76:79]\n\t"
"s_waitcnt lgkmcnt(2)\n\t"
"v_exp_f32 v104, v104\n\t"
"v_mfma_f32_16x16x32_bf16 v[64:67], a[208:211], a[160:163], v[64:67]\n\t"
"ds_read_b64_tr_b16 a[200:201], v7 offset:20480\n\t"
"v_cvt_pk_bf16_f32 v213, v102, v103\n\t"
"v_mfma_f32_16x16x32_bf16 v[80:83], a[212:215], a[160:163], v[80:83]\n\t"
"ds_read_b64_tr_b16 a[202:203], v7 offset:22528\n\t"
"v_cvt_pk_bf16_f32 v214, v116, v117\n\t"
"v_mfma_f32_16x16x32_bf16 v[84:87], a[212:215], a[164:167], v[84:87]\n\t"
"v_exp_f32 v105, v105\n\t"
"v_mfma_f32_16x16x32_bf16 v[68:71], a[208:211], a[164:167], v[68:71]\n\t"
"v_exp_f32 v106, v106\n\t"
"v_mfma_f32_16x16x32_bf16 v[72:75], a[208:211], a[168:171], v[72:75]\n\t"
"ds_read_b64_tr_b16 a[204:205], v13 offset:20480\n\t"
"v_cvt_pk_bf16_f32 v215, v118, v119\n\t"
"v_mfma_f32_16x16x32_bf16 v[88:91], a[212:215], a[168:171], v[88:91]\n\t"
"ds_read_b64_tr_b16 a[206:207], v13 offset:22528\n\t"
"v_cvt_pk_bf16_f32 v104, v104, v105\n\t"
"v_mfma_f32_16x16x32_bf16 v[92:95], a[212:215], a[172:175], v[92:95]\n\t"
"buffer_load_dwordx4 v10, s[16:19], s47 offen offset:1024 lds\n\t"
"v_mfma_f32_16x16x32_bf16 v[76:79], a[208:211], a[172:175], v[76:79]\n\t"
"s_waitcnt lgkmcnt(4)\n\t"
"v_exp_f32 v107, v107\n\t"
"v_mfma_f32_16x16x32_bf16 v[64:67], a[192:195], a[176:179], v[64:67]\n\t"
"ds_read_b128 a[208:211], v6 offset:49152\n\t"
"v_accvgpr_write_b32 a32, v104\n\t"
"v_mfma_f32_16x16x32_bf16 v[80:83], a[196:199], a[176:179], v[80:83]\n\t"
"v_exp_f32 v120, v120\n\t"
"v_mfma_f32_16x16x32_bf16 v[84:87], a[196:199], a[180:183], v[84:87]\n\t"
"v_exp_f32 v121, v121\n\t"
"v_mfma_f32_16x16x32_bf16 v[68:71], a[192:195], a[180:183], v[68:71]\n\t"
"v_cvt_pk_bf16_f32 v106, v106, v107\n\t"
"v_accvgpr_write_b32 a33, v106\n\t"
"v_mfma_f32_16x16x32_bf16 v[72:75], a[192:195], a[184:187], v[72:75]\n\t"
"ds_read_b128 a[212:215], v6 offset:55296\n\t"
"v_cvt_pk_bf16_f32 v120, v120, v121\n\t"
"v_mfma_f32_16x16x32_bf16 v[88:91], a[196:199], a[184:187], v[88:91]\n\t"
"v_exp_f32 v122, v122\n\t"
"v_mfma_f32_16x16x32_bf16 v[92:95], a[196:199], a[188:191], v[92:95]\n\t"
"v_exp_f32 v123, v123\n\t"
"v_mfma_f32_16x16x32_bf16 v[76:79], a[192:195], a[188:191], v[76:79]\n\t"
"v_exp_f32 v108, v108\n\t"
"v_smfmac_f32_16x16x64_bf16 a[224:227], a[244:247], v[200:207], v30\n\t"
"v_accvgpr_write_b32 a34, v120\n\t"
"v_smfmac_f32_16x16x64_bf16 a[228:231], a[244:247], a[24:31], v30\n\t"
"s_waitcnt lgkmcnt(2)\n\t"
"v_cvt_pk_bf16_f32 v122, v122, v123\n\t"
"v_mfma_f32_16x16x32_bf16 a[216:219], a[200:203], v[184:187], a[216:219]\n\t"
"ds_read_b128 a[192:195], v12 offset:49152\n\t"
"v_accvgpr_write_b32 a35, v122\n\t"
"v_mfma_f32_16x16x32_bf16 v[48:51], a[204:207], v[184:187], v[48:51]\n\t"
"buffer_load_dwordx4 v11, s[16:19], s47 offen offset:2048 lds\n\t"
"v_mfma_f32_16x16x32_bf16 v[52:55], a[204:207], v[188:191], v[52:55]\n\t"
"v_exp_f32 v109, v109\n\t"
"v_mfma_f32_16x16x32_bf16 a[220:223], a[200:203], v[188:191], a[220:223]\n\t"
"v_exp_f32 v110, v110\n\t"
"v_mfma_f32_16x16x32_bf16 a[232:235], a[200:203], a[8:11], a[232:235]\n\t"
"ds_read_b128 a[196:199], v12 offset:55296\n\t"
"v_cvt_pk_bf16_f32 v108, v108, v109\n\t"
"v_mfma_f32_16x16x32_bf16 v[56:59], a[204:207], a[8:11], v[56:59]\n\t"
"v_exp_f32 v111, v111\n\t"
"v_mfma_f32_16x16x32_bf16 v[60:63], a[204:207], a[12:15], v[60:63]\n\t"
"v_exp_f32 v124, v124\n\t"
"v_mfma_f32_16x16x32_bf16 a[248:251], a[200:203], a[12:15], a[248:251]\n\t"
"s_waitcnt lgkmcnt(2)\n\t"
"v_exp_f32 v125, v125\n\t"
"v_accvgpr_write_b32 a36, v108\n\t"
"v_mfma_f32_16x16x32_bf16 v[96:99], a[208:211], a[96:99], v[32:35]\n\t"
"ds_read_b128 a[200:203], v6 offset:50176\n\t"
"v_cvt_pk_bf16_f32 v110, v110, v111\n\t"
"v_mfma_f32_16x16x32_bf16 v[112:115], a[212:215], a[96:99], v[32:35]\n\t"
"v_exp_f32 v126, v126\n\t"
"v_mfma_f32_16x16x32_bf16 v[116:119], a[212:215], a[100:103], v[36:39]\n\t"
"v_exp_f32 v127, v127\n\t"
"v_mfma_f32_16x16x32_bf16 v[100:103], a[208:211], a[100:103], v[36:39]\n\t"
"v_accvgpr_write_b32 a37, v110\n\t"
"v_cvt_pk_bf16_f32 v124, v124, v125\n\t"
"v_mfma_f32_16x16x32_bf16 v[104:107], a[208:211], a[104:107], v[40:43]\n\t"
"ds_read_b128 a[204:207], v6 offset:56320\n\t"
"v_accvgpr_write_b32 a38, v124\n\t"
"v_mfma_f32_16x16x32_bf16 v[120:123], a[212:215], a[104:107], v[40:43]\n\t"
"v_cvt_pk_bf16_f32 v126, v126, v127\n\t"
"v_accvgpr_write_b32 a39, v126\n\t"
"v_mfma_f32_16x16x32_bf16 v[124:127], a[212:215], a[108:111], v[44:47]\n\t"
"s_waitcnt vmcnt(9)\n\t"
"s_barrier\n\t"
"v_mfma_f32_16x16x32_bf16 v[108:111], a[208:211], a[108:111], v[44:47]\n\t"
"s_waitcnt lgkmcnt(2)\n\t"
"v_exp_f32 v64, v64\n\t"
"v_mfma_f32_16x16x32_bf16 v[96:99], a[192:195], a[112:115], v[96:99]\n\t"
"ds_read_b128 a[208:211], v12 offset:50176\n\t"
"v_mfma_f32_16x16x32_bf16 v[112:115], a[196:199], a[112:115], v[112:115]\n\t"
"s_add_u32 m0, s44, 4096\n\t"
"v_exp_f32 v65, v65\n\t"
"v_mfma_f32_16x16x32_bf16 v[116:119], a[196:199], a[116:119], v[116:119]\n\t"
"s_addk_i32 s48, 12288\n\t"
"v_exp_f32 v66, v66\n\t"
"v_mfma_f32_16x16x32_bf16 v[100:103], a[192:195], a[116:119], v[100:103]\n\t"
"buffer_load_dwordx4 v4, s[20:23], s48 offen offset:0 lds\n\t"
"v_mfma_f32_16x16x32_bf16 v[104:107], a[192:195], a[120:123], v[104:107]\n\t"
"ds_read_b128 a[212:215], v12 offset:56320\n\t"
"v_cvt_pk_bf16_f32 v216, v64, v65\n\t"
"v_mfma_f32_16x16x32_bf16 v[120:123], a[196:199], a[120:123], v[120:123]\n\t"
"v_exp_f32 v67, v67\n\t"
"v_mfma_f32_16x16x32_bf16 v[124:127], a[196:199], a[124:127], v[124:127]\n\t"
"v_exp_f32 v80, v80\n\t"
"v_mfma_f32_16x16x32_bf16 v[108:111], a[192:195], a[124:127], v[108:111]\n\t"
"s_waitcnt lgkmcnt(2)\n\t"
"v_exp_f32 v81, v81\n\t"
"v_mfma_f32_16x16x32_bf16 v[96:99], a[200:203], a[128:131], v[96:99]\n\t"
"ds_read_b128 a[192:195], v6 offset:51200\n\t"
"v_cvt_pk_bf16_f32 v217, v66, v67\n\t"
"v_mfma_f32_16x16x32_bf16 v[112:115], a[204:207], a[128:131], v[112:115]\n\t"
"v_exp_f32 v82, v82\n\t"
"v_mfma_f32_16x16x32_bf16 v[116:119], a[204:207], a[132:135], v[116:119]\n\t"
"v_exp_f32 v83, v83\n\t"
"v_mfma_f32_16x16x32_bf16 v[100:103], a[200:203], a[132:135], v[100:103]\n\t"
"s_add_u32 m0, s43, 36864\n\t"
"v_exp_f32 v68, v68\n\t"
"v_cvt_pk_bf16_f32 v218, v80, v81\n\t"
"v_mfma_f32_16x16x32_bf16 v[104:107], a[200:203], a[136:139], v[104:107]\n\t"
"s_addk_i32 s47, 12288\n\t"
"ds_read_b128 a[196:199], v6 offset:57344\n\t"
"v_cvt_pk_bf16_f32 v219, v82, v83\n\t"
"v_mfma_f32_16x16x32_bf16 v[120:123], a[204:207], a[136:139], v[120:123]\n\t"
"buffer_load_dwordx4 v3, s[16:19], s47 offen offset:0 lds\n\t"
"v_mfma_f32_16x16x32_bf16 v[124:127], a[204:207], a[140:143], v[124:127]\n\t"
"v_exp_f32 v69, v69\n\t"
"v_mfma_f32_16x16x32_bf16 v[108:111], a[200:203], a[140:143], v[108:111]\n\t"
"s_waitcnt lgkmcnt(2)\n\t"
"v_exp_f32 v70, v70\n\t"
"v_mfma_f32_16x16x32_bf16 v[96:99], a[208:211], a[144:147], v[96:99]\n\t"
"ds_read_b128 a[200:203], v12 offset:51200\n\t"
"v_cvt_pk_bf16_f32 v220, v68, v69\n\t"
"v_mfma_f32_16x16x32_bf16 v[112:115], a[212:215], a[144:147], v[112:115]\n\t"
"v_exp_f32 v71, v71\n\t"
"v_mfma_f32_16x16x32_bf16 v[116:119], a[212:215], a[148:151], v[116:119]\n\t"
"v_exp_f32 v84, v84\n\t"
"v_mfma_f32_16x16x32_bf16 v[100:103], a[208:211], a[148:151], v[100:103]\n\t"
"v_exp_f32 v85, v85\n\t"
"v_mfma_f32_16x16x32_bf16 v[104:107], a[208:211], a[152:155], v[104:107]\n\t"
"ds_read_b128 a[204:207], v12 offset:57344\n\t"
"v_cvt_pk_bf16_f32 v221, v70, v71\n\t"
"v_mfma_f32_16x16x32_bf16 v[120:123], a[212:215], a[152:155], v[120:123]\n\t"
"v_exp_f32 v86, v86\n\t"
"v_mfma_f32_16x16x32_bf16 v[124:127], a[212:215], a[156:159], v[124:127]\n\t"
"v_exp_f32 v87, v87\n\t"
"v_mfma_f32_16x16x32_bf16 v[108:111], a[208:211], a[156:159], v[108:111]\n\t"
"s_waitcnt lgkmcnt(2)\n\t"
"buffer_load_dwordx4 v10, s[16:19], s47 offen offset:1024 lds\n\t"
"v_mfma_f32_16x16x32_bf16 v[96:99], a[192:195], a[160:163], v[96:99]\n\t"
"ds_read_b64_tr_b16 a[208:209], v7 offset:16384\n\t"
"v_cvt_pk_bf16_f32 v222, v84, v85\n\t"
"v_mfma_f32_16x16x32_bf16 v[112:115], a[196:199], a[160:163], v[112:115]\n\t"
"ds_read_b64_tr_b16 a[210:211], v7 offset:18432\n\t"
"v_cvt_pk_bf16_f32 v223, v86, v87\n\t"
"v_mfma_f32_16x16x32_bf16 v[116:119], a[196:199], a[164:167], v[116:119]\n\t"
"v_exp_f32 v72, v72\n\t"
"v_mfma_f32_16x16x32_bf16 v[100:103], a[192:195], a[164:167], v[100:103]\n\t"
"v_exp_f32 v73, v73\n\t"
"v_mfma_f32_16x16x32_bf16 v[104:107], a[192:195], a[168:171], v[104:107]\n\t"
"ds_read_b64_tr_b16 a[212:213], v13 offset:16384\n\t"
"v_cvt_pk_bf16_f32 v72, v72, v73\n\t"
"v_mfma_f32_16x16x32_bf16 v[120:123], a[196:199], a[168:171], v[120:123]\n\t"
"ds_read_b64_tr_b16 a[214:215], v13 offset:18432\n\t"
"v_accvgpr_write_b32 a40, v72\n\t"
"v_mfma_f32_16x16x32_bf16 v[124:127], a[196:199], a[172:175], v[124:127]\n\t"
"v_exp_f32 v74, v74\n\t"
"v_mfma_f32_16x16x32_bf16 v[108:111], a[192:195], a[172:175], v[108:111]\n\t"
"s_waitcnt lgkmcnt(4)\n\t"
"v_exp_f32 v75, v75\n\t"
"v_mfma_f32_16x16x32_bf16 v[96:99], a[200:203], a[176:179], v[96:99]\n\t"
"ds_read_b128 a[192:195], v6 offset:0\n\t"
"v_cvt_pk_bf16_f32 v74, v74, v75\n\t"
"v_mfma_f32_16x16x32_bf16 v[112:115], a[204:207], a[176:179], v[112:115]\n\t"
"buffer_load_dwordx4 v11, s[16:19], s47 offen offset:2048 lds\n\t"
"v_mfma_f32_16x16x32_bf16 v[116:119], a[204:207], a[180:183], v[116:119]\n\t"
"v_exp_f32 v88, v88\n\t"
"v_mfma_f32_16x16x32_bf16 v[100:103], a[200:203], a[180:183], v[100:103]\n\t"
"v_exp_f32 v89, v89\n\t"
"v_mfma_f32_16x16x32_bf16 v[104:107], a[200:203], a[184:187], v[104:107]\n\t"
"ds_read_b128 a[196:199], v6 offset:6144\n\t"
"v_accvgpr_write_b32 a41, v74\n\t"
"v_cvt_pk_bf16_f32 v88, v88, v89\n\t"
"v_mfma_f32_16x16x32_bf16 v[120:123], a[204:207], a[184:187], v[120:123]\n\t"
"v_exp_f32 v90, v90\n\t"
"v_mfma_f32_16x16x32_bf16 v[124:127], a[204:207], a[188:191], v[124:127]\n\t"
"v_exp_f32 v91, v91\n\t"
"v_mfma_f32_16x16x32_bf16 v[108:111], a[200:203], a[188:191], v[108:111]\n\t"
"v_exp_f32 v76, v76\n\t"
"v_smfmac_f32_16x16x64_bf16 a[224:227], a[244:247], v[208:215], v30\n\t"
"v_accvgpr_write_b32 a42, v88\n\t"
"v_smfmac_f32_16x16x64_bf16 a[228:231], a[244:247], a[32:39], v30\n\t"
"s_waitcnt lgkmcnt(2)\n\t"
"v_cvt_pk_bf16_f32 v90, v90, v91\n\t"
"v_mfma_f32_16x16x32_bf16 a[216:219], a[208:211], v[192:195], a[216:219]\n\t"
"ds_read_b128 a[200:203], v12 offset:0\n\t"
"v_accvgpr_write_b32 a43, v90\n\t"
"v_mfma_f32_16x16x32_bf16 v[48:51], a[212:215], v[192:195], v[48:51]\n\t"
"buffer_load_dword a252, v20, s[24:27], s80 offen offset:0\n\t"
"v_mfma_f32_16x16x32_bf16 v[52:55], a[212:215], v[196:199], v[52:55]\n\t"
"s_add_u32 s80, s80, 81920\n\t"
"v_exp_f32 v77, v77\n\t"
"v_mfma_f32_16x16x32_bf16 a[220:223], a[208:211], v[196:199], a[220:223]\n\t"
"v_exp_f32 v78, v78\n\t"
"v_mfma_f32_16x16x32_bf16 a[232:235], a[208:211], a[16:19], a[232:235]\n\t"
"ds_read_b128 a[204:207], v12 offset:6144\n\t"
"v_cvt_pk_bf16_f32 v76, v76, v77\n\t"
"v_mfma_f32_16x16x32_bf16 v[56:59], a[212:215], a[16:19], v[56:59]\n\t"
"v_exp_f32 v79, v79\n\t"
"v_mfma_f32_16x16x32_bf16 v[60:63], a[212:215], a[20:23], v[60:63]\n\t"
"v_exp_f32 v92, v92\n\t"
"v_mfma_f32_16x16x32_bf16 a[248:251], a[208:211], a[20:23], a[248:251]\n\t"
"s_waitcnt lgkmcnt(2)\n\t"
"v_exp_f32 v93, v93\n\t"
"v_accvgpr_write_b32 a44, v76\n\t"
"v_mfma_f32_16x16x32_bf16 v[64:67], a[192:195], a[96:99], v[32:35]\n\t"
"ds_read_b128 a[208:211], v6 offset:1024\n\t"
"v_cvt_pk_bf16_f32 v78, v78, v79\n\t"
"v_mfma_f32_16x16x32_bf16 v[80:83], a[196:199], a[96:99], v[32:35]\n\t"
"v_exp_f32 v94, v94\n\t"
"v_mfma_f32_16x16x32_bf16 v[84:87], a[196:199], a[100:103], v[36:39]\n\t"
"v_exp_f32 v95, v95\n\t"
"v_mfma_f32_16x16x32_bf16 v[68:71], a[192:195], a[100:103], v[36:39]\n\t"
"v_accvgpr_write_b32 a45, v78\n\t"
"v_cvt_pk_bf16_f32 v92, v92, v93\n\t"
"v_mfma_f32_16x16x32_bf16 v[72:75], a[192:195], a[104:107], v[40:43]\n\t"
"ds_read_b128 a[212:215], v6 offset:7168\n\t"
"v_accvgpr_write_b32 a46, v92\n\t"
"v_mfma_f32_16x16x32_bf16 v[88:91], a[196:199], a[104:107], v[40:43]\n\t"
"v_cvt_pk_bf16_f32 v94, v94, v95\n\t"
"v_accvgpr_write_b32 a47, v94\n\t"
"v_mfma_f32_16x16x32_bf16 v[92:95], a[196:199], a[108:111], v[44:47]\n\t"
"s_waitcnt vmcnt(10)\n\t"
"s_barrier\n\t"
"v_mfma_f32_16x16x32_bf16 v[76:79], a[192:195], a[108:111], v[44:47]\n\t"
"s_waitcnt lgkmcnt(2)\n\t"
"v_exp_f32 v96, v96\n\t"
"v_mfma_f32_16x16x32_bf16 v[64:67], a[200:203], a[112:115], v[64:67]\n\t"
"ds_read_b128 a[192:195], v12 offset:1024\n\t"
"v_mfma_f32_16x16x32_bf16 v[80:83], a[204:207], a[112:115], v[80:83]\n\t"
"v_exp_f32 v97, v97\n\t"
"v_mfma_f32_16x16x32_bf16 v[84:87], a[204:207], a[116:119], v[84:87]\n\t"
"v_exp_f32 v98, v98\n\t"
"v_mfma_f32_16x16x32_bf16 v[68:71], a[200:203], a[116:119], v[68:71]\n\t"
"s_add_u32 m0, s44, 0\n\t"
"v_exp_f32 v99, v99\n\t"
".Lpg8%=:\n"
"s_cbranch_execz .Lpg12%=\n\t"
"v_mfma_f32_16x16x32_bf16 v[72:75], a[200:203], a[120:123], v[72:75]\n\t"
"s_addk_i32 s48, 12288\n\t"
"ds_read_b128 a[196:199], v12 offset:7168\n\t"
"v_cvt_pk_bf16_f32 v224, v96, v97\n\t"
"v_mfma_f32_16x16x32_bf16 v[88:91], a[204:207], a[120:123], v[88:91]\n\t"
"buffer_load_dwordx4 v4, s[20:23], s48 offen offset:0 lds\n\t"
"v_mfma_f32_16x16x32_bf16 v[92:95], a[204:207], a[124:127], v[92:95]\n\t"
"v_exp_f32 v112, v112\n\t"
"v_mfma_f32_16x16x32_bf16 v[76:79], a[200:203], a[124:127], v[76:79]\n\t"
"s_waitcnt lgkmcnt(2)\n\t"
"v_exp_f32 v113, v113\n\t"
"v_mfma_f32_16x16x32_bf16 v[64:67], a[208:211], a[128:131], v[64:67]\n\t"
"ds_read_b128 a[200:203], v6 offset:2048\n\t"
"v_cvt_pk_bf16_f32 v225, v98, v99\n\t"
"v_mfma_f32_16x16x32_bf16 v[80:83], a[212:215], a[128:131], v[80:83]\n\t"
"v_exp_f32 v114, v114\n\t"
"v_mfma_f32_16x16x32_bf16 v[84:87], a[212:215], a[132:135], v[84:87]\n\t"
"v_exp_f32 v115, v115\n\t"
"v_mfma_f32_16x16x32_bf16 v[68:71], a[208:211], a[132:135], v[68:71]\n\t"
"v_exp_f32 v100, v100\n\t"
"v_mfma_f32_16x16x32_bf16 v[72:75], a[208:211], a[136:139], v[72:75]\n\t"
"ds_read_b128 a[204:207], v6 offset:8192\n\t"
"v_cvt_pk_bf16_f32 v226, v112, v113\n\t"
"v_mfma_f32_16x16x32_bf16 v[88:91], a[212:215], a[136:139], v[88:91]\n\t"
"v_exp_f32 v101, v101\n\t"
"v_mfma_f32_16x16x32_bf16 v[92:95], a[212:215], a[140:143], v[92:95]\n\t"
"s_add_u32 m0, s43, 49152\n\t"
"v_exp_f32 v102, v102\n\t"
"v_mfma_f32_16x16x32_bf16 v[76:79], a[208:211], a[140:143], v[76:79]\n\t"
"s_waitcnt lgkmcnt(2)\n\t"
"v_exp_f32 v103, v103\n\t"
"v_mfma_f32_16x16x32_bf16 v[64:67], a[192:195], a[144:147], v[64:67]\n\t"
"s_addk_i32 s47, 12288\n\t"
"ds_read_b128 a[208:211], v12 offset:2048\n\t"
"v_cvt_pk_bf16_f32 v227, v114, v115\n\t"
"v_mfma_f32_16x16x32_bf16 v[80:83], a[196:199], a[144:147], v[80:83]\n\t"
"buffer_load_dwordx4 v3, s[16:19], s47 offen offset:0 lds\n\t"
"v_mfma_f32_16x16x32_bf16 v[84:87], a[196:199], a[148:151], v[84:87]\n\t"
"v_exp_f32 v116, v116\n\t"
"v_mfma_f32_16x16x32_bf16 v[68:71], a[192:195], a[148:151], v[68:71]\n\t"
"v_exp_f32 v117, v117\n\t"
"v_mfma_f32_16x16x32_bf16 v[72:75], a[192:195], a[152:155], v[72:75]\n\t"
"ds_read_b128 a[212:215], v12 offset:8192\n\t"
"v_cvt_pk_bf16_f32 v228, v100, v101\n\t"
"v_mfma_f32_16x16x32_bf16 v[88:91], a[196:199], a[152:155], v[88:91]\n\t"
"v_exp_f32 v118, v118\n\t"
"v_mfma_f32_16x16x32_bf16 v[92:95], a[196:199], a[156:159], v[92:95]\n\t"
"v_exp_f32 v119, v119\n\t"
"v_mfma_f32_16x16x32_bf16 v[76:79], a[192:195], a[156:159], v[76:79]\n\t"
"s_waitcnt lgkmcnt(2)\n\t"
"v_exp_f32 v104, v104\n\t"
"v_mfma_f32_16x16x32_bf16 v[64:67], a[200:203], a[160:163], v[64:67]\n\t"
"ds_read_b64_tr_b16 a[192:193], v7 offset:12288\n\t"
"v_cvt_pk_bf16_f32 v229, v102, v103\n\t"
"v_mfma_f32_16x16x32_bf16 v[80:83], a[204:207], a[160:163], v[80:83]\n\t"
"ds_read_b64_tr_b16 a[194:195], v7 offset:14336\n\t"
"v_cvt_pk_bf16_f32 v230, v116, v117\n\t"
"v_mfma_f32_16x16x32_bf16 v[84:87], a[204:207], a[164:167], v[84:87]\n\t"
"v_exp_f32 v105, v105\n\t"
"v_mfma_f32_16x16x32_bf16 v[68:71], a[200:203], a[164:167], v[68:71]\n\t"
"v_exp_f32 v106, v106\n\t"
"v_mfma_f32_16x16x32_bf16 v[72:75], a[200:203], a[168:171], v[72:75]\n\t"
"ds_read_b64_tr_b16 a[196:197], v13 offset:12288\n\t"
"v_cvt_pk_bf16_f32 v231, v118, v119\n\t"
"v_mfma_f32_16x16x32_bf16 v[88:91], a[204:207], a[168:171], v[88:91]\n\t"
"ds_read_b64_tr_b16 a[198:199], v13 offset:14336\n\t"
"v_cvt_pk_bf16_f32 v104, v104, v105\n\t"
"v_mfma_f32_16x16x32_bf16 v[92:95], a[204:207], a[172:175], v[92:95]\n\t"
"buffer_load_dwordx4 v10, s[16:19], s47 offen offset:1024 lds\n\t"
"v_mfma_f32_16x16x32_bf16 v[76:79], a[200:203], a[172:175], v[76:79]\n\t"
"s_waitcnt lgkmcnt(4)\n\t"
"v_exp_f32 v107, v107\n\t"
"v_mfma_f32_16x16x32_bf16 v[64:67], a[208:211], a[176:179], v[64:67]\n\t"
"ds_read_b128 a[200:203], v6 offset:12288\n\t"
"v_accvgpr_write_b32 a48, v104\n\t"
"v_mfma_f32_16x16x32_bf16 v[80:83], a[212:215], a[176:179], v[80:83]\n\t"
"v_exp_f32 v120, v120\n\t"
"v_mfma_f32_16x16x32_bf16 v[84:87], a[212:215], a[180:183], v[84:87]\n\t"
"v_exp_f32 v121, v121\n\t"
"v_mfma_f32_16x16x32_bf16 v[68:71], a[208:211], a[180:183], v[68:71]\n\t"
"v_cvt_pk_bf16_f32 v106, v106, v107\n\t"
"v_accvgpr_write_b32 a49, v106\n\t"
"v_mfma_f32_16x16x32_bf16 v[72:75], a[208:211], a[184:187], v[72:75]\n\t"
"ds_read_b128 a[204:207], v6 offset:18432\n\t"
"v_cvt_pk_bf16_f32 v120, v120, v121\n\t"
"v_mfma_f32_16x16x32_bf16 v[88:91], a[212:215], a[184:187], v[88:91]\n\t"
"v_exp_f32 v122, v122\n\t"
"v_mfma_f32_16x16x32_bf16 v[92:95], a[212:215], a[188:191], v[92:95]\n\t"
"v_exp_f32 v123, v123\n\t"
"v_mfma_f32_16x16x32_bf16 v[76:79], a[208:211], a[188:191], v[76:79]\n\t"
"v_exp_f32 v108, v108\n\t"
"v_smfmac_f32_16x16x64_bf16 a[224:227], a[244:247], v[216:223], v30\n\t"
"v_accvgpr_write_b32 a50, v120\n\t"
"v_smfmac_f32_16x16x64_bf16 a[228:231], a[244:247], a[40:47], v30\n\t"
"s_waitcnt lgkmcnt(2)\n\t"
"v_cvt_pk_bf16_f32 v122, v122, v123\n\t"
"v_mfma_f32_16x16x32_bf16 a[216:219], a[192:195], v[200:203], a[216:219]\n\t"
"ds_read_b128 a[208:211], v12 offset:12288\n\t"
"v_accvgpr_write_b32 a51, v122\n\t"
"v_mfma_f32_16x16x32_bf16 v[48:51], a[196:199], v[200:203], v[48:51]\n\t"
"buffer_load_dwordx4 v11, s[16:19], s47 offen offset:2048 lds\n\t"
"v_mfma_f32_16x16x32_bf16 v[52:55], a[196:199], v[204:207], v[52:55]\n\t"
"v_exp_f32 v109, v109\n\t"
"v_mfma_f32_16x16x32_bf16 a[220:223], a[192:195], v[204:207], a[220:223]\n\t"
"v_exp_f32 v110, v110\n\t"
"v_mfma_f32_16x16x32_bf16 a[232:235], a[192:195], a[24:27], a[232:235]\n\t"
"ds_read_b128 a[212:215], v12 offset:18432\n\t"
"v_cvt_pk_bf16_f32 v108, v108, v109\n\t"
"v_mfma_f32_16x16x32_bf16 v[56:59], a[196:199], a[24:27], v[56:59]\n\t"
"v_exp_f32 v111, v111\n\t"
"v_mfma_f32_16x16x32_bf16 v[60:63], a[196:199], a[28:31], v[60:63]\n\t"
"v_exp_f32 v124, v124\n\t"
"v_mfma_f32_16x16x32_bf16 a[248:251], a[192:195], a[28:31], a[248:251]\n\t"
"s_waitcnt lgkmcnt(2)\n\t"
"v_exp_f32 v125, v125\n\t"
"v_accvgpr_write_b32 a52, v108\n\t"
"v_mfma_f32_16x16x32_bf16 v[96:99], a[200:203], a[96:99], v[32:35]\n\t"
"ds_read_b128 a[192:195], v6 offset:13312\n\t"
"v_cvt_pk_bf16_f32 v110, v110, v111\n\t"
"v_mfma_f32_16x16x32_bf16 v[112:115], a[204:207], a[96:99], v[32:35]\n\t"
"v_exp_f32 v126, v126\n\t"
"v_mfma_f32_16x16x32_bf16 v[116:119], a[204:207], a[100:103], v[36:39]\n\t"
"v_exp_f32 v127, v127\n\t"
"v_mfma_f32_16x16x32_bf16 v[100:103], a[200:203], a[100:103], v[36:39]\n\t"
"v_accvgpr_write_b32 a53, v110\n\t"
"v_cvt_pk_bf16_f32 v124, v124, v125\n\t"
"v_mfma_f32_16x16x32_bf16 v[104:107], a[200:203], a[104:107], v[40:43]\n\t"
"ds_read_b128 a[196:199], v6 offset:19456\n\t"
"v_accvgpr_write_b32 a54, v124\n\t"
"v_mfma_f32_16x16x32_bf16 v[120:123], a[204:207], a[104:107], v[40:43]\n\t"
"v_cvt_pk_bf16_f32 v126, v126, v127\n\t"
"v_accvgpr_write_b32 a55, v126\n\t"
"v_mfma_f32_16x16x32_bf16 v[124:127], a[204:207], a[108:111], v[44:47]\n\t"
"s_waitcnt vmcnt(9)\n\t"
"s_barrier\n\t"
"v_mfma_f32_16x16x32_bf16 v[108:111], a[200:203], a[108:111], v[44:47]\n\t"
"s_waitcnt lgkmcnt(2)\n\t"
"v_exp_f32 v64, v64\n\t"
"v_mfma_f32_16x16x32_bf16 v[96:99], a[208:211], a[112:115], v[96:99]\n\t"
"ds_read_b128 a[200:203], v12 offset:13312\n\t"
"v_mfma_f32_16x16x32_bf16 v[112:115], a[212:215], a[112:115], v[112:115]\n\t"
"s_add_u32 m0, s44, 73728\n\t"
"v_exp_f32 v65, v65\n\t"
"v_mfma_f32_16x16x32_bf16 v[116:119], a[212:215], a[116:119], v[116:119]\n\t"
"s_addk_i32 s48, 12288\n\t"
"v_exp_f32 v66, v66\n\t"
"v_mfma_f32_16x16x32_bf16 v[100:103], a[208:211], a[116:119], v[100:103]\n\t"
"buffer_load_dwordx4 v4, s[20:23], s48 offen offset:0 lds\n\t"
"v_mfma_f32_16x16x32_bf16 v[104:107], a[208:211], a[120:123], v[104:107]\n\t"
"ds_read_b128 a[204:207], v12 offset:19456\n\t"
"v_cvt_pk_bf16_f32 v232, v64, v65\n\t"
"v_mfma_f32_16x16x32_bf16 v[120:123], a[212:215], a[120:123], v[120:123]\n\t"
"v_exp_f32 v67, v67\n\t"
"v_mfma_f32_16x16x32_bf16 v[124:127], a[212:215], a[124:127], v[124:127]\n\t"
"v_exp_f32 v80, v80\n\t"
"v_mfma_f32_16x16x32_bf16 v[108:111], a[208:211], a[124:127], v[108:111]\n\t"
"s_waitcnt lgkmcnt(2)\n\t"
"v_exp_f32 v81, v81\n\t"
"v_mfma_f32_16x16x32_bf16 v[96:99], a[192:195], a[128:131], v[96:99]\n\t"
"ds_read_b128 a[208:211], v6 offset:14336\n\t"
"v_cvt_pk_bf16_f32 v233, v66, v67\n\t"
"v_mfma_f32_16x16x32_bf16 v[112:115], a[196:199], a[128:131], v[112:115]\n\t"
"v_exp_f32 v82, v82\n\t"
"v_mfma_f32_16x16x32_bf16 v[116:119], a[196:199], a[132:135], v[116:119]\n\t"
"v_exp_f32 v83, v83\n\t"
"v_mfma_f32_16x16x32_bf16 v[100:103], a[192:195], a[132:135], v[100:103]\n\t"
"s_add_u32 m0, s43, 0\n\t"
"v_exp_f32 v68, v68\n\t"
"v_cvt_pk_bf16_f32 v234, v80, v81\n\t"
"v_mfma_f32_16x16x32_bf16 v[104:107], a[192:195], a[136:139], v[104:107]\n\t"
"s_addk_i32 s47, 12288\n\t"
"ds_read_b128 a[212:215], v6 offset:20480\n\t"
"v_cvt_pk_bf16_f32 v235, v82, v83\n\t"
"v_mfma_f32_16x16x32_bf16 v[120:123], a[196:199], a[136:139], v[120:123]\n\t"
"buffer_load_dwordx4 v3, s[16:19], s47 offen offset:0 lds\n\t"
"v_mfma_f32_16x16x32_bf16 v[124:127], a[196:199], a[140:143], v[124:127]\n\t"
"v_exp_f32 v69, v69\n\t"
"v_mfma_f32_16x16x32_bf16 v[108:111], a[192:195], a[140:143], v[108:111]\n\t"
"s_waitcnt lgkmcnt(2)\n\t"
"v_exp_f32 v70, v70\n\t"
"v_mfma_f32_16x16x32_bf16 v[96:99], a[200:203], a[144:147], v[96:99]\n\t"
"ds_read_b128 a[192:195], v12 offset:14336\n\t"
"v_cvt_pk_bf16_f32 v236, v68, v69\n\t"
"v_mfma_f32_16x16x32_bf16 v[112:115], a[204:207], a[144:147], v[112:115]\n\t"
"v_exp_f32 v71, v71\n\t"
"v_mfma_f32_16x16x32_bf16 v[116:119], a[204:207], a[148:151], v[116:119]\n\t"
"v_exp_f32 v84, v84\n\t"
"v_mfma_f32_16x16x32_bf16 v[100:103], a[200:203], a[148:151], v[100:103]\n\t"
"v_exp_f32 v85, v85\n\t"
"v_mfma_f32_16x16x32_bf16 v[104:107], a[200:203], a[152:155], v[104:107]\n\t"
"ds_read_b128 a[196:199], v12 offset:20480\n\t"
"v_cvt_pk_bf16_f32 v237, v70, v71\n\t"
"v_mfma_f32_16x16x32_bf16 v[120:123], a[204:207], a[152:155], v[120:123]\n\t"
"v_exp_f32 v86, v86\n\t"
"v_mfma_f32_16x16x32_bf16 v[124:127], a[204:207], a[156:159], v[124:127]\n\t"
"v_exp_f32 v87, v87\n\t"
"v_mfma_f32_16x16x32_bf16 v[108:111], a[200:203], a[156:159], v[108:111]\n\t"
"s_waitcnt lgkmcnt(2)\n\t"
"buffer_load_dwordx4 v10, s[16:19], s47 offen offset:1024 lds\n\t"
"v_mfma_f32_16x16x32_bf16 v[96:99], a[208:211], a[160:163], v[96:99]\n\t"
"ds_read_b64_tr_b16 a[200:201], v7 offset:8192\n\t"
"v_cvt_pk_bf16_f32 v238, v84, v85\n\t"
"v_mfma_f32_16x16x32_bf16 v[112:115], a[212:215], a[160:163], v[112:115]\n\t"
"ds_read_b64_tr_b16 a[202:203], v7 offset:10240\n\t"
"v_cvt_pk_bf16_f32 v239, v86, v87\n\t"
"v_mfma_f32_16x16x32_bf16 v[116:119], a[212:215], a[164:167], v[116:119]\n\t"
"v_exp_f32 v72, v72\n\t"
"v_mfma_f32_16x16x32_bf16 v[100:103], a[208:211], a[164:167], v[100:103]\n\t"
"v_exp_f32 v73, v73\n\t"
"v_mfma_f32_16x16x32_bf16 v[104:107], a[208:211], a[168:171], v[104:107]\n\t"
"ds_read_b64_tr_b16 a[204:205], v13 offset:8192\n\t"
"v_cvt_pk_bf16_f32 v72, v72, v73\n\t"
"v_mfma_f32_16x16x32_bf16 v[120:123], a[212:215], a[168:171], v[120:123]\n\t"
"ds_read_b64_tr_b16 a[206:207], v13 offset:10240\n\t"
"v_accvgpr_write_b32 a56, v72\n\t"
"v_mfma_f32_16x16x32_bf16 v[124:127], a[212:215], a[172:175], v[124:127]\n\t"
"v_exp_f32 v74, v74\n\t"
"v_mfma_f32_16x16x32_bf16 v[108:111], a[208:211], a[172:175], v[108:111]\n\t"
"s_waitcnt lgkmcnt(4)\n\t"
"v_exp_f32 v75, v75\n\t"
"v_mfma_f32_16x16x32_bf16 v[96:99], a[192:195], a[176:179], v[96:99]\n\t"
"ds_read_b128 a[208:211], v6 offset:24576\n\t"
"v_cvt_pk_bf16_f32 v74, v74, v75\n\t"
"v_mfma_f32_16x16x32_bf16 v[112:115], a[196:199], a[176:179], v[112:115]\n\t"
"buffer_load_dwordx4 v11, s[16:19], s47 offen offset:2048 lds\n\t"
"v_mfma_f32_16x16x32_bf16 v[116:119], a[196:199], a[180:183], v[116:119]\n\t"
"v_exp_f32 v88, v88\n\t"
"v_mfma_f32_16x16x32_bf16 v[100:103], a[192:195], a[180:183], v[100:103]\n\t"
"v_exp_f32 v89, v89\n\t"
"v_mfma_f32_16x16x32_bf16 v[104:107], a[192:195], a[184:187], v[104:107]\n\t"
"ds_read_b128 a[212:215], v6 offset:30720\n\t"
"v_accvgpr_write_b32 a57, v74\n\t"
"v_cvt_pk_bf16_f32 v88, v88, v89\n\t"
"v_mfma_f32_16x16x32_bf16 v[120:123], a[196:199], a[184:187], v[120:123]\n\t"
"v_exp_f32 v90, v90\n\t"
"v_mfma_f32_16x16x32_bf16 v[124:127], a[196:199], a[188:191], v[124:127]\n\t"
"v_exp_f32 v91, v91\n\t"
"v_mfma_f32_16x16x32_bf16 v[108:111], a[192:195], a[188:191], v[108:111]\n\t"
"v_exp_f32 v76, v76\n\t"
"v_smfmac_f32_16x16x64_bf16 a[224:227], a[244:247], v[224:231], v30\n\t"
"v_accvgpr_write_b32 a58, v88\n\t"
"v_smfmac_f32_16x16x64_bf16 a[228:231], a[244:247], a[48:55], v30\n\t"
"s_waitcnt lgkmcnt(2)\n\t"
"v_cvt_pk_bf16_f32 v90, v90, v91\n\t"
"v_mfma_f32_16x16x32_bf16 a[216:219], a[200:203], v[208:211], a[216:219]\n\t"
"ds_read_b128 a[192:195], v12 offset:24576\n\t"
"v_accvgpr_write_b32 a59, v90\n\t"
"v_mfma_f32_16x16x32_bf16 v[48:51], a[204:207], v[208:211], v[48:51]\n\t"
"buffer_load_dword a252, v20, s[24:27], s80 offen offset:0\n\t"
"v_mfma_f32_16x16x32_bf16 v[52:55], a[204:207], v[212:215], v[52:55]\n\t"
"s_add_u32 s80, s80, 81920\n\t"
"v_exp_f32 v77, v77\n\t"
"v_mfma_f32_16x16x32_bf16 a[220:223], a[200:203], v[212:215], a[220:223]\n\t"
"v_exp_f32 v78, v78\n\t"
"v_mfma_f32_16x16x32_bf16 a[232:235], a[200:203], a[32:35], a[232:235]\n\t"
"ds_read_b128 a[196:199], v12 offset:30720\n\t"
"v_cvt_pk_bf16_f32 v76, v76, v77\n\t"
"v_mfma_f32_16x16x32_bf16 v[56:59], a[204:207], a[32:35], v[56:59]\n\t"
"v_exp_f32 v79, v79\n\t"
"v_mfma_f32_16x16x32_bf16 v[60:63], a[204:207], a[36:39], v[60:63]\n\t"
"v_exp_f32 v92, v92\n\t"
"v_mfma_f32_16x16x32_bf16 a[248:251], a[200:203], a[36:39], a[248:251]\n\t"
"s_waitcnt lgkmcnt(2)\n\t"
"v_exp_f32 v93, v93\n\t"
"v_accvgpr_write_b32 a60, v76\n\t"
"v_mfma_f32_16x16x32_bf16 v[64:67], a[208:211], a[96:99], v[32:35]\n\t"
"ds_read_b128 a[200:203], v6 offset:25600\n\t"
"v_cvt_pk_bf16_f32 v78, v78, v79\n\t"
"v_mfma_f32_16x16x32_bf16 v[80:83], a[212:215], a[96:99], v[32:35]\n\t"
"v_exp_f32 v94, v94\n\t"
"v_mfma_f32_16x16x32_bf16 v[84:87], a[212:215], a[100:103], v[36:39]\n\t"
"v_exp_f32 v95, v95\n\t"
"v_mfma_f32_16x16x32_bf16 v[68:71], a[208:211], a[100:103], v[36:39]\n\t"
"v_accvgpr_write_b32 a61, v78\n\t"
"v_cvt_pk_bf16_f32 v92, v92, v93\n\t"
"v_mfma_f32_16x16x32_bf16 v[72:75], a[208:211], a[104:107], v[40:43]\n\t"
"ds_read_b128 a[204:207], v6 offset:31744\n\t"
"v_accvgpr_write_b32 a62, v92\n\t"
"v_mfma_f32_16x16x32_bf16 v[88:91], a[212:215], a[104:107], v[40:43]\n\t"
"v_cvt_pk_bf16_f32 v94, v94, v95\n\t"
"v_accvgpr_write_b32 a63, v94\n\t"
"v_mfma_f32_16x16x32_bf16 v[92:95], a[212:215], a[108:111], v[44:47]\n\t"
"s_waitcnt vmcnt(10)\n\t"
"s_barrier\n\t"
"v_mfma_f32_16x16x32_bf16 v[76:79], a[208:211], a[108:111], v[44:47]\n\t"
"s_waitcnt lgkmcnt(2)\n\t"
"v_exp_f32 v96, v96\n\t"
"v_mfma_f32_16x16x32_bf16 v[64:67], a[192:195], a[112:115], v[64:67]\n\t"
"ds_read_b128 a[208:211], v12 offset:25600\n\t"
"v_mfma_f32_16x16x32_bf16 v[80:83], a[196:199], a[112:115], v[80:83]\n\t"
"s_add_u32 m0, s44, 69632\n\t"
"v_exp_f32 v97, v97\n\t"
"v_mfma_f32_16x16x32_bf16 v[84:87], a[196:199], a[116:119], v[84:87]\n\t"
"s_addk_i32 s48, 12288\n\t"
"v_exp_f32 v98, v98\n\t"
"v_mfma_f32_16x16x32_bf16 v[68:71], a[192:195], a[116:119], v[68:71]\n\t"
"buffer_load_dwordx4 v4, s[20:23], s48 offen offset:0 lds\n\t"
"v_mfma_f32_16x16x32_bf16 v[72:75], a[192:195], a[120:123], v[72:75]\n\t"
"ds_read_b128 a[212:215], v12 offset:31744\n\t"
"v_cvt_pk_bf16_f32 v240, v96, v97\n\t"
"v_mfma_f32_16x16x32_bf16 v[88:91], a[196:199], a[120:123], v[88:91]\n\t"
"v_exp_f32 v99, v99\n\t"
"v_mfma_f32_16x16x32_bf16 v[92:95], a[196:199], a[124:127], v[92:95]\n\t"
"v_exp_f32 v112, v112\n\t"
"v_mfma_f32_16x16x32_bf16 v[76:79], a[192:195], a[124:127], v[76:79]\n\t"
"s_waitcnt lgkmcnt(2)\n\t"
"v_exp_f32 v113, v113\n\t"
"v_mfma_f32_16x16x32_bf16 v[64:67], a[200:203], a[128:131], v[64:67]\n\t"
"ds_read_b128 a[192:195], v6 offset:26624\n\t"
"v_cvt_pk_bf16_f32 v241, v98, v99\n\t"
"v_mfma_f32_16x16x32_bf16 v[80:83], a[204:207], a[128:131], v[80:83]\n\t"
"v_exp_f32 v114, v114\n\t"
"v_mfma_f32_16x16x32_bf16 v[84:87], a[204:207], a[132:135], v[84:87]\n\t"
"v_exp_f32 v115, v115\n\t"
"v_mfma_f32_16x16x32_bf16 v[68:71], a[200:203], a[132:135], v[68:71]\n\t"
"s_addk_i32 m0, -4096\n\t"
"v_exp_f32 v100, v100\n\t"
"v_cvt_pk_bf16_f32 v242, v112, v113\n\t"
"v_mfma_f32_16x16x32_bf16 v[72:75], a[200:203], a[136:139], v[72:75]\n\t"
"s_addk_i32 s48, 12288\n\t"
"ds_read_b128 a[196:199], v6 offset:32768\n\t"
"v_cvt_pk_bf16_f32 v243, v114, v115\n\t"
"v_mfma_f32_16x16x32_bf16 v[88:91], a[204:207], a[136:139], v[88:91]\n\t"
"buffer_load_dwordx4 v4, s[20:23], s48 offen offset:0 lds\n\t"
"v_mfma_f32_16x16x32_bf16 v[92:95], a[204:207], a[140:143], v[92:95]\n\t"
"v_exp_f32 v101, v101\n\t"
"v_mfma_f32_16x16x32_bf16 v[76:79], a[200:203], a[140:143], v[76:79]\n\t"
"s_waitcnt lgkmcnt(2)\n\t"
"v_exp_f32 v102, v102\n\t"
"v_mfma_f32_16x16x32_bf16 v[64:67], a[208:211], a[144:147], v[64:67]\n\t"
"ds_read_b128 a[200:203], v12 offset:26624\n\t"
"v_cvt_pk_bf16_f32 v244, v100, v101\n\t"
"v_mfma_f32_16x16x32_bf16 v[80:83], a[212:215], a[144:147], v[80:83]\n\t"
"v_exp_f32 v103, v103\n\t"
"v_mfma_f32_16x16x32_bf16 v[84:87], a[212:215], a[148:151], v[84:87]\n\t"
"v_exp_f32 v116, v116\n\t"
"v_mfma_f32_16x16x32_bf16 v[68:71], a[208:211], a[148:151], v[68:71]\n\t"
"v_exp_f32 v117, v117\n\t"
"v_mfma_f32_16x16x32_bf16 v[72:75], a[208:211], a[152:155], v[72:75]\n\t"
"ds_read_b128 a[204:207], v12 offset:32768\n\t"
"v_cvt_pk_bf16_f32 v245, v102, v103\n\t"
"v_mfma_f32_16x16x32_bf16 v[88:91], a[212:215], a[152:155], v[88:91]\n\t"
"s_addk_i32 m0, -4096\n\t"
"v_exp_f32 v118, v118\n\t"
"v_mfma_f32_16x16x32_bf16 v[92:95], a[212:215], a[156:159], v[92:95]\n\t"
"s_addk_i32 s48, 12288\n\t"
"v_exp_f32 v119, v119\n\t"
"v_mfma_f32_16x16x32_bf16 v[76:79], a[208:211], a[156:159], v[76:79]\n\t"
"s_waitcnt lgkmcnt(2)\n\t"
"buffer_load_dwordx4 v4, s[20:23], s48 offen offset:0 lds\n\t"
"v_mfma_f32_16x16x32_bf16 v[64:67], a[192:195], a[160:163], v[64:67]\n\t"
"ds_read_b64_tr_b16 a[208:209], v7 offset:4096\n\t"
"v_cvt_pk_bf16_f32 v246, v116, v117\n\t"
"v_mfma_f32_16x16x32_bf16 v[80:83], a[196:199], a[160:163], v[80:83]\n\t"
"ds_read_b64_tr_b16 a[210:211], v7 offset:6144\n\t"
"v_cvt_pk_bf16_f32 v247, v118, v119\n\t"
"v_mfma_f32_16x16x32_bf16 v[84:87], a[196:199], a[164:167], v[84:87]\n\t"
"v_exp_f32 v104, v104\n\t"
"v_mfma_f32_16x16x32_bf16 v[68:71], a[192:195], a[164:167], v[68:71]\n\t"
"v_exp_f32 v105, v105\n\t"
"v_mfma_f32_16x16x32_bf16 v[72:75], a[192:195], a[168:171], v[72:75]\n\t"
"ds_read_b64_tr_b16 a[212:213], v13 offset:4096\n\t"
"v_cvt_pk_bf16_f32 v104, v104, v105\n\t"
"v_mfma_f32_16x16x32_bf16 v[88:91], a[196:199], a[168:171], v[88:91]\n\t"
"ds_read_b64_tr_b16 a[214:215], v13 offset:6144\n\t"
"v_accvgpr_write_b32 a64, v104\n\t"
"v_mfma_f32_16x16x32_bf16 v[92:95], a[196:199], a[172:175], v[92:95]\n\t"
"s_addk_i32 m0, -4096\n\t"
"v_exp_f32 v106, v106\n\t"
"v_mfma_f32_16x16x32_bf16 v[76:79], a[192:195], a[172:175], v[76:79]\n\t"
"s_waitcnt lgkmcnt(4)\n\t"
"v_exp_f32 v107, v107\n\t"
"v_mfma_f32_16x16x32_bf16 v[64:67], a[200:203], a[176:179], v[64:67]\n\t"
"s_addk_i32 s48, 12288\n\t"
"ds_read_b128 a[192:195], v6 offset:36864\n\t"
"v_cvt_pk_bf16_f32 v106, v106, v107\n\t"
"v_mfma_f32_16x16x32_bf16 v[80:83], a[204:207], a[176:179], v[80:83]\n\t"
"buffer_load_dwordx4 v4, s[20:23], s48 offen offset:0 lds\n\t"
"v_mfma_f32_16x16x32_bf16 v[84:87], a[204:207], a[180:183], v[84:87]\n\t"
"v_exp_f32 v120, v120\n\t"
"v_mfma_f32_16x16x32_bf16 v[68:71], a[200:203], a[180:183], v[68:71]\n\t"
"v_exp_f32 v121, v121\n\t"
"v_mfma_f32_16x16x32_bf16 v[72:75], a[200:203], a[184:187], v[72:75]\n\t"
"ds_read_b128 a[196:199], v6 offset:43008\n\t"
"v_accvgpr_write_b32 a65, v106\n\t"
"v_cvt_pk_bf16_f32 v120, v120, v121\n\t"
"v_mfma_f32_16x16x32_bf16 v[88:91], a[204:207], a[184:187], v[88:91]\n\t"
"v_exp_f32 v122, v122\n\t"
"v_mfma_f32_16x16x32_bf16 v[92:95], a[204:207], a[188:191], v[92:95]\n\t"
"v_exp_f32 v123, v123\n\t"
"v_mfma_f32_16x16x32_bf16 v[76:79], a[200:203], a[188:191], v[76:79]\n\t"
"v_exp_f32 v108, v108\n\t"
"v_smfmac_f32_16x16x64_bf16 a[224:227], a[244:247], v[232:239], v30\n\t"
"s_addk_i32 m0, -4096\n\t"
"v_accvgpr_write_b32 a66, v120\n\t"
"v_smfmac_f32_16x16x64_bf16 a[228:231], a[244:247], a[56:63], v30\n\t"
"s_waitcnt lgkmcnt(2)\n\t"
"v_cvt_pk_bf16_f32 v122, v122, v123\n\t"
"v_mfma_f32_16x16x32_bf16 a[216:219], a[208:211], v[216:219], a[216:219]\n\t"
"s_addk_i32 s48, 12288\n\t"
"ds_read_b128 a[200:203], v12 offset:36864\n\t"
"v_accvgpr_write_b32 a67, v122\n\t"
"v_mfma_f32_16x16x32_bf16 v[48:51], a[212:215], v[216:219], v[48:51]\n\t"
"buffer_load_dwordx4 v4, s[20:23], s48 offen offset:0 lds\n\t"
"v_mfma_f32_16x16x32_bf16 v[52:55], a[212:215], v[220:223], v[52:55]\n\t"
"v_exp_f32 v109, v109\n\t"
"v_mfma_f32_16x16x32_bf16 a[220:223], a[208:211], v[220:223], a[220:223]\n\t"
"v_exp_f32 v110, v110\n\t"
"v_mfma_f32_16x16x32_bf16 a[232:235], a[208:211], a[40:43], a[232:235]\n\t"
"ds_read_b128 a[204:207], v12 offset:43008\n\t"
"v_cvt_pk_bf16_f32 v108, v108, v109\n\t"
"v_mfma_f32_16x16x32_bf16 v[56:59], a[212:215], a[40:43], v[56:59]\n\t"
"v_exp_f32 v111, v111\n\t"
"v_mfma_f32_16x16x32_bf16 v[60:63], a[212:215], a[44:47], v[60:63]\n\t"
"v_exp_f32 v124, v124\n\t"
"v_mfma_f32_16x16x32_bf16 a[248:251], a[208:211], a[44:47], a[248:251]\n\t"
"s_waitcnt lgkmcnt(2)\n\t"
"v_exp_f32 v125, v125\n\t"
"v_accvgpr_write_b32 a68, v108\n\t"
"v_mfma_f32_16x16x32_bf16 v[96:99], a[192:195], a[96:99], v[32:35]\n\t"
"ds_read_b128 a[208:211], v6 offset:37888\n\t"
"v_cvt_pk_bf16_f32 v110, v110, v111\n\t"
"v_mfma_f32_16x16x32_bf16 v[112:115], a[196:199], a[96:99], v[32:35]\n\t"
"v_exp_f32 v126, v126\n\t"
"v_mfma_f32_16x16x32_bf16 v[116:119], a[196:199], a[100:103], v[36:39]\n\t"
"v_exp_f32 v127, v127\n\t"
"v_mfma_f32_16x16x32_bf16 v[100:103], a[192:195], a[100:103], v[36:39]\n\t"
"v_accvgpr_write_b32 a69, v110\n\t"
"v_cvt_pk_bf16_f32 v124, v124, v125\n\t"
"v_mfma_f32_16x16x32_bf16 v[104:107], a[192:195], a[104:107], v[40:43]\n\t"
"ds_read_b128 a[212:215], v6 offset:44032\n\t"
"v_accvgpr_write_b32 a70, v124\n\t"
"v_mfma_f32_16x16x32_bf16 v[120:123], a[196:199], a[104:107], v[40:43]\n\t"
"v_cvt_pk_bf16_f32 v126, v126, v127\n\t"
"v_accvgpr_write_b32 a71, v126\n\t"
"v_mfma_f32_16x16x32_bf16 v[124:127], a[196:199], a[108:111], v[44:47]\n\t"
"s_waitcnt vmcnt(10)\n\t"
"s_barrier\n\t"
"v_mfma_f32_16x16x32_bf16 v[108:111], a[192:195], a[108:111], v[44:47]\n\t"
"s_waitcnt lgkmcnt(2)\n\t"
"v_exp_f32 v64, v64\n\t"
"v_mfma_f32_16x16x32_bf16 v[96:99], a[200:203], a[112:115], v[96:99]\n\t"
"ds_read_b128 a[192:195], v12 offset:37888\n\t"
"v_mfma_f32_16x16x32_bf16 v[112:115], a[204:207], a[112:115], v[112:115]\n\t"
"v_exp_f32 v65, v65\n\t"
"v_mfma_f32_16x16x32_bf16 v[116:119], a[204:207], a[116:119], v[116:119]\n\t"
"v_exp_f32 v66, v66\n\t"
"v_mfma_f32_16x16x32_bf16 v[100:103], a[200:203], a[116:119], v[100:103]\n\t"
"s_addk_i32 m0, -4096\n\t"
"v_exp_f32 v67, v67\n\t"
"v_mfma_f32_16x16x32_bf16 v[104:107], a[200:203], a[120:123], v[104:107]\n\t"
"s_mov_b32 s48, 128\n\t"
"ds_read_b128 a[196:199], v12 offset:44032\n\t"
"v_cvt_pk_bf16_f32 v248, v64, v65\n\t"
"v_mfma_f32_16x16x32_bf16 v[120:123], a[204:207], a[120:123], v[120:123]\n\t"
"buffer_load_dwordx4 v4, s[20:23], s48 offen offset:0 lds\n\t"
"v_mfma_f32_16x16x32_bf16 v[124:127], a[204:207], a[124:127], v[124:127]\n\t"
"v_exp_f32 v80, v80\n\t"
"v_mfma_f32_16x16x32_bf16 v[108:111], a[200:203], a[124:127], v[108:111]\n\t"
"s_waitcnt lgkmcnt(2)\n\t"
"v_exp_f32 v81, v81\n\t"
"v_mfma_f32_16x16x32_bf16 v[96:99], a[208:211], a[128:131], v[96:99]\n\t"
"ds_read_b128 a[200:203], v6 offset:38912\n\t"
"v_cvt_pk_bf16_f32 v249, v66, v67\n\t"
"v_mfma_f32_16x16x32_bf16 v[112:115], a[212:215], a[128:131], v[112:115]\n\t"
"v_exp_f32 v82, v82\n\t"
"v_mfma_f32_16x16x32_bf16 v[116:119], a[212:215], a[132:135], v[116:119]\n\t"
"v_exp_f32 v83, v83\n\t"
"v_mfma_f32_16x16x32_bf16 v[100:103], a[208:211], a[132:135], v[100:103]\n\t"
"v_exp_f32 v68, v68\n\t"
"v_cvt_pk_bf16_f32 v250, v80, v81\n\t"
"v_mfma_f32_16x16x32_bf16 v[104:107], a[208:211], a[136:139], v[104:107]\n\t"
"ds_read_b128 a[204:207], v6 offset:45056\n\t"
"v_cvt_pk_bf16_f32 v251, v82, v83\n\t"
"v_mfma_f32_16x16x32_bf16 v[120:123], a[212:215], a[136:139], v[120:123]\n\t"
"v_exp_f32 v69, v69\n\t"
"v_mfma_f32_16x16x32_bf16 v[124:127], a[212:215], a[140:143], v[124:127]\n\t"
"s_addk_i32 m0, -4096\n\t"
"v_exp_f32 v70, v70\n\t"
"v_mfma_f32_16x16x32_bf16 v[108:111], a[208:211], a[140:143], v[108:111]\n\t"
"s_waitcnt lgkmcnt(2)\n\t"
"v_exp_f32 v71, v71\n\t"
"v_mfma_f32_16x16x32_bf16 v[96:99], a[192:195], a[144:147], v[96:99]\n\t"
"s_addk_i32 s48, 12288\n\t"
"ds_read_b128 a[208:211], v12 offset:38912\n\t"
"v_cvt_pk_bf16_f32 v252, v68, v69\n\t"
"v_mfma_f32_16x16x32_bf16 v[112:115], a[196:199], a[144:147], v[112:115]\n\t"
"buffer_load_dwordx4 v4, s[20:23], s48 offen offset:0 lds\n\t"
"v_mfma_f32_16x16x32_bf16 v[116:119], a[196:199], a[148:151], v[116:119]\n\t"
"v_exp_f32 v84, v84\n\t"
"v_mfma_f32_16x16x32_bf16 v[100:103], a[192:195], a[148:151], v[100:103]\n\t"
"v_exp_f32 v85, v85\n\t"
"v_mfma_f32_16x16x32_bf16 v[104:107], a[192:195], a[152:155], v[104:107]\n\t"
"ds_read_b128 a[212:215], v12 offset:45056\n\t"
"v_cvt_pk_bf16_f32 v253, v70, v71\n\t"
"v_exp_f32 v86, v86\n\t"
"v_mfma_f32_16x16x32_bf16 v[120:123], a[196:199], a[152:155], v[120:123]\n\t"
"v_exp_f32 v87, v87\n\t"
"v_mfma_f32_16x16x32_bf16 v[124:127], a[196:199], a[156:159], v[124:127]\n\t"
"v_exp_f32 v72, v72\n\t"
"v_mfma_f32_16x16x32_bf16 v[108:111], a[192:195], a[156:159], v[108:111]\n\t"
"s_waitcnt lgkmcnt(2)\n\t"
"v_exp_f32 v73, v73\n\t"
"v_mfma_f32_16x16x32_bf16 v[96:99], a[200:203], a[160:163], v[96:99]\n\t"
"ds_read_b64_tr_b16 a[192:193], v7 offset:0\n\t"
"v_cvt_pk_bf16_f32 v254, v84, v85\n\t"
"v_mfma_f32_16x16x32_bf16 v[112:115], a[204:207], a[160:163], v[112:115]\n\t"
"ds_read_b64_tr_b16 a[194:195], v7 offset:2048\n\t"
"v_cvt_pk_bf16_f32 v255, v86, v87\n\t"
"v_mfma_f32_16x16x32_bf16 v[116:119], a[204:207], a[164:167], v[116:119]\n\t"
"v_exp_f32 v74, v74\n\t"
"v_mfma_f32_16x16x32_bf16 v[100:103], a[200:203], a[164:167], v[100:103]\n\t"
"v_exp_f32 v75, v75\n\t"
"v_mfma_f32_16x16x32_bf16 v[104:107], a[200:203], a[168:171], v[104:107]\n\t"
"s_addk_i32 m0, -4096\n\t"
"ds_read_b64_tr_b16 a[196:197], v13 offset:0\n\t"
"v_cvt_pk_bf16_f32 v72, v72, v73\n\t"
"v_mfma_f32_16x16x32_bf16 v[120:123], a[204:207], a[168:171], v[120:123]\n\t"
"s_addk_i32 s48, 12288\n\t"
"ds_read_b64_tr_b16 a[198:199], v13 offset:2048\n\t"
"v_accvgpr_write_b32 a72, v72\n\t"
"v_mfma_f32_16x16x32_bf16 v[124:127], a[204:207], a[172:175], v[124:127]\n\t"
"buffer_load_dwordx4 v4, s[20:23], s48 offen offset:0 lds\n\t"
"v_mfma_f32_16x16x32_bf16 v[108:111], a[200:203], a[172:175], v[108:111]\n\t"
"s_waitcnt lgkmcnt(4)\n\t"
"v_exp_f32 v88, v88\n\t"
"v_cvt_pk_bf16_f32 v74, v74, v75\n\t"
"v_mfma_f32_16x16x32_bf16 v[96:99], a[208:211], a[176:179], v[96:99]\n\t"
"ds_read_b128 a[200:203], v6 offset:49152\n\t"
"v_mul_u32_u24 v20, 12, v1\n\t"
"v_mfma_f32_16x16x32_bf16 v[112:115], a[212:215], a[176:179], v[112:115]\n\t"
"v_exp_f32 v89, v89\n\t"
"v_mfma_f32_16x16x32_bf16 v[116:119], a[212:215], a[180:183], v[116:119]\n\t"
"v_exp_f32 v90, v90\n\t"
"v_mfma_f32_16x16x32_bf16 v[100:103], a[208:211], a[180:183], v[100:103]\n\t"
"v_exp_f32 v91, v91\n\t"
"v_mfma_f32_16x16x32_bf16 v[104:107], a[208:211], a[184:187], v[104:107]\n\t"
"ds_read_b128 a[204:207], v6 offset:55296\n\t"
"v_cmp_gt_u32 vcc, 512, v1\n\t"
"v_mfma_f32_16x16x32_bf16 v[120:123], a[212:215], a[184:187], v[120:123]\n\t"
"v_accvgpr_write_b32 a73, v74\n\t"
"v_cvt_pk_bf16_f32 v88, v88, v89\n\t"
"v_mfma_f32_16x16x32_bf16 v[124:127], a[212:215], a[188:191], v[124:127]\n\t"
"v_exp_f32 v76, v76\n\t"
"v_mfma_f32_16x16x32_bf16 v[108:111], a[208:211], a[188:191], v[108:111]\n\t"
"v_exp_f32 v77, v77\n\t"
"v_smfmac_f32_16x16x64_bf16 a[224:227], a[244:247], v[240:247], v30\n\t"
"v_accvgpr_write_b32 a74, v88\n\t"
"v_cvt_pk_bf16_f32 v90, v90, v91\n\t"
"v_smfmac_f32_16x16x64_bf16 a[228:231], a[244:247], a[64:71], v30\n\t"
"s_waitcnt lgkmcnt(2)\n\t"
"v_accvgpr_write_b32 a75, v90\n\t"
"v_mfma_f32_16x16x32_bf16 a[216:219], a[192:195], v[224:227], a[216:219]\n\t"
"ds_read_b128 a[208:211], v12 offset:49152\n\t"
"v_mov_b32 v21, 1073741824\n\t"
"v_mfma_f32_16x16x32_bf16 v[48:51], a[196:199], v[224:227], v[48:51]\n\t"
"v_exp_f32 v78, v78\n\t"
"v_mfma_f32_16x16x32_bf16 v[52:55], a[196:199], v[228:231], v[52:55]\n\t"
"v_exp_f32 v79, v79\n\t"
"v_mfma_f32_16x16x32_bf16 a[220:223], a[192:195], v[228:231], a[220:223]\n\t"
"s_mul_i32 s46, s45, 6144\n\t"
"v_cvt_pk_bf16_f32 v76, v76, v77\n\t"
"v_accvgpr_write_b32 a76, v76\n\t"
"v_mfma_f32_16x16x32_bf16 a[232:235], a[192:195], a[48:51], a[232:235]\n\t"
"ds_read_b128 a[212:215], v12 offset:55296\n\t"
"v_cndmask_b32 v20, v21, v20, vcc\n\t"
"v_mfma_f32_16x16x32_bf16 v[56:59], a[196:199], a[48:51], v[56:59]\n\t"
"buffer_load_dword a252, v20, s[20:23], s46 offen offset:128\n\t"
"v_mfma_f32_16x16x32_bf16 v[60:63], a[196:199], a[52:55], v[60:63]\n\t"
"v_exp_f32 v92, v92\n\t"
"v_mfma_f32_16x16x32_bf16 a[248:251], a[192:195], a[52:55], a[248:251]\n\t"
"s_waitcnt lgkmcnt(2)\n\t"
"v_exp_f32 v93, v93\n\t"
"v_cvt_pk_bf16_f32 v78, v78, v79\n\t"
"v_mfma_f32_16x16x32_bf16 v[64:67], a[200:203], a[96:99], v[32:35]\n\t"
"s_mov_b32 s56, 81920\n\t"
"ds_read_b128 a[192:195], v6 offset:50176\n\t"
"v_accvgpr_write_b32 a77, v78\n\t"
"v_mfma_f32_16x16x32_bf16 v[80:83], a[204:207], a[96:99], v[32:35]\n\t"
"v_exp_f32 v94, v94\n\t"
"v_mfma_f32_16x16x32_bf16 v[84:87], a[204:207], a[100:103], v[36:39]\n\t"
"v_exp_f32 v95, v95\n\t"
"v_mfma_f32_16x16x32_bf16 v[68:71], a[200:203], a[100:103], v[36:39]\n\t"
"v_cvt_pk_bf16_f32 v92, v92, v93\n\t"
"v_accvgpr_write_b32 a78, v92\n\t"
"v_mfma_f32_16x16x32_bf16 v[72:75], a[200:203], a[104:107], v[40:43]\n\t"
"ds_read_b128 a[196:199], v6 offset:56320\n\t"
"v_cvt_pk_bf16_f32 v94, v94, v95\n\t"
"v_mfma_f32_16x16x32_bf16 v[88:91], a[204:207], a[104:107], v[40:43]\n\t"
"v_accvgpr_write_b32 a79, v94\n\t"
"v_mfma_f32_16x16x32_bf16 v[92:95], a[204:207], a[108:111], v[44:47]\n\t"
"s_waitcnt vmcnt(10)\n\t"
"s_barrier\n\t"
"v_mfma_f32_16x16x32_bf16 v[76:79], a[200:203], a[108:111], v[44:47]\n\t"
"s_waitcnt lgkmcnt(2)\n\t"
"v_exp_f32 v96, v96\n\t"
"v_mfma_f32_16x16x32_bf16 v[64:67], a[208:211], a[112:115], v[64:67]\n\t"
"ds_read_b128 a[200:203], v12 offset:50176\n\t"
"v_lshrrev_b32 v22, 1, v1\n\t"
"v_mfma_f32_16x16x32_bf16 v[80:83], a[212:215], a[112:115], v[80:83]\n\t"
"v_exp_f32 v97, v97\n\t"
"v_mfma_f32_16x16x32_bf16 v[84:87], a[212:215], a[116:119], v[84:87]\n\t"
"v_exp_f32 v98, v98\n\t"
"v_cvt_pk_bf16_f32 v96, v96, v97\n\t"
"v_mfma_f32_16x16x32_bf16 v[68:71], a[208:211], a[116:119], v[68:71]\n\t"
".Lpg9%=:\n"
"s_cbranch_execz .Lpg13%=\n\t"
"v_exp_f32 v99, v99\n\t"
"v_mfma_f32_16x16x32_bf16 v[72:75], a[208:211], a[120:123], v[72:75]\n\t"
"ds_read_b128 a[204:207], v12 offset:56320\n\t"
"v_add_u32 v22, s87, v22\n\t"
"v_mfma_f32_16x16x32_bf16 v[88:91], a[212:215], a[120:123], v[88:91]\n\t"
"v_exp_f32 v112, v112\n\t"
"v_mfma_f32_16x16x32_bf16 v[92:95], a[212:215], a[124:127], v[92:95]\n\t"
"ds_read_b64_tr_b16 a[236:237], v22 offset:0\n\t"
"v_accvgpr_write_b32 a80, v96\n\t"
"v_mfma_f32_16x16x32_bf16 v[76:79], a[208:211], a[124:127], v[76:79]\n\t"
"s_waitcnt lgkmcnt(3)\n\t"
"v_exp_f32 v113, v113\n\t"
"v_mfma_f32_16x16x32_bf16 v[64:67], a[192:195], a[128:131], v[64:67]\n\t"
"ds_read_b128 a[208:211], v6 offset:51200\n\t"
"v_cvt_pk_bf16_f32 v98, v98, v99\n\t"
"v_mfma_f32_16x16x32_bf16 v[80:83], a[196:199], a[128:131], v[80:83]\n\t"
"ds_read_b64_tr_b16 a[238:239], v22 offset:512\n\t"
"v_accvgpr_write_b32 a81, v98\n\t"
"v_mfma_f32_16x16x32_bf16 v[84:87], a[196:199], a[132:135], v[84:87]\n\t"
"v_exp_f32 v114, v114\n\t"
"v_mfma_f32_16x16x32_bf16 v[68:71], a[192:195], a[132:135], v[68:71]\n\t"
"ds_read_b64_tr_b16 a[240:241], v22 offset:1024\n\t"
"v_cvt_pk_bf16_f32 v112, v112, v113\n\t"
"v_mfma_f32_16x16x32_bf16 v[72:75], a[192:195], a[136:139], v[72:75]\n\t"
"ds_read_b128 a[212:215], v6 offset:57344\n\t"
"v_accvgpr_write_b32 a82, v112\n\t"
"v_mfma_f32_16x16x32_bf16 v[88:91], a[196:199], a[136:139], v[88:91]\n\t"
"ds_read_b64_tr_b16 a[242:243], v22 offset:1536\n\t"
"v_exp_f32 v115, v115\n\t"
"v_mfma_f32_16x16x32_bf16 v[92:95], a[196:199], a[140:143], v[92:95]\n\t"
"v_exp_f32 v100, v100\n\t"
"v_mfma_f32_16x16x32_bf16 v[76:79], a[192:195], a[140:143], v[76:79]\n\t"
"s_waitcnt lgkmcnt(6)\n\t"
"v_exp_f32 v101, v101\n\t"
"v_mfma_f32_16x16x32_bf16 v[64:67], a[200:203], a[144:147], v[64:67]\n\t"
"ds_read_b128 a[192:195], v12 offset:51200\n\t"
"v_cvt_pk_bf16_f32 v114, v114, v115\n\t"
"v_mfma_f32_16x16x32_bf16 v[80:83], a[204:207], a[144:147], v[80:83]\n\t"
"v_exp_f32 v102, v102\n\t"
"v_mfma_f32_16x16x32_bf16 v[84:87], a[204:207], a[148:151], v[84:87]\n\t"
"v_exp_f32 v103, v103\n\t"
"v_mfma_f32_16x16x32_bf16 v[68:71], a[200:203], a[148:151], v[68:71]\n\t"
"v_accvgpr_write_b32 a83, v114\n\t"
"v_cvt_pk_bf16_f32 v100, v100, v101\n\t"
"v_mfma_f32_16x16x32_bf16 v[72:75], a[200:203], a[152:155], v[72:75]\n\t"
"ds_read_b128 a[196:199], v12 offset:57344\n\t"
"v_accvgpr_write_b32 a84, v100\n\t"
"v_cvt_pk_bf16_f32 v102, v102, v103\n\t"
"v_mfma_f32_16x16x32_bf16 v[88:91], a[204:207], a[152:155], v[88:91]\n\t"
"v_exp_f32 v116, v116\n\t"
"v_mfma_f32_16x16x32_bf16 v[92:95], a[204:207], a[156:159], v[92:95]\n\t"
"v_exp_f32 v117, v117\n\t"
"v_mfma_f32_16x16x32_bf16 v[76:79], a[200:203], a[156:159], v[76:79]\n\t"
"s_waitcnt lgkmcnt(3)\n\t"
"v_exp_f32 v118, v118\n\t"
"v_mfma_f32_16x16x32_bf16 v[64:67], a[208:211], a[160:163], v[64:67]\n\t"
"ds_read_b64_tr_b16 a[200:201], v16 offset:8192\n\t"
"v_accvgpr_write_b32 a85, v102\n\t"
"v_mfma_f32_16x16x32_bf16 v[80:83], a[212:215], a[160:163], v[80:83]\n\t"
"ds_read_b64_tr_b16 a[202:203], v16 offset:10240\n\t"
"v_cvt_pk_bf16_f32 v116, v116, v117\n\t"
"v_mfma_f32_16x16x32_bf16 v[84:87], a[212:215], a[164:167], v[84:87]\n\t"
"v_exp_f32 v119, v119\n\t"
"v_mfma_f32_16x16x32_bf16 v[68:71], a[208:211], a[164:167], v[68:71]\n\t"
"v_exp_f32 v104, v104\n\t"
"v_mfma_f32_16x16x32_bf16 v[72:75], a[208:211], a[168:171], v[72:75]\n\t"
"ds_read_b64_tr_b16 a[204:205], v17 offset:8192\n\t"
"v_accvgpr_write_b32 a86, v116\n\t"
"v_mfma_f32_16x16x32_bf16 v[88:91], a[212:215], a[168:171], v[88:91]\n\t"
"ds_read_b64_tr_b16 a[206:207], v17 offset:10240\n\t"
"v_cvt_pk_bf16_f32 v118, v118, v119\n\t"
"v_mfma_f32_16x16x32_bf16 v[92:95], a[212:215], a[172:175], v[92:95]\n\t"
"v_exp_f32 v105, v105\n\t"
"v_mfma_f32_16x16x32_bf16 v[76:79], a[208:211], a[172:175], v[76:79]\n\t"
"s_waitcnt lgkmcnt(4)\n\t"
"v_exp_f32 v106, v106\n\t"
"v_accvgpr_write_b32 a87, v118\n\t"
"v_mfma_f32_16x16x32_bf16 v[64:67], a[192:195], a[176:179], v[64:67]\n\t"
"ds_read_b128 a[208:211], v6 offset:0\n\t"
"v_cvt_pk_bf16_f32 v104, v104, v105\n\t"
"v_mfma_f32_16x16x32_bf16 v[80:83], a[196:199], a[176:179], v[80:83]\n\t"
"v_exp_f32 v107, v107\n\t"
"v_mfma_f32_16x16x32_bf16 v[84:87], a[196:199], a[180:183], v[84:87]\n\t"
"v_exp_f32 v120, v120\n\t"
"v_mfma_f32_16x16x32_bf16 v[68:71], a[192:195], a[180:183], v[68:71]\n\t"
"v_exp_f32 v121, v121\n\t"
"v_mfma_f32_16x16x32_bf16 v[72:75], a[192:195], a[184:187], v[72:75]\n\t"
"ds_read_b128 a[212:215], v6 offset:6144\n\t"
"v_accvgpr_write_b32 a88, v104\n\t"
"v_mfma_f32_16x16x32_bf16 v[88:91], a[196:199], a[184:187], v[88:91]\n\t"
"v_cvt_pk_bf16_f32 v106, v106, v107\n\t"
"v_accvgpr_write_b32 a89, v106\n\t"
"v_mfma_f32_16x16x32_bf16 v[92:95], a[196:199], a[188:191], v[92:95]\n\t"
"v_exp_f32 v122, v122\n\t"
"v_mfma_f32_16x16x32_bf16 v[76:79], a[192:195], a[188:191], v[76:79]\n\t"
"v_exp_f32 v123, v123\n\t"
"v_smfmac_f32_16x16x64_bf16 a[224:227], a[244:247], v[248:255], v30\n\t"
"v_cvt_pk_bf16_f32 v120, v120, v121\n\t"
"v_accvgpr_write_b32 a90, v120\n\t"
"v_smfmac_f32_16x16x64_bf16 a[228:231], a[244:247], a[72:79], v30\n\t"
"s_waitcnt lgkmcnt(2)\n\t"
"v_cvt_pk_bf16_f32 v122, v122, v123\n\t"
"v_mfma_f32_16x16x32_bf16 a[216:219], a[200:203], v[232:235], a[216:219]\n\t"
"ds_read_b128 a[192:195], v12 offset:0\n\t"
"v_accvgpr_write_b32 a91, v122\n\t"
"v_mfma_f32_16x16x32_bf16 v[48:51], a[204:207], v[232:235], v[48:51]\n\t"
"v_exp_f32 v108, v108\n\t"
"v_mfma_f32_16x16x32_bf16 v[52:55], a[204:207], v[236:239], v[52:55]\n\t"
"v_exp_f32 v109, v109\n\t"
"v_mfma_f32_16x16x32_bf16 a[220:223], a[200:203], v[236:239], a[220:223]\n\t"
"v_exp_f32 v110, v110\n\t"
"v_mfma_f32_16x16x32_bf16 a[232:235], a[200:203], a[56:59], a[232:235]\n\t"
"ds_read_b128 a[196:199], v12 offset:6144\n\t"
"v_cvt_pk_bf16_f32 v108, v108, v109\n\t"
"v_mfma_f32_16x16x32_bf16 v[56:59], a[204:207], a[56:59], v[56:59]\n\t"
"v_exp_f32 v111, v111\n\t"
"v_mfma_f32_16x16x32_bf16 v[60:63], a[204:207], a[60:63], v[60:63]\n\t"
"v_exp_f32 v124, v124\n\t"
"v_mfma_f32_16x16x32_bf16 a[248:251], a[200:203], a[60:63], a[248:251]\n\t"
"s_waitcnt lgkmcnt(2)\n\t"
"v_exp_f32 v125, v125\n\t"
"v_accvgpr_write_b32 a92, v108\n\t"
"v_mfma_f32_16x16x32_bf16 v[96:99], a[208:211], a[96:99], v[32:35]\n\t"
"ds_read_b128 a[200:203], v6 offset:1024\n\t"
"v_cvt_pk_bf16_f32 v110, v110, v111\n\t"
"v_mfma_f32_16x16x32_bf16 v[112:115], a[212:215], a[96:99], v[32:35]\n\t"
"v_exp_f32 v126, v126\n\t"
"v_mfma_f32_16x16x32_bf16 v[116:119], a[212:215], a[100:103], v[36:39]\n\t"
"v_exp_f32 v127, v127\n\t"
"v_mfma_f32_16x16x32_bf16 v[100:103], a[208:211], a[100:103], v[36:39]\n\t"
"v_accvgpr_write_b32 a93, v110\n\t"
"v_cvt_pk_bf16_f32 v124, v124, v125\n\t"
"v_mfma_f32_16x16x32_bf16 v[104:107], a[208:211], a[104:107], v[40:43]\n\t"
"ds_read_b128 a[204:207], v6 offset:7168\n\t"
"v_accvgpr_write_b32 a94, v124\n\t"
"v_mfma_f32_16x16x32_bf16 v[120:123], a[212:215], a[104:107], v[40:43]\n\t"
"v_cvt_pk_bf16_f32 v126, v126, v127\n\t"
"v_accvgpr_write_b32 a95, v126\n\t"
"v_mfma_f32_16x16x32_bf16 v[124:127], a[212:215], a[108:111], v[44:47]\n\t"
"s_waitcnt vmcnt(4)\n\t"
"s_barrier\n\t"
"v_mfma_f32_16x16x32_bf16 v[108:111], a[208:211], a[108:111], v[44:47]\n\t"
"s_waitcnt lgkmcnt(2)\n\t"
"v_exp_f32 v64, v64\n\t"
"v_mfma_f32_16x16x32_bf16 v[96:99], a[192:195], a[112:115], v[96:99]\n\t"
"s_load_dword s3, %[karg], s85 offset:35996\n\t"
"ds_read_b128 a[208:211], v12 offset:1024\n\t"
"v_exp_f32 v65, v65\n\t"
"v_mfma_f32_16x16x32_bf16 v[112:115], a[196:199], a[112:115], v[112:115]\n\t"
"v_exp_f32 v66, v66\n\t"
"v_mfma_f32_16x16x32_bf16 v[116:119], a[196:199], a[116:119], v[116:119]\n\t"
"v_exp_f32 v67, v67\n\t"
"v_mfma_f32_16x16x32_bf16 v[100:103], a[192:195], a[116:119], v[100:103]\n\t"
"v_exp_f32 v80, v80\n\t"
"v_mfma_f32_16x16x32_bf16 v[104:107], a[192:195], a[120:123], v[104:107]\n\t"
"ds_read_b128 a[212:215], v12 offset:7168\n\t"
"v_cvt_pk_bf16_f32 v64, v64, v65\n\t"
"v_mfma_f32_16x16x32_bf16 v[120:123], a[196:199], a[120:123], v[120:123]\n\t"
"v_exp_f32 v81, v81\n\t"
"v_mfma_f32_16x16x32_bf16 v[124:127], a[196:199], a[124:127], v[124:127]\n\t"
"v_exp_f32 v82, v82\n\t"
"v_mfma_f32_16x16x32_bf16 v[108:111], a[192:195], a[124:127], v[108:111]\n\t"
"s_waitcnt lgkmcnt(2)\n\t"
"v_exp_f32 v83, v83\n\t"
"v_mfma_f32_16x16x32_bf16 v[96:99], a[200:203], a[128:131], v[96:99]\n\t"
"ds_read_b128 a[192:195], v6 offset:2048\n\t"
"v_cvt_pk_bf16_f32 v65, v66, v67\n\t"
"v_cvt_pk_bf16_f32 v66, v80, v81\n\t"
"v_mfma_f32_16x16x32_bf16 v[112:115], a[204:207], a[128:131], v[112:115]\n\t"
"v_exp_f32 v68, v68\n\t"
"v_mfma_f32_16x16x32_bf16 v[116:119], a[204:207], a[132:135], v[116:119]\n\t"
"v_exp_f32 v69, v69\n\t"
"v_mfma_f32_16x16x32_bf16 v[100:103], a[200:203], a[132:135], v[100:103]\n\t"
"v_exp_f32 v70, v70\n\t"
"v_mfma_f32_16x16x32_bf16 v[104:107], a[200:203], a[136:139], v[104:107]\n\t"
"ds_read_b128 a[196:199], v6 offset:8192\n\t"
"v_cvt_pk_bf16_f32 v67, v82, v83\n\t"
"v_mfma_f32_16x16x32_bf16 v[120:123], a[204:207], a[136:139], v[120:123]\n\t"
"v_exp_f32 v71, v71\n\t"
"v_mfma_f32_16x16x32_bf16 v[124:127], a[204:207], a[140:143], v[124:127]\n\t"
"v_exp_f32 v84, v84\n\t"
"v_mfma_f32_16x16x32_bf16 v[108:111], a[200:203], a[140:143], v[108:111]\n\t"
"s_waitcnt lgkmcnt(2)\n\t"
"v_exp_f32 v85, v85\n\t"
"v_mfma_f32_16x16x32_bf16 v[96:99], a[208:211], a[144:147], v[96:99]\n\t"
"ds_read_b128 a[200:203], v12 offset:2048\n\t"
"v_cvt_pk_bf16_f32 v68, v68, v69\n\t"
"v_cvt_pk_bf16_f32 v69, v70, v71\n\t"
"v_mfma_f32_16x16x32_bf16 v[112:115], a[212:215], a[144:147], v[112:115]\n\t"
"v_exp_f32 v86, v86\n\t"
"v_mfma_f32_16x16x32_bf16 v[116:119], a[212:215], a[148:151], v[116:119]\n\t"
"v_exp_f32 v87, v87\n\t"
"v_mfma_f32_16x16x32_bf16 v[100:103], a[208:211], a[148:151], v[100:103]\n\t"
"v_exp_f32 v72, v72\n\t"
"v_mfma_f32_16x16x32_bf16 v[104:107], a[208:211], a[152:155], v[104:107]\n\t"
"s_add_u32 m0, s73, 142784\n\t"
"ds_read_b128 a[204:207], v12 offset:8192\n\t"
"v_cvt_pk_bf16_f32 v70, v84, v85\n\t"
"v_mfma_f32_16x16x32_bf16 v[120:123], a[212:215], a[152:155], v[120:123]\n\t"
"buffer_load_dwordx4 v2, s[24:27], s60 offen offset:0 lds\n\t"
"v_mfma_f32_16x16x32_bf16 v[124:127], a[212:215], a[156:159], v[124:127]\n\t"
"v_exp_f32 v73, v73\n\t"
"v_mfma_f32_16x16x32_bf16 v[108:111], a[208:211], a[156:159], v[108:111]\n\t"
"s_waitcnt lgkmcnt(2)\n\t"
"v_exp_f32 v74, v74\n\t"
"v_mfma_f32_16x16x32_bf16 v[96:99], a[192:195], a[160:163], v[96:99]\n\t"
"ds_read_b64_tr_b16 a[208:209], v16 offset:4096\n\t"
"v_cvt_pk_bf16_f32 v71, v86, v87\n\t"
"v_mfma_f32_16x16x32_bf16 v[112:115], a[196:199], a[160:163], v[112:115]\n\t"
"ds_read_b64_tr_b16 a[210:211], v16 offset:6144\n\t"
"v_cvt_pk_bf16_f32 v72, v72, v73\n\t"
"v_mfma_f32_16x16x32_bf16 v[116:119], a[196:199], a[164:167], v[116:119]\n\t"
"s_addk_i32 m0, 832\n\t"
"v_exp_f32 v75, v75\n\t"
"v_mfma_f32_16x16x32_bf16 v[100:103], a[192:195], a[164:167], v[100:103]\n\t"
"buffer_load_dwordx4 v2, s[24:27], s60 offen offset:1280 lds\n\t"
"v_mfma_f32_16x16x32_bf16 v[104:107], a[192:195], a[168:171], v[104:107]\n\t"
"ds_read_b64_tr_b16 a[212:213], v17 offset:4096\n\t"
"v_cvt_pk_bf16_f32 v73, v74, v75\n\t"
"v_mfma_f32_16x16x32_bf16 v[120:123], a[196:199], a[168:171], v[120:123]\n\t"
"ds_read_b64_tr_b16 a[214:215], v17 offset:6144\n\t"
"v_exp_f32 v88, v88\n\t"
"v_mfma_f32_16x16x32_bf16 v[124:127], a[196:199], a[172:175], v[124:127]\n\t"
"v_exp_f32 v89, v89\n\t"
"v_mfma_f32_16x16x32_bf16 v[108:111], a[192:195], a[172:175], v[108:111]\n\t"
"s_waitcnt lgkmcnt(4)\n\t"
"v_exp_f32 v90, v90\n\t"
"v_mfma_f32_16x16x32_bf16 v[96:99], a[200:203], a[176:179], v[96:99]\n\t"
"ds_read_b64_tr_b16 a[192:193], v16 offset:0\n\t"
"v_cvt_pk_bf16_f32 v74, v88, v89\n\t"
"v_mfma_f32_16x16x32_bf16 v[112:115], a[204:207], a[176:179], v[112:115]\n\t"
"s_addk_i32 m0, 832\n\t"
"ds_read_b64_tr_b16 a[194:195], v16 offset:2048\n\t"
"v_exp_f32 v91, v91\n\t"
"v_mfma_f32_16x16x32_bf16 v[116:119], a[204:207], a[180:183], v[116:119]\n\t"
"buffer_load_dwordx4 v2, s[24:27], s60 offen offset:2560 lds\n\t"
"v_mfma_f32_16x16x32_bf16 v[100:103], a[200:203], a[180:183], v[100:103]\n\t"
"v_exp_f32 v76, v76\n\t"
"v_mfma_f32_16x16x32_bf16 v[104:107], a[200:203], a[184:187], v[104:107]\n\t"
"ds_read_b64_tr_b16 a[196:197], v17 offset:0\n\t"
"v_cvt_pk_bf16_f32 v75, v90, v91\n\t"
"v_mfma_f32_16x16x32_bf16 v[120:123], a[204:207], a[184:187], v[120:123]\n\t"
"ds_read_b64_tr_b16 a[198:199], v17 offset:2048\n\t"
"v_mfma_f32_16x16x32_bf16 v[124:127], a[204:207], a[188:191], v[124:127]\n\t"
"v_exp_f32 v77, v77\n\t"
"v_mfma_f32_16x16x32_bf16 v[108:111], a[200:203], a[188:191], v[108:111]\n\t"
"v_exp_f32 v78, v78\n\t"
"v_smfmac_f32_16x16x64_bf16 a[224:227], a[244:247], a[80:87], v30\n\t"
"v_cvt_pk_bf16_f32 v76, v76, v77\n\t"
"v_smfmac_f32_16x16x64_bf16 a[228:231], a[244:247], a[88:95], v30\n\t"
"s_waitcnt lgkmcnt(4)\n\t"
"v_exp_f32 v79, v79\n\t"
"v_mfma_f32_16x16x32_bf16 a[216:219], a[208:211], v[240:243], a[216:219]\n\t"
"ds_read_b64_tr_b16 a[200:201], v7 offset:61440\n\t"
"v_cvt_pk_bf16_f32 v77, v78, v79\n\t"
"v_mfma_f32_16x16x32_bf16 v[48:51], a[212:215], v[240:243], v[48:51]\n\t"
"ds_read_b64_tr_b16 a[202:203], v7 offset:63488\n\t"
"s_addk_i32 m0, 832\n\t"
"v_mfma_f32_16x16x32_bf16 v[52:55], a[212:215], v[244:247], v[52:55]\n\t"
"buffer_load_dwordx4 v2, s[24:27], s60 offen offset:3840 lds\n\t"
"v_mfma_f32_16x16x32_bf16 a[220:223], a[208:211], v[244:247], a[220:223]\n\t"
"v_exp_f32 v92, v92\n\t"
"v_mfma_f32_16x16x32_bf16 a[232:235], a[208:211], a[64:67], a[232:235]\n\t"
"ds_read_b64_tr_b16 a[204:205], v13 offset:61440\n\t"
"v_mfma_f32_16x16x32_bf16 v[56:59], a[212:215], a[64:67], v[56:59]\n\t"
"ds_read_b64_tr_b16 a[206:207], v13 offset:63488\n\t"
"s_addk_i32 m0, 5888\n\t"
"v_mfma_f32_16x16x32_bf16 v[60:63], a[212:215], a[68:71], v[60:63]\n\t"
"buffer_load_dwordx4 v9, s[24:27], s60 offen offset:64 lds\n\t"
"v_mfma_f32_16x16x32_bf16 a[248:251], a[208:211], a[68:71], a[248:251]\n\t"
"s_waitcnt lgkmcnt(4)\n\t"
"v_exp_f32 v93, v93\n\t"
"v_mfma_f32_16x16x32_bf16 a[216:219], a[192:195], v[248:251], a[216:219]\n\t"
"ds_read_b64_tr_b16 a[208:209], v7 offset:57344\n\t"
"v_cvt_pk_bf16_f32 v78, v92, v93\n\t"
"v_mfma_f32_16x16x32_bf16 v[48:51], a[196:199], v[248:251], v[48:51]\n\t"
"s_add_u32 s60, s60, s56\n\t"
"ds_read_b64_tr_b16 a[210:211], v7 offset:59392\n\t"
"v_exp_f32 v94, v94\n\t"
"v_mfma_f32_16x16x32_bf16 v[52:55], a[196:199], v[252:255], v[52:55]\n\t"
"s_addk_i32 m0, -29440\n\t"
"v_exp_f32 v95, v95\n\t"
"v_mfma_f32_16x16x32_bf16 a[220:223], a[192:195], v[252:255], a[220:223]\n\t"
"buffer_load_dwordx4 v2, s[24:27], s60 offen offset:0 lds\n\t"
"v_mfma_f32_16x16x32_bf16 a[232:235], a[192:195], a[72:75], a[232:235]\n\t"
"ds_read_b64_tr_b16 a[212:213], v13 offset:57344\n\t"
"v_cvt_pk_bf16_f32 v79, v94, v95\n\t"
"v_mfma_f32_16x16x32_bf16 v[56:59], a[196:199], a[72:75], v[56:59]\n\t"
"ds_read_b64_tr_b16 a[214:215], v13 offset:59392\n\t"
"v_exp_f32 v96, v96\n\t"
"v_mfma_f32_16x16x32_bf16 v[60:63], a[196:199], a[76:79], v[60:63]\n\t"
"v_exp_f32 v97, v97\n\t"
"v_mfma_f32_16x16x32_bf16 a[248:251], a[192:195], a[76:79], a[248:251]\n\t"
"v_exp_f32 v98, v98\n\t"
"v_smfmac_f32_16x16x64_bf16 a[224:227], a[244:247], v[64:71], v30\n\t"
"v_cvt_pk_bf16_f32 v96, v96, v97\n\t"
"v_smfmac_f32_16x16x64_bf16 a[228:231], a[244:247], v[72:79], v30\n\t"
"s_waitcnt lgkmcnt(4)\n\t"
"v_exp_f32 v99, v99\n\t"
"v_mfma_f32_16x16x32_bf16 a[216:219], a[200:203], a[80:83], a[216:219]\n\t"
"ds_read_b64_tr_b16 a[192:193], v14 offset:36864\n\t"
"v_cvt_pk_bf16_f32 v97, v98, v99\n\t"
"v_mfma_f32_16x16x32_bf16 v[48:51], a[204:207], a[80:83], v[48:51]\n\t"
"ds_read_b64_tr_b16 a[194:195], v14 offset:38912\n\t"
"s_addk_i32 m0, 832\n\t"
"v_mfma_f32_16x16x32_bf16 v[52:55], a[204:207], a[84:87], v[52:55]\n\t"
"buffer_load_dwordx4 v2, s[24:27], s60 offen offset:1280 lds\n\t"
"v_mfma_f32_16x16x32_bf16 a[220:223], a[200:203], a[84:87], a[220:223]\n\t"
"v_exp_f32 v112, v112\n\t"
"v_mfma_f32_16x16x32_bf16 a[232:235], a[200:203], a[88:91], a[232:235]\n\t"
"ds_read_b64_tr_b16 a[196:197], v15 offset:36864\n\t"
"v_exp_f32 v113, v113\n\t"
"v_mfma_f32_16x16x32_bf16 v[56:59], a[204:207], a[88:91], v[56:59]\n\t"
"ds_read_b64_tr_b16 a[198:199], v15 offset:38912\n\t"
"v_cvt_pk_bf16_f32 v98, v112, v113\n\t"
"v_mfma_f32_16x16x32_bf16 v[60:63], a[204:207], a[92:95], v[60:63]\n\t"
"s_addk_i32 m0, 832\n\t"
"v_exp_f32 v114, v114\n\t"
"v_mfma_f32_16x16x32_bf16 a[248:251], a[200:203], a[92:95], a[248:251]\n\t"
"s_waitcnt lgkmcnt(4)\n\t"
"buffer_load_dwordx4 v2, s[24:27], s60 offen offset:2560 lds\n\t"
"v_mfma_f32_16x16x32_bf16 a[216:219], a[208:211], v[64:67], a[216:219]\n\t"
"ds_read_b64_tr_b16 a[200:201], v14 offset:32768\n\t"
"v_exp_f32 v115, v115\n\t"
"v_mfma_f32_16x16x32_bf16 v[48:51], a[212:215], v[64:67], v[48:51]\n\t"
"ds_read_b64_tr_b16 a[202:203], v14 offset:34816\n\t"
"v_cvt_pk_bf16_f32 v99, v114, v115\n\t"
"v_mfma_f32_16x16x32_bf16 v[52:55], a[212:215], v[68:71], v[52:55]\n\t"
"v_exp_f32 v100, v100\n\t"
"v_mfma_f32_16x16x32_bf16 a[220:223], a[208:211], v[68:71], a[220:223]\n\t"
"v_exp_f32 v101, v101\n\t"
"v_mfma_f32_16x16x32_bf16 a[232:235], a[208:211], v[72:75], a[232:235]\n\t"
"ds_read_b64_tr_b16 a[204:205], v15 offset:32768\n\t"
"v_cvt_pk_bf16_f32 v100, v100, v101\n\t"
"v_mfma_f32_16x16x32_bf16 v[56:59], a[212:215], v[72:75], v[56:59]\n\t"
"s_addk_i32 m0, 832\n\t"
"ds_read_b64_tr_b16 a[206:207], v15 offset:34816\n\t"
"v_exp_f32 v102, v102\n\t"
"v_mfma_f32_16x16x32_bf16 v[60:63], a[212:215], v[76:79], v[60:63]\n\t"
"buffer_load_dwordx4 v2, s[24:27], s60 offen offset:3840 lds\n\t"
"v_mfma_f32_16x16x32_bf16 a[248:251], a[208:211], v[76:79], a[248:251]\n\t"
"s_waitcnt lgkmcnt(4)\n\t"
"v_exp_f32 v103, v103\n\t"
"v_mfma_f32_16x16x32_bf16 v[32:35], a[192:195], v[128:131], 0\n\t"
"ds_read_b64_tr_b16 a[208:209], v14 offset:28672\n\t"
"v_cvt_pk_bf16_f32 v101, v102, v103\n\t"
"v_mfma_f32_16x16x32_bf16 v[80:83], a[196:199], v[128:131], 0\n\t"
"ds_read_b64_tr_b16 a[210:211], v14 offset:30720\n\t"
"v_mfma_f32_16x16x32_bf16 v[84:87], a[196:199], v[132:135], 0\n\t"
"v_exp_f32 v116, v116\n\t"
"v_mfma_f32_16x16x32_bf16 v[36:39], a[192:195], v[132:135], 0\n\t"
"v_exp_f32 v117, v117\n\t"
"v_mfma_f32_16x16x32_bf16 v[40:43], a[192:195], v[136:139], 0\n\t"
"ds_read_b64_tr_b16 a[212:213], v15 offset:28672\n\t"
"v_cvt_pk_bf16_f32 v102, v116, v117\n\t"
"v_mfma_f32_16x16x32_bf16 v[88:91], a[196:199], v[136:139], 0\n\t"
"s_addk_i32 m0, 5888\n\t"
"ds_read_b64_tr_b16 a[214:215], v15 offset:30720\n\t"
"v_exp_f32 v118, v118\n\t"
"v_mfma_f32_16x16x32_bf16 v[92:95], a[196:199], v[140:143], 0\n\t"
"buffer_load_dwordx4 v9, s[24:27], s60 offen offset:64 lds\n\t"
"v_mfma_f32_16x16x32_bf16 v[44:47], a[192:195], v[140:143], 0\n\t"
"s_waitcnt lgkmcnt(4)\n\t"
"v_exp_f32 v119, v119\n\t"
"v_mfma_f32_16x16x32_bf16 v[32:35], a[200:203], v[144:147], v[32:35]\n\t"
"s_add_u32 s60, s60, s56\n\t"
"ds_read_b64_tr_b16 a[192:193], v14 offset:24576\n\t"
"v_cvt_pk_bf16_f32 v103, v118, v119\n\t"
"v_mfma_f32_16x16x32_bf16 v[80:83], a[204:207], v[144:147], v[80:83]\n\t"
"s_addk_i32 m0, -29440\n\t"
"ds_read_b64_tr_b16 a[194:195], v14 offset:26624\n\t"
"v_exp_f32 v104, v104\n\t"
"v_mfma_f32_16x16x32_bf16 v[84:87], a[204:207], v[148:151], v[84:87]\n\t"
"buffer_load_dwordx4 v2, s[24:27], s60 offen offset:0 lds\n\t"
"v_mfma_f32_16x16x32_bf16 v[36:39], a[200:203], v[148:151], v[36:39]\n\t"
"v_exp_f32 v105, v105\n\t"
"v_mfma_f32_16x16x32_bf16 v[40:43], a[200:203], v[152:155], v[40:43]\n\t"
"ds_read_b64_tr_b16 a[196:197], v15 offset:24576\n\t"
"v_cvt_pk_bf16_f32 v104, v104, v105\n\t"
"v_mfma_f32_16x16x32_bf16 v[88:91], a[204:207], v[152:155], v[88:91]\n\t"
"ds_read_b64_tr_b16 a[198:199], v15 offset:26624\n\t"
"v_exp_f32 v106, v106\n\t"
"v_mfma_f32_16x16x32_bf16 v[92:95], a[204:207], v[156:159], v[92:95]\n\t"
"v_exp_f32 v107, v107\n\t"
"v_mfma_f32_16x16x32_bf16 v[44:47], a[200:203], v[156:159], v[44:47]\n\t"
"s_waitcnt lgkmcnt(4)\n\t"
"v_exp_f32 v120, v120\n\t"
"v_mfma_f32_16x16x32_bf16 v[32:35], a[208:211], v[160:163], v[32:35]\n\t"
"ds_read_b64_tr_b16 a[200:201], v14 offset:20480\n\t"
"v_cvt_pk_bf16_f32 v105, v106, v107\n\t"
"v_mfma_f32_16x16x32_bf16 v[80:83], a[212:215], v[160:163], v[80:83]\n\t"
"s_addk_i32 m0, 832\n\t"
"ds_read_b64_tr_b16 a[202:203], v14 offset:22528\n\t"
"v_exp_f32 v121, v121\n\t"
"v_mfma_f32_16x16x32_bf16 v[84:87], a[212:215], v[164:167], v[84:87]\n\t"
"buffer_load_dwordx4 v2, s[24:27], s60 offen offset:1280 lds\n\t"
"v_mfma_f32_16x16x32_bf16 v[36:39], a[208:211], v[164:167], v[36:39]\n\t"
"v_exp_f32 v122, v122\n\t"
"v_mfma_f32_16x16x32_bf16 v[40:43], a[208:211], v[168:171], v[40:43]\n\t"
"ds_read_b64_tr_b16 a[204:205], v15 offset:20480\n\t"
"v_cvt_pk_bf16_f32 v106, v120, v121\n\t"
"v_mfma_f32_16x16x32_bf16 v[88:91], a[212:215], v[168:171], v[88:91]\n\t"
"ds_read_b64_tr_b16 a[206:207], v15 offset:22528\n\t"
"v_exp_f32 v123, v123\n\t"
"v_mfma_f32_16x16x32_bf16 v[92:95], a[212:215], v[172:175], v[92:95]\n\t"
"s_addk_i32 m0, 832\n\t"
"v_exp_f32 v108, v108\n\t"
"v_mfma_f32_16x16x32_bf16 v[44:47], a[208:211], v[172:175], v[44:47]\n\t"
"s_waitcnt lgkmcnt(4)\n\t"
"buffer_load_dwordx4 v2, s[24:27], s60 offen offset:2560 lds\n\t"
"v_mfma_f32_16x16x32_bf16 v[32:35], a[192:195], v[176:179], v[32:35]\n\t"
"ds_read_b64_tr_b16 a[208:209], v14 offset:16384\n\t"
"v_cvt_pk_bf16_f32 v107, v122, v123\n\t"
"v_mfma_f32_16x16x32_bf16 v[80:83], a[196:199], v[176:179], v[80:83]\n\t"
"ds_read_b64_tr_b16 a[210:211], v14 offset:18432\n\t"
"v_exp_f32 v109, v109\n\t"
"v_mfma_f32_16x16x32_bf16 v[84:87], a[196:199], v[180:183], v[84:87]\n\t"
"v_exp_f32 v110, v110\n\t"
"v_mfma_f32_16x16x32_bf16 v[36:39], a[192:195], v[180:183], v[36:39]\n\t"
"v_exp_f32 v111, v111\n\t"
"v_mfma_f32_16x16x32_bf16 v[40:43], a[192:195], a[0:3], v[40:43]\n\t"
"ds_read_b64_tr_b16 a[212:213], v15 offset:16384\n\t"
"v_cvt_pk_bf16_f32 v108, v108, v109\n\t"
"v_mfma_f32_16x16x32_bf16 v[88:91], a[196:199], a[0:3], v[88:91]\n\t"
"s_addk_i32 m0, 832\n\t"
"ds_read_b64_tr_b16 a[214:215], v15 offset:18432\n\t"
"v_cvt_pk_bf16_f32 v109, v110, v111\n\t"
"v_mfma_f32_16x16x32_bf16 v[92:95], a[196:199], a[4:7], v[92:95]\n\t"
"buffer_load_dwordx4 v2, s[24:27], s60 offen offset:3840 lds\n\t"
"v_mfma_f32_16x16x32_bf16 v[44:47], a[192:195], a[4:7], v[44:47]\n\t"
"s_waitcnt lgkmcnt(4)\n\t"
"v_exp_f32 v124, v124\n\t"
"v_mfma_f32_16x16x32_bf16 v[32:35], a[200:203], v[184:187], v[32:35]\n\t"
"ds_read_b64_tr_b16 a[192:193], v7 offset:53248\n\t"
"v_exp_f32 v125, v125\n\t"
"v_mfma_f32_16x16x32_bf16 v[80:83], a[204:207], v[184:187], v[80:83]\n\t"
"ds_read_b64_tr_b16 a[194:195], v7 offset:55296\n\t"
"v_cvt_pk_bf16_f32 v110, v124, v125\n\t"
"v_mfma_f32_16x16x32_bf16 v[84:87], a[204:207], v[188:191], v[84:87]\n\t"
"v_exp_f32 v126, v126\n\t"
"v_mfma_f32_16x16x32_bf16 v[36:39], a[200:203], v[188:191], v[36:39]\n\t"
"v_exp_f32 v127, v127\n\t"
"v_mfma_f32_16x16x32_bf16 v[40:43], a[200:203], a[8:11], v[40:43]\n\t"
"ds_read_b64_tr_b16 a[196:197], v13 offset:53248\n\t"
"v_cvt_pk_bf16_f32 v111, v126, v127\n\t"
"v_mfma_f32_16x16x32_bf16 v[88:91], a[204:207], a[8:11], v[88:91]\n\t"
"s_addk_i32 m0, 5888\n\t"
"ds_read_b64_tr_b16 a[198:199], v13 offset:55296\n\t"
"v_accvgpr_read_b32 v112, a216\n\t"
"v_mfma_f32_16x16x32_bf16 v[92:95], a[204:207], a[12:15], v[92:95]\n\t"
"buffer_load_dwordx4 v9, s[24:27], s60 offen offset:64 lds\n\t"
"v_mfma_f32_16x16x32_bf16 v[44:47], a[200:203], a[12:15], v[44:47]\n\t"
"v_accvgpr_read_b32 v113, a217\n\t"
"v_accvgpr_read_b32 v114, a218\n\t"
"v_smfmac_f32_16x16x64_bf16 a[224:227], a[244:247], v[96:103], v30\n\t"
"v_accvgpr_read_b32 v115, a219\n\t"
"v_smfmac_f32_16x16x64_bf16 a[228:231], a[244:247], v[104:111], v30\n\t"
"s_waitcnt lgkmcnt(4)\n\t"
"v_accvgpr_read_b32 v116, a220\n\t"
"v_mfma_f32_16x16x32_bf16 v[32:35], a[208:211], v[192:195], v[32:35]\n\t"
"ds_read_b64_tr_b16 a[200:201], v14 offset:12288\n\t"
"v_accvgpr_read_b32 v117, a221\n\t"
"v_mfma_f32_16x16x32_bf16 v[80:83], a[212:215], v[192:195], v[80:83]\n\t"
"s_addk_i32 s48, 12288\n\t"
"ds_read_b64_tr_b16 a[202:203], v14 offset:14336\n\t"
"v_accvgpr_read_b32 v118, a222\n\t"
"s_barrier\n\t"
"v_mfma_f32_16x16x32_bf16 v[84:87], a[212:215], v[196:199], v[84:87]\n\t"
"s_add_u32 m0, s44, 36864\n\t"
"v_accvgpr_read_b32 v119, a223\n\t"
"v_accvgpr_read_b32 v120, a232\n\t"
"v_mfma_f32_16x16x32_bf16 v[36:39], a[208:211], v[196:199], v[36:39]\n\t"
"buffer_load_dwordx4 v4, s[20:23], s48 offen offset:0 lds\n\t"
"v_accvgpr_read_b32 v121, a233\n\t"
"v_accvgpr_read_b32 v122, a234\n\t"
"v_mfma_f32_16x16x32_bf16 v[40:43], a[208:211], a[16:19], v[40:43]\n\t"
"ds_read_b64_tr_b16 a[204:205], v15 offset:12288\n\t"
"v_accvgpr_read_b32 v123, a235\n\t"
"v_mfma_f32_16x16x32_bf16 v[88:91], a[212:215], a[16:19], v[88:91]\n\t"
"s_addk_i32 s48, 12288\n\t"
"ds_read_b64_tr_b16 a[206:207], v15 offset:14336\n\t"
"v_accvgpr_read_b32 v124, a248\n\t"
"v_mfma_f32_16x16x32_bf16 v[92:95], a[212:215], a[20:23], v[92:95]\n\t"
"s_addk_i32 m0, -4096\n\t"
"v_accvgpr_read_b32 v125, a249\n\t"
"v_accvgpr_read_b32 v126, a250\n\t"
"v_mfma_f32_16x16x32_bf16 v[44:47], a[208:211], a[20:23], v[44:47]\n\t"
"s_waitcnt lgkmcnt(4)\n\t"
"buffer_load_dwordx4 v4, s[20:23], s48 offen offset:0 lds\n\t"
"v_accvgpr_read_b32 v127, a251\n\t"
"v_accvgpr_read_b32 v20, a224\n\t"
"v_mfma_f32_16x16x32_bf16 v[112:115], a[192:195], v[96:99], v[112:115]\n\t"
"ds_read_b64_tr_b16 a[208:209], v14 offset:8192\n\t"
"v_accvgpr_read_b32 v10, a225\n\t"
"v_mfma_f32_16x16x32_bf16 v[48:51], a[196:199], v[96:99], v[48:51]\n\t"
"s_addk_i32 s48, 12288\n\t"
"ds_read_b64_tr_b16 a[210:211], v14 offset:10240\n\t"
"v_accvgpr_read_b32 v21, a226\n\t"
"v_mfma_f32_16x16x32_bf16 v[52:55], a[196:199], v[100:103], v[52:55]\n\t"
"s_addk_i32 m0, -4096\n\t"
"v_accvgpr_read_b32 v11, a227\n\t"
"v_accvgpr_read_b32 v22, a228\n\t"
"v_mfma_f32_16x16x32_bf16 v[116:119], a[192:195], v[100:103], v[116:119]\n\t"
"buffer_load_dwordx4 v4, s[20:23], s48 offen offset:0 lds\n\t"
"v_accvgpr_read_b32 v12, a229\n\t"
"v_accvgpr_read_b32 v23, a230\n\t"
"v_mfma_f32_16x16x32_bf16 v[120:123], a[192:195], v[104:107], v[120:123]\n\t"
"ds_read_b64_tr_b16 a[212:213], v15 offset:8192\n\t"
"v_accvgpr_read_b32 v30, a231\n\t"
"v_mfma_f32_16x16x32_bf16 v[56:59], a[196:199], v[104:107], v[56:59]\n\t"
"s_addk_i32 s48, 12288\n\t"
"ds_read_b64_tr_b16 a[214:215], v15 offset:10240\n\t"
"v_add_f32 v20, v20, v10\n\t"
"v_mfma_f32_16x16x32_bf16 v[60:63], a[196:199], v[108:111], v[60:63]\n\t"
"s_addk_i32 m0, -4096\n\t"
"v_add_f32 v21, v21, v11\n\t"
"v_add_f32 v22, v22, v12\n\t"
"v_mfma_f32_16x16x32_bf16 v[124:127], a[192:195], v[108:111], v[124:127]\n\t"
"s_waitcnt lgkmcnt(4)\n\t"
"v_add_f32 v23, v23, v30\n\t"
"v_add_f32 v10, v20, v21\n\t"
"v_mfma_f32_16x16x32_bf16 v[32:35], a[200:203], v[200:203], v[32:35]\n\t"
"ds_read_b64_tr_b16 a[192:193], v14 offset:4096\n\t"
"v_add_f32 v30, v22, v23\n\t"
"v_mfma_f32_16x16x32_bf16 v[80:83], a[204:207], v[200:203], v[80:83]\n\t"
"buffer_load_dwordx4 v4, s[20:23], s48 offen offset:0 lds\n\t"
"ds_read_b64_tr_b16 a[194:195], v14 offset:6144\n\t"
"v_mfma_f32_16x16x32_bf16 v[84:87], a[204:207], v[204:207], v[84:87]\n\t"
"s_addk_i32 m0, -4096\n\t"
"v_rcp_f32 v20, v20\n\t"
"v_mfma_f32_16x16x32_bf16 v[36:39], a[200:203], v[204:207], v[36:39]\n\t"
"s_addk_i32 s48, 12288\n\t"
"v_rcp_f32 v21, v21\n\t"
"v_mfma_f32_16x16x32_bf16 v[40:43], a[200:203], a[24:27], v[40:43]\n\t"
"buffer_load_dwordx4 v4, s[20:23], s48 offen offset:0 lds\n\t"
"ds_read_b64_tr_b16 a[196:197], v15 offset:4096\n\t"
"v_mfma_f32_16x16x32_bf16 v[88:91], a[204:207], a[24:27], v[88:91]\n\t"
"ds_read_b64_tr_b16 a[198:199], v15 offset:6144\n\t"
"v_mfma_f32_16x16x32_bf16 v[92:95], a[204:207], a[28:31], v[92:95]\n\t"
"v_rcp_f32 v22, v22\n\t"
"v_mfma_f32_16x16x32_bf16 v[44:47], a[200:203], a[28:31], v[44:47]\n\t"
"s_waitcnt lgkmcnt(4)\n\t"
"v_rcp_f32 v23, v23\n\t"
"v_mfma_f32_16x16x32_bf16 v[32:35], a[208:211], v[208:211], v[32:35]\n\t"
"ds_read_b64_tr_b16 a[200:201], v14 offset:0\n\t"
"v_add_f32 v30, v30, v10\n\t"
"v_mfma_f32_16x16x32_bf16 v[80:83], a[212:215], v[208:211], v[80:83]\n\t"
"ds_read_b64_tr_b16 a[202:203], v14 offset:2048\n\t"
"v_mov_b32 v10, 0x4e800000\n\t"
"v_mfma_f32_16x16x32_bf16 v[84:87], a[212:215], v[212:215], v[84:87]\n\t"
"s_lshl_b32 s75, s38, 2\n\t"
"v_cmp_nlt_f32 vcc, v30, v10\n\t"
"s_add_u32 s75, s75, 79360\n\t"
"v_mfma_f32_16x16x32_bf16 v[36:39], a[208:211], v[212:215], v[36:39]\n\t"
"s_cmp_lg_u64 vcc, 0\n\t"
"v_mov_b32 v10, s75\n\t"
"s_cselect_b32 s46, 1, 0\n\t"
"v_mfma_f32_16x16x32_bf16 v[40:43], a[208:211], a[32:35], v[40:43]\n\t"
"ds_read_b64_tr_b16 a[204:205], v15 offset:0\n\t"
"v_mov_b32 v30, s46\n\t"
"v_mfma_f32_16x16x32_bf16 v[88:91], a[212:215], a[32:35], v[88:91]\n\t"
"ds_read_b64_tr_b16 a[206:207], v15 offset:2048\n\t"
"v_and_b32 v11, 12, %[vT]\n\t"
"v_mfma_f32_16x16x32_bf16 v[92:95], a[212:215], a[36:39], v[92:95]\n\t"
"ds_write_b32 v10, v30 offset:0\n\t"
"v_add_u32 v11, 79360, v11\n\t"
"v_mfma_f32_16x16x32_bf16 v[44:47], a[208:211], a[36:39], v[44:47]\n\t"
"s_waitcnt lgkmcnt(5)\n\t"
"v_mul_f32 v112, v112, v20\n\t"
"v_mul_f32 v113, v113, v20\n\t"
"v_mfma_f32_16x16x32_bf16 v[32:35], a[192:195], v[216:219], v[32:35]\n\t"
"ds_read_b64_tr_b16 a[208:209], v18 offset:8192\n\t"
"v_mul_f32 v114, v114, v20\n\t"
"v_mfma_f32_16x16x32_bf16 v[80:83], a[196:199], v[216:219], v[80:83]\n\t"
"ds_read_b64_tr_b16 a[210:211], v18 offset:10240\n\t"
"v_mul_f32 v115, v115, v20\n\t"
"v_mfma_f32_16x16x32_bf16 v[84:87], a[196:199], v[220:223], v[84:87]\n\t"
"s_waitcnt lgkmcnt(2)\n\t"
"s_barrier\n\t"
"v_mfma_f32_16x16x32_bf16 v[36:39], a[192:195], v[220:223], v[36:39]\n\t"
"s_addk_i32 m0, -4096\n\t"
"ds_read_b32 v30, v11 offset:0\n\t"
"v_mul_f32 v48, v48, v20\n\t"
"v_mfma_f32_16x16x32_bf16 v[40:43], a[192:195], a[40:43], v[40:43]\n\t"
"s_addk_i32 s48, 12288\n\t"
"ds_read_b64_tr_b16 a[212:213], v19 offset:8192\n\t"
"buffer_load_dwordx4 v4, s[20:23], s48 offen offset:0 lds\n\t"
"v_mul_f32 v49, v49, v20\n\t"
"v_mfma_f32_16x16x32_bf16 v[88:91], a[196:199], a[40:43], v[88:91]\n\t"
"ds_read_b64_tr_b16 a[214:215], v19 offset:10240\n\t"
"v_mul_f32 v50, v50, v20\n\t"
"v_mfma_f32_16x16x32_bf16 v[92:95], a[196:199], a[44:47], v[92:95]\n\t"
"v_mul_f32 v51, v51, v20\n\t"
"v_cvt_pk_bf16_f32 v112, v112, v113\n\t"
"v_mfma_f32_16x16x32_bf16 v[44:47], a[192:195], a[44:47], v[44:47]\n\t"
"v_cvt_pk_bf16_f32 v113, v114, v115\n\t"
"v_cvt_pk_bf16_f32 v114, v48, v49\n\t"
"v_mfma_f32_16x16x32_bf16 v[32:35], a[200:203], v[224:227], v[32:35]\n\t"
"ds_read_b64_tr_b16 a[192:193], v18 offset:4096\n\t"
"v_cvt_pk_bf16_f32 v115, v50, v51\n\t"
".Lpg10%=:\n"
"s_cbranch_execz .Lpg14%=\n\t"
"v_mfma_f32_16x16x32_bf16 v[80:83], a[204:207], v[224:227], v[80:83]\n\t"
"s_waitcnt lgkmcnt(3)\n\t"
"ds_read_b64_tr_b16 a[194:195], v18 offset:6144\n\t"
"buffer_store_dwordx4 v[112:115], v1, s[8:11], s52 offen offset:0 sc1\n\t"
"v_cmp_lt_u32 vcc, s49, v30\n\t"
"v_mfma_f32_16x16x32_bf16 v[84:87], a[204:207], v[228:231], v[84:87]\n\t"
"s_cbranch_vccnz .Lredo%=\n\t"
"v_mul_f32 v116, v116, v21\n\t"
"v_mul_f32 v117, v117, v21\n\t"
"v_mfma_f32_16x16x32_bf16 v[36:39], a[200:203], v[228:231], v[36:39]\n\t"
"v_mul_f32 v118, v118, v21\n\t"
"v_mul_f32 v119, v119, v21\n\t"
"v_mfma_f32_16x16x32_bf16 v[40:43], a[200:203], a[48:51], v[40:43]\n\t"
"ds_read_b64_tr_b16 a[196:197], v19 offset:4096\n\t"
"v_mul_f32 v52, v52, v21\n\t"
"v_mfma_f32_16x16x32_bf16 v[88:91], a[204:207], a[48:51], v[88:91]\n\t"
"ds_read_b64_tr_b16 a[198:199], v19 offset:6144\n\t"
"v_mul_f32 v53, v53, v21\n\t"
"v_mfma_f32_16x16x32_bf16 v[92:95], a[204:207], a[52:55], v[92:95]\n\t"
"v_mul_f32 v54, v54, v21\n\t"
"v_mul_f32 v55, v55, v21\n\t"
"v_mfma_f32_16x16x32_bf16 v[44:47], a[200:203], a[52:55], v[44:47]\n\t"
"s_waitcnt lgkmcnt(4)\n\t"
"v_cvt_pk_bf16_f32 v116, v116, v117\n\t"
"v_cvt_pk_bf16_f32 v117, v118, v119\n\t"
"v_mfma_f32_16x16x32_bf16 v[32:35], a[208:211], v[232:235], v[32:35]\n\t"
"ds_read_b64_tr_b16 a[200:201], v18 offset:0\n\t"
"v_cvt_pk_bf16_f32 v118, v52, v53\n\t"
"v_mfma_f32_16x16x32_bf16 v[80:83], a[212:215], v[232:235], v[80:83]\n\t"
"ds_read_b64_tr_b16 a[202:203], v18 offset:2048\n\t"
"v_cvt_pk_bf16_f32 v119, v54, v55\n\t"
"v_mfma_f32_16x16x32_bf16 v[84:87], a[212:215], v[236:239], v[84:87]\n\t"
"buffer_store_dwordx4 v[116:119], v1, s[8:11], s52 offen offset:1024 sc1\n\t"
"v_mfma_f32_16x16x32_bf16 v[36:39], a[208:211], v[236:239], v[36:39]\n\t"
"v_mul_f32 v120, v120, v22\n\t"
"v_mul_f32 v121, v121, v22\n\t"
"v_mfma_f32_16x16x32_bf16 v[40:43], a[208:211], a[56:59], v[40:43]\n\t"
"ds_read_b64_tr_b16 a[204:205], v19 offset:0\n\t"
"v_mul_f32 v122, v122, v22\n\t"
"v_mfma_f32_16x16x32_bf16 v[88:91], a[212:215], a[56:59], v[88:91]\n\t"
"ds_read_b64_tr_b16 a[206:207], v19 offset:2048\n\t"
"v_mul_f32 v123, v123, v22\n\t"
"v_mfma_f32_16x16x32_bf16 v[92:95], a[212:215], a[60:63], v[92:95]\n\t"
"v_mul_f32 v56, v56, v22\n\t"
"v_mul_f32 v57, v57, v22\n\t"
"v_mfma_f32_16x16x32_bf16 v[44:47], a[208:211], a[60:63], v[44:47]\n\t"
"s_waitcnt lgkmcnt(4)\n\t"
"v_mul_f32 v58, v58, v22\n\t"
"v_mul_f32 v59, v59, v22\n\t"
"v_mfma_f32_16x16x32_bf16 v[32:35], a[192:195], v[240:243], v[32:35]\n\t"
"ds_read_b64_tr_b16 a[208:209], v14 offset:61440\n\t"
"v_cvt_pk_bf16_f32 v120, v120, v121\n\t"
"v_mfma_f32_16x16x32_bf16 v[80:83], a[196:199], v[240:243], v[80:83]\n\t"
"ds_read_b64_tr_b16 a[210:211], v14 offset:63488\n\t"
"v_cvt_pk_bf16_f32 v121, v122, v123\n\t"
"v_mfma_f32_16x16x32_bf16 v[84:87], a[196:199], v[244:247], v[84:87]\n\t"
"v_cvt_pk_bf16_f32 v122, v56, v57\n\t"
"v_cvt_pk_bf16_f32 v123, v58, v59\n\t"
"v_mfma_f32_16x16x32_bf16 v[36:39], a[192:195], v[244:247], v[36:39]\n\t"
"buffer_store_dwordx4 v[120:123], v1, s[8:11], s52 offen offset:2048 sc1\n\t"
"v_mfma_f32_16x16x32_bf16 v[40:43], a[192:195], a[64:67], v[40:43]\n\t"
"ds_read_b64_tr_b16 a[212:213], v15 offset:61440\n\t"
"v_mul_f32 v124, v124, v23\n\t"
"v_mfma_f32_16x16x32_bf16 v[88:91], a[196:199], a[64:67], v[88:91]\n\t"
"ds_read_b64_tr_b16 a[214:215], v15 offset:63488\n\t"
"v_mul_f32 v125, v125, v23\n\t"
"v_mfma_f32_16x16x32_bf16 v[92:95], a[196:199], a[68:71], v[92:95]\n\t"
"v_mul_f32 v126, v126, v23\n\t"
"v_mul_f32 v127, v127, v23\n\t"
"v_mfma_f32_16x16x32_bf16 v[44:47], a[192:195], a[68:71], v[44:47]\n\t"
"s_waitcnt lgkmcnt(4)\n\t"
"v_mul_f32 v60, v60, v23\n\t"
"v_mul_f32 v61, v61, v23\n\t"
"v_mfma_f32_16x16x32_bf16 v[32:35], a[200:203], v[248:251], v[32:35]\n\t"
"ds_read_b64_tr_b16 a[192:193], v14 offset:57344\n\t"
"v_mul_f32 v62, v62, v23\n\t"
"v_mfma_f32_16x16x32_bf16 v[80:83], a[204:207], v[248:251], v[80:83]\n\t"
"ds_read_b64_tr_b16 a[194:195], v14 offset:59392\n\t"
"v_mul_f32 v63, v63, v23\n\t"
"v_mfma_f32_16x16x32_bf16 v[84:87], a[204:207], v[252:255], v[84:87]\n\t"
"v_cvt_pk_bf16_f32 v124, v124, v125\n\t"
"v_cvt_pk_bf16_f32 v125, v126, v127\n\t"
"v_mfma_f32_16x16x32_bf16 v[36:39], a[200:203], v[252:255], v[36:39]\n\t"
"v_cvt_pk_bf16_f32 v126, v60, v61\n\t"
"v_cvt_pk_bf16_f32 v127, v62, v63\n\t"
"v_mfma_f32_16x16x32_bf16 v[40:43], a[200:203], a[72:75], v[40:43]\n\t"
"ds_read_b64_tr_b16 a[196:197], v15 offset:57344\n\t"
"buffer_store_dwordx4 v[124:127], v1, s[8:11], s52 offen offset:3072 sc1\n\t"
"v_mfma_f32_16x16x32_bf16 v[88:91], a[204:207], a[72:75], v[88:91]\n\t"
"ds_read_b64_tr_b16 a[198:199], v15 offset:59392\n\t"
"v_accvgpr_write_b32 a128, v112\n\t"
"v_mfma_f32_16x16x32_bf16 v[92:95], a[204:207], a[76:79], v[92:95]\n\t"
"v_accvgpr_write_b32 a129, v113\n\t"
"v_accvgpr_write_b32 a130, v114\n\t"
"v_mfma_f32_16x16x32_bf16 v[44:47], a[200:203], a[76:79], v[44:47]\n\t"
"s_waitcnt lgkmcnt(4)\n\t"
"v_accvgpr_write_b32 a131, v115\n\t"
"v_accvgpr_write_b32 a132, v116\n\t"
"v_mfma_f32_16x16x32_bf16 v[32:35], a[208:211], a[80:83], v[32:35]\n\t"
"ds_read_b64_tr_b16 a[200:201], v14 offset:53248\n\t"
"v_accvgpr_write_b32 a133, v117\n\t"
"v_mfma_f32_16x16x32_bf16 v[80:83], a[212:215], a[80:83], v[80:83]\n\t"
"ds_read_b64_tr_b16 a[202:203], v14 offset:55296\n\t"
"v_accvgpr_write_b32 a134, v118\n\t"
"v_mfma_f32_16x16x32_bf16 v[84:87], a[212:215], a[84:87], v[84:87]\n\t"
"v_accvgpr_write_b32 a135, v119\n\t"
"v_accvgpr_write_b32 a136, v120\n\t"
"v_mfma_f32_16x16x32_bf16 v[36:39], a[208:211], a[84:87], v[36:39]\n\t"
"s_addk_i32 s48, 12288\n\t"
"v_accvgpr_write_b32 a137, v121\n\t"
"v_accvgpr_write_b32 a138, v122\n\t"
"v_mfma_f32_16x16x32_bf16 v[40:43], a[208:211], a[88:91], v[40:43]\n\t"
"s_addk_i32 m0, -4096\n\t"
"ds_read_b64_tr_b16 a[204:205], v15 offset:53248\n\t"
"v_accvgpr_write_b32 a139, v123\n\t"
"v_mfma_f32_16x16x32_bf16 v[88:91], a[212:215], a[88:91], v[88:91]\n\t"
"ds_read_b64_tr_b16 a[206:207], v15 offset:55296\n\t"
"buffer_load_dwordx4 v4, s[20:23], s48 offen offset:0 lds\n\t"
"v_accvgpr_write_b32 a140, v124\n\t"
"v_mfma_f32_16x16x32_bf16 v[92:95], a[212:215], a[92:95], v[92:95]\n\t"
"v_accvgpr_write_b32 a141, v125\n\t"
"v_accvgpr_write_b32 a142, v126\n\t"
"v_mfma_f32_16x16x32_bf16 v[44:47], a[208:211], a[92:95], v[44:47]\n\t"
"s_waitcnt vmcnt(6)\n\t"
"v_accvgpr_write_b32 a143, v127\n\t"
"v_bfe_u32 v11, %[vT], 2, 1\n\t"
"s_barrier\n\t"
"s_waitcnt lgkmcnt(4)\n\t"
"v_mfma_f32_16x16x32_bf16 v[32:35], a[192:195], v[64:67], v[32:35]\n\t"
"s_addk_i32 m0, -4096\n\t"
"ds_read_b64_tr_b16 a[208:209], v7 offset:49152\n\t"
"v_mul_u32_u24 v11, 96, v11\n\t"
"v_mfma_f32_16x16x32_bf16 v[80:83], a[196:199], v[64:67], v[80:83]\n\t"
"s_addk_i32 s48, 12288\n\t"
"ds_read_b64_tr_b16 a[210:211], v7 offset:51200\n\t"
"buffer_load_dwordx4 v4, s[20:23], s48 offen offset:0 lds\n\t"
"v_and_b32 v12, 3, %[vT]\n\t"
"v_mfma_f32_16x16x32_bf16 v[84:87], a[196:199], v[68:71], v[84:87]\n\t"
"s_lshl_b32 s46, s83, 4\n\t"
"v_add_u32 v11, v11, v12\n\t"
"s_add_u32 s46, s46, 28340224\n\t"
"v_mfma_f32_16x16x32_bf16 v[36:39], a[192:195], v[68:71], v[36:39]\n\t"
"s_addk_i32 s48, 12288\n\t"
"v_lshl_add_u32 v11, v11, 10, s46\n\t"
"v_and_b32 v12, 8, %[vT]\n\t"
"v_mfma_f32_16x16x32_bf16 v[40:43], a[192:195], v[72:75], v[40:43]\n\t"
"s_addk_i32 m0, -4096\n\t"
"ds_read_b64_tr_b16 a[212:213], v13 offset:49152\n\t"
"v_cmp_eq_u32 vcc, 0, v12\n\t"
"v_mfma_f32_16x16x32_bf16 v[88:91], a[196:199], v[72:75], v[88:91]\n\t"
"s_lshl_b32 s83, s83, 4\n\t"
"s_add_u32 s83, s83, 28602368\n\t"
"ds_read_b64_tr_b16 a[214:215], v13 offset:51200\n\t"
"buffer_load_dwordx4 v4, s[20:23], s48 offen offset:0 lds\n\t"
"v_mfma_f32_16x16x32_bf16 v[92:95], a[196:199], v[76:79], v[92:95]\n\t"
"s_cmp_lt_u32 s50, 240\n\t"
"s_cselect_b32 s83, s83, 1073741824\n\t"
"v_cndmask_b32 v10, v11, v29, vcc\n\t"
"v_mfma_f32_16x16x32_bf16 v[44:47], a[192:195], v[76:79], v[44:47]\n\t"
"s_waitcnt lgkmcnt(4)\n\t"
"v_bfe_u32 v11, %[vT], 3, 2\n\t"
"v_bfe_u32 v12, %[vT], 5, 1\n\t"
"v_mfma_f32_16x16x32_bf16 v[32:35], a[200:203], v[96:99], v[32:35]\n\t"
"s_add_u32 m0, s44, 0\n\t"
"ds_read_b64_tr_b16 a[192:193], v7 offset:45056\n\t"
"v_lshl_add_u32 v11, v11, 1, v12\n\t"
"v_mfma_f32_16x16x32_bf16 v[80:83], a[204:207], v[96:99], v[80:83]\n\t"
"s_addk_i32 s48, 12288\n\t"
"ds_read_b64_tr_b16 a[194:195], v7 offset:47104\n\t"
"v_and_b32 v12, 7, %[vT]\n\t"
"v_mfma_f32_16x16x32_bf16 v[84:87], a[204:207], v[100:103], v[84:87]\n\t"
"v_lshl_add_u32 v11, v11, 3, v12\n\t"
"v_lshlrev_b32 v11, 3, v11\n\t"
"v_mfma_f32_16x16x32_bf16 v[36:39], a[200:203], v[100:103], v[36:39]\n\t"
"buffer_load_dwordx4 v4, s[20:23], s48 offen offset:0 lds\n\t"
"v_mfma_f32_16x16x32_bf16 v[40:43], a[200:203], v[104:107], v[40:43]\n\t"
"s_add_u32 m0, s44, 73728\n\t"
"ds_read_b64_tr_b16 a[196:197], v13 offset:45056\n\t"
"s_addk_i32 s48, 12288\n\t"
"v_mfma_f32_16x16x32_bf16 v[88:91], a[204:207], v[104:107], v[88:91]\n\t"
"buffer_load_dwordx4 v4, s[20:23], s48 offen offset:0 lds\n\t"
"v_mfma_f32_16x16x32_bf16 v[92:95], a[204:207], v[108:111], v[92:95]\n\t"
"ds_read_b64_tr_b16 a[198:199], v13 offset:47104\n\t"
"v_mfma_f32_16x16x32_bf16 v[44:47], a[200:203], v[108:111], v[44:47]\n\t"
"s_waitcnt lgkmcnt(4)\n\t"
"v_mfma_f32_16x16x32_bf16 v[112:115], a[208:211], v[128:131], 0\n\t"
"s_addk_i32 m0, -4096\n\t"
"ds_read_b64_tr_b16 a[200:201], v7 offset:40960\n\t"
"s_addk_i32 s48, 12288\n\t"
"v_mfma_f32_16x16x32_bf16 v[48:51], a[212:215], v[128:131], 0\n\t"
"buffer_load_dwordx4 v4, s[20:23], s48 offen offset:0 lds\n\t"
"v_mfma_f32_16x16x32_bf16 v[52:55], a[212:215], v[132:135], 0\n\t"
"ds_read_b64_tr_b16 a[202:203], v7 offset:43008\n\t"
"v_mfma_f32_16x16x32_bf16 v[116:119], a[208:211], v[132:135], 0\n\t"
"v_mul_f32 v32, v32, v20\n\t"
"v_mfma_f32_16x16x32_bf16 v[120:123], a[208:211], v[136:139], 0\n\t"
"s_addk_i32 m0, -4096\n\t"
"ds_read_b64_tr_b16 a[204:205], v13 offset:40960\n\t"
"s_addk_i32 s48, 12288\n\t"
"v_mfma_f32_16x16x32_bf16 v[56:59], a[212:215], v[136:139], 0\n\t"
"ds_read_b64_tr_b16 a[206:207], v13 offset:43008\n\t"
"buffer_load_dwordx4 v4, s[20:23], s48 offen offset:0 lds\n\t"
"v_mfma_f32_16x16x32_bf16 v[60:63], a[212:215], v[140:143], 0\n\t"
"v_mul_f32 v33, v33, v20\n\t"
"v_mul_f32 v34, v34, v20\n\t"
"v_mfma_f32_16x16x32_bf16 v[124:127], a[208:211], v[140:143], 0\n\t"
"s_waitcnt lgkmcnt(4)\n\t"
"v_mul_f32 v35, v35, v20\n\t"
"v_mul_f32 v80, v80, v20\n\t"
"v_mfma_f32_16x16x32_bf16 v[112:115], a[192:195], v[144:147], v[112:115]\n\t"
"s_addk_i32 m0, -4096\n\t"
"ds_read_b64_tr_b16 a[208:209], v7 offset:36864\n\t"
"v_mul_f32 v81, v81, v20\n\t"
"v_mfma_f32_16x16x32_bf16 v[48:51], a[196:199], v[144:147], v[48:51]\n\t"
"s_mov_b32 s48, 256\n\t"
"ds_read_b64_tr_b16 a[210:211], v7 offset:38912\n\t"
"buffer_load_dwordx4 v4, s[20:23], s48 offen offset:0 lds\n\t"
"v_mul_f32 v82, v82, v20\n\t"
"v_mfma_f32_16x16x32_bf16 v[52:55], a[196:199], v[148:151], v[52:55]\n\t"
"v_mul_f32 v83, v83, v20\n\t"
"v_cvt_pk_bf16_f32 v32, v32, v33\n\t"
"v_mfma_f32_16x16x32_bf16 v[116:119], a[192:195], v[148:151], v[116:119]\n\t"
"v_cvt_pk_bf16_f32 v33, v34, v35\n\t"
"v_cvt_pk_bf16_f32 v34, v80, v81\n\t"
"v_mfma_f32_16x16x32_bf16 v[120:123], a[192:195], v[152:155], v[120:123]\n\t"
"ds_read_b64_tr_b16 a[212:213], v13 offset:36864\n\t"
"v_cvt_pk_bf16_f32 v35, v82, v83\n\t"
"v_mfma_f32_16x16x32_bf16 v[56:59], a[196:199], v[152:155], v[56:59]\n\t"
"s_addk_i32 s52, 4096\n\t"
"ds_read_b64_tr_b16 a[214:215], v13 offset:38912\n\t"
"v_mul_f32 v36, v36, v21\n\t"
"v_mfma_f32_16x16x32_bf16 v[60:63], a[196:199], v[156:159], v[60:63]\n\t"
"buffer_store_dwordx4 v[32:35], v1, s[8:11], s52 offen offset:0 sc1\n\t"
"v_mfma_f32_16x16x32_bf16 v[124:127], a[192:195], v[156:159], v[124:127]\n\t"
"s_waitcnt lgkmcnt(4)\n\t"
"v_mul_f32 v37, v37, v21\n\t"
"v_mul_f32 v38, v38, v21\n\t"
"v_mfma_f32_16x16x32_bf16 v[112:115], a[200:203], v[160:163], v[112:115]\n\t"
"ds_read_b64_tr_b16 a[192:193], v7 offset:32768\n\t"
"v_mul_f32 v39, v39, v21\n\t"
"v_mfma_f32_16x16x32_bf16 v[48:51], a[204:207], v[160:163], v[48:51]\n\t"
"ds_read_b64_tr_b16 a[194:195], v7 offset:34816\n\t"
"v_mul_f32 v84, v84, v21\n\t"
"v_mfma_f32_16x16x32_bf16 v[52:55], a[204:207], v[164:167], v[52:55]\n\t"
"v_mul_f32 v85, v85, v21\n\t"
"v_mul_f32 v86, v86, v21\n\t"
"v_mfma_f32_16x16x32_bf16 v[116:119], a[200:203], v[164:167], v[116:119]\n\t"
"v_mul_f32 v87, v87, v21\n\t"
"v_cvt_pk_bf16_f32 v36, v36, v37\n\t"
"v_mfma_f32_16x16x32_bf16 v[120:123], a[200:203], v[168:171], v[120:123]\n\t"
"ds_read_b64_tr_b16 a[196:197], v13 offset:32768\n\t"
"v_cvt_pk_bf16_f32 v37, v38, v39\n\t"
"v_mfma_f32_16x16x32_bf16 v[56:59], a[204:207], v[168:171], v[56:59]\n\t"
"ds_read_b64_tr_b16 a[198:199], v13 offset:34816\n\t"
"v_cvt_pk_bf16_f32 v38, v84, v85\n\t"
"v_mfma_f32_16x16x32_bf16 v[60:63], a[204:207], v[172:175], v[60:63]\n\t"
"v_cvt_pk_bf16_f32 v39, v86, v87\n\t"
"v_mul_f32 v40, v40, v22\n\t"
"v_mfma_f32_16x16x32_bf16 v[124:127], a[200:203], v[172:175], v[124:127]\n\t"
"s_waitcnt lgkmcnt(4)\n\t"
"buffer_store_dwordx4 v[36:39], v1, s[8:11], s52 offen offset:1024 sc1\n\t"
"v_mfma_f32_16x16x32_bf16 v[112:115], a[208:211], v[176:179], v[112:115]\n\t"
"ds_read_b64_tr_b16 a[200:201], v7 offset:28672\n\t"
"v_mul_f32 v41, v41, v22\n\t"
"v_mfma_f32_16x16x32_bf16 v[48:51], a[212:215], v[176:179], v[48:51]\n\t"
"ds_read_b64_tr_b16 a[202:203], v7 offset:30720\n\t"
"v_mul_f32 v42, v42, v22\n\t"
"v_mfma_f32_16x16x32_bf16 v[52:55], a[212:215], v[180:183], v[52:55]\n\t"
"v_mul_f32 v43, v43, v22\n\t"
"v_mul_f32 v88, v88, v22\n\t"
"v_mfma_f32_16x16x32_bf16 v[116:119], a[208:211], v[180:183], v[116:119]\n\t"
"v_mul_f32 v89, v89, v22\n\t"
"v_mul_f32 v90, v90, v22\n\t"
"v_mfma_f32_16x16x32_bf16 v[120:123], a[208:211], a[0:3], v[120:123]\n\t"
"ds_read_b64_tr_b16 a[204:205], v13 offset:28672\n\t"
"v_mul_f32 v91, v91, v22\n\t"
"v_mfma_f32_16x16x32_bf16 v[56:59], a[212:215], a[0:3], v[56:59]\n\t"
"ds_read_b64_tr_b16 a[206:207], v13 offset:30720\n\t"
"v_cvt_pk_bf16_f32 v40, v40, v41\n\t"
"v_mfma_f32_16x16x32_bf16 v[60:63], a[212:215], a[4:7], v[60:63]\n\t"
"v_cvt_pk_bf16_f32 v41, v42, v43\n\t"
"v_cvt_pk_bf16_f32 v42, v88, v89\n\t"
"v_mfma_f32_16x16x32_bf16 v[124:127], a[208:211], a[4:7], v[124:127]\n\t"
"s_waitcnt lgkmcnt(4)\n\t"
"v_cvt_pk_bf16_f32 v43, v90, v91\n\t"
"v_mul_f32 v44, v44, v23\n\t"
"v_mfma_f32_16x16x32_bf16 v[112:115], a[192:195], v[184:187], v[112:115]\n\t"
"ds_read_b64_tr_b16 a[208:209], v7 offset:24576\n\t"
"buffer_store_dwordx4 v[40:43], v1, s[8:11], s52 offen offset:2048 sc1\n\t"
"v_mfma_f32_16x16x32_bf16 v[48:51], a[196:199], v[184:187], v[48:51]\n\t"
"ds_read_b64_tr_b16 a[210:211], v7 offset:26624\n\t"
"v_mul_f32 v45, v45, v23\n\t"
"v_mfma_f32_16x16x32_bf16 v[52:55], a[196:199], v[188:191], v[52:55]\n\t"
"v_mul_f32 v46, v46, v23\n\t"
"v_mul_f32 v47, v47, v23\n\t"
"v_mfma_f32_16x16x32_bf16 v[116:119], a[192:195], v[188:191], v[116:119]\n\t"
"v_mul_f32 v92, v92, v23\n\t"
"v_mul_f32 v93, v93, v23\n\t"
"v_mfma_f32_16x16x32_bf16 v[120:123], a[192:195], a[8:11], v[120:123]\n\t"
"ds_read_b64_tr_b16 a[212:213], v13 offset:24576\n\t"
"v_mul_f32 v94, v94, v23\n\t"
"v_mfma_f32_16x16x32_bf16 v[56:59], a[196:199], a[8:11], v[56:59]\n\t"
"ds_read_b64_tr_b16 a[214:215], v13 offset:26624\n\t"
"v_mul_f32 v95, v95, v23\n\t"
"v_mfma_f32_16x16x32_bf16 v[60:63], a[196:199], a[12:15], v[60:63]\n\t"
"v_cvt_pk_bf16_f32 v44, v44, v45\n\t"
"v_cvt_pk_bf16_f32 v45, v46, v47\n\t"
"v_mfma_f32_16x16x32_bf16 v[124:127], a[192:195], a[12:15], v[124:127]\n\t"
"s_waitcnt lgkmcnt(4)\n\t"
"v_cvt_pk_bf16_f32 v46, v92, v93\n\t"
"v_cvt_pk_bf16_f32 v47, v94, v95\n\t"
"v_mfma_f32_16x16x32_bf16 v[112:115], a[200:203], v[192:195], v[112:115]\n\t"
"ds_read_b64_tr_b16 a[192:193], v7 offset:20480\n\t"
"v_accvgpr_write_b32 a144, v32\n\t"
"v_mfma_f32_16x16x32_bf16 v[48:51], a[204:207], v[192:195], v[48:51]\n\t"
"ds_read_b64_tr_b16 a[194:195], v7 offset:22528\n\t"
"buffer_store_dwordx4 v[44:47], v1, s[8:11], s52 offen offset:3072 sc1\n\t"
"v_mfma_f32_16x16x32_bf16 v[52:55], a[204:207], v[196:199], v[52:55]\n\t"
"v_accvgpr_write_b32 a145, v33\n\t"
"v_accvgpr_write_b32 a146, v34\n\t"
"v_mfma_f32_16x16x32_bf16 v[116:119], a[200:203], v[196:199], v[116:119]\n\t"
"v_accvgpr_write_b32 a147, v35\n\t"
"v_accvgpr_write_b32 a148, v36\n\t"
"v_mfma_f32_16x16x32_bf16 v[120:123], a[200:203], a[16:19], v[120:123]\n\t"
"ds_read_b64_tr_b16 a[196:197], v13 offset:20480\n\t"
"v_accvgpr_write_b32 a149, v37\n\t"
"v_mfma_f32_16x16x32_bf16 v[56:59], a[204:207], a[16:19], v[56:59]\n\t"
"ds_read_b64_tr_b16 a[198:199], v13 offset:22528\n\t"
"v_accvgpr_write_b32 a150, v38\n\t"
"v_mfma_f32_16x16x32_bf16 v[60:63], a[204:207], a[20:23], v[60:63]\n\t"
"v_accvgpr_write_b32 a151, v39\n\t"
"v_accvgpr_write_b32 a152, v40\n\t"
"v_mfma_f32_16x16x32_bf16 v[124:127], a[200:203], a[20:23], v[124:127]\n\t"
"s_waitcnt vmcnt(9)\n\t"
"v_accvgpr_write_b32 a153, v41\n\t"
"v_accvgpr_write_b32 a154, v42\n\t"
"s_barrier\n\t"
"s_waitcnt lgkmcnt(4)\n\t"
"v_mfma_f32_16x16x32_bf16 v[112:115], a[208:211], v[200:203], v[112:115]\n\t"
"s_addk_i32 s48, 12288\n\t"
"ds_read_b64_tr_b16 a[200:201], v7 offset:16384\n\t"
"v_accvgpr_write_b32 a155, v43\n\t"
"v_mfma_f32_16x16x32_bf16 v[48:51], a[212:215], v[200:203], v[48:51]\n\t"
"s_addk_i32 m0, -4096\n\t"
"ds_read_b64_tr_b16 a[202:203], v7 offset:18432\n\t"
"v_accvgpr_write_b32 a156, v44\n\t"
"v_mfma_f32_16x16x32_bf16 v[52:55], a[212:215], v[204:207], v[52:55]\n\t"
"v_accvgpr_write_b32 a157, v45\n\t"
"v_accvgpr_write_b32 a158, v46\n\t"
"v_mfma_f32_16x16x32_bf16 v[116:119], a[208:211], v[204:207], v[116:119]\n\t"
"buffer_load_dwordx4 v4, s[20:23], s48 offen offset:0 lds\n\t"
"v_mfma_f32_16x16x32_bf16 v[120:123], a[208:211], a[24:27], v[120:123]\n\t"
"ds_read_b64_tr_b16 a[204:205], v13 offset:16384\n\t"
"v_accvgpr_write_b32 a159, v47\n\t"
"v_mfma_f32_16x16x32_bf16 v[56:59], a[212:215], a[24:27], v[56:59]\n\t"
"s_addk_i32 m0, -4096\n\t"
"ds_read_b64_tr_b16 a[206:207], v13 offset:18432\n\t"
"s_addk_i32 s48, 12288\n\t"
"v_mfma_f32_16x16x32_bf16 v[60:63], a[212:215], a[28:31], v[60:63]\n\t"
"buffer_load_dwordx4 v4, s[20:23], s48 offen offset:0 lds\n\t"
"v_mfma_f32_16x16x32_bf16 v[124:127], a[208:211], a[28:31], v[124:127]\n\t"
"s_waitcnt lgkmcnt(4)\n\t"
"v_mfma_f32_16x16x32_bf16 v[112:115], a[192:195], v[208:211], v[112:115]\n\t"
"ds_read_b64_tr_b16 a[208:209], v7 offset:12288\n\t"
"v_mfma_f32_16x16x32_bf16 v[48:51], a[196:199], v[208:211], v[48:51]\n\t"
"ds_read_b64_tr_b16 a[210:211], v7 offset:14336\n\t"
"s_add_u32 m0, s73, 79616\n\t"
"v_mfma_f32_16x16x32_bf16 v[52:55], a[196:199], v[212:215], v[52:55]\n\t"
"buffer_load_dwordx4 v2, s[24:27], s61 offen offset:0 lds\n\t"
"v_mfma_f32_16x16x32_bf16 v[116:119], a[192:195], v[212:215], v[116:119]\n\t"
"v_mfma_f32_16x16x32_bf16 v[120:123], a[192:195], a[32:35], v[120:123]\n\t"
"ds_read_b64_tr_b16 a[212:213], v13 offset:12288\n\t"
"v_mfma_f32_16x16x32_bf16 v[56:59], a[196:199], a[32:35], v[56:59]\n\t"
"ds_read_b64_tr_b16 a[214:215], v13 offset:14336\n\t"
"v_mfma_f32_16x16x32_bf16 v[60:63], a[196:199], a[36:39], v[60:63]\n\t"
"v_mfma_f32_16x16x32_bf16 v[124:127], a[192:195], a[36:39], v[124:127]\n\t"
"s_waitcnt lgkmcnt(4)\n\t"
"v_mfma_f32_16x16x32_bf16 v[112:115], a[200:203], v[216:219], v[112:115]\n\t"
"ds_read_b64_tr_b16 a[192:193], v7 offset:8192\n\t"
"v_mfma_f32_16x16x32_bf16 v[48:51], a[204:207], v[216:219], v[48:51]\n\t"
"ds_read_b64_tr_b16 a[194:195], v7 offset:10240\n\t"
"s_addk_i32 m0, 832\n\t"
"v_mfma_f32_16x16x32_bf16 v[52:55], a[204:207], v[220:223], v[52:55]\n\t"
"buffer_load_dwordx4 v2, s[24:27], s61 offen offset:1280 lds\n\t"
"v_mfma_f32_16x16x32_bf16 v[116:119], a[200:203], v[220:223], v[116:119]\n\t"
"v_mfma_f32_16x16x32_bf16 v[120:123], a[200:203], a[40:43], v[120:123]\n\t"
"ds_read_b64_tr_b16 a[196:197], v13 offset:8192\n\t"
"v_mfma_f32_16x16x32_bf16 v[56:59], a[204:207], a[40:43], v[56:59]\n\t"
"ds_read_b64_tr_b16 a[198:199], v13 offset:10240\n\t"
"v_mfma_f32_16x16x32_bf16 v[60:63], a[204:207], a[44:47], v[60:63]\n\t"
"v_mfma_f32_16x16x32_bf16 v[124:127], a[200:203], a[44:47], v[124:127]\n\t"
"s_waitcnt lgkmcnt(4)\n\t"
"v_mfma_f32_16x16x32_bf16 v[112:115], a[208:211], v[224:227], v[112:115]\n\t"
"ds_read_b64_tr_b16 a[200:201], v7 offset:4096\n\t"
"v_mfma_f32_16x16x32_bf16 v[48:51], a[212:215], v[224:227], v[48:51]\n\t"
"ds_read_b64_tr_b16 a[202:203], v7 offset:6144\n\t"
"s_addk_i32 m0, 832\n\t"
"v_mfma_f32_16x16x32_bf16 v[52:55], a[212:215], v[228:231], v[52:55]\n\t"
"buffer_load_dwordx4 v2, s[24:27], s61 offen offset:2560 lds\n\t"
"v_mfma_f32_16x16x32_bf16 v[116:119], a[208:211], v[228:231], v[116:119]\n\t"
"v_mfma_f32_16x16x32_bf16 v[120:123], a[208:211], a[48:51], v[120:123]\n\t"
"ds_read_b64_tr_b16 a[204:205], v13 offset:4096\n\t"
"v_mfma_f32_16x16x32_bf16 v[56:59], a[212:215], a[48:51], v[56:59]\n\t"
"ds_read_b64_tr_b16 a[206:207], v13 offset:6144\n\t"
"v_mfma_f32_16x16x32_bf16 v[60:63], a[212:215], a[52:55], v[60:63]\n\t"
"s_waitcnt vmcnt(10)\n\t"
"v_mfma_f32_16x16x32_bf16 v[124:127], a[208:211], a[52:55], v[124:127]\n\t"
"s_barrier\n\t"
"s_waitcnt lgkmcnt(4)\n\t"
"v_mfma_f32_16x16x32_bf16 v[112:115], a[192:195], v[232:235], v[112:115]\n\t"
"ds_read_b64_tr_b16 a[208:209], v7 offset:0\n\t"
"v_mfma_f32_16x16x32_bf16 v[48:51], a[196:199], v[232:235], v[48:51]\n\t"
"ds_read_b64_tr_b16 a[210:211], v7 offset:2048\n\t"
"s_addk_i32 m0, 832\n\t"
"v_mfma_f32_16x16x32_bf16 v[52:55], a[196:199], v[236:239], v[52:55]\n\t"
"buffer_load_dwordx4 v2, s[24:27], s61 offen offset:3840 lds\n\t"
"v_mfma_f32_16x16x32_bf16 v[116:119], a[192:195], v[236:239], v[116:119]\n\t"
"v_mfma_f32_16x16x32_bf16 v[120:123], a[192:195], a[56:59], v[120:123]\n\t"
"ds_read_b64_tr_b16 a[212:213], v13 offset:0\n\t"
"v_mfma_f32_16x16x32_bf16 v[56:59], a[196:199], a[56:59], v[56:59]\n\t"
"ds_read_b64_tr_b16 a[214:215], v13 offset:2048\n\t"
"v_mfma_f32_16x16x32_bf16 v[60:63], a[196:199], a[60:63], v[60:63]\n\t"
"v_mfma_f32_16x16x32_bf16 v[124:127], a[192:195], a[60:63], v[124:127]\n\t"
"s_waitcnt lgkmcnt(4)\n\t"
"v_mfma_f32_16x16x32_bf16 v[112:115], a[200:203], v[240:243], v[112:115]\n\t"
"ds_read_b64_tr_b16 a[192:193], v16 offset:8192\n\t"
"v_mfma_f32_16x16x32_bf16 v[48:51], a[204:207], v[240:243], v[48:51]\n\t"
"ds_read_b64_tr_b16 a[194:195], v16 offset:10240\n\t"
"s_addk_i32 m0, 5888\n\t"
"v_mfma_f32_16x16x32_bf16 v[52:55], a[204:207], v[244:247], v[52:55]\n\t"
"buffer_load_dwordx4 v9, s[24:27], s61 offen offset:64 lds\n\t"
"v_mfma_f32_16x16x32_bf16 v[116:119], a[200:203], v[244:247], v[116:119]\n\t"
"v_mfma_f32_16x16x32_bf16 v[120:123], a[200:203], a[64:67], v[120:123]\n\t"
"ds_read_b64_tr_b16 a[196:197], v17 offset:8192\n\t"
"v_mfma_f32_16x16x32_bf16 v[56:59], a[204:207], a[64:67], v[56:59]\n\t"
"ds_read_b64_tr_b16 a[198:199], v17 offset:10240\n\t"
"s_sub_u32 s98, s39, 6\n\t"
"v_mfma_f32_16x16x32_bf16 v[60:63], a[204:207], a[68:71], v[60:63]\n\t"
"v_accvgpr_write_b32 a232, s98\n\t"
"v_mfma_f32_16x16x32_bf16 v[124:127], a[200:203], a[68:71], v[124:127]\n\t"
"s_waitcnt lgkmcnt(4)\n\t"
"ds_read_b64_tr_b16 a[200:201], v16 offset:4096\n\t"
"v_mfma_f32_16x16x32_bf16 v[112:115], a[208:211], v[248:251], v[112:115]\n\t"
"s_waitcnt vmcnt(7)\n\t"
"s_barrier\n\t"
"v_mfma_f32_16x16x32_bf16 v[48:51], a[212:215], v[248:251], v[48:51]\n\t"
"buffer_store_dword a232, v28, s[8:11], 0 offen offset:0 sc1\n\t"
"v_mfma_f32_16x16x32_bf16 v[52:55], a[212:215], v[252:255], v[52:55]\n\t"
"ds_read_b64_tr_b16 a[202:203], v16 offset:6144\n\t"
"v_mfma_f32_16x16x32_bf16 v[116:119], a[208:211], v[252:255], v[116:119]\n\t"
"v_mfma_f32_16x16x32_bf16 v[120:123], a[208:211], a[72:75], v[120:123]\n\t"
"ds_read_b64_tr_b16 a[204:205], v17 offset:4096\n\t"
"v_mfma_f32_16x16x32_bf16 v[56:59], a[212:215], a[72:75], v[56:59]\n\t"
"ds_read_b64_tr_b16 a[206:207], v17 offset:6144\n\t"
"v_mfma_f32_16x16x32_bf16 v[60:63], a[212:215], a[76:79], v[60:63]\n\t"
"v_mfma_f32_16x16x32_bf16 v[124:127], a[208:211], a[76:79], v[124:127]\n\t"
"s_waitcnt lgkmcnt(4)\n\t"
"v_mfma_f32_16x16x32_bf16 v[112:115], a[192:195], a[80:83], v[112:115]\n\t"
"ds_read_b64_tr_b16 a[208:209], v16 offset:0\n\t"
"v_mfma_f32_16x16x32_bf16 v[48:51], a[196:199], a[80:83], v[48:51]\n\t"
"ds_read_b64_tr_b16 a[210:211], v16 offset:2048\n\t"
"v_mfma_f32_16x16x32_bf16 v[52:55], a[196:199], a[84:87], v[52:55]\n\t"
"v_mfma_f32_16x16x32_bf16 v[116:119], a[192:195], a[84:87], v[116:119]\n\t"
"v_mfma_f32_16x16x32_bf16 v[120:123], a[192:195], a[88:91], v[120:123]\n\t"
"ds_read_b64_tr_b16 a[212:213], v17 offset:0\n\t"
"v_mfma_f32_16x16x32_bf16 v[56:59], a[196:199], a[88:91], v[56:59]\n\t"
"ds_read_b64_tr_b16 a[214:215], v17 offset:2048\n\t"
"v_mfma_f32_16x16x32_bf16 v[60:63], a[196:199], a[92:95], v[60:63]\n\t"
"v_mfma_f32_16x16x32_bf16 v[124:127], a[192:195], a[92:95], v[124:127]\n\t"
"s_waitcnt lgkmcnt(4)\n\t"
"v_mfma_f32_16x16x32_bf16 v[112:115], a[200:203], v[64:67], v[112:115]\n\t"
"ds_read_b64_tr_b16 a[192:193], v14 offset:49152\n\t"
"v_mfma_f32_16x16x32_bf16 v[48:51], a[204:207], v[64:67], v[48:51]\n\t"
"ds_read_b64_tr_b16 a[194:195], v14 offset:51200\n\t"
"v_mfma_f32_16x16x32_bf16 v[52:55], a[204:207], v[68:71], v[52:55]\n\t"
"v_mfma_f32_16x16x32_bf16 v[116:119], a[200:203], v[68:71], v[116:119]\n\t"
"v_mfma_f32_16x16x32_bf16 v[120:123], a[200:203], v[72:75], v[120:123]\n\t"
"ds_read_b64_tr_b16 a[196:197], v15 offset:49152\n\t"
"v_mfma_f32_16x16x32_bf16 v[56:59], a[204:207], v[72:75], v[56:59]\n\t"
"ds_read_b64_tr_b16 a[198:199], v15 offset:51200\n\t"
"v_mfma_f32_16x16x32_bf16 v[60:63], a[204:207], v[76:79], v[60:63]\n\t"
"v_mfma_f32_16x16x32_bf16 v[124:127], a[200:203], v[76:79], v[124:127]\n\t"
"s_waitcnt lgkmcnt(4)\n\t"
"v_mfma_f32_16x16x32_bf16 v[112:115], a[208:211], v[96:99], v[112:115]\n\t"
"ds_read_b64_tr_b16 a[200:201], v14 offset:45056\n\t"
"v_mfma_f32_16x16x32_bf16 v[48:51], a[212:215], v[96:99], v[48:51]\n\t"
"ds_read_b64_tr_b16 a[202:203], v14 offset:47104\n\t"
"v_mfma_f32_16x16x32_bf16 v[52:55], a[212:215], v[100:103], v[52:55]\n\t"
"v_mfma_f32_16x16x32_bf16 v[116:119], a[208:211], v[100:103], v[116:119]\n\t"
"v_mfma_f32_16x16x32_bf16 v[120:123], a[208:211], v[104:107], v[120:123]\n\t"
"ds_read_b64_tr_b16 a[204:205], v15 offset:45056\n\t"
"v_mfma_f32_16x16x32_bf16 v[56:59], a[212:215], v[104:107], v[56:59]\n\t"
"ds_read_b64_tr_b16 a[206:207], v15 offset:47104\n\t"
"v_mfma_f32_16x16x32_bf16 v[60:63], a[212:215], v[108:111], v[60:63]\n\t"
"v_mfma_f32_16x16x32_bf16 v[124:127], a[208:211], v[108:111], v[124:127]\n\t"
"s_waitcnt lgkmcnt(4)\n\t"
"v_mfma_f32_16x16x32_bf16 v[32:35], a[192:195], v[128:131], 0\n\t"
"ds_read_b64_tr_b16 a[208:209], v14 offset:40960\n\t"
"v_mfma_f32_16x16x32_bf16 v[80:83], a[196:199], v[128:131], 0\n\t"
"ds_read_b64_tr_b16 a[210:211], v14 offset:43008\n\t"
"v_mfma_f32_16x16x32_bf16 v[84:87], a[196:199], v[132:135], 0\n\t"
"v_mfma_f32_16x16x32_bf16 v[36:39], a[192:195], v[132:135], 0\n\t"
"v_mfma_f32_16x16x32_bf16 v[40:43], a[192:195], v[136:139], 0\n\t"
"ds_read_b64_tr_b16 a[212:213], v15 offset:40960\n\t"
"v_mfma_f32_16x16x32_bf16 v[88:91], a[196:199], v[136:139], 0\n\t"
"ds_read_b64_tr_b16 a[214:215], v15 offset:43008\n\t"
"v_mul_f32 v112, v112, v20\n\t"
"v_mfma_f32_16x16x32_bf16 v[92:95], a[196:199], v[140:143], 0\n\t"
"v_mul_f32 v113, v113, v20\n\t"
"v_mul_f32 v114, v114, v20\n\t"
"v_mfma_f32_16x16x32_bf16 v[44:47], a[192:195], v[140:143], 0\n\t"
"s_waitcnt lgkmcnt(4)\n\t"
"v_mul_f32 v115, v115, v20\n\t"
"v_mul_f32 v48, v48, v20\n\t"
"v_mfma_f32_16x16x32_bf16 v[32:35], a[200:203], v[144:147], v[32:35]\n\t"
"ds_read_b64_tr_b16 a[192:193], v14 offset:36864\n\t"
"v_mul_f32 v49, v49, v20\n\t"
"v_mfma_f32_16x16x32_bf16 v[80:83], a[204:207], v[144:147], v[80:83]\n\t"
"ds_read_b64_tr_b16 a[194:195], v14 offset:38912\n\t"
"v_mul_f32 v50, v50, v20\n\t"
"v_mfma_f32_16x16x32_bf16 v[84:87], a[204:207], v[148:151], v[84:87]\n\t"
"v_mul_f32 v51, v51, v20\n\t"
"v_cvt_pk_bf16_f32 v112, v112, v113\n\t"
"v_mfma_f32_16x16x32_bf16 v[36:39], a[200:203], v[148:151], v[36:39]\n\t"
"v_cvt_pk_bf16_f32 v113, v114, v115\n\t"
"v_cvt_pk_bf16_f32 v114, v48, v49\n\t"
"v_mfma_f32_16x16x32_bf16 v[40:43], a[200:203], v[152:155], v[40:43]\n\t"
"ds_read_b64_tr_b16 a[196:197], v15 offset:36864\n\t"
"v_cvt_pk_bf16_f32 v115, v50, v51\n\t"
"v_mfma_f32_16x16x32_bf16 v[88:91], a[204:207], v[152:155], v[88:91]\n\t"
"s_addk_i32 s52, 4096\n\t"
"ds_read_b64_tr_b16 a[198:199], v15 offset:38912\n\t"
"v_mul_f32 v116, v116, v21\n\t"
"v_mfma_f32_16x16x32_bf16 v[92:95], a[204:207], v[156:159], v[92:95]\n\t"
"buffer_store_dwordx4 v[112:115], v1, s[8:11], s52 offen offset:0 sc1\n\t"
"v_mfma_f32_16x16x32_bf16 v[44:47], a[200:203], v[156:159], v[44:47]\n\t"
"s_waitcnt lgkmcnt(4)\n\t"
"v_mul_f32 v117, v117, v21\n\t"
"v_mul_f32 v118, v118, v21\n\t"
"v_mfma_f32_16x16x32_bf16 v[32:35], a[208:211], v[160:163], v[32:35]\n\t"
"ds_read_b64_tr_b16 a[200:201], v14 offset:32768\n\t"
"v_mul_f32 v119, v119, v21\n\t"
"v_mfma_f32_16x16x32_bf16 v[80:83], a[212:215], v[160:163], v[80:83]\n\t"
"ds_read_b64_tr_b16 a[202:203], v14 offset:34816\n\t"
"v_mul_f32 v52, v52, v21\n\t"
"v_mfma_f32_16x16x32_bf16 v[84:87], a[212:215], v[164:167], v[84:87]\n\t"
"v_mul_f32 v53, v53, v21\n\t"
"v_mul_f32 v54, v54, v21\n\t"
"v_mfma_f32_16x16x32_bf16 v[36:39], a[208:211], v[164:167], v[36:39]\n\t"
"v_mul_f32 v55, v55, v21\n\t"
"v_cvt_pk_bf16_f32 v116, v116, v117\n\t"
"v_mfma_f32_16x16x32_bf16 v[40:43], a[208:211], v[168:171], v[40:43]\n\t"
"ds_read_b64_tr_b16 a[204:205], v15 offset:32768\n\t"
"v_cvt_pk_bf16_f32 v117, v118, v119\n\t"
"v_mfma_f32_16x16x32_bf16 v[88:91], a[212:215], v[168:171], v[88:91]\n\t"
"ds_read_b64_tr_b16 a[206:207], v15 offset:34816\n\t"
"v_cvt_pk_bf16_f32 v118, v52, v53\n\t"
"v_mfma_f32_16x16x32_bf16 v[92:95], a[212:215], v[172:175], v[92:95]\n\t"
"v_cvt_pk_bf16_f32 v119, v54, v55\n\t"
"v_mul_f32 v120, v120, v22\n\t"
"v_mfma_f32_16x16x32_bf16 v[44:47], a[208:211], v[172:175], v[44:47]\n\t"
"s_waitcnt lgkmcnt(4)\n\t"
"buffer_store_dwordx4 v[116:119], v1, s[8:11], s52 offen offset:1024 sc1\n\t"
"v_mfma_f32_16x16x32_bf16 v[32:35], a[192:195], v[176:179], v[32:35]\n\t"
"ds_read_b64_tr_b16 a[208:209], v14 offset:28672\n\t"
"v_mul_f32 v121, v121, v22\n\t"
"v_mfma_f32_16x16x32_bf16 v[80:83], a[196:199], v[176:179], v[80:83]\n\t"
"ds_read_b64_tr_b16 a[210:211], v14 offset:30720\n\t"
"v_mul_f32 v122, v122, v22\n\t"
"v_mfma_f32_16x16x32_bf16 v[84:87], a[196:199], v[180:183], v[84:87]\n\t"
"v_mul_f32 v123, v123, v22\n\t"
"v_mul_f32 v56, v56, v22\n\t"
"v_mfma_f32_16x16x32_bf16 v[36:39], a[192:195], v[180:183], v[36:39]\n\t"
"v_mul_f32 v57, v57, v22\n\t"
"v_mul_f32 v58, v58, v22\n\t"
"v_mfma_f32_16x16x32_bf16 v[40:43], a[192:195], a[0:3], v[40:43]\n\t"
"ds_read_b64_tr_b16 a[212:213], v15 offset:28672\n\t"
"v_mul_f32 v59, v59, v22\n\t"
"v_mfma_f32_16x16x32_bf16 v[88:91], a[196:199], a[0:3], v[88:91]\n\t"
"ds_read_b64_tr_b16 a[214:215], v15 offset:30720\n\t"
"v_cvt_pk_bf16_f32 v120, v120, v121\n\t"
"v_mfma_f32_16x16x32_bf16 v[92:95], a[196:199], a[4:7], v[92:95]\n\t"
"v_cvt_pk_bf16_f32 v121, v122, v123\n\t"
"v_cvt_pk_bf16_f32 v122, v56, v57\n\t"
"v_mfma_f32_16x16x32_bf16 v[44:47], a[192:195], a[4:7], v[44:47]\n\t"
"s_waitcnt lgkmcnt(4)\n\t"
"v_cvt_pk_bf16_f32 v123, v58, v59\n\t"
"v_mul_f32 v124, v124, v23\n\t"
".Lpg11%=:\n"
"s_cbranch_execz .Lpg15%=\n\t"
"v_mfma_f32_16x16x32_bf16 v[32:35], a[200:203], v[184:187], v[32:35]\n\t"
"ds_read_b64_tr_b16 a[192:193], v14 offset:24576\n\t"
"buffer_store_dwordx4 v[120:123], v1, s[8:11], s52 offen offset:2048 sc1\n\t"
"v_mfma_f32_16x16x32_bf16 v[80:83], a[204:207], v[184:187], v[80:83]\n\t"
"ds_read_b64_tr_b16 a[194:195], v14 offset:26624\n\t"
"v_mul_f32 v125, v125, v23\n\t"
"s_barrier\n\t"
"v_mfma_f32_16x16x32_bf16 v[84:87], a[204:207], v[188:191], v[84:87]\n\t"
"v_mul_f32 v126, v126, v23\n\t"
"v_mul_f32 v127, v127, v23\n\t"
"v_mfma_f32_16x16x32_bf16 v[36:39], a[200:203], v[188:191], v[36:39]\n\t"
"v_mul_f32 v60, v60, v23\n\t"
"v_mul_f32 v61, v61, v23\n\t"
"v_mfma_f32_16x16x32_bf16 v[40:43], a[200:203], a[8:11], v[40:43]\n\t"
"ds_read_b64_tr_b16 a[196:197], v15 offset:24576\n\t"
"v_mul_f32 v62, v62, v23\n\t"
"v_mfma_f32_16x16x32_bf16 v[88:91], a[204:207], a[8:11], v[88:91]\n\t"
"ds_read_b64_tr_b16 a[198:199], v15 offset:26624\n\t"
"v_mul_f32 v63, v63, v23\n\t"
"v_mfma_f32_16x16x32_bf16 v[92:95], a[204:207], a[12:15], v[92:95]\n\t"
"v_cvt_pk_bf16_f32 v124, v124, v125\n\t"
"v_cvt_pk_bf16_f32 v125, v126, v127\n\t"
"v_mfma_f32_16x16x32_bf16 v[44:47], a[200:203], a[12:15], v[44:47]\n\t"
"s_waitcnt lgkmcnt(4)\n\t"
"v_cvt_pk_bf16_f32 v126, v60, v61\n\t"
"v_cvt_pk_bf16_f32 v127, v62, v63\n\t"
"v_mfma_f32_16x16x32_bf16 v[32:35], a[208:211], v[192:195], v[32:35]\n\t"
"ds_read_b64_tr_b16 a[200:201], v14 offset:20480\n\t"
"v_accvgpr_write_b32 a160, v112\n\t"
"v_mfma_f32_16x16x32_bf16 v[80:83], a[212:215], v[192:195], v[80:83]\n\t"
"ds_read_b64_tr_b16 a[202:203], v14 offset:22528\n\t"
"buffer_store_dwordx4 v[124:127], v1, s[8:11], s52 offen offset:3072 sc1\n\t"
"v_mfma_f32_16x16x32_bf16 v[84:87], a[212:215], v[196:199], v[84:87]\n\t"
"v_accvgpr_write_b32 a161, v113\n\t"
"v_accvgpr_write_b32 a162, v114\n\t"
"v_mfma_f32_16x16x32_bf16 v[36:39], a[208:211], v[196:199], v[36:39]\n\t"
"v_accvgpr_write_b32 a163, v115\n\t"
"v_accvgpr_write_b32 a164, v116\n\t"
"v_mfma_f32_16x16x32_bf16 v[40:43], a[208:211], a[16:19], v[40:43]\n\t"
"ds_read_b64_tr_b16 a[204:205], v15 offset:20480\n\t"
"v_accvgpr_write_b32 a165, v117\n\t"
"v_mfma_f32_16x16x32_bf16 v[88:91], a[212:215], a[16:19], v[88:91]\n\t"
"ds_read_b64_tr_b16 a[206:207], v15 offset:22528\n\t"
"v_accvgpr_write_b32 a166, v118\n\t"
"v_mfma_f32_16x16x32_bf16 v[92:95], a[212:215], a[20:23], v[92:95]\n\t"
"v_accvgpr_write_b32 a167, v119\n\t"
"v_accvgpr_write_b32 a168, v120\n\t"
"v_mfma_f32_16x16x32_bf16 v[44:47], a[208:211], a[20:23], v[44:47]\n\t"
"s_waitcnt lgkmcnt(4)\n\t"
"v_accvgpr_write_b32 a169, v121\n\t"
"v_accvgpr_write_b32 a170, v122\n\t"
"v_mfma_f32_16x16x32_bf16 v[32:35], a[192:195], v[200:203], v[32:35]\n\t"
"s_addk_i32 s48, 12288\n\t"
"ds_read_b64_tr_b16 a[208:209], v14 offset:16384\n\t"
"v_accvgpr_write_b32 a171, v123\n\t"
"v_mfma_f32_16x16x32_bf16 v[80:83], a[196:199], v[200:203], v[80:83]\n\t"
"s_add_u32 m0, s44, 49152\n\t"
"ds_read_b64_tr_b16 a[210:211], v14 offset:18432\n\t"
"v_accvgpr_write_b32 a172, v124\n\t"
"v_mfma_f32_16x16x32_bf16 v[84:87], a[196:199], v[204:207], v[84:87]\n\t"
"v_accvgpr_write_b32 a173, v125\n\t"
"v_accvgpr_write_b32 a174, v126\n\t"
"v_mfma_f32_16x16x32_bf16 v[36:39], a[192:195], v[204:207], v[36:39]\n\t"
"buffer_load_dwordx4 v4, s[20:23], s48 offen offset:0 lds\n\t"
"v_mfma_f32_16x16x32_bf16 v[40:43], a[192:195], a[24:27], v[40:43]\n\t"
"ds_read_b64_tr_b16 a[212:213], v15 offset:16384\n\t"
"v_accvgpr_write_b32 a175, v127\n\t"
"v_mfma_f32_16x16x32_bf16 v[88:91], a[196:199], a[24:27], v[88:91]\n\t"
"s_addk_i32 m0, -4096\n\t"
"ds_read_b64_tr_b16 a[214:215], v15 offset:18432\n\t"
"s_addk_i32 s48, 12288\n\t"
"v_mfma_f32_16x16x32_bf16 v[92:95], a[196:199], a[28:31], v[92:95]\n\t"
"buffer_load_dwordx4 v4, s[20:23], s48 offen offset:0 lds\n\t"
"v_mfma_f32_16x16x32_bf16 v[44:47], a[192:195], a[28:31], v[44:47]\n\t"
"s_waitcnt lgkmcnt(4)\n\t"
"v_mfma_f32_16x16x32_bf16 v[32:35], a[200:203], v[208:211], v[32:35]\n\t"
"ds_read_b64_tr_b16 a[192:193], v14 offset:12288\n\t"
"v_mfma_f32_16x16x32_bf16 v[80:83], a[204:207], v[208:211], v[80:83]\n\t"
"ds_read_b64_tr_b16 a[194:195], v14 offset:14336\n\t"
"v_mfma_f32_16x16x32_bf16 v[84:87], a[204:207], v[212:215], v[84:87]\n\t"
"buffer_load_dword v30, v10, s[8:11], 0 offen offset:0 sc1\n\t"
"v_mfma_f32_16x16x32_bf16 v[36:39], a[200:203], v[212:215], v[36:39]\n\t"
"s_addk_i32 m0, -4096\n\t"
"s_addk_i32 s48, 12288\n\t"
"ds_read_b64_tr_b16 a[196:197], v15 offset:12288\n\t"
"v_mfma_f32_16x16x32_bf16 v[40:43], a[200:203], a[32:35], v[40:43]\n\t"
"buffer_load_dwordx4 v4, s[20:23], s48 offen offset:0 lds\n\t"
"v_mfma_f32_16x16x32_bf16 v[88:91], a[204:207], a[32:35], v[88:91]\n\t"
"ds_read_b64_tr_b16 a[198:199], v15 offset:14336\n\t"
"v_mfma_f32_16x16x32_bf16 v[92:95], a[204:207], a[36:39], v[92:95]\n\t"
"v_mfma_f32_16x16x32_bf16 v[44:47], a[200:203], a[36:39], v[44:47]\n\t"
"s_addk_i32 m0, -4096\n\t"
"s_addk_i32 s48, 12288\n\t"
"s_waitcnt lgkmcnt(4)\n\t"
"v_mfma_f32_16x16x32_bf16 v[32:35], a[208:211], v[216:219], v[32:35]\n\t"
"ds_read_b64_tr_b16 a[200:201], v14 offset:8192\n\t"
"buffer_load_dwordx4 v4, s[20:23], s48 offen offset:0 lds\n\t"
"v_mfma_f32_16x16x32_bf16 v[80:83], a[212:215], v[216:219], v[80:83]\n\t"
"ds_read_b64_tr_b16 a[202:203], v14 offset:10240\n\t"
"s_barrier\n\t"
"v_mfma_f32_16x16x32_bf16 v[84:87], a[212:215], v[220:223], v[84:87]\n\t"
"v_mfma_f32_16x16x32_bf16 v[36:39], a[208:211], v[220:223], v[36:39]\n\t"
"s_addk_i32 m0, -4096\n\t"
"s_addk_i32 s48, 12288\n\t"
"ds_read_b64_tr_b16 a[204:205], v15 offset:8192\n\t"
"v_mfma_f32_16x16x32_bf16 v[40:43], a[208:211], a[40:43], v[40:43]\n\t"
"buffer_load_dwordx4 v4, s[20:23], s48 offen offset:0 lds\n\t"
"v_mfma_f32_16x16x32_bf16 v[88:91], a[212:215], a[40:43], v[88:91]\n\t"
"ds_read_b64_tr_b16 a[206:207], v15 offset:10240\n\t"
"v_mfma_f32_16x16x32_bf16 v[92:95], a[212:215], a[44:47], v[92:95]\n\t"
"v_mfma_f32_16x16x32_bf16 v[44:47], a[208:211], a[44:47], v[44:47]\n\t"
"s_addk_i32 m0, -4096\n\t"
"s_addk_i32 s48, 12288\n\t"
"s_waitcnt lgkmcnt(4)\n\t"
"v_mfma_f32_16x16x32_bf16 v[32:35], a[192:195], v[224:227], v[32:35]\n\t"
"ds_read_b64_tr_b16 a[208:209], v14 offset:4096\n\t"
"v_mfma_f32_16x16x32_bf16 v[80:83], a[196:199], v[224:227], v[80:83]\n\t"
"ds_read_b64_tr_b16 a[210:211], v14 offset:6144\n\t"
"v_mfma_f32_16x16x32_bf16 v[84:87], a[196:199], v[228:231], v[84:87]\n\t"
"buffer_load_dwordx4 v4, s[20:23], s48 offen offset:0 lds\n\t"
"v_mfma_f32_16x16x32_bf16 v[36:39], a[192:195], v[228:231], v[36:39]\n\t"
"s_addk_i32 m0, -4096\n\t"
"s_addk_i32 s48, 12288\n\t"
"ds_read_b64_tr_b16 a[212:213], v15 offset:4096\n\t"
"v_mfma_f32_16x16x32_bf16 v[40:43], a[192:195], a[48:51], v[40:43]\n\t"
"buffer_load_dwordx4 v4, s[20:23], s48 offen offset:0 lds\n\t"
"v_mfma_f32_16x16x32_bf16 v[88:91], a[196:199], a[48:51], v[88:91]\n\t"
"ds_read_b64_tr_b16 a[214:215], v15 offset:6144\n\t"
"v_mfma_f32_16x16x32_bf16 v[92:95], a[196:199], a[52:55], v[92:95]\n\t"
"v_mfma_f32_16x16x32_bf16 v[44:47], a[192:195], a[52:55], v[44:47]\n\t"
"s_addk_i32 m0, -4096\n\t"
"s_addk_i32 s48, 12288\n\t"
"s_waitcnt lgkmcnt(4)\n\t"
"v_mfma_f32_16x16x32_bf16 v[32:35], a[200:203], v[232:235], v[32:35]\n\t"
"ds_read_b64_tr_b16 a[192:193], v14 offset:0\n\t"
"v_mfma_f32_16x16x32_bf16 v[80:83], a[204:207], v[232:235], v[80:83]\n\t"
"ds_read_b64_tr_b16 a[194:195], v14 offset:2048\n\t"
"v_mfma_f32_16x16x32_bf16 v[84:87], a[204:207], v[236:239], v[84:87]\n\t"
"buffer_load_dwordx4 v4, s[20:23], s48 offen offset:0 lds\n\t"
"v_mfma_f32_16x16x32_bf16 v[36:39], a[200:203], v[236:239], v[36:39]\n\t"
"v_mfma_f32_16x16x32_bf16 v[40:43], a[200:203], a[56:59], v[40:43]\n\t"
"ds_read_b64_tr_b16 a[196:197], v15 offset:0\n\t"
"v_mfma_f32_16x16x32_bf16 v[88:91], a[204:207], a[56:59], v[88:91]\n\t"
"ds_read_b64_tr_b16 a[198:199], v15 offset:2048\n\t"
"v_mfma_f32_16x16x32_bf16 v[92:95], a[204:207], a[60:63], v[92:95]\n\t"
"v_mfma_f32_16x16x32_bf16 v[44:47], a[200:203], a[60:63], v[44:47]\n\t"
"s_waitcnt lgkmcnt(4)\n\t"
"v_mfma_f32_16x16x32_bf16 v[32:35], a[208:211], v[240:243], v[32:35]\n\t"
"ds_read_b64_tr_b16 a[200:201], v18 offset:8192\n\t"
"v_mfma_f32_16x16x32_bf16 v[80:83], a[212:215], v[240:243], v[80:83]\n\t"
"ds_read_b64_tr_b16 a[202:203], v18 offset:10240\n\t"
"v_mfma_f32_16x16x32_bf16 v[84:87], a[212:215], v[244:247], v[84:87]\n\t"
"v_mfma_f32_16x16x32_bf16 v[36:39], a[208:211], v[244:247], v[36:39]\n\t"
"v_mfma_f32_16x16x32_bf16 v[40:43], a[208:211], a[64:67], v[40:43]\n\t"
"ds_read_b64_tr_b16 a[204:205], v19 offset:8192\n\t"
"v_mfma_f32_16x16x32_bf16 v[88:91], a[212:215], a[64:67], v[88:91]\n\t"
"ds_read_b64_tr_b16 a[206:207], v19 offset:10240\n\t"
"v_mfma_f32_16x16x32_bf16 v[92:95], a[212:215], a[68:71], v[92:95]\n\t"
"v_mfma_f32_16x16x32_bf16 v[44:47], a[208:211], a[68:71], v[44:47]\n\t"
"s_waitcnt lgkmcnt(4)\n\t"
"ds_read_b64_tr_b16 a[208:209], v18 offset:4096\n\t"
"v_mfma_f32_16x16x32_bf16 v[32:35], a[192:195], v[248:251], v[32:35]\n\t"
"ds_read_b64_tr_b16 a[210:211], v18 offset:6144\n\t"
"v_mfma_f32_16x16x32_bf16 v[80:83], a[196:199], v[248:251], v[80:83]\n\t"
"s_barrier\n\t"
"v_mfma_f32_16x16x32_bf16 v[84:87], a[196:199], v[252:255], v[84:87]\n\t"
"s_addk_i32 m0, -4096\n\t"
"s_addk_i32 s48, 12288\n\t"
"v_mfma_f32_16x16x32_bf16 v[36:39], a[192:195], v[252:255], v[36:39]\n\t"
"buffer_load_dwordx4 v4, s[20:23], s48 offen offset:0 lds\n\t"
"v_mfma_f32_16x16x32_bf16 v[40:43], a[192:195], a[72:75], v[40:43]\n\t"
"ds_read_b64_tr_b16 a[212:213], v19 offset:4096\n\t"
"v_mfma_f32_16x16x32_bf16 v[88:91], a[196:199], a[72:75], v[88:91]\n\t"
"ds_read_b64_tr_b16 a[214:215], v19 offset:6144\n\t"
"v_mfma_f32_16x16x32_bf16 v[92:95], a[196:199], a[76:79], v[92:95]\n\t"
"s_addk_i32 m0, -4096\n\t"
"s_addk_i32 s48, 12288\n\t"
"v_mfma_f32_16x16x32_bf16 v[44:47], a[192:195], a[76:79], v[44:47]\n\t"
"s_waitcnt lgkmcnt(4)\n\t"
"buffer_load_dwordx4 v4, s[20:23], s48 offen offset:0 lds\n\t"
"v_mfma_f32_16x16x32_bf16 v[32:35], a[200:203], a[80:83], v[32:35]\n\t"
"ds_read_b64_tr_b16 a[192:193], v18 offset:0\n\t"
"v_mfma_f32_16x16x32_bf16 v[80:83], a[204:207], a[80:83], v[80:83]\n\t"
"ds_read_b64_tr_b16 a[194:195], v18 offset:2048\n\t"
"v_mfma_f32_16x16x32_bf16 v[84:87], a[204:207], a[84:87], v[84:87]\n\t"
"v_mfma_f32_16x16x32_bf16 v[36:39], a[200:203], a[84:87], v[36:39]\n\t"
"ds_read_b64_tr_b16 a[196:197], v19 offset:0\n\t"
"v_mfma_f32_16x16x32_bf16 v[40:43], a[200:203], a[88:91], v[40:43]\n\t"
"s_waitcnt vmcnt(8)\n\t"
"v_sub_u32 v30, s39, v30\n\t"
"v_cmp_lt_u32 vcc, 6, v30\n\t"
"s_cbranch_vccnz .LslB%=\n\t"
".LbkB%=:\n"
"v_mfma_f32_16x16x32_bf16 v[88:91], a[204:207], a[88:91], v[88:91]\n\t"
"ds_read_b64_tr_b16 a[198:199], v19 offset:2048\n\t"
"v_mfma_f32_16x16x32_bf16 v[92:95], a[204:207], a[92:95], v[92:95]\n\t"
"buffer_load_dwordx2 a[96:97], v11, s[8:11], s83 offen offset:0 sc1\n\t"
"v_mfma_f32_16x16x32_bf16 v[44:47], a[200:203], a[92:95], v[44:47]\n\t"
"s_barrier\n\t"
"s_waitcnt lgkmcnt(4)\n\t"
"v_mfma_f32_16x16x32_bf16 v[32:35], a[208:211], v[64:67], v[32:35]\n\t"
"ds_read_b64_tr_b16 a[200:201], v7 offset:61440\n\t"
"buffer_load_dwordx2 a[98:99], v11, s[8:11], s83 offen offset:512 sc1\n\t"
"v_mfma_f32_16x16x32_bf16 v[80:83], a[212:215], v[64:67], v[80:83]\n\t"
"ds_read_b64_tr_b16 a[202:203], v7 offset:63488\n\t"
"v_mfma_f32_16x16x32_bf16 v[84:87], a[212:215], v[68:71], v[84:87]\n\t"
"buffer_load_dwordx2 a[100:101], v11, s[8:11], s83 offen offset:1024 sc1\n\t"
"v_mfma_f32_16x16x32_bf16 v[36:39], a[208:211], v[68:71], v[36:39]\n\t"
"ds_read_b64_tr_b16 a[204:205], v13 offset:61440\n\t"
"v_mfma_f32_16x16x32_bf16 v[40:43], a[208:211], v[72:75], v[40:43]\n\t"
"buffer_load_dwordx2 a[102:103], v11, s[8:11], s83 offen offset:1536 sc1\n\t"
"v_mfma_f32_16x16x32_bf16 v[88:91], a[212:215], v[72:75], v[88:91]\n\t"
"ds_read_b64_tr_b16 a[206:207], v13 offset:63488\n\t"
"v_mfma_f32_16x16x32_bf16 v[92:95], a[212:215], v[76:79], v[92:95]\n\t"
"buffer_load_dwordx2 a[104:105], v11, s[8:11], s83 offen offset:2048 sc1\n\t"
"v_mfma_f32_16x16x32_bf16 v[44:47], a[208:211], v[76:79], v[44:47]\n\t"
"s_waitcnt lgkmcnt(4)\n\t"
"ds_read_b64_tr_b16 a[208:209], v7 offset:57344\n\t"
"v_mfma_f32_16x16x32_bf16 v[32:35], a[192:195], v[96:99], v[32:35]\n\t"
"buffer_load_dwordx2 a[106:107], v11, s[8:11], s83 offen offset:2560 sc1\n\t"
"v_mfma_f32_16x16x32_bf16 v[80:83], a[196:199], v[96:99], v[80:83]\n\t"
"ds_read_b64_tr_b16 a[210:211], v7 offset:59392\n\t"
"v_mfma_f32_16x16x32_bf16 v[84:87], a[196:199], v[100:103], v[84:87]\n\t"
"buffer_load_dwordx2 a[108:109], v11, s[8:11], s83 offen offset:3072 sc1\n\t"
"v_mfma_f32_16x16x32_bf16 v[36:39], a[192:195], v[100:103], v[36:39]\n\t"
"ds_read_b64_tr_b16 a[212:213], v13 offset:57344\n\t"
"v_mfma_f32_16x16x32_bf16 v[40:43], a[192:195], v[104:107], v[40:43]\n\t"
"buffer_load_dwordx2 a[110:111], v11, s[8:11], s83 offen offset:3584 sc1\n\t"
"v_mfma_f32_16x16x32_bf16 v[88:91], a[196:199], v[104:107], v[88:91]\n\t"
"s_addk_i32 m0, -4096\n\t"
"ds_read_b64_tr_b16 a[214:215], v13 offset:59392\n\t"
"s_addk_i32 s48, 12288\n\t"
"v_mfma_f32_16x16x32_bf16 v[92:95], a[196:199], v[108:111], v[92:95]\n\t"
"buffer_load_dwordx4 v4, s[20:23], s48 offen offset:0 lds\n\t"
"v_mfma_f32_16x16x32_bf16 v[44:47], a[192:195], v[108:111], v[44:47]\n\t"
"s_waitcnt lgkmcnt(4)\n\t"
"v_mfma_f32_16x16x32_bf16 v[112:115], a[200:203], v[128:131], 0\n\t"
"ds_read_b64_tr_b16 a[192:193], v7 offset:53248\n\t"
"v_mfma_f32_16x16x32_bf16 v[48:51], a[204:207], v[128:131], 0\n\t"
"s_addk_i32 m0, -4096\n\t"
"ds_read_b64_tr_b16 a[194:195], v7 offset:55296\n\t"
"s_addk_i32 s48, 12288\n\t"
"v_mfma_f32_16x16x32_bf16 v[52:55], a[204:207], v[132:135], 0\n\t"
"buffer_load_dwordx4 v4, s[20:23], s48 offen offset:0 lds\n\t"
"v_mfma_f32_16x16x32_bf16 v[116:119], a[200:203], v[132:135], 0\n\t"
"v_mul_f32 v33, v33, v20\n\t"
"v_mul_f32 v34, v34, v20\n\t"
"v_mfma_f32_16x16x32_bf16 v[120:123], a[200:203], v[136:139], 0\n\t"
"ds_read_b64_tr_b16 a[196:197], v13 offset:53248\n\t"
"v_mul_f32 v32, v32, v20\n\t"
"v_mfma_f32_16x16x32_bf16 v[56:59], a[204:207], v[136:139], 0\n\t"
"s_add_u32 m0, s44, 0\n\t"
"ds_read_b64_tr_b16 a[198:199], v13 offset:55296\n\t"
"s_addk_i32 s48, 12288\n\t"
"v_mfma_f32_16x16x32_bf16 v[60:63], a[204:207], v[140:143], 0\n\t"
"buffer_load_dwordx4 v4, s[20:23], s48 offen offset:0 lds\n\t"
"v_mfma_f32_16x16x32_bf16 v[124:127], a[200:203], v[140:143], 0\n\t"
"s_waitcnt lgkmcnt(4)\n\t"
"v_mul_f32 v35, v35, v20\n\t"
"v_mul_f32 v80, v80, v20\n\t"
"v_mfma_f32_16x16x32_bf16 v[112:115], a[208:211], v[144:147], v[112:115]\n\t"
"ds_read_b64_tr_b16 a[200:201], v7 offset:49152\n\t"
"v_mul_f32 v81, v81, v20\n\t"
"v_mfma_f32_16x16x32_bf16 v[48:51], a[212:215], v[144:147], v[48:51]\n\t"
"ds_read_b64_tr_b16 a[202:203], v7 offset:51200\n\t"
"v_mul_f32 v82, v82, v20\n\t"
"v_mfma_f32_16x16x32_bf16 v[52:55], a[212:215], v[148:151], v[52:55]\n\t"
"buffer_load_dwordx4 a[216:219], v1, s[8:11], s57 offen offset:0 sc1\n\t"
"v_mul_f32 v83, v83, v20\n\t"
"v_cvt_pk_bf16_f32 v32, v32, v33\n\t"
"v_mfma_f32_16x16x32_bf16 v[116:119], a[208:211], v[148:151], v[116:119]\n\t"
"v_cvt_pk_bf16_f32 v33, v34, v35\n\t"
"v_cvt_pk_bf16_f32 v34, v80, v81\n\t"
"v_mfma_f32_16x16x32_bf16 v[120:123], a[208:211], v[152:155], v[120:123]\n\t"
"ds_read_b64_tr_b16 a[204:205], v13 offset:49152\n\t"
"v_cvt_pk_bf16_f32 v35, v82, v83\n\t"
"v_mfma_f32_16x16x32_bf16 v[56:59], a[212:215], v[152:155], v[56:59]\n\t"
"s_addk_i32 s52, 4096\n\t"
"ds_read_b64_tr_b16 a[206:207], v13 offset:51200\n\t"
"v_mul_f32 v36, v36, v21\n\t"
"v_mfma_f32_16x16x32_bf16 v[60:63], a[212:215], v[156:159], v[60:63]\n\t"
"buffer_store_dwordx4 v[32:35], v1, s[8:11], s52 offen offset:0 sc1\n\t"
"v_mfma_f32_16x16x32_bf16 v[124:127], a[208:211], v[156:159], v[124:127]\n\t"
"s_waitcnt vmcnt(18)\n\t"
"v_mul_f32 v37, v37, v21\n\t"
"v_mul_f32 v38, v38, v21\n\t"
"s_barrier\n\t"
"s_waitcnt lgkmcnt(4)\n\t"
"v_mfma_f32_16x16x32_bf16 v[112:115], a[192:195], v[160:163], v[112:115]\n\t"
"ds_read_b64_tr_b16 a[208:209], v7 offset:45056\n\t"
"v_mul_f32 v39, v39, v21\n\t"
"v_mfma_f32_16x16x32_bf16 v[48:51], a[196:199], v[160:163], v[48:51]\n\t"
"ds_read_b64_tr_b16 a[210:211], v7 offset:47104\n\t"
"v_mul_f32 v84, v84, v21\n\t"
"v_mfma_f32_16x16x32_bf16 v[52:55], a[196:199], v[164:167], v[52:55]\n\t"
"v_mul_f32 v85, v85, v21\n\t"
"v_mul_f32 v86, v86, v21\n\t"
"v_mfma_f32_16x16x32_bf16 v[116:119], a[192:195], v[164:167], v[116:119]\n\t"
"v_mul_f32 v87, v87, v21\n\t"
"v_cvt_pk_bf16_f32 v36, v36, v37\n\t"
"v_mfma_f32_16x16x32_bf16 v[120:123], a[192:195], v[168:171], v[120:123]\n\t"
"ds_read_b64_tr_b16 a[212:213], v13 offset:45056\n\t"
"v_cvt_pk_bf16_f32 v37, v38, v39\n\t"
"v_mfma_f32_16x16x32_bf16 v[56:59], a[196:199], v[168:171], v[56:59]\n\t"
"ds_read_b64_tr_b16 a[214:215], v13 offset:47104\n\t"
"v_cvt_pk_bf16_f32 v38, v84, v85\n\t"
"v_mfma_f32_16x16x32_bf16 v[60:63], a[196:199], v[172:175], v[60:63]\n\t"
"v_cvt_pk_bf16_f32 v39, v86, v87\n\t"
"v_mul_f32 v40, v40, v22\n\t"
"v_mfma_f32_16x16x32_bf16 v[124:127], a[192:195], v[172:175], v[124:127]\n\t"
"s_waitcnt lgkmcnt(4)\n\t"
"buffer_store_dwordx4 v[36:39], v1, s[8:11], s52 offen offset:1024 sc1\n\t"
"v_mfma_f32_16x16x32_bf16 v[112:115], a[200:203], v[176:179], v[112:115]\n\t"
"ds_read_b64_tr_b16 a[192:193], v7 offset:40960\n\t"
"v_mul_f32 v41, v41, v22\n\t"
"v_mfma_f32_16x16x32_bf16 v[48:51], a[204:207], v[176:179], v[48:51]\n\t"
"ds_read_b64_tr_b16 a[194:195], v7 offset:43008\n\t"
"v_mul_f32 v42, v42, v22\n\t"
"v_mfma_f32_16x16x32_bf16 v[52:55], a[204:207], v[180:183], v[52:55]\n\t"
"v_mul_f32 v43, v43, v22\n\t"
"v_mul_f32 v88, v88, v22\n\t"
"v_mfma_f32_16x16x32_bf16 v[116:119], a[200:203], v[180:183], v[116:119]\n\t"
"v_mul_f32 v89, v89, v22\n\t"
"v_mul_f32 v90, v90, v22\n\t"
"v_mfma_f32_16x16x32_bf16 v[120:123], a[200:203], a[0:3], v[120:123]\n\t"
"s_load_dword s3, %[karg], s85 offset:45452\n\t"
"ds_read_b64_tr_b16 a[196:197], v13 offset:40960\n\t"
"v_mul_f32 v91, v91, v22\n\t"
"v_mfma_f32_16x16x32_bf16 v[56:59], a[204:207], a[0:3], v[56:59]\n\t"
"ds_read_b64_tr_b16 a[198:199], v13 offset:43008\n\t"
"v_cvt_pk_bf16_f32 v40, v40, v41\n\t"
"v_mfma_f32_16x16x32_bf16 v[60:63], a[204:207], a[4:7], v[60:63]\n\t"
"v_cvt_pk_bf16_f32 v41, v42, v43\n\t"
"v_cvt_pk_bf16_f32 v42, v88, v89\n\t"
"v_mfma_f32_16x16x32_bf16 v[124:127], a[200:203], a[4:7], v[124:127]\n\t"
"s_waitcnt lgkmcnt(4)\n\t"
"v_cvt_pk_bf16_f32 v43, v90, v91\n\t"
"v_mul_f32 v44, v44, v23\n\t"
"v_mfma_f32_16x16x32_bf16 v[112:115], a[208:211], v[184:187], v[112:115]\n\t"
"ds_read_b64_tr_b16 a[200:201], v7 offset:36864\n\t"
"buffer_store_dwordx4 v[40:43], v1, s[8:11], s52 offen offset:2048 sc1\n\t"
"v_mfma_f32_16x16x32_bf16 v[48:51], a[212:215], v[184:187], v[48:51]\n\t"
"ds_read_b64_tr_b16 a[202:203], v7 offset:38912\n\t"
"v_mul_f32 v45, v45, v23\n\t"
"v_mfma_f32_16x16x32_bf16 v[52:55], a[212:215], v[188:191], v[52:55]\n\t"
"v_mul_f32 v46, v46, v23\n\t"
"v_mul_f32 v47, v47, v23\n\t"
"v_mfma_f32_16x16x32_bf16 v[116:119], a[208:211], v[188:191], v[116:119]\n\t"
"v_mul_f32 v92, v92, v23\n\t"
"v_mul_f32 v93, v93, v23\n\t"
"v_mfma_f32_16x16x32_bf16 v[120:123], a[208:211], a[8:11], v[120:123]\n\t"
"ds_read_b64_tr_b16 a[204:205], v13 offset:36864\n\t"
"v_mul_f32 v94, v94, v23\n\t"
"v_mfma_f32_16x16x32_bf16 v[56:59], a[212:215], a[8:11], v[56:59]\n\t"
"ds_read_b64_tr_b16 a[206:207], v13 offset:38912\n\t"
"v_mul_f32 v95, v95, v23\n\t"
"v_mfma_f32_16x16x32_bf16 v[60:63], a[212:215], a[12:15], v[60:63]\n\t"
"v_cvt_pk_bf16_f32 v44, v44, v45\n\t"
"v_cvt_pk_bf16_f32 v45, v46, v47\n\t"
"v_mfma_f32_16x16x32_bf16 v[124:127], a[208:211], a[12:15], v[124:127]\n\t"
"s_waitcnt lgkmcnt(4)\n\t"
"v_cvt_pk_bf16_f32 v46, v92, v93\n\t"
"v_cvt_pk_bf16_f32 v47, v94, v95\n\t"
"v_mfma_f32_16x16x32_bf16 v[112:115], a[192:195], v[192:195], v[112:115]\n\t"
"ds_read_b64_tr_b16 a[208:209], v7 offset:32768\n\t"
"v_accvgpr_write_b32 a176, v32\n\t"
"v_mfma_f32_16x16x32_bf16 v[48:51], a[196:199], v[192:195], v[48:51]\n\t"
"ds_read_b64_tr_b16 a[210:211], v7 offset:34816\n\t"
"buffer_store_dwordx4 v[44:47], v1, s[8:11], s52 offen offset:3072 sc1\n\t"
"v_mfma_f32_16x16x32_bf16 v[52:55], a[196:199], v[196:199], v[52:55]\n\t"
"v_accvgpr_write_b32 a177, v33\n\t"
"v_accvgpr_write_b32 a178, v34\n\t"
"v_mfma_f32_16x16x32_bf16 v[116:119], a[192:195], v[196:199], v[116:119]\n\t"
"v_accvgpr_write_b32 a179, v35\n\t"
"v_accvgpr_write_b32 a180, v36\n\t"
"v_mfma_f32_16x16x32_bf16 v[120:123], a[192:195], a[16:19], v[120:123]\n\t"
"ds_read_b64_tr_b16 a[212:213], v13 offset:32768\n\t"
"v_accvgpr_write_b32 a181, v37\n\t"
"v_mfma_f32_16x16x32_bf16 v[56:59], a[196:199], a[16:19], v[56:59]\n\t"
"ds_read_b64_tr_b16 a[214:215], v13 offset:34816\n\t"
"v_accvgpr_write_b32 a182, v38\n\t"
"v_mfma_f32_16x16x32_bf16 v[60:63], a[196:199], a[20:23], v[60:63]\n\t"
"v_accvgpr_write_b32 a183, v39\n\t"
"v_accvgpr_write_b32 a184, v40\n\t"
"v_mfma_f32_16x16x32_bf16 v[124:127], a[192:195], a[20:23], v[124:127]\n\t"
"s_waitcnt vmcnt(17)\n\t"
"v_accvgpr_write_b32 a185, v41\n\t"
"v_accvgpr_write_b32 a186, v42\n\t"
"s_barrier\n\t"
"s_waitcnt lgkmcnt(4)\n\t"
"v_mfma_f32_16x16x32_bf16 v[112:115], a[200:203], v[200:203], v[112:115]\n\t"
"s_waitcnt vmcnt(8)\n\t"
"ds_read_b64_tr_b16 a[192:193], v7 offset:28672\n\t"
"v_accvgpr_write_b32 a187, v43\n\t"
"v_mfma_f32_16x16x32_bf16 v[48:51], a[204:207], v[200:203], v[48:51]\n\t"
"ds_read_b64_tr_b16 a[194:195], v7 offset:30720\n\t"
"v_accvgpr_write_b32 a188, v44\n\t"
"v_mfma_f32_16x16x32_bf16 v[52:55], a[204:207], v[204:207], v[52:55]\n\t"
"buffer_load_dwordx4 a[220:223], v1, s[8:11], s57 offen offset:1024 sc1\n\t"
"v_accvgpr_write_b32 a189, v45\n\t"
"v_accvgpr_write_b32 a190, v46\n\t"
"v_mfma_f32_16x16x32_bf16 v[116:119], a[200:203], v[204:207], v[116:119]\n\t"
"v_accvgpr_write_b32 a191, v47\n\t"
"v_accvgpr_read_b32 v16, a96\n\t"
"v_mfma_f32_16x16x32_bf16 v[120:123], a[200:203], a[24:27], v[120:123]\n\t"
"ds_read_b64_tr_b16 a[196:197], v13 offset:28672\n\t"
"v_accvgpr_read_b32 v17, a97\n\t"
"v_mfma_f32_16x16x32_bf16 v[56:59], a[204:207], a[24:27], v[56:59]\n\t"
"ds_read_b64_tr_b16 a[198:199], v13 offset:30720\n\t"
"v_accvgpr_read_b32 v10, a98\n\t"
"v_mfma_f32_16x16x32_bf16 v[60:63], a[204:207], a[28:31], v[60:63]\n\t"
"buffer_load_dwordx4 a[224:227], v1, s[8:11], s57 offen offset:2048 sc1\n\t"
"v_accvgpr_read_b32 v12, a99\n\t"
"v_add_f32 v16, v16, v10\n\t"
"v_mfma_f32_16x16x32_bf16 v[124:127], a[200:203], a[28:31], v[124:127]\n\t"
"s_waitcnt lgkmcnt(4)\n\t"
"v_add_f32 v17, v17, v12\n\t"
"v_accvgpr_read_b32 v10, a100\n\t"
"v_mfma_f32_16x16x32_bf16 v[112:115], a[208:211], v[208:211], v[112:115]\n\t"
"ds_read_b64_tr_b16 a[200:201], v7 offset:24576\n\t"
"v_accvgpr_read_b32 v12, a101\n\t"
"v_mfma_f32_16x16x32_bf16 v[48:51], a[212:215], v[208:211], v[48:51]\n\t"
"ds_read_b64_tr_b16 a[202:203], v7 offset:26624\n\t"
"v_add_f32 v16, v16, v10\n\t"
"v_mfma_f32_16x16x32_bf16 v[52:55], a[212:215], v[212:215], v[52:55]\n\t"
"v_add_f32 v17, v17, v12\n\t"
"v_accvgpr_read_b32 v10, a102\n\t"
"v_mfma_f32_16x16x32_bf16 v[116:119], a[208:211], v[212:215], v[116:119]\n\t"
"buffer_load_dwordx4 a[228:231], v1, s[8:11], s57 offen offset:3072 sc1\n\t"
"v_accvgpr_read_b32 v12, a103\n\t"
"v_add_f32 v16, v16, v10\n\t"
"v_mfma_f32_16x16x32_bf16 v[120:123], a[208:211], a[32:35], v[120:123]\n\t"
"ds_read_b64_tr_b16 a[204:205], v13 offset:24576\n\t"
"v_add_f32 v17, v17, v12\n\t"
"v_mfma_f32_16x16x32_bf16 v[56:59], a[212:215], a[32:35], v[56:59]\n\t"
"ds_read_b64_tr_b16 a[206:207], v13 offset:26624\n\t"
"v_accvgpr_read_b32 v18, a104\n\t"
"v_mfma_f32_16x16x32_bf16 v[60:63], a[212:215], a[36:39], v[60:63]\n\t"
"v_accvgpr_read_b32 v19, a105\n\t"
"v_accvgpr_read_b32 v10, a106\n\t"
"v_mfma_f32_16x16x32_bf16 v[124:127], a[208:211], a[36:39], v[124:127]\n\t"
"s_waitcnt lgkmcnt(4)\n\t"
"v_accvgpr_read_b32 v12, a107\n\t"
"v_add_f32 v18, v18, v10\n\t"
"v_mfma_f32_16x16x32_bf16 v[112:115], a[192:195], v[216:219], v[112:115]\n\t"
"ds_read_b64_tr_b16 a[208:209], v7 offset:20480\n\t"
"buffer_load_dwordx4 a[244:247], v1, s[8:11], s58 offen offset:0 sc1\n\t"
"v_add_f32 v19, v19, v12\n\t"
"v_mfma_f32_16x16x32_bf16 v[48:51], a[196:199], v[216:219], v[48:51]\n\t"
"ds_read_b64_tr_b16 a[210:211], v7 offset:22528\n\t"
"v_accvgpr_read_b32 v10, a108\n\t"
"v_mfma_f32_16x16x32_bf16 v[52:55], a[196:199], v[220:223], v[52:55]\n\t"
"v_accvgpr_read_b32 v12, a109\n\t"
"v_add_f32 v18, v18, v10\n\t"
"v_mfma_f32_16x16x32_bf16 v[116:119], a[192:195], v[220:223], v[116:119]\n\t"
"v_add_f32 v19, v19, v12\n\t"
"v_accvgpr_read_b32 v10, a110\n\t"
"v_mfma_f32_16x16x32_bf16 v[120:123], a[192:195], a[40:43], v[120:123]\n\t"
"ds_read_b64_tr_b16 a[212:213], v13 offset:20480\n\t"
"buffer_load_dwordx4 a[248:251], v1, s[8:11], s58 offen offset:1024 sc1\n\t"
"v_accvgpr_read_b32 v12, a111\n\t"
"v_mfma_f32_16x16x32_bf16 v[56:59], a[196:199], a[40:43], v[56:59]\n\t"
"ds_read_b64_tr_b16 a[214:215], v13 offset:22528\n\t"
"v_add_f32 v18, v18, v10\n\t"
"v_mfma_f32_16x16x32_bf16 v[60:63], a[196:199], a[44:47], v[60:63]\n\t"
"v_add_f32 v19, v19, v12\n\t"
"v_mul_f32 v10, 0xbfb8aa3b, v16\n\t"
"v_mfma_f32_16x16x32_bf16 v[124:127], a[192:195], a[44:47], v[124:127]\n\t"
"v_mul_f32 v12, 0xbfb8aa3b, v17\n\t"
"s_waitcnt lgkmcnt(4)\n\t"
"v_mfma_f32_16x16x32_bf16 v[112:115], a[200:203], v[224:227], v[112:115]\n\t"
"ds_read_b64_tr_b16 a[192:193], v7 offset:16384\n\t"
"v_mfma_f32_16x16x32_bf16 v[48:51], a[204:207], v[224:227], v[48:51]\n\t"
"ds_read_b64_tr_b16 a[194:195], v7 offset:18432\n\t"
"buffer_load_dwordx4 a[252:255], v1, s[8:11], s58 offen offset:2048 sc1\n\t"
"v_mfma_f32_16x16x32_bf16 v[52:55], a[204:207], v[228:231], v[52:55]\n\t"
"v_exp_f32 v10, v10\n\t"
"v_mfma_f32_16x16x32_bf16 v[116:119], a[200:203], v[228:231], v[116:119]\n\t"
"v_exp_f32 v12, v12\n\t"
"v_mfma_f32_16x16x32_bf16 v[120:123], a[200:203], a[48:51], v[120:123]\n\t"
"ds_read_b64_tr_b16 a[196:197], v13 offset:16384\n\t"
"v_mul_f32 v16, v16, v18\n\t"
"v_mfma_f32_16x16x32_bf16 v[56:59], a[204:207], a[48:51], v[56:59]\n\t"
"ds_read_b64_tr_b16 a[198:199], v13 offset:18432\n\t"
"v_mul_f32 v17, v17, v19\n\t"
"v_mfma_f32_16x16x32_bf16 v[60:63], a[204:207], a[52:55], v[60:63]\n\t"
"s_waitcnt vmcnt(11)\n\t"
"v_add_f32 v10, 1.0, v10\n\t"
"v_add_f32 v12, 1.0, v12\n\t"
"v_mfma_f32_16x16x32_bf16 v[124:127], a[200:203], a[52:55], v[124:127]\n\t"
"s_barrier\n\t"
"s_waitcnt lgkmcnt(4)\n\t"
"v_mfma_f32_16x16x32_bf16 v[112:115], a[208:211], v[232:235], v[112:115]\n\t"
"ds_read_b64_tr_b16 a[200:201], v7 offset:12288\n\t"
"v_mfma_f32_16x16x32_bf16 v[48:51], a[212:215], v[232:235], v[48:51]\n\t"
"ds_read_b64_tr_b16 a[202:203], v7 offset:14336\n\t"
"v_mfma_f32_16x16x32_bf16 v[52:55], a[212:215], v[236:239], v[52:55]\n\t"
"v_rcp_f32 v10, v10\n\t"
"v_mfma_f32_16x16x32_bf16 v[116:119], a[208:211], v[236:239], v[116:119]\n\t"
"v_rcp_f32 v12, v12\n\t"
"v_mfma_f32_16x16x32_bf16 v[120:123], a[208:211], a[56:59], v[120:123]\n\t"
"ds_read_b64_tr_b16 a[204:205], v13 offset:12288\n\t"
"v_lshlrev_b32 v18, 2, %[vT]\n\t"
"v_mfma_f32_16x16x32_bf16 v[56:59], a[212:215], a[56:59], v[56:59]\n\t"
"ds_read_b64_tr_b16 a[206:207], v13 offset:14336\n\t"
"v_add_u32 v18, 78336, v18\n\t"
"v_mfma_f32_16x16x32_bf16 v[60:63], a[212:215], a[60:63], v[60:63]\n\t"
"v_mul_f32 v16, v16, v10\n\t"
"v_mul_f32 v17, v17, v12\n\t"
"v_cvt_pk_bf16_f32 v16, v16, v17\n\t"
"v_mfma_f32_16x16x32_bf16 v[124:127], a[208:211], a[60:63], v[124:127]\n\t"
"s_waitcnt lgkmcnt(4)\n\t"
"ds_write_b32 v18, v16 offset:0\n\t"
"v_mfma_f32_16x16x32_bf16 v[112:115], a[192:195], v[240:243], v[112:115]\n\t"
"ds_read_b64_tr_b16 a[208:209], v7 offset:8192\n\t"
"v_and_b32 v18, 4294967040, v18\n\t"
"v_mfma_f32_16x16x32_bf16 v[48:51], a[196:199], v[240:243], v[48:51]\n\t"
"ds_read_b64_tr_b16 a[210:211], v7 offset:10240\n\t"
"v_and_b32 v19, 1, %[vT]\n\t"
"v_mfma_f32_16x16x32_bf16 v[52:55], a[196:199], v[244:247], v[52:55]\n\t"
"v_lshl_add_u32 v18, v19, 7, v18\n\t"
"v_bfe_u32 v19, %[vT], 4, 2\n\t"
"v_lshl_add_u32 v18, v19, 3, v18\n\t"
"v_mfma_f32_16x16x32_bf16 v[116:119], a[192:195], v[244:247], v[116:119]\n\t"
"ds_read2_b64 a[96:99], v18 offset0:0 offset1:4\n\t"
"v_mfma_f32_16x16x32_bf16 v[120:123], a[192:195], a[64:67], v[120:123]\n\t"
"ds_read_b64_tr_b16 a[212:213], v13 offset:8192\n\t"
"ds_read2_b64 a[104:107], v18 offset0:8 offset1:12\n\t"
"v_mfma_f32_16x16x32_bf16 v[56:59], a[196:199], a[64:67], v[56:59]\n\t"
"s_sub_u32 s98, s39, 5\n\t"
"ds_read_b64_tr_b16 a[214:215], v13 offset:10240\n\t"
"v_and_b32 v11, 15, %[vT]\n\t"
"v_mfma_f32_16x16x32_bf16 v[60:63], a[196:199], a[68:71], v[60:63]\n\t"
"v_accvgpr_write_b32 a232, s98\n\t"
"v_lshlrev_b32 v12, 3, v11\n\t"
"v_mfma_f32_16x16x32_bf16 v[124:127], a[192:195], a[68:71], v[124:127]\n\t"
"s_waitcnt lgkmcnt(7)\n\t"
"ds_read_b64_tr_b16 a[192:193], v7 offset:4096\n\t"
"v_mfma_f32_16x16x32_bf16 v[112:115], a[200:203], v[248:251], v[112:115]\n\t"
"s_waitcnt vmcnt(6)\n\t"
"s_barrier\n\t"
"v_mfma_f32_16x16x32_bf16 v[48:51], a[204:207], v[248:251], v[48:51]\n\t"
"buffer_store_dword a232, v28, s[8:11], 0 offen offset:0 sc1\n\t"
"v_mfma_f32_16x16x32_bf16 v[52:55], a[204:207], v[252:255], v[52:55]\n\t"
"ds_read_b64_tr_b16 a[194:195], v7 offset:6144\n\t"
"v_mfma_f32_16x16x32_bf16 v[116:119], a[200:203], v[252:255], v[116:119]\n\t"
"v_mfma_f32_16x16x32_bf16 v[120:123], a[200:203], a[72:75], v[120:123]\n\t"
"ds_read_b64_tr_b16 a[196:197], v13 offset:4096\n\t"
"s_waitcnt lgkmcnt(4)\n\t"
"v_mfma_f32_16x16x32_bf16 v[56:59], a[204:207], a[72:75], v[56:59]\n\t"
"ds_read_b64_tr_b16 a[198:199], v13 offset:6144\n\t"
"v_mfma_f32_16x16x32_bf16 v[60:63], a[204:207], a[76:79], v[60:63]\n\t"
"v_mfma_f32_16x16x32_bf16 a[112:115], a[96:99], a[236:239], 0\n\t"
"v_mfma_f32_16x16x32_bf16 v[124:127], a[200:203], a[76:79], v[124:127]\n\t"
"v_mfma_f32_16x16x32_bf16 a[112:115], a[104:107], a[240:243], a[112:115]\n\t"
"s_waitcnt lgkmcnt(4)\n\t"
"v_mfma_f32_16x16x32_bf16 v[112:115], a[208:211], a[80:83], v[112:115]\n\t"
"ds_read_b64_tr_b16 a[200:201], v7 offset:0\n\t"
"v_add_u32 v12, 77824, v12\n\t"
"v_mfma_f32_16x16x32_bf16 v[48:51], a[212:215], a[80:83], v[48:51]\n\t"
"s_lshl_b32 s46, s38, 7\n\t"
"ds_read_b64_tr_b16 a[202:203], v7 offset:2048\n\t"
"v_add_u32 v10, s46, v12\n\t"
"v_mfma_f32_16x16x32_bf16 v[52:55], a[212:215], a[84:87], v[52:55]\n\t"
"v_lshlrev_b32 v11, 3, v11\n\t"
"v_cmp_gt_u32 vcc, 16, %[vT]\n\t"
"v_mfma_f32_16x16x32_bf16 v[116:119], a[208:211], a[84:87], v[116:119]\n\t"
"v_mov_b32 v16, 1073741824\n\t"
"v_mfma_f32_16x16x32_bf16 v[120:123], a[208:211], a[88:91], v[120:123]\n\t"
"ds_read_b64_tr_b16 a[204:205], v13 offset:0\n\t"
"v_mfma_f32_16x16x32_bf16 v[56:59], a[212:215], a[88:91], v[56:59]\n\t"
"ds_read_b64_tr_b16 a[206:207], v13 offset:2048\n\t"
"v_cndmask_b32 v11, v16, v11, vcc\n\t"
"v_mfma_f32_16x16x32_bf16 v[60:63], a[212:215], a[92:95], v[60:63]\n\t"
"ds_write_b64 v10, a[112:113] offset:0\n\t"
"v_mfma_f32_16x16x32_bf16 v[124:127], a[208:211], a[92:95], v[124:127]\n\t"
"s_waitcnt lgkmcnt(5)\n\t"
"v_mfma_f32_16x16x32_bf16 v[112:115], a[192:195], v[64:67], v[112:115]\n\t"
"ds_read_b64_tr_b16 a[208:209], v14 offset:61440\n\t"
"v_mfma_f32_16x16x32_bf16 v[48:51], a[196:199], v[64:67], v[48:51]\n\t"
"ds_read_b64_tr_b16 a[210:211], v14 offset:63488\n\t"
"v_mfma_f32_16x16x32_bf16 v[52:55], a[196:199], v[68:71], v[52:55]\n\t"
"s_waitcnt lgkmcnt(2)\n\t"
"s_barrier\n\t"
"v_mfma_f32_16x16x32_bf16 v[116:119], a[192:195], v[68:71], v[116:119]\n\t"
".Lpg12%=:\n"
"s_cbranch_execz .Lpg16%=\n\t"
"ds_read2_b64 v[16:19], v12 offset0:0 offset1:16\n\t"
"v_mov_b32 v4, v11\n\t"
"v_mfma_f32_16x16x32_bf16 v[120:123], a[192:195], v[72:75], v[120:123]\n\t"
"buffer_load_dwordx4 a[96:99], v1, s[8:11], s58 offen offset:3072 sc1\n\t"
"ds_read_b64_tr_b16 a[212:213], v15 offset:61440\n\t"
"v_mfma_f32_16x16x32_bf16 v[56:59], a[196:199], v[72:75], v[56:59]\n\t"
"ds_read_b64_tr_b16 a[214:215], v15 offset:63488\n\t"
"v_mfma_f32_16x16x32_bf16 v[60:63], a[196:199], v[76:79], v[60:63]\n\t"
"buffer_load_dwordx4 a[100:103], v1, s[8:11], s59 offen offset:0 sc1\n\t"
"v_mfma_f32_16x16x32_bf16 v[124:127], a[192:195], v[76:79], v[124:127]\n\t"
"v_mfma_f32_16x16x32_bf16 v[112:115], a[200:203], v[96:99], v[112:115]\n\t"
"ds_read_b64_tr_b16 a[192:193], v14 offset:57344\n\t"
"v_mfma_f32_16x16x32_bf16 v[48:51], a[204:207], v[96:99], v[48:51]\n\t"
"s_waitcnt lgkmcnt(3)\n\t"
"ds_read_b64_tr_b16 a[194:195], v14 offset:59392\n\t"
"v_add_f32 v10, v16, v18\n\t"
"v_add_f32 v11, v17, v19\n\t"
"v_mfma_f32_16x16x32_bf16 v[52:55], a[204:207], v[100:103], v[52:55]\n\t"
"buffer_load_dwordx4 a[104:107], v1, s[8:11], s59 offen offset:1024 sc1\n\t"
"v_mfma_f32_16x16x32_bf16 v[116:119], a[200:203], v[100:103], v[116:119]\n\t"
"ds_read2_b64 v[16:19], v12 offset0:32 offset1:48\n\t"
"v_mfma_f32_16x16x32_bf16 v[120:123], a[200:203], v[104:107], v[120:123]\n\t"
"ds_read_b64_tr_b16 a[196:197], v15 offset:57344\n\t"
"v_mfma_f32_16x16x32_bf16 v[56:59], a[204:207], v[104:107], v[56:59]\n\t"
"ds_read_b64_tr_b16 a[198:199], v15 offset:59392\n\t"
"v_mfma_f32_16x16x32_bf16 v[60:63], a[204:207], v[108:111], v[60:63]\n\t"
"v_mfma_f32_16x16x32_bf16 v[124:127], a[200:203], v[108:111], v[124:127]\n\t"
"s_waitcnt lgkmcnt(5)\n\t"
"buffer_load_dwordx4 a[108:111], v1, s[8:11], s59 offen offset:2048 sc1\n\t"
"v_mfma_f32_16x16x32_bf16 v[32:35], a[208:211], v[128:131], 0\n\t"
"s_waitcnt lgkmcnt(2)\n\t"
"ds_read_b64_tr_b16 a[200:201], v14 offset:53248\n\t"
"v_add_f32 v16, v16, v18\n\t"
"v_mfma_f32_16x16x32_bf16 v[80:83], a[212:215], v[128:131], 0\n\t"
"ds_read_b64_tr_b16 a[202:203], v14 offset:55296\n\t"
"buffer_load_dwordx4 a[112:115], v1, s[8:11], s59 offen offset:3072 sc1\n\t"
"v_mfma_f32_16x16x32_bf16 v[84:87], a[212:215], v[132:135], 0\n\t"
"v_add_f32 v17, v17, v19\n\t"
"v_add_f32 v10, v10, v16\n\t"
"v_mfma_f32_16x16x32_bf16 v[36:39], a[208:211], v[132:135], 0\n\t"
"v_add_f32 v11, v11, v17\n\t"
"v_cvt_pk_bf16_f32 v16, v10, v10\n\t"
"v_mfma_f32_16x16x32_bf16 v[40:43], a[208:211], v[136:139], 0\n\t"
"ds_read_b64_tr_b16 a[204:205], v15 offset:53248\n\t"
"v_cvt_pk_bf16_f32 v17, v11, v11\n\t"
"v_mfma_f32_16x16x32_bf16 v[88:91], a[212:215], v[136:139], 0\n\t"
"ds_read_b64_tr_b16 a[206:207], v15 offset:55296\n\t"
"v_mul_f32 v112, v112, v20\n\t"
"v_mfma_f32_16x16x32_bf16 v[92:95], a[212:215], v[140:143], 0\n\t"
"s_waitcnt lgkmcnt(4)\n\t"
"v_mul_f32 v113, v113, v20\n\t"
"v_mul_f32 v114, v114, v20\n\t"
"v_mfma_f32_16x16x32_bf16 v[44:47], a[208:211], v[140:143], 0\n\t"
"s_addk_i32 s57, 4096\n\t"
"v_mul_f32 v115, v115, v20\n\t"
"v_mul_f32 v48, v48, v20\n\t"
"v_mfma_f32_16x16x32_bf16 v[32:35], a[192:195], v[144:147], v[32:35]\n\t"
"ds_read_b64_tr_b16 a[208:209], v14 offset:49152\n\t"
"buffer_load_dwordx4 a[116:119], v1, s[8:11], s57 offen offset:0 sc1\n\t"
"v_mul_f32 v49, v49, v20\n\t"
"v_mfma_f32_16x16x32_bf16 v[80:83], a[196:199], v[144:147], v[80:83]\n\t"
"ds_read_b64_tr_b16 a[210:211], v14 offset:51200\n\t"
"v_mul_f32 v50, v50, v20\n\t"
"v_mfma_f32_16x16x32_bf16 v[84:87], a[196:199], v[148:151], v[84:87]\n\t"
"v_mul_f32 v51, v51, v20\n\t"
"v_cvt_pk_bf16_f32 v112, v112, v113\n\t"
"v_mfma_f32_16x16x32_bf16 v[36:39], a[192:195], v[148:151], v[36:39]\n\t"
"v_cvt_pk_bf16_f32 v113, v114, v115\n\t"
"v_cvt_pk_bf16_f32 v114, v48, v49\n\t"
"v_mfma_f32_16x16x32_bf16 v[40:43], a[192:195], v[152:155], v[40:43]\n\t"
"ds_read_b64_tr_b16 a[212:213], v15 offset:49152\n\t"
"buffer_load_dwordx4 a[120:123], v1, s[8:11], s57 offen offset:1024 sc1\n\t"
"v_cvt_pk_bf16_f32 v115, v50, v51\n\t"
"v_mfma_f32_16x16x32_bf16 v[88:91], a[196:199], v[152:155], v[88:91]\n\t"
"s_addk_i32 s52, 4096\n\t"
"ds_read_b64_tr_b16 a[214:215], v15 offset:51200\n\t"
"v_mul_f32 v116, v116, v21\n\t"
"v_mfma_f32_16x16x32_bf16 v[92:95], a[196:199], v[156:159], v[92:95]\n\t"
"buffer_store_dwordx4 v[112:115], v1, s[8:11], s52 offen offset:0 sc1\n\t"
"v_mfma_f32_16x16x32_bf16 v[44:47], a[192:195], v[156:159], v[44:47]\n\t"
"s_waitcnt lgkmcnt(4)\n\t"
"v_mul_f32 v117, v117, v21\n\t"
"v_mul_f32 v118, v118, v21\n\t"
"v_mfma_f32_16x16x32_bf16 v[32:35], a[200:203], v[160:163], v[32:35]\n\t"
"ds_read_b64_tr_b16 a[192:193], v14 offset:45056\n\t"
"v_mul_f32 v119, v119, v21\n\t"
"v_mfma_f32_16x16x32_bf16 v[80:83], a[204:207], v[160:163], v[80:83]\n\t"
"ds_read_b64_tr_b16 a[194:195], v14 offset:47104\n\t"
"v_mul_f32 v52, v52, v21\n\t"
"s_barrier\n\t"
"v_mfma_f32_16x16x32_bf16 v[84:87], a[204:207], v[164:167], v[84:87]\n\t"
"v_mul_f32 v53, v53, v21\n\t"
"v_mul_f32 v54, v54, v21\n\t"
"v_mfma_f32_16x16x32_bf16 v[36:39], a[200:203], v[164:167], v[36:39]\n\t"
"v_mul_f32 v55, v55, v21\n\t"
"v_cvt_pk_bf16_f32 v116, v116, v117\n\t"
"v_mfma_f32_16x16x32_bf16 v[40:43], a[200:203], v[168:171], v[40:43]\n\t"
"ds_read_b64_tr_b16 a[196:197], v15 offset:45056\n\t"
"v_cvt_pk_bf16_f32 v117, v118, v119\n\t"
"v_mfma_f32_16x16x32_bf16 v[88:91], a[204:207], v[168:171], v[88:91]\n\t"
"ds_read_b64_tr_b16 a[198:199], v15 offset:47104\n\t"
"v_cvt_pk_bf16_f32 v118, v52, v53\n\t"
"v_mfma_f32_16x16x32_bf16 v[92:95], a[204:207], v[172:175], v[92:95]\n\t"
"v_cvt_pk_bf16_f32 v119, v54, v55\n\t"
"v_mul_f32 v120, v120, v22\n\t"
"v_mfma_f32_16x16x32_bf16 v[44:47], a[200:203], v[172:175], v[44:47]\n\t"
"s_waitcnt lgkmcnt(4)\n\t"
"buffer_store_dwordx4 v[116:119], v1, s[8:11], s52 offen offset:1024 sc1\n\t"
"v_mfma_f32_16x16x32_bf16 v[32:35], a[208:211], v[176:179], v[32:35]\n\t"
"ds_read_b64_tr_b16 a[200:201], v14 offset:40960\n\t"
"v_mul_f32 v121, v121, v22\n\t"
"v_mfma_f32_16x16x32_bf16 v[80:83], a[212:215], v[176:179], v[80:83]\n\t"
"ds_read_b64_tr_b16 a[202:203], v14 offset:43008\n\t"
"v_mul_f32 v122, v122, v22\n\t"
"v_mfma_f32_16x16x32_bf16 v[84:87], a[212:215], v[180:183], v[84:87]\n\t"
"v_mul_f32 v123, v123, v22\n\t"
"v_mul_f32 v56, v56, v22\n\t"
"v_mfma_f32_16x16x32_bf16 v[36:39], a[208:211], v[180:183], v[36:39]\n\t"
"v_mul_f32 v57, v57, v22\n\t"
"v_mul_f32 v58, v58, v22\n\t"
"v_mfma_f32_16x16x32_bf16 v[40:43], a[208:211], a[0:3], v[40:43]\n\t"
"ds_read_b64_tr_b16 a[204:205], v15 offset:40960\n\t"
"v_mul_f32 v59, v59, v22\n\t"
"v_mfma_f32_16x16x32_bf16 v[88:91], a[212:215], a[0:3], v[88:91]\n\t"
"ds_read_b64_tr_b16 a[206:207], v15 offset:43008\n\t"
"v_cvt_pk_bf16_f32 v120, v120, v121\n\t"
"v_mfma_f32_16x16x32_bf16 v[92:95], a[212:215], a[4:7], v[92:95]\n\t"
"v_cvt_pk_bf16_f32 v121, v122, v123\n\t"
"v_cvt_pk_bf16_f32 v122, v56, v57\n\t"
"v_mfma_f32_16x16x32_bf16 v[44:47], a[208:211], a[4:7], v[44:47]\n\t"
"s_waitcnt lgkmcnt(4)\n\t"
"v_cvt_pk_bf16_f32 v123, v58, v59\n\t"
"v_mul_f32 v124, v124, v23\n\t"
"v_mfma_f32_16x16x32_bf16 v[32:35], a[192:195], v[184:187], v[32:35]\n\t"
"ds_read_b64_tr_b16 a[208:209], v14 offset:36864\n\t"
"buffer_store_dwordx4 v[120:123], v1, s[8:11], s52 offen offset:2048 sc1\n\t"
"v_mfma_f32_16x16x32_bf16 v[80:83], a[196:199], v[184:187], v[80:83]\n\t"
"ds_read_b64_tr_b16 a[210:211], v14 offset:38912\n\t"
"v_mul_f32 v125, v125, v23\n\t"
"v_mfma_f32_16x16x32_bf16 v[84:87], a[196:199], v[188:191], v[84:87]\n\t"
"v_mul_f32 v126, v126, v23\n\t"
"v_mul_f32 v127, v127, v23\n\t"
"v_mfma_f32_16x16x32_bf16 v[36:39], a[192:195], v[188:191], v[36:39]\n\t"
"v_mul_f32 v60, v60, v23\n\t"
"v_mul_f32 v61, v61, v23\n\t"
"v_mfma_f32_16x16x32_bf16 v[40:43], a[192:195], a[8:11], v[40:43]\n\t"
"ds_read_b64_tr_b16 a[212:213], v15 offset:36864\n\t"
"v_mul_f32 v62, v62, v23\n\t"
"v_mfma_f32_16x16x32_bf16 v[88:91], a[196:199], a[8:11], v[88:91]\n\t"
"ds_read_b64_tr_b16 a[214:215], v15 offset:38912\n\t"
"v_mul_f32 v63, v63, v23\n\t"
"v_mfma_f32_16x16x32_bf16 v[92:95], a[196:199], a[12:15], v[92:95]\n\t"
"v_cvt_pk_bf16_f32 v124, v124, v125\n\t"
"v_cvt_pk_bf16_f32 v125, v126, v127\n\t"
"v_mfma_f32_16x16x32_bf16 v[44:47], a[192:195], a[12:15], v[44:47]\n\t"
"s_waitcnt lgkmcnt(4)\n\t"
"v_cvt_pk_bf16_f32 v126, v60, v61\n\t"
"v_cvt_pk_bf16_f32 v127, v62, v63\n\t"
"v_mfma_f32_16x16x32_bf16 v[32:35], a[200:203], v[192:195], v[32:35]\n\t"
"ds_read_b64_tr_b16 a[192:193], v14 offset:32768\n\t"
"v_lshlrev_b32 v16, 16, v16\n\t"
"v_mfma_f32_16x16x32_bf16 v[80:83], a[204:207], v[192:195], v[80:83]\n\t"
"ds_read_b64_tr_b16 a[194:195], v14 offset:34816\n\t"
"buffer_store_dwordx4 v[124:127], v1, s[8:11], s52 offen offset:3072 sc1\n\t"
"v_mfma_f32_16x16x32_bf16 v[84:87], a[204:207], v[196:199], v[84:87]\n\t"
"v_lshlrev_b32 v17, 16, v17\n\t"
"v_sub_f32 v16, v10, v16\n\t"
"v_mfma_f32_16x16x32_bf16 v[36:39], a[200:203], v[196:199], v[36:39]\n\t"
"v_sub_f32 v17, v11, v17\n\t"
"v_cvt_pk_bf16_f32 v10, v10, v16\n\t"
"v_mfma_f32_16x16x32_bf16 v[40:43], a[200:203], a[16:19], v[40:43]\n\t"
"ds_read_b64_tr_b16 a[196:197], v15 offset:32768\n\t"
"v_cvt_pk_bf16_f32 v11, v11, v17\n\t"
"v_mfma_f32_16x16x32_bf16 v[88:91], a[204:207], a[16:19], v[88:91]\n\t"
"ds_read_b64_tr_b16 a[198:199], v15 offset:34816\n\t"
"v_bfe_u32 v16, %[vT], 4, 1\n\t"
"v_mfma_f32_16x16x32_bf16 v[92:95], a[204:207], a[20:23], v[92:95]\n\t"
"v_mul_u32_u24 v12, 1056, v16\n\t"
"v_bfe_u32 v16, %[vT], 5, 1\n\t"
"v_mfma_f32_16x16x32_bf16 v[44:47], a[200:203], a[20:23], v[44:47]\n\t"
"s_waitcnt lgkmcnt(4)\n\t"
"v_lshl_add_u32 v12, v16, 8, v12\n\t"
"v_and_b32 v16, 3, %[vT]\n\t"
"v_mfma_f32_16x16x32_bf16 v[32:35], a[208:211], v[200:203], v[32:35]\n\t"
"ds_read_b64_tr_b16 a[200:201], v14 offset:28672\n\t"
"v_lshl_add_u32 v12, v16, 3, v12\n\t"
"v_mfma_f32_16x16x32_bf16 v[80:83], a[212:215], v[200:203], v[80:83]\n\t"
"ds_read_b64_tr_b16 a[202:203], v14 offset:30720\n\t"
"v_bfe_u32 v16, %[vT], 2, 2\n\t"
"v_mfma_f32_16x16x32_bf16 v[84:87], a[212:215], v[204:207], v[84:87]\n\t"
"buffer_load_dword v30, v29, s[8:11], 0 offen offset:0 sc1\n\t"
"v_mfma_f32_16x16x32_bf16 v[36:39], a[208:211], v[204:207], v[36:39]\n\t"
"v_lshl_add_u32 v12, v16, 6, v12\n\t"
"v_add_u32 v17, 100672, v12\n\t"
"v_mfma_f32_16x16x32_bf16 v[40:43], a[208:211], a[24:27], v[40:43]\n\t"
"ds_read_b64_tr_b16 a[204:205], v15 offset:28672\n\t"
"buffer_store_dwordx2 v[10:11], v4, s[8:11], s84 offen offset:0 sc1\n\t"
"v_mfma_f32_16x16x32_bf16 v[88:91], a[212:215], a[24:27], v[88:91]\n\t"
"s_add_u32 m0, s73, 58560\n\t"
"ds_read_b64_tr_b16 a[206:207], v15 offset:30720\n\t"
"v_add_u32 v19, 37504, v12\n\t"
"v_mfma_f32_16x16x32_bf16 v[92:95], a[212:215], a[28:31], v[92:95]\n\t"
"v_lshl_add_u32 v18, v16, 11, v19\n\t"
"v_lshl_add_u32 v16, v16, 11, v17\n\t"
"v_mfma_f32_16x16x32_bf16 v[44:47], a[208:211], a[28:31], v[44:47]\n\t"
"s_waitcnt lgkmcnt(4)\n\t"
"buffer_load_dwordx4 v2, s[24:27], s62 offen offset:0 lds\n\t"
"v_mfma_f32_16x16x32_bf16 v[32:35], a[192:195], v[208:211], v[32:35]\n\t"
"ds_read_b64_tr_b16 a[208:209], v14 offset:24576\n\t"
"ds_read_b64_tr_b16 a[210:211], v14 offset:26624\n\t"
"v_mfma_f32_16x16x32_bf16 v[80:83], a[196:199], v[208:211], v[80:83]\n\t"
"s_barrier\n\t"
"v_mfma_f32_16x16x32_bf16 v[84:87], a[196:199], v[212:215], v[84:87]\n\t"
"v_mfma_f32_16x16x32_bf16 v[36:39], a[192:195], v[212:215], v[36:39]\n\t"
"s_addk_i32 m0, 832\n\t"
"ds_read_b64_tr_b16 a[212:213], v15 offset:24576\n\t"
"v_mfma_f32_16x16x32_bf16 v[40:43], a[192:195], a[32:35], v[40:43]\n\t"
"buffer_load_dwordx4 v2, s[24:27], s62 offen offset:1280 lds\n\t"
"v_mfma_f32_16x16x32_bf16 v[88:91], a[196:199], a[32:35], v[88:91]\n\t"
"ds_read_b64_tr_b16 a[214:215], v15 offset:26624\n\t"
"v_mfma_f32_16x16x32_bf16 v[92:95], a[196:199], a[36:39], v[92:95]\n\t"
"v_mfma_f32_16x16x32_bf16 v[44:47], a[192:195], a[36:39], v[44:47]\n\t"
"s_waitcnt lgkmcnt(4)\n\t"
"v_mfma_f32_16x16x32_bf16 v[32:35], a[200:203], v[216:219], v[32:35]\n\t"
"ds_read_b64_tr_b16 a[192:193], v14 offset:20480\n\t"
"v_mfma_f32_16x16x32_bf16 v[80:83], a[204:207], v[216:219], v[80:83]\n\t"
"ds_read_b64_tr_b16 a[194:195], v14 offset:22528\n\t"
"s_addk_i32 m0, 832\n\t"
"v_mfma_f32_16x16x32_bf16 v[84:87], a[204:207], v[220:223], v[84:87]\n\t"
"buffer_load_dwordx4 v2, s[24:27], s62 offen offset:2560 lds\n\t"
"v_mfma_f32_16x16x32_bf16 v[36:39], a[200:203], v[220:223], v[36:39]\n\t"
"v_mfma_f32_16x16x32_bf16 v[40:43], a[200:203], a[40:43], v[40:43]\n\t"
"ds_read_b64_tr_b16 a[196:197], v15 offset:20480\n\t"
"v_mfma_f32_16x16x32_bf16 v[88:91], a[204:207], a[40:43], v[88:91]\n\t"
"ds_read_b64_tr_b16 a[198:199], v15 offset:22528\n\t"
"v_mfma_f32_16x16x32_bf16 v[92:95], a[204:207], a[44:47], v[92:95]\n\t"
"v_mfma_f32_16x16x32_bf16 v[44:47], a[200:203], a[44:47], v[44:47]\n\t"
"s_addk_i32 m0, 832\n\t"
"s_waitcnt lgkmcnt(4)\n\t"
"ds_read_b64_tr_b16 a[200:201], v14 offset:16384\n\t"
"v_mfma_f32_16x16x32_bf16 v[32:35], a[208:211], v[224:227], v[32:35]\n\t"
"buffer_load_dwordx4 v2, s[24:27], s62 offen offset:3840 lds\n\t"
"v_mfma_f32_16x16x32_bf16 v[80:83], a[212:215], v[224:227], v[80:83]\n\t"
"ds_read_b64_tr_b16 a[202:203], v14 offset:18432\n\t"
"v_mfma_f32_16x16x32_bf16 v[84:87], a[212:215], v[228:231], v[84:87]\n\t"
"v_mfma_f32_16x16x32_bf16 v[36:39], a[208:211], v[228:231], v[36:39]\n\t"
"v_mfma_f32_16x16x32_bf16 v[40:43], a[208:211], a[48:51], v[40:43]\n\t"
"ds_read_b64_tr_b16 a[204:205], v15 offset:16384\n\t"
"v_mfma_f32_16x16x32_bf16 v[88:91], a[212:215], a[48:51], v[88:91]\n\t"
"ds_read_b64_tr_b16 a[206:207], v15 offset:18432\n\t"
"s_addk_i32 m0, 5888\n\t"
"v_mfma_f32_16x16x32_bf16 v[92:95], a[212:215], a[52:55], v[92:95]\n\t"
"buffer_load_dwordx4 v9, s[24:27], s62 offen offset:64 lds\n\t"
"v_mfma_f32_16x16x32_bf16 v[44:47], a[208:211], a[52:55], v[44:47]\n\t"
"s_waitcnt lgkmcnt(4)\n\t"
"v_mfma_f32_16x16x32_bf16 v[32:35], a[192:195], v[232:235], v[32:35]\n\t"
"ds_read_b64_tr_b16 a[208:209], v14 offset:12288\n\t"
"v_mfma_f32_16x16x32_bf16 v[80:83], a[196:199], v[232:235], v[80:83]\n\t"
"ds_read_b64_tr_b16 a[210:211], v14 offset:14336\n\t"
"v_mfma_f32_16x16x32_bf16 v[84:87], a[196:199], v[236:239], v[84:87]\n\t"
"v_mfma_f32_16x16x32_bf16 v[36:39], a[192:195], v[236:239], v[36:39]\n\t"
"s_addk_i32 m0, -29440\n\t"
"ds_read_b64_tr_b16 a[212:213], v15 offset:12288\n\t"
"v_mfma_f32_16x16x32_bf16 v[40:43], a[192:195], a[56:59], v[40:43]\n\t"
"buffer_load_dwordx4 v2, s[24:27], s63 offen offset:0 lds\n\t"
"v_mfma_f32_16x16x32_bf16 v[88:91], a[196:199], a[56:59], v[88:91]\n\t"
"ds_read_b64_tr_b16 a[214:215], v15 offset:14336\n\t"
"v_mfma_f32_16x16x32_bf16 v[92:95], a[196:199], a[60:63], v[92:95]\n\t"
"v_mfma_f32_16x16x32_bf16 v[44:47], a[192:195], a[60:63], v[44:47]\n\t"
"s_waitcnt lgkmcnt(4)\n\t"
"v_mfma_f32_16x16x32_bf16 v[32:35], a[200:203], v[240:243], v[32:35]\n\t"
"ds_read_b64_tr_b16 a[192:193], v14 offset:8192\n\t"
"v_mfma_f32_16x16x32_bf16 v[80:83], a[204:207], v[240:243], v[80:83]\n\t"
"ds_read_b64_tr_b16 a[194:195], v14 offset:10240\n\t"
"s_addk_i32 m0, 832\n\t"
"v_mfma_f32_16x16x32_bf16 v[84:87], a[204:207], v[244:247], v[84:87]\n\t"
"buffer_load_dwordx4 v2, s[24:27], s63 offen offset:1280 lds\n\t"
"v_mfma_f32_16x16x32_bf16 v[36:39], a[200:203], v[244:247], v[36:39]\n\t"
"v_mfma_f32_16x16x32_bf16 v[40:43], a[200:203], a[64:67], v[40:43]\n\t"
"ds_read_b64_tr_b16 a[196:197], v15 offset:8192\n\t"
"v_mfma_f32_16x16x32_bf16 v[88:91], a[204:207], a[64:67], v[88:91]\n\t"
"ds_read_b64_tr_b16 a[198:199], v15 offset:10240\n\t"
"v_mfma_f32_16x16x32_bf16 v[92:95], a[204:207], a[68:71], v[92:95]\n\t"
"v_mfma_f32_16x16x32_bf16 v[44:47], a[200:203], a[68:71], v[44:47]\n\t"
"s_addk_i32 m0, 832\n\t"
"s_waitcnt lgkmcnt(4)\n\t"
"ds_read_b64_tr_b16 a[200:201], v14 offset:4096\n\t"
"v_mfma_f32_16x16x32_bf16 v[32:35], a[208:211], v[248:251], v[32:35]\n\t"
"buffer_load_dwordx4 v2, s[24:27], s63 offen offset:2560 lds\n\t"
"ds_read_b64_tr_b16 a[202:203], v14 offset:6144\n\t"
"v_mfma_f32_16x16x32_bf16 v[80:83], a[212:215], v[248:251], v[80:83]\n\t"
"s_barrier\n\t"
"v_mfma_f32_16x16x32_bf16 v[84:87], a[212:215], v[252:255], v[84:87]\n\t"
"v_mfma_f32_16x16x32_bf16 v[36:39], a[208:211], v[252:255], v[36:39]\n\t"
"ds_read_b64_tr_b16 a[204:205], v15 offset:4096\n\t"
"v_mfma_f32_16x16x32_bf16 v[40:43], a[208:211], a[72:75], v[40:43]\n\t"
"s_waitcnt vmcnt(9)\n\t"
"v_sub_u32 v30, s39, v30\n\t"
"v_cmp_lt_u32 vcc, 5, v30\n\t"
"s_cbranch_vccnz .LslC%=\n\t"
".LbkC%=:\n"
"v_mfma_f32_16x16x32_bf16 v[88:91], a[212:215], a[72:75], v[88:91]\n\t"
"ds_read_b64_tr_b16 a[206:207], v15 offset:6144\n\t"
"s_addk_i32 m0, 832\n\t"
"v_mfma_f32_16x16x32_bf16 v[92:95], a[212:215], a[76:79], v[92:95]\n\t"
"buffer_load_dwordx4 v2, s[24:27], s63 offen offset:3840 lds\n\t"
"v_mfma_f32_16x16x32_bf16 v[44:47], a[208:211], a[76:79], v[44:47]\n\t"
"s_waitcnt lgkmcnt(4)\n\t"
"v_mfma_f32_16x16x32_bf16 v[32:35], a[192:195], a[80:83], v[32:35]\n\t"
"ds_read_b64_tr_b16 a[208:209], v14 offset:0\n\t"
"v_mfma_f32_16x16x32_bf16 v[80:83], a[196:199], a[80:83], v[80:83]\n\t"
"ds_read_b64_tr_b16 a[210:211], v14 offset:2048\n\t"
"v_mfma_f32_16x16x32_bf16 v[84:87], a[196:199], a[84:87], v[84:87]\n\t"
"v_mfma_f32_16x16x32_bf16 v[36:39], a[192:195], a[84:87], v[36:39]\n\t"
"s_addk_i32 m0, 5888\n\t"
"ds_read_b64_tr_b16 a[212:213], v15 offset:0\n\t"
"v_mfma_f32_16x16x32_bf16 v[40:43], a[192:195], a[88:91], v[40:43]\n\t"
"buffer_load_dwordx4 v9, s[24:27], s63 offen offset:64 lds\n\t"
"v_mfma_f32_16x16x32_bf16 v[88:91], a[196:199], a[88:91], v[88:91]\n\t"
"ds_read_b64_tr_b16 a[214:215], v15 offset:2048\n\t"
"v_mfma_f32_16x16x32_bf16 v[92:95], a[196:199], a[92:95], v[92:95]\n\t"
"v_mfma_f32_16x16x32_bf16 v[44:47], a[192:195], a[92:95], v[44:47]\n\t"
"s_barrier\n\t"
"s_waitcnt lgkmcnt(4)\n\t"
"v_mfma_f32_16x16x32_bf16 v[32:35], a[200:203], v[64:67], v[32:35]\n\t"
"ds_read_b64_tr_b16 a[192:193], v16 offset:42112\n\t"
"v_mfma_f32_16x16x32_bf16 v[80:83], a[204:207], v[64:67], v[80:83]\n\t"
"ds_read_b64_tr_b16 a[194:195], v16 offset:42624\n\t"
"v_mfma_f32_16x16x32_bf16 v[84:87], a[204:207], v[68:71], v[84:87]\n\t"
"buffer_load_dwordx4 a[124:127], v1, s[8:11], s57 offen offset:2048 sc1\n\t"
"v_mfma_f32_16x16x32_bf16 v[36:39], a[200:203], v[68:71], v[36:39]\n\t"
"v_mfma_f32_16x16x32_bf16 v[40:43], a[200:203], v[72:75], v[40:43]\n\t"
"ds_read_b64_tr_b16 a[196:197], v16 offset:42144\n\t"
"v_mfma_f32_16x16x32_bf16 v[88:91], a[204:207], v[72:75], v[88:91]\n\t"
"ds_read_b64_tr_b16 a[198:199], v16 offset:42656\n\t"
"v_mfma_f32_16x16x32_bf16 v[92:95], a[204:207], v[76:79], v[92:95]\n\t"
"buffer_load_dwordx4 a[236:239], v1, s[8:11], s57 offen offset:3072 sc1\n\t"
"v_mfma_f32_16x16x32_bf16 v[44:47], a[200:203], v[76:79], v[44:47]\n\t"
"s_waitcnt lgkmcnt(4)\n\t"
"v_mfma_f32_16x16x32_bf16 v[32:35], a[208:211], v[96:99], v[32:35]\n\t"
"ds_read_b64_tr_b16 a[200:201], v16 offset:42176\n\t"
"v_mfma_f32_16x16x32_bf16 v[80:83], a[212:215], v[96:99], v[80:83]\n\t"
"s_add_u32 s61, s61, s56\n\t"
"ds_read_b64_tr_b16 a[202:203], v16 offset:42688\n\t"
"s_addk_i32 m0, -29440\n\t"
"v_mfma_f32_16x16x32_bf16 v[84:87], a[212:215], v[100:103], v[84:87]\n\t"
"buffer_load_dwordx4 v2, s[24:27], s61 offen offset:0 lds\n\t"
"v_mfma_f32_16x16x32_bf16 v[36:39], a[208:211], v[100:103], v[36:39]\n\t"
"v_mfma_f32_16x16x32_bf16 v[40:43], a[208:211], v[104:107], v[40:43]\n\t"
"ds_read_b64_tr_b16 a[204:205], v16 offset:42208\n\t"
"v_mfma_f32_16x16x32_bf16 v[88:91], a[212:215], v[104:107], v[88:91]\n\t"
"ds_read_b64_tr_b16 a[206:207], v16 offset:42720\n\t"
"v_mfma_f32_16x16x32_bf16 v[92:95], a[212:215], v[108:111], v[92:95]\n\t"
"v_mfma_f32_16x16x32_bf16 v[44:47], a[208:211], v[108:111], v[44:47]\n\t"
"s_addk_i32 m0, 832\n\t"
"s_waitcnt lgkmcnt(4)\n\t"
"v_mfma_f32_16x16x32_bf16 v[128:131], a[192:195], a[128:131], 0\n\t"
"ds_read_b64_tr_b16 a[208:209], v17 offset:50560\n\t"
"v_mov_b32 v14, v12\n\t"
"v_mfma_f32_16x16x32_bf16 v[144:147], a[196:199], a[128:131], 0\n\t"
"ds_read_b64_tr_b16 a[210:211], v17 offset:51072\n\t"
"v_subrev_u32 v13, 37504, v18\n\t"
"v_mfma_f32_16x16x32_bf16 v[148:151], a[196:199], a[132:135], 0\n\t"
"buffer_load_dwordx4 v2, s[24:27], s61 offen offset:1280 lds\n\t"
"v_mfma_f32_16x16x32_bf16 v[132:135], a[192:195], a[132:135], 0\n\t"
"v_mfma_f32_16x16x32_bf16 v[136:139], a[192:195], a[136:139], 0\n\t"
"ds_read_b64_tr_b16 a[212:213], v16 offset:42240\n\t"
"v_mul_f32 v35, v35, v20\n\t"
"v_mfma_f32_16x16x32_bf16 v[152:155], a[196:199], a[136:139], 0\n\t"
"ds_read_b64_tr_b16 a[214:215], v16 offset:42752\n\t"
"v_mul_f32 v32, v32, v20\n\t"
"v_mfma_f32_16x16x32_bf16 v[156:159], a[196:199], a[140:143], 0\n\t"
"s_addk_i32 m0, 832\n\t"
"v_mul_f32 v33, v33, v20\n\t"
"v_mul_f32 v34, v34, v20\n\t"
"v_mfma_f32_16x16x32_bf16 v[140:143], a[192:195], a[140:143], 0\n\t"
"s_waitcnt lgkmcnt(4)\n\t"
"buffer_load_dwordx4 v2, s[24:27], s61 offen offset:2560 lds\n\t"
"v_mul_f32 v80, v80, v20\n\t"
"v_mfma_f32_16x16x32_bf16 v[172:175], a[200:203], a[140:143], 0\n\t"
"ds_read_b64_tr_b16 a[192:193], v16 offset:42272\n\t"
"v_mul_f32 v81, v81, v20\n\t"
"v_mfma_f32_16x16x32_bf16 v[188:191], a[204:207], a[140:143], 0\n\t"
"ds_read_b64_tr_b16 a[194:195], v16 offset:42784\n\t"
"v_mul_f32 v82, v82, v20\n\t"
"v_mfma_f32_16x16x32_bf16 v[184:187], a[204:207], a[136:139], 0\n\t"
"v_mul_f32 v83, v83, v20\n\t"
"v_cvt_pk_bf16_f32 v32, v32, v33\n\t"
"v_mfma_f32_16x16x32_bf16 v[168:171], a[200:203], a[136:139], 0\n\t"
"v_cvt_pk_bf16_f32 v33, v34, v35\n\t"
"v_cvt_pk_bf16_f32 v34, v80, v81\n\t"
"v_mfma_f32_16x16x32_bf16 v[164:167], a[200:203], a[132:135], 0\n\t"
"ds_read_b64_tr_b16 a[196:197], v16 offset:42304\n\t"
"v_cvt_pk_bf16_f32 v35, v82, v83\n\t"
"v_mfma_f32_16x16x32_bf16 v[180:183], a[204:207], a[132:135], 0\n\t"
"s_addk_i32 s52, 4096\n\t"
"ds_read_b64_tr_b16 a[198:199], v16 offset:42816\n\t"
"v_mul_f32 v36, v36, v21\n\t"
"v_mfma_f32_16x16x32_bf16 v[176:179], a[204:207], a[128:131], 0\n\t"
"buffer_store_dwordx4 v[32:35], v1, s[8:11], s52 offen offset:0 sc1\n\t"
"v_mfma_f32_16x16x32_bf16 v[160:163], a[200:203], a[128:131], 0\n\t"
"s_waitcnt lgkmcnt(4)\n\t"
"v_mul_f32 v37, v37, v21\n\t"
"v_mul_f32 v38, v38, v21\n\t"
"v_mfma_f32_16x16x32_bf16 v[192:195], a[208:211], a[128:131], 0\n\t"
"ds_read_b64_tr_b16 a[200:201], v16 offset:42336\n\t"
"v_mul_f32 v39, v39, v21\n\t"
"v_mfma_f32_16x16x32_bf16 v[208:211], a[212:215], a[128:131], 0\n\t"
"ds_read_b64_tr_b16 a[202:203], v16 offset:42848\n\t"
"v_mul_f32 v84, v84, v21\n\t"
"v_mfma_f32_16x16x32_bf16 v[212:215], a[212:215], a[132:135], 0\n\t"
"v_mul_f32 v85, v85, v21\n\t"
"v_mul_f32 v86, v86, v21\n\t"
"v_mfma_f32_16x16x32_bf16 v[196:199], a[208:211], a[132:135], 0\n\t"
"v_mul_f32 v87, v87, v21\n\t"
"v_cvt_pk_bf16_f32 v36, v36, v37\n\t"
"v_mfma_f32_16x16x32_bf16 v[200:203], a[208:211], a[136:139], 0\n\t"
"ds_read_b64_tr_b16 a[204:205], v17 offset:50592\n\t"
"v_cvt_pk_bf16_f32 v37, v38, v39\n\t"
"v_mfma_f32_16x16x32_bf16 v[216:219], a[212:215], a[136:139], 0\n\t"
"ds_read_b64_tr_b16 a[206:207], v17 offset:51104\n\t"
"v_cvt_pk_bf16_f32 v38, v84, v85\n\t"
"v_mfma_f32_16x16x32_bf16 v[220:223], a[212:215], a[140:143], 0\n\t"
"v_cvt_pk_bf16_f32 v39, v86, v87\n\t"
"v_mul_f32 v40, v40, v22\n\t"
"v_mfma_f32_16x16x32_bf16 v[204:207], a[208:211], a[140:143], 0\n\t"
"s_waitcnt lgkmcnt(4)\n\t"
"buffer_store_dwordx4 v[36:39], v1, s[8:11], s52 offen offset:1024 sc1\n\t"
"v_mfma_f32_16x16x32_bf16 v[236:239], a[192:195], a[140:143], 0\n\t"
"ds_read_b64_tr_b16 a[208:209], v16 offset:52640\n\t"
"v_mul_f32 v41, v41, v22\n\t"
"v_mfma_f32_16x16x32_bf16 v[252:255], a[196:199], a[140:143], 0\n\t"
"ds_read_b64_tr_b16 a[210:211], v16 offset:53152\n\t"
"v_mul_f32 v42, v42, v22\n\t"
"v_mfma_f32_16x16x32_bf16 v[248:251], a[196:199], a[136:139], 0\n\t"
"v_mul_f32 v43, v43, v22\n\t"
"v_mul_f32 v88, v88, v22\n\t"
"v_mfma_f32_16x16x32_bf16 v[232:235], a[192:195], a[136:139], 0\n\t"
"v_mul_f32 v89, v89, v22\n\t"
"v_mul_f32 v90, v90, v22\n\t"
"v_mfma_f32_16x16x32_bf16 v[228:231], a[192:195], a[132:135], 0\n\t"
"ds_read_b64_tr_b16 a[212:213], v16 offset:52672\n\t"
"v_mul_f32 v91, v91, v22\n\t"
"v_mfma_f32_16x16x32_bf16 v[244:247], a[196:199], a[132:135], 0\n\t"
"ds_read_b64_tr_b16 a[214:215], v16 offset:53184\n\t"
"v_cvt_pk_bf16_f32 v40, v40, v41\n\t"
"v_mfma_f32_16x16x32_bf16 v[240:243], a[196:199], a[128:131], 0\n\t"
"v_cvt_pk_bf16_f32 v41, v42, v43\n\t"
"v_cvt_pk_bf16_f32 v42, v88, v89\n\t"
"v_mfma_f32_16x16x32_bf16 v[224:227], a[192:195], a[128:131], 0\n\t"
"s_waitcnt lgkmcnt(4)\n\t"
"v_cvt_pk_bf16_f32 v43, v90, v91\n\t"
"v_mul_f32 v44, v44, v23\n\t"
"v_mfma_f32_16x16x32_bf16 v[64:67], a[200:203], a[128:131], 0\n\t"
"ds_read_b64_tr_b16 a[192:193], v16 offset:52704\n\t"
"buffer_store_dwordx4 v[40:43], v1, s[8:11], s52 offen offset:2048 sc1\n\t"
"v_mfma_f32_16x16x32_bf16 v[96:99], a[204:207], a[128:131], 0\n\t"
"ds_read_b64_tr_b16 a[194:195], v16 offset:53216\n\t"
"v_mul_f32 v45, v45, v23\n\t"
"v_mfma_f32_16x16x32_bf16 v[100:103], a[204:207], a[132:135], 0\n\t"
"v_mul_f32 v46, v46, v23\n\t"
"v_mul_f32 v47, v47, v23\n\t"
"v_mfma_f32_16x16x32_bf16 v[68:71], a[200:203], a[132:135], 0\n\t"
"v_mul_f32 v92, v92, v23\n\t"
"v_mul_f32 v93, v93, v23\n\t"
"v_mfma_f32_16x16x32_bf16 v[72:75], a[200:203], a[136:139], 0\n\t"
"ds_read_b64_tr_b16 a[196:197], v16 offset:52736\n\t"
"v_mul_f32 v94, v94, v23\n\t"
"v_mfma_f32_16x16x32_bf16 v[104:107], a[204:207], a[136:139], 0\n\t"
"ds_read_b64_tr_b16 a[198:199], v16 offset:53248\n\t"
"v_mul_f32 v95, v95, v23\n\t"
"v_mfma_f32_16x16x32_bf16 v[108:111], a[204:207], a[140:143], 0\n\t"
"v_cvt_pk_bf16_f32 v44, v44, v45\n\t"
"v_cvt_pk_bf16_f32 v45, v46, v47\n\t"
"v_mfma_f32_16x16x32_bf16 v[76:79], a[200:203], a[140:143], 0\n\t"
"s_waitcnt lgkmcnt(4)\n\t"
"v_cvt_pk_bf16_f32 v46, v92, v93\n\t"
"v_cvt_pk_bf16_f32 v47, v94, v95\n\t"
"v_mfma_f32_16x16x32_bf16 v[128:131], a[208:211], a[144:147], v[128:131]\n\t"
"ds_read_b64_tr_b16 a[200:201], v17 offset:61088\n\t"
"v_mfma_f32_16x16x32_bf16 v[144:147], a[212:215], a[144:147], v[144:147]\n\t"
"buffer_store_dwordx4 v[44:47], v1, s[8:11], s52 offen offset:3072 sc1\n\t"
"v_mfma_f32_16x16x32_bf16 v[148:151], a[212:215], a[148:151], v[148:151]\n\t"
"ds_read_b64_tr_b16 a[202:203], v17 offset:61600\n\t"
"v_mfma_f32_16x16x32_bf16 v[132:135], a[208:211], a[148:151], v[132:135]\n\t"
"v_mfma_f32_16x16x32_bf16 v[136:139], a[208:211], a[152:155], v[136:139]\n\t"
"ds_read_b64_tr_b16 a[204:205], v16 offset:52768\n\t"
"v_mfma_f32_16x16x32_bf16 v[152:155], a[212:215], a[152:155], v[152:155]\n\t"
"ds_read_b64_tr_b16 a[206:207], v16 offset:53280\n\t"
"v_mfma_f32_16x16x32_bf16 v[156:159], a[212:215], a[156:159], v[156:159]\n\t"
"v_mfma_f32_16x16x32_bf16 v[140:143], a[208:211], a[156:159], v[140:143]\n\t"
"s_waitcnt lgkmcnt(4)\n\t"
"v_mfma_f32_16x16x32_bf16 v[172:175], a[192:195], a[156:159], v[172:175]\n\t"
"ds_read_b64_tr_b16 a[208:209], v16 offset:52800\n\t"
"v_mfma_f32_16x16x32_bf16 v[188:191], a[196:199], a[156:159], v[188:191]\n\t"
"ds_read_b64_tr_b16 a[210:211], v16 offset:53312\n\t"
"s_addk_i32 m0, 832\n\t"
"v_mfma_f32_16x16x32_bf16 v[184:187], a[196:199], a[152:155], v[184:187]\n\t"
"buffer_load_dwordx4 v2, s[24:27], s61 offen offset:3840 lds\n\t"
"v_mfma_f32_16x16x32_bf16 v[168:171], a[192:195], a[152:155], v[168:171]\n\t"
"v_mfma_f32_16x16x32_bf16 v[164:167], a[192:195], a[148:151], v[164:167]\n\t"
"ds_read_b64_tr_b16 a[212:213], v16 offset:52832\n\t"
"v_mfma_f32_16x16x32_bf16 v[180:183], a[196:199], a[148:151], v[180:183]\n\t"
"ds_read_b64_tr_b16 a[214:215], v16 offset:53344\n\t"
"v_mfma_f32_16x16x32_bf16 v[176:179], a[196:199], a[144:147], v[176:179]\n\t"
"s_addk_i32 m0, 5888\n\t"
"v_mfma_f32_16x16x32_bf16 v[160:163], a[192:195], a[144:147], v[160:163]\n\t"
"s_waitcnt lgkmcnt(4)\n\t"
"buffer_load_dwordx4 v9, s[24:27], s61 offen offset:64 lds\n\t"
"v_mfma_f32_16x16x32_bf16 v[192:195], a[200:203], a[144:147], v[192:195]\n\t"
"ds_read_b64_tr_b16 a[192:193], v16 offset:52864\n\t"
"v_mfma_f32_16x16x32_bf16 v[208:211], a[204:207], a[144:147], v[208:211]\n\t"
"ds_read_b64_tr_b16 a[194:195], v16 offset:53376\n\t"
"v_mfma_f32_16x16x32_bf16 v[212:215], a[204:207], a[148:151], v[212:215]\n\t"
"v_mfma_f32_16x16x32_bf16 v[196:199], a[200:203], a[148:151], v[196:199]\n\t"
"v_mfma_f32_16x16x32_bf16 v[200:203], a[200:203], a[152:155], v[200:203]\n\t"
"ds_read_b64_tr_b16 a[196:197], v17 offset:61120\n\t"
"s_addk_i32 s58, 4096\n\t"
"v_mfma_f32_16x16x32_bf16 v[216:219], a[204:207], a[152:155], v[216:219]\n\t"
"buffer_load_dwordx4 a[240:243], v1, s[8:11], s58 offen offset:0 sc1\n\t"
"v_mfma_f32_16x16x32_bf16 v[220:223], a[204:207], a[156:159], v[220:223]\n\t"
"ds_read_b64_tr_b16 a[198:199], v17 offset:61632\n\t"
"v_mfma_f32_16x16x32_bf16 v[204:207], a[200:203], a[156:159], v[204:207]\n\t"
"s_waitcnt lgkmcnt(4)\n\t"
"v_mfma_f32_16x16x32_bf16 v[236:239], a[208:211], a[156:159], v[236:239]\n\t"
"ds_read_b64_tr_b16 a[200:201], v16 offset:21056\n\t"
"v_mfma_f32_16x16x32_bf16 v[252:255], a[212:215], a[156:159], v[252:255]\n\t"
"ds_read_b64_tr_b16 a[202:203], v16 offset:21568\n\t"
"v_mfma_f32_16x16x32_bf16 v[248:251], a[212:215], a[152:155], v[248:251]\n\t"
"buffer_load_dwordx4 a[0:3], v1, s[8:11], s58 offen offset:1024 sc1\n\t"
"v_mfma_f32_16x16x32_bf16 v[232:235], a[208:211], a[152:155], v[232:235]\n\t"
"v_mfma_f32_16x16x32_bf16 v[228:231], a[208:211], a[148:151], v[228:231]\n\t"
"ds_read_b64_tr_b16 a[204:205], v16 offset:21088\n\t"
"v_mfma_f32_16x16x32_bf16 v[244:247], a[212:215], a[148:151], v[244:247]\n\t"
"ds_read_b64_tr_b16 a[206:207], v16 offset:21600\n\t"
"v_mfma_f32_16x16x32_bf16 v[240:243], a[212:215], a[144:147], v[240:243]\n\t"
"buffer_load_dwordx4 a[4:7], v1, s[8:11], s58 offen offset:2048 sc1\n\t"
"v_mfma_f32_16x16x32_bf16 v[224:227], a[208:211], a[144:147], v[224:227]\n\t"
"s_waitcnt lgkmcnt(4)\n\t"
"v_mfma_f32_16x16x32_bf16 v[64:67], a[192:195], a[144:147], v[64:67]\n\t"
"ds_read_b64_tr_b16 a[208:209], v16 offset:21120\n\t"
"v_mfma_f32_16x16x32_bf16 v[96:99], a[196:199], a[144:147], v[96:99]\n\t"
"ds_read_b64_tr_b16 a[210:211], v16 offset:21632\n\t"
"v_mfma_f32_16x16x32_bf16 v[100:103], a[196:199], a[148:151], v[100:103]\n\t"
"s_barrier\n\t"
"v_mfma_f32_16x16x32_bf16 v[68:71], a[192:195], a[148:151], v[68:71]\n\t"
"buffer_load_dwordx4 a[8:11], v1, s[8:11], s58 offen offset:3072 sc1\n\t"
"v_mfma_f32_16x16x32_bf16 v[72:75], a[192:195], a[152:155], v[72:75]\n\t"
"ds_read_b64_tr_b16 a[212:213], v16 offset:21152\n\t"
"v_mfma_f32_16x16x32_bf16 v[104:107], a[196:199], a[152:155], v[104:107]\n\t"
"ds_read_b64_tr_b16 a[214:215], v16 offset:21664\n\t"
"v_mfma_f32_16x16x32_bf16 v[108:111], a[196:199], a[156:159], v[108:111]\n\t"
"s_add_u32 s62, s62, s56\n\t"
"s_add_u32 m0, s73, 142784\n\t"
"v_mfma_f32_16x16x32_bf16 v[76:79], a[192:195], a[156:159], v[76:79]\n\t"
"s_waitcnt lgkmcnt(4)\n\t"
"buffer_load_dwordx4 v2, s[24:27], s62 offen offset:0 lds\n\t"
"v_mfma_f32_16x16x32_bf16 v[128:131], a[200:203], a[160:163], v[128:131]\n\t"
"ds_read_b64_tr_b16 a[192:193], v17 offset:29504\n\t"
"v_mfma_f32_16x16x32_bf16 v[144:147], a[204:207], a[160:163], v[144:147]\n\t"
"ds_read_b64_tr_b16 a[194:195], v17 offset:30016\n\t"
"v_mfma_f32_16x16x32_bf16 v[148:151], a[204:207], a[164:167], v[148:151]\n\t"
"v_mfma_f32_16x16x32_bf16 v[132:135], a[200:203], a[164:167], v[132:135]\n\t"
"v_mfma_f32_16x16x32_bf16 v[136:139], a[200:203], a[168:171], v[136:139]\n\t"
"ds_read_b64_tr_b16 a[196:197], v16 offset:21184\n\t"
"s_addk_i32 m0, 832\n\t"
"v_mfma_f32_16x16x32_bf16 v[152:155], a[204:207], a[168:171], v[152:155]\n\t"
"buffer_load_dwordx4 v2, s[24:27], s62 offen offset:1280 lds\n\t"
"v_mfma_f32_16x16x32_bf16 v[156:159], a[204:207], a[172:175], v[156:159]\n\t"
"ds_read_b64_tr_b16 a[198:199], v16 offset:21696\n\t"
"v_mfma_f32_16x16x32_bf16 v[140:143], a[200:203], a[172:175], v[140:143]\n\t"
"s_waitcnt lgkmcnt(4)\n\t"
"v_mfma_f32_16x16x32_bf16 v[172:175], a[208:211], a[172:175], v[172:175]\n\t"
"ds_read_b64_tr_b16 a[200:201], v16 offset:21216\n\t"
"v_mfma_f32_16x16x32_bf16 v[188:191], a[212:215], a[172:175], v[188:191]\n\t"
"ds_read_b64_tr_b16 a[202:203], v16 offset:21728\n\t"
"v_mfma_f32_16x16x32_bf16 v[184:187], a[212:215], a[168:171], v[184:187]\n\t"
"s_addk_i32 m0, 832\n\t"
"v_mfma_f32_16x16x32_bf16 v[168:171], a[208:211], a[168:171], v[168:171]\n\t"
"buffer_load_dwordx4 v2, s[24:27], s62 offen offset:2560 lds\n\t"
"v_mfma_f32_16x16x32_bf16 v[164:167], a[208:211], a[164:167], v[164:167]\n\t"
".Lpg13%=:\n"
"s_cbranch_execz .Lhr0%=\n\t"
"ds_read_b64_tr_b16 a[204:205], v16 offset:21248\n\t"
"v_mfma_f32_16x16x32_bf16 v[180:183], a[212:215], a[164:167], v[180:183]\n\t"
"ds_read_b64_tr_b16 a[206:207], v16 offset:21760\n\t"
"v_mfma_f32_16x16x32_bf16 v[176:179], a[212:215], a[160:163], v[176:179]\n\t"
"v_mfma_f32_16x16x32_bf16 v[160:163], a[208:211], a[160:163], v[160:163]\n\t"
"s_waitcnt lgkmcnt(4)\n\t"
"v_mfma_f32_16x16x32_bf16 v[192:195], a[192:195], a[160:163], v[192:195]\n\t"
"ds_read_b64_tr_b16 a[208:209], v16 offset:21280\n\t"
"s_addk_i32 m0, 832\n\t"
"v_mfma_f32_16x16x32_bf16 v[208:211], a[196:199], a[160:163], v[208:211]\n\t"
"buffer_load_dwordx4 v2, s[24:27], s62 offen offset:3840 lds\n\t"
"v_mfma_f32_16x16x32_bf16 v[212:215], a[196:199], a[164:167], v[212:215]\n\t"
"ds_read_b64_tr_b16 a[210:211], v16 offset:21792\n\t"
"v_mfma_f32_16x16x32_bf16 v[196:199], a[192:195], a[164:167], v[196:199]\n\t"
"v_mfma_f32_16x16x32_bf16 v[200:203], a[192:195], a[168:171], v[200:203]\n\t"
"ds_read_b64_tr_b16 a[212:213], v17 offset:29536\n\t"
"v_mfma_f32_16x16x32_bf16 v[216:219], a[196:199], a[168:171], v[216:219]\n\t"
"ds_read_b64_tr_b16 a[214:215], v17 offset:30048\n\t"
"v_mfma_f32_16x16x32_bf16 v[220:223], a[196:199], a[172:175], v[220:223]\n\t"
"s_addk_i32 m0, 5888\n\t"
"v_mfma_f32_16x16x32_bf16 v[204:207], a[192:195], a[172:175], v[204:207]\n\t"
"s_waitcnt lgkmcnt(4)\n\t"
"buffer_load_dwordx4 v9, s[24:27], s62 offen offset:64 lds\n\t"
"v_mfma_f32_16x16x32_bf16 v[236:239], a[200:203], a[172:175], v[236:239]\n\t"
"ds_read_b64_tr_b16 a[192:193], v16 offset:31584\n\t"
"v_mfma_f32_16x16x32_bf16 v[252:255], a[204:207], a[172:175], v[252:255]\n\t"
"ds_read_b64_tr_b16 a[194:195], v16 offset:32096\n\t"
"v_mfma_f32_16x16x32_bf16 v[248:251], a[204:207], a[168:171], v[248:251]\n\t"
"v_mfma_f32_16x16x32_bf16 v[232:235], a[200:203], a[168:171], v[232:235]\n\t"
"v_mfma_f32_16x16x32_bf16 v[228:231], a[200:203], a[164:167], v[228:231]\n\t"
"s_sub_u32 s98, s39, 4\n\t"
"ds_read_b64_tr_b16 a[196:197], v16 offset:31616\n\t"
"s_addk_i32 s59, 4096\n\t"
"v_mfma_f32_16x16x32_bf16 v[244:247], a[204:207], a[164:167], v[244:247]\n\t"
"ds_read_b64_tr_b16 a[198:199], v16 offset:32128\n\t"
"v_accvgpr_write_b32 a232, s98\n\t"
"v_mfma_f32_16x16x32_bf16 v[240:243], a[204:207], a[160:163], v[240:243]\n\t"
"s_waitcnt vmcnt(11)\n\t"
"buffer_load_dwordx4 a[12:15], v1, s[8:11], s59 offen offset:0 sc1\n\t"
"v_mfma_f32_16x16x32_bf16 v[224:227], a[200:203], a[160:163], v[224:227]\n\t"
"s_barrier\n\t"
"s_waitcnt lgkmcnt(4)\n\t"
"v_mfma_f32_16x16x32_bf16 v[64:67], a[208:211], a[160:163], v[64:67]\n\t"
"ds_read_b64_tr_b16 a[200:201], v16 offset:31648\n\t"
"buffer_store_dword a232, v28, s[8:11], 0 offen offset:0 sc1\n\t"
"v_mfma_f32_16x16x32_bf16 v[96:99], a[212:215], a[160:163], v[96:99]\n\t"
"ds_read_b64_tr_b16 a[202:203], v16 offset:32160\n\t"
"v_mfma_f32_16x16x32_bf16 v[100:103], a[212:215], a[164:167], v[100:103]\n\t"
"buffer_load_dwordx4 a[16:19], v1, s[8:11], s59 offen offset:1024 sc1\n\t"
"v_mfma_f32_16x16x32_bf16 v[68:71], a[208:211], a[164:167], v[68:71]\n\t"
"v_mfma_f32_16x16x32_bf16 v[72:75], a[208:211], a[168:171], v[72:75]\n\t"
"ds_read_b64_tr_b16 a[204:205], v16 offset:31680\n\t"
"v_mfma_f32_16x16x32_bf16 v[104:107], a[212:215], a[168:171], v[104:107]\n\t"
"ds_read_b64_tr_b16 a[206:207], v16 offset:32192\n\t"
"v_mfma_f32_16x16x32_bf16 v[108:111], a[212:215], a[172:175], v[108:111]\n\t"
"buffer_load_dwordx4 a[20:23], v1, s[8:11], s59 offen offset:2048 sc1\n\t"
"v_mfma_f32_16x16x32_bf16 v[76:79], a[208:211], a[172:175], v[76:79]\n\t"
"s_waitcnt lgkmcnt(4)\n\t"
"v_mfma_f32_16x16x32_bf16 v[128:131], a[192:195], a[176:179], v[128:131]\n\t"
"ds_read_b64_tr_b16 a[208:209], v17 offset:40032\n\t"
"v_mfma_f32_16x16x32_bf16 v[144:147], a[196:199], a[176:179], v[144:147]\n\t"
"ds_read_b64_tr_b16 a[210:211], v17 offset:40544\n\t"
"v_mfma_f32_16x16x32_bf16 v[148:151], a[196:199], a[180:183], v[148:151]\n\t"
"v_mfma_f32_16x16x32_bf16 v[132:135], a[192:195], a[180:183], v[132:135]\n\t"
"buffer_load_dwordx4 a[24:27], v1, s[8:11], s59 offen offset:3072 sc1\n\t"
"v_mfma_f32_16x16x32_bf16 v[136:139], a[192:195], a[184:187], v[136:139]\n\t"
"ds_read_b64_tr_b16 a[212:213], v16 offset:31712\n\t"
"v_mfma_f32_16x16x32_bf16 v[152:155], a[196:199], a[184:187], v[152:155]\n\t"
"ds_read_b64_tr_b16 a[214:215], v16 offset:32224\n\t"
"v_mfma_f32_16x16x32_bf16 v[156:159], a[196:199], a[188:191], v[156:159]\n\t"
"s_addk_i32 s57, 4096\n\t"
"v_mfma_f32_16x16x32_bf16 v[140:143], a[192:195], a[188:191], v[140:143]\n\t"
"s_waitcnt lgkmcnt(4)\n\t"
"buffer_load_dwordx4 a[28:31], v1, s[8:11], s57 offen offset:0 sc1\n\t"
"v_mfma_f32_16x16x32_bf16 v[172:175], a[200:203], a[188:191], v[172:175]\n\t"
"ds_read_b64_tr_b16 a[192:193], v16 offset:31744\n\t"
"v_mfma_f32_16x16x32_bf16 v[188:191], a[204:207], a[188:191], v[188:191]\n\t"
"ds_read_b64_tr_b16 a[194:195], v16 offset:32256\n\t"
"v_mfma_f32_16x16x32_bf16 v[184:187], a[204:207], a[184:187], v[184:187]\n\t"
"v_mfma_f32_16x16x32_bf16 v[168:171], a[200:203], a[184:187], v[168:171]\n\t"
"ds_read_b64_tr_b16 a[196:197], v16 offset:31776\n\t"
"v_mfma_f32_16x16x32_bf16 v[164:167], a[200:203], a[180:183], v[164:167]\n\t"
"buffer_load_dwordx4 a[32:35], v1, s[8:11], s57 offen offset:1024 sc1\n\t"
"v_mfma_f32_16x16x32_bf16 v[180:183], a[204:207], a[180:183], v[180:183]\n\t"
"ds_read_b64_tr_b16 a[198:199], v16 offset:32288\n\t"
"v_mfma_f32_16x16x32_bf16 v[176:179], a[204:207], a[176:179], v[176:179]\n\t"
"v_mfma_f32_16x16x32_bf16 v[160:163], a[200:203], a[176:179], v[160:163]\n\t"
"s_waitcnt lgkmcnt(4)\n\t"
"ds_read_b64_tr_b16 a[200:201], v16 offset:31808\n\t"
"v_mfma_f32_16x16x32_bf16 v[192:195], a[208:211], a[176:179], v[192:195]\n\t"
"buffer_load_dwordx4 a[36:39], v1, s[8:11], s57 offen offset:2048 sc1\n\t"
"v_mfma_f32_16x16x32_bf16 v[208:211], a[212:215], a[176:179], v[208:211]\n\t"
"ds_read_b64_tr_b16 a[202:203], v16 offset:32320\n\t"
"v_mfma_f32_16x16x32_bf16 v[212:215], a[212:215], a[180:183], v[212:215]\n\t"
"v_mfma_f32_16x16x32_bf16 v[196:199], a[208:211], a[180:183], v[196:199]\n\t"
"v_mfma_f32_16x16x32_bf16 v[200:203], a[208:211], a[184:187], v[200:203]\n\t"
"ds_read_b64_tr_b16 a[204:205], v17 offset:40064\n\t"
"v_mfma_f32_16x16x32_bf16 v[216:219], a[212:215], a[184:187], v[216:219]\n\t"
"buffer_load_dwordx4 a[40:43], v1, s[8:11], s57 offen offset:3072 sc1\n\t"
"v_mfma_f32_16x16x32_bf16 v[220:223], a[212:215], a[188:191], v[220:223]\n\t"
"ds_read_b64_tr_b16 a[206:207], v17 offset:40576\n\t"
"v_mfma_f32_16x16x32_bf16 v[204:207], a[208:211], a[188:191], v[204:207]\n\t"
"s_waitcnt lgkmcnt(4)\n\t"
"v_mfma_f32_16x16x32_bf16 v[236:239], a[192:195], a[188:191], v[236:239]\n\t"
"ds_read_b64_tr_b16 a[208:209], v16 offset:0\n\t"
"v_mfma_f32_16x16x32_bf16 v[252:255], a[196:199], a[188:191], v[252:255]\n\t"
"ds_read_b64_tr_b16 a[210:211], v16 offset:512\n\t"
"s_addk_i32 s58, 4096\n\t"
"v_mfma_f32_16x16x32_bf16 v[248:251], a[196:199], a[184:187], v[248:251]\n\t"
"buffer_load_dwordx4 a[44:47], v1, s[8:11], s58 offen offset:0 sc1\n\t"
"v_mfma_f32_16x16x32_bf16 v[232:235], a[192:195], a[184:187], v[232:235]\n\t"
"v_mfma_f32_16x16x32_bf16 v[228:231], a[192:195], a[180:183], v[228:231]\n\t"
"ds_read_b64_tr_b16 a[212:213], v16 offset:32\n\t"
"v_mfma_f32_16x16x32_bf16 v[244:247], a[196:199], a[180:183], v[244:247]\n\t"
"ds_read_b64_tr_b16 a[214:215], v16 offset:544\n\t"
"v_mfma_f32_16x16x32_bf16 v[240:243], a[196:199], a[176:179], v[240:243]\n\t"
"buffer_load_dwordx4 a[48:51], v1, s[8:11], s58 offen offset:1024 sc1\n\t"
"v_mfma_f32_16x16x32_bf16 v[224:227], a[192:195], a[176:179], v[224:227]\n\t"
"s_waitcnt lgkmcnt(4)\n\t"
"v_mfma_f32_16x16x32_bf16 v[64:67], a[200:203], a[176:179], v[64:67]\n\t"
"ds_read_b64_tr_b16 a[192:193], v16 offset:64\n\t"
"v_mfma_f32_16x16x32_bf16 v[96:99], a[204:207], a[176:179], v[96:99]\n\t"
"ds_read_b64_tr_b16 a[194:195], v16 offset:576\n\t"
"v_mfma_f32_16x16x32_bf16 v[100:103], a[204:207], a[180:183], v[100:103]\n\t"
"s_barrier\n\t"
"v_mfma_f32_16x16x32_bf16 v[68:71], a[200:203], a[180:183], v[68:71]\n\t"
"buffer_load_dwordx4 a[52:55], v1, s[8:11], s58 offen offset:2048 sc1\n\t"
"v_mfma_f32_16x16x32_bf16 v[72:75], a[200:203], a[184:187], v[72:75]\n\t"
"ds_read_b64_tr_b16 a[196:197], v16 offset:96\n\t"
"v_mfma_f32_16x16x32_bf16 v[104:107], a[204:207], a[184:187], v[104:107]\n\t"
"ds_read_b64_tr_b16 a[198:199], v16 offset:608\n\t"
"v_mfma_f32_16x16x32_bf16 v[108:111], a[204:207], a[188:191], v[108:111]\n\t"
"s_add_u32 s63, s63, s56\n\t"
"s_addk_i32 m0, -29440\n\t"
"v_mfma_f32_16x16x32_bf16 v[76:79], a[200:203], a[188:191], v[76:79]\n\t"
"s_waitcnt lgkmcnt(4)\n\t"
"buffer_load_dwordx4 v2, s[24:27], s63 offen offset:0 lds\n\t"
"v_mfma_f32_16x16x32_bf16 v[128:131], a[208:211], v[112:115], v[128:131]\n\t"
"ds_read_b64_tr_b16 a[200:201], v17 offset:8448\n\t"
"v_mfma_f32_16x16x32_bf16 v[144:147], a[212:215], v[112:115], v[144:147]\n\t"
"ds_read_b64_tr_b16 a[202:203], v17 offset:8960\n\t"
"v_mfma_f32_16x16x32_bf16 v[148:151], a[212:215], v[116:119], v[148:151]\n\t"
"v_mfma_f32_16x16x32_bf16 v[132:135], a[208:211], v[116:119], v[132:135]\n\t"
"v_mfma_f32_16x16x32_bf16 v[136:139], a[208:211], v[120:123], v[136:139]\n\t"
"ds_read_b64_tr_b16 a[204:205], v16 offset:128\n\t"
"s_addk_i32 m0, 832\n\t"
"v_mfma_f32_16x16x32_bf16 v[152:155], a[212:215], v[120:123], v[152:155]\n\t"
"buffer_load_dwordx4 v2, s[24:27], s63 offen offset:1280 lds\n\t"
"v_mfma_f32_16x16x32_bf16 v[156:159], a[212:215], v[124:127], v[156:159]\n\t"
"ds_read_b64_tr_b16 a[206:207], v16 offset:640\n\t"
"v_mfma_f32_16x16x32_bf16 v[140:143], a[208:211], v[124:127], v[140:143]\n\t"
"s_waitcnt lgkmcnt(4)\n\t"
"v_mfma_f32_16x16x32_bf16 v[172:175], a[192:195], v[124:127], v[172:175]\n\t"
"ds_read_b64_tr_b16 a[208:209], v16 offset:160\n\t"
"v_mfma_f32_16x16x32_bf16 v[188:191], a[196:199], v[124:127], v[188:191]\n\t"
"ds_read_b64_tr_b16 a[210:211], v16 offset:672\n\t"
"v_mfma_f32_16x16x32_bf16 v[184:187], a[196:199], v[120:123], v[184:187]\n\t"
"s_addk_i32 m0, 832\n\t"
"v_mfma_f32_16x16x32_bf16 v[168:171], a[192:195], v[120:123], v[168:171]\n\t"
"buffer_load_dwordx4 v2, s[24:27], s63 offen offset:2560 lds\n\t"
"v_mfma_f32_16x16x32_bf16 v[164:167], a[192:195], v[116:119], v[164:167]\n\t"
"ds_read_b64_tr_b16 a[212:213], v16 offset:192\n\t"
"v_mfma_f32_16x16x32_bf16 v[180:183], a[196:199], v[116:119], v[180:183]\n\t"
"ds_read_b64_tr_b16 a[214:215], v16 offset:704\n\t"
"v_mfma_f32_16x16x32_bf16 v[176:179], a[196:199], v[112:115], v[176:179]\n\t"
"v_mfma_f32_16x16x32_bf16 v[160:163], a[192:195], v[112:115], v[160:163]\n\t"
"s_waitcnt lgkmcnt(4)\n\t"
"v_mfma_f32_16x16x32_bf16 v[192:195], a[200:203], v[112:115], v[192:195]\n\t"
"ds_read_b64_tr_b16 a[192:193], v16 offset:224\n\t"
"s_addk_i32 m0, 832\n\t"
"v_mfma_f32_16x16x32_bf16 v[208:211], a[204:207], v[112:115], v[208:211]\n\t"
"buffer_load_dwordx4 v2, s[24:27], s63 offen offset:3840 lds\n\t"
"v_mfma_f32_16x16x32_bf16 v[212:215], a[204:207], v[116:119], v[212:215]\n\t"
"ds_read_b64_tr_b16 a[194:195], v16 offset:736\n\t"
"v_mfma_f32_16x16x32_bf16 v[196:199], a[200:203], v[116:119], v[196:199]\n\t"
"v_mfma_f32_16x16x32_bf16 v[200:203], a[200:203], v[120:123], v[200:203]\n\t"
"ds_read_b64_tr_b16 a[196:197], v17 offset:8480\n\t"
"v_mfma_f32_16x16x32_bf16 v[216:219], a[204:207], v[120:123], v[216:219]\n\t"
"ds_read_b64_tr_b16 a[198:199], v17 offset:8992\n\t"
"v_mfma_f32_16x16x32_bf16 v[220:223], a[204:207], v[124:127], v[220:223]\n\t"
"s_addk_i32 m0, 5888\n\t"
"v_mfma_f32_16x16x32_bf16 v[204:207], a[200:203], v[124:127], v[204:207]\n\t"
"s_waitcnt lgkmcnt(4)\n\t"
"buffer_load_dwordx4 v9, s[24:27], s63 offen offset:64 lds\n\t"
"v_mfma_f32_16x16x32_bf16 v[236:239], a[208:211], v[124:127], v[236:239]\n\t"
"ds_read_b64_tr_b16 a[200:201], v16 offset:10528\n\t"
"v_mfma_f32_16x16x32_bf16 v[252:255], a[212:215], v[124:127], v[252:255]\n\t"
"ds_read_b64_tr_b16 a[202:203], v16 offset:11040\n\t"
"v_mfma_f32_16x16x32_bf16 v[248:251], a[212:215], v[120:123], v[248:251]\n\t"
"v_mfma_f32_16x16x32_bf16 v[232:235], a[208:211], v[120:123], v[232:235]\n\t"
"v_mfma_f32_16x16x32_bf16 v[228:231], a[208:211], v[116:119], v[228:231]\n\t"
"ds_read_b64_tr_b16 a[204:205], v16 offset:10560\n\t"
"v_mfma_f32_16x16x32_bf16 v[244:247], a[212:215], v[116:119], v[244:247]\n\t"
"buffer_load_dwordx4 a[56:59], v1, s[8:11], s58 offen offset:3072 sc1\n\t"
"v_mfma_f32_16x16x32_bf16 v[240:243], a[212:215], v[112:115], v[240:243]\n\t"
"ds_read_b64_tr_b16 a[206:207], v16 offset:11072\n\t"
"v_mfma_f32_16x16x32_bf16 v[224:227], a[208:211], v[112:115], v[224:227]\n\t"
"s_waitcnt lgkmcnt(4)\n\t"
"buffer_load_dword v30, v29, s[8:11], 0 offen offset:0 sc1\n\t"
"v_mfma_f32_16x16x32_bf16 v[64:67], a[192:195], v[112:115], v[64:67]\n\t"
"ds_read_b64_tr_b16 a[208:209], v16 offset:10592\n\t"
"v_mfma_f32_16x16x32_bf16 v[96:99], a[196:199], v[112:115], v[96:99]\n\t"
"ds_read_b64_tr_b16 a[210:211], v16 offset:11104\n\t"
"s_addk_i32 s59, 4096\n\t"
"v_mfma_f32_16x16x32_bf16 v[100:103], a[196:199], v[116:119], v[100:103]\n\t"
"buffer_load_dwordx4 a[60:63], v1, s[8:11], s59 offen offset:0 sc1\n\t"
"v_mfma_f32_16x16x32_bf16 v[68:71], a[192:195], v[116:119], v[68:71]\n\t"
"v_mfma_f32_16x16x32_bf16 v[72:75], a[192:195], v[120:123], v[72:75]\n\t"
"ds_read_b64_tr_b16 a[212:213], v16 offset:10624\n\t"
"v_mfma_f32_16x16x32_bf16 v[104:107], a[196:199], v[120:123], v[104:107]\n\t"
"ds_read_b64_tr_b16 a[214:215], v16 offset:11136\n\t"
"v_mfma_f32_16x16x32_bf16 v[108:111], a[196:199], v[124:127], v[108:111]\n\t"
"buffer_load_dwordx4 a[64:67], v1, s[8:11], s59 offen offset:1024 sc1\n\t"
"v_mfma_f32_16x16x32_bf16 v[76:79], a[192:195], v[124:127], v[76:79]\n\t"
"s_waitcnt lgkmcnt(4)\n\t"
"v_mfma_f32_16x16x32_bf16 v[128:131], a[200:203], v[32:35], v[128:131]\n\t"
"ds_read_b64_tr_b16 a[192:193], v17 offset:18976\n\t"
"v_mfma_f32_16x16x32_bf16 v[144:147], a[204:207], v[32:35], v[144:147]\n\t"
"ds_read_b64_tr_b16 a[194:195], v17 offset:19488\n\t"
"v_mfma_f32_16x16x32_bf16 v[148:151], a[204:207], v[36:39], v[148:151]\n\t"
"v_mfma_f32_16x16x32_bf16 v[132:135], a[200:203], v[36:39], v[132:135]\n\t"
"buffer_load_dwordx4 a[68:71], v1, s[8:11], s59 offen offset:2048 sc1\n\t"
"v_mfma_f32_16x16x32_bf16 v[136:139], a[200:203], v[40:43], v[136:139]\n\t"
"ds_read_b64_tr_b16 a[196:197], v16 offset:10656\n\t"
"v_mfma_f32_16x16x32_bf16 v[152:155], a[204:207], v[40:43], v[152:155]\n\t"
"ds_read_b64_tr_b16 a[198:199], v16 offset:11168\n\t"
"v_mfma_f32_16x16x32_bf16 v[156:159], a[204:207], v[44:47], v[156:159]\n\t"
"v_mfma_f32_16x16x32_bf16 v[140:143], a[200:203], v[44:47], v[140:143]\n\t"
"s_waitcnt lgkmcnt(4)\n\t"
"buffer_load_dwordx4 a[72:75], v1, s[8:11], s59 offen offset:3072 sc1\n\t"
"v_mfma_f32_16x16x32_bf16 v[172:175], a[208:211], v[44:47], v[172:175]\n\t"
"ds_read_b64_tr_b16 a[200:201], v16 offset:10688\n\t"
"v_mfma_f32_16x16x32_bf16 v[188:191], a[212:215], v[44:47], v[188:191]\n\t"
"ds_read_b64_tr_b16 a[202:203], v16 offset:11200\n\t"
"v_mfma_f32_16x16x32_bf16 v[184:187], a[212:215], v[40:43], v[184:187]\n\t"
"v_mfma_f32_16x16x32_bf16 v[168:171], a[208:211], v[40:43], v[168:171]\n\t"
"s_addk_i32 s57, 4096\n\t"
"ds_read_b64_tr_b16 a[204:205], v16 offset:10720\n\t"
"v_mfma_f32_16x16x32_bf16 v[164:167], a[208:211], v[36:39], v[164:167]\n\t"
"buffer_load_dwordx4 a[76:79], v1, s[8:11], s57 offen offset:0 sc1\n\t"
"v_mfma_f32_16x16x32_bf16 v[180:183], a[212:215], v[36:39], v[180:183]\n\t"
"ds_read_b64_tr_b16 a[206:207], v16 offset:11232\n\t"
"v_mfma_f32_16x16x32_bf16 v[176:179], a[212:215], v[32:35], v[176:179]\n\t"
"v_mfma_f32_16x16x32_bf16 v[160:163], a[208:211], v[32:35], v[160:163]\n\t"
"s_waitcnt lgkmcnt(4)\n\t"
"v_mfma_f32_16x16x32_bf16 v[192:195], a[192:195], v[32:35], v[192:195]\n\t"
"ds_read_b64_tr_b16 a[208:209], v16 offset:10752\n\t"
"v_mfma_f32_16x16x32_bf16 v[208:211], a[196:199], v[32:35], v[208:211]\n\t"
"buffer_load_dwordx4 a[80:83], v1, s[8:11], s57 offen offset:1024 sc1\n\t"
"v_mfma_f32_16x16x32_bf16 v[212:215], a[196:199], v[36:39], v[212:215]\n\t"
"ds_read_b64_tr_b16 a[210:211], v16 offset:11264\n\t"
"v_mfma_f32_16x16x32_bf16 v[196:199], a[192:195], v[36:39], v[196:199]\n\t"
"v_mfma_f32_16x16x32_bf16 v[200:203], a[192:195], v[40:43], v[200:203]\n\t"
"ds_read_b64_tr_b16 a[212:213], v17 offset:19008\n\t"
"v_mfma_f32_16x16x32_bf16 v[216:219], a[196:199], v[40:43], v[216:219]\n\t"
"buffer_load_dwordx4 a[84:87], v1, s[8:11], s57 offen offset:2048 sc1\n\t"
"v_mfma_f32_16x16x32_bf16 v[220:223], a[196:199], v[44:47], v[220:223]\n\t"
"ds_read_b64_tr_b16 a[214:215], v17 offset:19520\n\t"
"v_mfma_f32_16x16x32_bf16 v[204:207], a[192:195], v[44:47], v[204:207]\n\t"
"s_waitcnt lgkmcnt(4)\n\t"
"v_mfma_f32_16x16x32_bf16 v[236:239], a[200:203], v[44:47], v[236:239]\n\t"
"ds_read_b64_tr_b16 a[192:193], v18 offset:42112\n\t"
"v_mfma_f32_16x16x32_bf16 v[252:255], a[204:207], v[44:47], v[252:255]\n\t"
"ds_read_b64_tr_b16 a[194:195], v18 offset:42624\n\t"
"v_mfma_f32_16x16x32_bf16 v[248:251], a[204:207], v[40:43], v[248:251]\n\t"
"buffer_load_dwordx4 a[88:91], v1, s[8:11], s57 offen offset:3072 sc1\n\t"
"v_mfma_f32_16x16x32_bf16 v[232:235], a[200:203], v[40:43], v[232:235]\n\t"
"v_mfma_f32_16x16x32_bf16 v[228:231], a[200:203], v[36:39], v[228:231]\n\t"
"ds_read_b64_tr_b16 a[196:197], v18 offset:42144\n\t"
"v_mfma_f32_16x16x32_bf16 v[244:247], a[204:207], v[36:39], v[244:247]\n\t"
"ds_read_b64_tr_b16 a[198:199], v18 offset:42656\n\t"
"s_addk_i32 s58, 4096\n\t"
"v_mfma_f32_16x16x32_bf16 v[240:243], a[204:207], v[32:35], v[240:243]\n\t"
"buffer_load_dwordx4 a[92:95], v1, s[8:11], s58 offen offset:0 sc1\n\t"
"v_mfma_f32_16x16x32_bf16 v[224:227], a[200:203], v[32:35], v[224:227]\n\t"
"s_waitcnt lgkmcnt(4)\n\t"
"v_mfma_f32_16x16x32_bf16 v[64:67], a[208:211], v[32:35], v[64:67]\n\t"
"ds_read_b64_tr_b16 a[200:201], v18 offset:42176\n\t"
"v_mfma_f32_16x16x32_bf16 v[96:99], a[212:215], v[32:35], v[96:99]\n\t"
"ds_read_b64_tr_b16 a[202:203], v18 offset:42688\n\t"
"v_mfma_f32_16x16x32_bf16 v[100:103], a[212:215], v[36:39], v[100:103]\n\t"
"s_barrier\n\t"
"v_mfma_f32_16x16x32_bf16 v[68:71], a[208:211], v[36:39], v[68:71]\n\t"
"buffer_load_dwordx4 a[128:131], v1, s[8:11], s58 offen offset:1024 sc1\n\t"
"v_mfma_f32_16x16x32_bf16 v[72:75], a[208:211], v[40:43], v[72:75]\n\t"
"ds_read_b64_tr_b16 a[204:205], v18 offset:42208\n\t"
"v_mfma_f32_16x16x32_bf16 v[104:107], a[212:215], v[40:43], v[104:107]\n\t"
"ds_read_b64_tr_b16 a[206:207], v18 offset:42720\n\t"
"v_mfma_f32_16x16x32_bf16 v[108:111], a[212:215], v[44:47], v[108:111]\n\t"
"s_add_u32 s61, s61, s56\n\t"
"s_addk_i32 m0, -29440\n\t"
"v_mfma_f32_16x16x32_bf16 v[76:79], a[208:211], v[44:47], v[76:79]\n\t"
"s_waitcnt lgkmcnt(4)\n\t"
"buffer_load_dwordx4 v2, s[24:27], s61 offen offset:0 lds\n\t"
"v_mfma_f32_16x16x32_bf16 v[128:131], a[192:195], a[216:219], v[128:131]\n\t"
"ds_read_b64_tr_b16 a[208:209], v19 offset:50560\n\t"
"v_mfma_f32_16x16x32_bf16 v[144:147], a[196:199], a[216:219], v[144:147]\n\t"
"s_waitcnt vmcnt(11)\n\t"
"ds_read_b64_tr_b16 a[210:211], v19 offset:51072\n\t"
"v_sub_u32 v30, s39, v30\n\t"
"v_cmp_lt_u32 vcc, 4, v30\n\t"
"s_cbranch_vccnz .LslD%=\n\t"
".LbkD%=:\n"
"v_mfma_f32_16x16x32_bf16 v[148:151], a[196:199], a[220:223], v[148:151]\n\t"
"v_mfma_f32_16x16x32_bf16 v[132:135], a[192:195], a[220:223], v[132:135]\n\t"
"v_mfma_f32_16x16x32_bf16 v[136:139], a[192:195], a[224:227], v[136:139]\n\t"
"ds_read_b64_tr_b16 a[212:213], v18 offset:42240\n\t"
"s_addk_i32 m0, 832\n\t"
"v_mfma_f32_16x16x32_bf16 v[152:155], a[196:199], a[224:227], v[152:155]\n\t"
"buffer_load_dwordx4 v2, s[24:27], s61 offen offset:1280 lds\n\t"
"v_mfma_f32_16x16x32_bf16 v[156:159], a[196:199], a[228:231], v[156:159]\n\t"
"ds_read_b64_tr_b16 a[214:215], v18 offset:42752\n\t"
"v_mfma_f32_16x16x32_bf16 v[140:143], a[192:195], a[228:231], v[140:143]\n\t"
"s_waitcnt lgkmcnt(4)\n\t"
"v_mfma_f32_16x16x32_bf16 v[172:175], a[200:203], a[228:231], v[172:175]\n\t"
"ds_read_b64_tr_b16 a[192:193], v18 offset:42272\n\t"
"v_mfma_f32_16x16x32_bf16 v[188:191], a[204:207], a[228:231], v[188:191]\n\t"
"ds_read_b64_tr_b16 a[194:195], v18 offset:42784\n\t"
"v_mfma_f32_16x16x32_bf16 v[184:187], a[204:207], a[224:227], v[184:187]\n\t"
"s_addk_i32 m0, 832\n\t"
"v_mfma_f32_16x16x32_bf16 v[168:171], a[200:203], a[224:227], v[168:171]\n\t"
"buffer_load_dwordx4 v2, s[24:27], s61 offen offset:2560 lds\n\t"
"v_mfma_f32_16x16x32_bf16 v[164:167], a[200:203], a[220:223], v[164:167]\n\t"
"ds_read_b64_tr_b16 a[196:197], v18 offset:42304\n\t"
"v_mfma_f32_16x16x32_bf16 v[180:183], a[204:207], a[220:223], v[180:183]\n\t"
"ds_read_b64_tr_b16 a[198:199], v18 offset:42816\n\t"
"v_mfma_f32_16x16x32_bf16 v[176:179], a[204:207], a[216:219], v[176:179]\n\t"
"v_mfma_f32_16x16x32_bf16 v[160:163], a[200:203], a[216:219], v[160:163]\n\t"
"s_waitcnt lgkmcnt(4)\n\t"
"v_mfma_f32_16x16x32_bf16 v[192:195], a[208:211], a[216:219], v[192:195]\n\t"
"ds_read_b64_tr_b16 a[200:201], v18 offset:42336\n\t"
"v_mfma_f32_16x16x32_bf16 v[208:211], a[212:215], a[216:219], v[208:211]\n\t"
"ds_read_b64_tr_b16 a[202:203], v18 offset:42848\n\t"
"s_addk_i32 m0, 832\n\t"
"v_mfma_f32_16x16x32_bf16 v[212:215], a[212:215], a[220:223], v[212:215]\n\t"
"buffer_load_dwordx4 v2, s[24:27], s61 offen offset:3840 lds\n\t"
"v_mfma_f32_16x16x32_bf16 v[196:199], a[208:211], a[220:223], v[196:199]\n\t"
"v_mfma_f32_16x16x32_bf16 v[200:203], a[208:211], a[224:227], v[200:203]\n\t"
"s_mul_i32 s43, s38, 3200\n\t"
"ds_read_b64_tr_b16 a[204:205], v19 offset:50592\n\t"
"s_movk_i32 s46, 1152\n\t"
"v_mfma_f32_16x16x32_bf16 v[216:219], a[212:215], a[224:227], v[216:219]\n\t"
"ds_read_b64_tr_b16 a[206:207], v19 offset:51104\n\t"
"v_add_u32 v95, s43, v1\n\t"
"v_mfma_f32_16x16x32_bf16 v[220:223], a[212:215], a[228:231], v[220:223]\n\t"
"v_mov_b32 v21, 1073741824\n\t"
"v_lshrrev_b32 v20, 7, v1\n\t"
"v_mfma_f32_16x16x32_bf16 v[204:207], a[208:211], a[228:231], v[204:207]\n\t"
"s_addk_i32 m0, 5888\n\t"
"v_mad_u32_u24 v92, v20, s46, v1\n\t"
"s_waitcnt lgkmcnt(4)\n\t"
"v_mfma_f32_16x16x32_bf16 v[236:239], a[192:195], a[228:231], v[236:239]\n\t"
"ds_read_b64_tr_b16 a[208:209], v18 offset:52640\n\t"
"buffer_load_dwordx4 v9, s[24:27], s61 offen offset:64 lds\n\t"
"v_mfma_f32_16x16x32_bf16 v[252:255], a[196:199], a[228:231], v[252:255]\n\t"
"s_movk_i32 s46, 1248\n\t"
"ds_read_b64_tr_b16 a[210:211], v18 offset:53152\n\t"
"v_and_b32 v20, 48, %[vT]\n\t"
"v_mfma_f32_16x16x32_bf16 v[248:251], a[196:199], a[224:227], v[248:251]\n\t"
"v_xor_b32 v92, v20, v92\n\t"
"v_add_u32 v93, 9216, v92\n\t"
"v_mfma_f32_16x16x32_bf16 v[232:235], a[192:195], a[224:227], v[232:235]\n\t"
"v_lshrrev_b32 v20, 5, v1\n\t"
"v_mad_u32_u24 v94, v20, s46, v1\n\t"
"v_mfma_f32_16x16x32_bf16 v[228:231], a[192:195], a[220:223], v[228:231]\n\t"
"s_lshr_b32 s46, s72, 16\n\t"
"ds_read_b64_tr_b16 a[212:213], v18 offset:52672\n\t"
"v_add_u32 v94, s46, v94\n\t"
"v_mfma_f32_16x16x32_bf16 v[244:247], a[196:199], a[220:223], v[244:247]\n\t"
"ds_read_b64_tr_b16 a[214:215], v18 offset:53184\n\t"
"v_mfma_f32_16x16x32_bf16 v[240:243], a[196:199], a[216:219], v[240:243]\n\t"
"buffer_load_dwordx4 a[132:135], v1, s[8:11], s58 offen offset:2048 sc1\n\t"
"v_mfma_f32_16x16x32_bf16 v[224:227], a[192:195], a[216:219], v[224:227]\n\t"
"s_waitcnt lgkmcnt(4)\n\t"
"v_mov_b32_dpp v94, v21 quad_perm:[0,1,2,3] row_mask:0xc bank_mask:0xf\n\t"
"v_and_b32 v20, 15, %[vT]\n\t"
"v_mfma_f32_16x16x32_bf16 v[64:67], a[200:203], a[216:219], v[64:67]\n\t"
"ds_read_b64_tr_b16 a[192:193], v18 offset:52704\n\t"
"v_bfe_u32 v22, %[vT], 4, 2\n\t"
"v_mfma_f32_16x16x32_bf16 v[96:99], a[204:207], a[216:219], v[96:99]\n\t"
"ds_read_b64_tr_b16 a[194:195], v18 offset:53216\n\t"
"v_lshl_add_u32 v4, v22, 3, s43\n\t"
"v_mfma_f32_16x16x32_bf16 v[100:103], a[204:207], a[220:223], v[100:103]\n\t"
"buffer_load_dwordx4 a[136:139], v1, s[8:11], s58 offen offset:3072 sc1\n\t"
"v_mfma_f32_16x16x32_bf16 v[68:71], a[200:203], a[220:223], v[68:71]\n\t"
"v_lshl_add_u32 v5, v20, 5, v4\n\t"
"v_lshl_add_u32 v4, v20, 7, v4\n\t"
"v_mfma_f32_16x16x32_bf16 v[72:75], a[200:203], a[224:227], v[72:75]\n\t"
"ds_read_b64_tr_b16 a[196:197], v18 offset:52736\n\t"
"v_and_b32 v21, 14, %[vT]\n\t"
"v_mfma_f32_16x16x32_bf16 v[104:107], a[204:207], a[224:227], v[104:107]\n\t"
"ds_read_b64_tr_b16 a[198:199], v18 offset:53248\n\t"
"v_lshlrev_b32 v21, 3, v21\n\t"
"v_mfma_f32_16x16x32_bf16 v[108:111], a[204:207], a[228:231], v[108:111]\n\t"
"s_addk_i32 s59, 4096\n\t"
"v_xor_b32 v4, v21, v4\n\t"
"v_xor_b32 v6, 32, v4\n\t"
"v_mfma_f32_16x16x32_bf16 v[76:79], a[200:203], a[228:231], v[76:79]\n\t"
"s_waitcnt lgkmcnt(4)\n\t"
"buffer_load_dwordx4 a[140:143], v1, s[8:11], s59 offen offset:0 sc1\n\t"
"v_mfma_f32_16x16x32_bf16 v[128:131], a[208:211], a[244:247], v[128:131]\n\t"
"ds_read_b64_tr_b16 a[200:201], v19 offset:61088\n\t"
"v_add_u32 v21, s71, v20\n\t"
"v_mfma_f32_16x16x32_bf16 v[144:147], a[212:215], a[244:247], v[144:147]\n\t"
"ds_read_b64_tr_b16 a[202:203], v19 offset:61600\n\t"
"v_lshlrev_b32 v7, 3, v21\n\t"
"v_mfma_f32_16x16x32_bf16 v[148:151], a[212:215], a[248:251], v[148:151]\n\t"
"s_mul_i32 s45, s71, 1280\n\t"
"v_add_u32 v7, 2621440, v7\n\t"
"s_and_b32 s46, s72, 7\n\t"
"v_mfma_f32_16x16x32_bf16 v[132:135], a[208:211], a[248:251], v[132:135]\n\t"
"buffer_load_dwordx4 a[144:147], v1, s[8:11], s59 offen offset:1024 sc1\n\t"
"v_mfma_f32_16x16x32_bf16 v[136:139], a[208:211], a[252:255], v[136:139]\n\t"
"s_lshl_b32 s46, s46, 7\n\t"
"ds_read_b64_tr_b16 a[204:205], v18 offset:52768\n\t"
"s_add_u32 s47, s45, s46\n\t"
"v_mfma_f32_16x16x32_bf16 v[152:155], a[212:215], a[252:255], v[152:155]\n\t"
"ds_read_b64_tr_b16 a[206:207], v18 offset:53280\n\t"
"v_lshrrev_b32 v11, 1, v20\n\t"
"v_mfma_f32_16x16x32_bf16 v[156:159], a[212:215], a[96:99], v[156:159]\n\t"
"v_lshl_add_u32 v22, v22, 1, 0\n\t"
"v_sub_u32 v22, v11, v22\n\t"
"v_mfma_f32_16x16x32_bf16 v[140:143], a[208:211], a[96:99], v[140:143]\n\t"
"s_waitcnt lgkmcnt(4)\n\t"
"v_and_b32 v20, 1, v20\n\t"
"v_lshlrev_b32 v20, 4, v20\n\t"
"v_mfma_f32_16x16x32_bf16 v[172:175], a[192:195], a[96:99], v[172:175]\n\t"
"ds_read_b64_tr_b16 a[208:209], v18 offset:52800\n\t"
"v_mfma_f32_16x16x32_bf16 v[188:191], a[196:199], a[96:99], v[188:191]\n\t"
"s_mov_b32 s46, 16256\n\t"
"ds_read_b64_tr_b16 a[210:211], v18 offset:53312\n\t"
"v_lshlrev_b32 v20, v20, s46\n\t"
"v_mfma_f32_16x16x32_bf16 v[184:187], a[196:199], a[252:255], v[184:187]\n\t"
"v_cmp_eq_u32 vcc, 0, v22\n\t"
"v_cmp_eq_u32 s[68:69], 1, v22\n\t"
"v_mfma_f32_16x16x32_bf16 v[168:171], a[192:195], a[252:255], v[168:171]\n\t"
"buffer_load_dwordx4 a[148:151], v1, s[8:11], s59 offen offset:2048 sc1\n\t"
"v_mfma_f32_16x16x32_bf16 v[164:167], a[192:195], a[248:251], v[164:167]\n\t"
"ds_read_b64_tr_b16 a[212:213], v18 offset:52832\n\t"
"buffer_load_dwordx4 a[152:155], v1, s[8:11], s59 offen offset:3072 sc1\n\t"
"v_mfma_f32_16x16x32_bf16 v[180:183], a[196:199], a[248:251], v[180:183]\n\t"
"ds_read_b64_tr_b16 a[214:215], v18 offset:53344\n\t"
"v_cndmask_b32 v10, 0, v20, vcc\n\t"
"v_mfma_f32_16x16x32_bf16 v[176:179], a[196:199], a[244:247], v[176:179]\n\t"
"s_and_b32 s46, %[sB], 7\n\t"
"v_cndmask_b32 v11, 0, v20, s[68:69]\n\t"
"s_lshl_b32 s46, s46, 17\n\t"
"v_mfma_f32_16x16x32_bf16 v[160:163], a[192:195], a[244:247], v[160:163]\n\t"
"s_lshr_b32 s75, %[sB], 3\n\t"
"s_lshl_b32 s75, s75, 12\n\t"
"s_waitcnt lgkmcnt(4)\n\t"
"v_mfma_f32_16x16x32_bf16 v[192:195], a[200:203], a[244:247], v[192:195]\n\t"
"s_addk_i32 s57, 4096\n\t"
"ds_read_b64_tr_b16 a[192:193], v18 offset:52864\n\t"
"s_add_u32 s46, s46, s75\n\t"
"v_mfma_f32_16x16x32_bf16 v[208:211], a[204:207], a[244:247], v[208:211]\n\t"
"ds_read_b64_tr_b16 a[194:195], v18 offset:53376\n\t"
"buffer_load_dwordx4 a[156:159], v1, s[8:11], s57 offen offset:0 sc1\n\t"
"v_mfma_f32_16x16x32_bf16 v[212:215], a[204:207], a[248:251], v[212:215]\n\t"
"s_add_u32 s74, s46, 33554432\n\t"
"s_add_u32 s74, s74, s44\n\t"
"v_lshrrev_b32 v20, 2, v1\n\t"
"v_mfma_f32_16x16x32_bf16 v[196:199], a[200:203], a[248:251], v[196:199]\n\t"
"s_andn2_b32 s46, 2032, s28\n\t"
"v_sub_u32 v20, v1, v20\n\t"
"s_andn2_b32 s75, 2032, s29\n\t"
"v_mfma_f32_16x16x32_bf16 v[200:203], a[200:203], a[252:255], v[200:203]\n\t"
"s_sub_u32 s46, s46, s71\n\t"
"ds_read_b64_tr_b16 a[196:197], v19 offset:61120\n\t"
"s_sub_u32 s75, s75, s71\n\t"
"v_mfma_f32_16x16x32_bf16 v[216:219], a[204:207], a[252:255], v[216:219]\n\t"
"s_mul_i32 s99, s46, s75\n\t"
"ds_read_b64_tr_b16 a[198:199], v19 offset:61632\n\t"
"s_mov_b32 s70, 0xbfb8aa3b\n\t"
"v_mfma_f32_16x16x32_bf16 v[220:223], a[204:207], a[96:99], v[220:223]\n\t"
"buffer_load_dwordx4 a[160:163], v1, s[8:11], s57 offen offset:1024 sc1\n\t"
"v_mfma_f32_16x16x32_bf16 v[204:207], a[200:203], a[96:99], v[204:207]\n\t"
"s_mov_b32 s80, 4294901760\n\t"
"s_mov_b32 s53, 1048576\n\t"
"s_waitcnt lgkmcnt(4)\n\t"
"v_mfma_f32_16x16x32_bf16 v[236:239], a[208:211], a[96:99], v[236:239]\n\t"
"s_add_u32 s51, s53, 131072\n\t"
"ds_read_b64_tr_b16 a[200:201], v18 offset:21056\n\t"
"s_mul_i32 s52, s51, 3\n\t"
"v_mfma_f32_16x16x32_bf16 v[252:255], a[212:215], a[96:99], v[252:255]\n\t"
"s_cmp_eq_u32 s99, 0\n\t"
"ds_read_b64_tr_b16 a[202:203], v18 offset:21568\n\t"
"s_cbranch_scc1 .Ly0%=\n\t"
".Lyb0%=:\n"
"v_mfma_f32_16x16x32_bf16 v[248:251], a[212:215], a[252:255], v[248:251]\n\t"
"buffer_load_dwordx4 a[164:167], v1, s[8:11], s57 offen offset:2048 sc1\n\t"
"v_mfma_f32_16x16x32_bf16 v[232:235], a[208:211], a[252:255], v[232:235]\n\t"
"s_lshl_b32 s46, %[sB], 11\n\t"
"v_mov_b32 v25, s39\n\t"
"s_lshl_b32 s75, s38, 9\n\t"
"v_mfma_f32_16x16x32_bf16 v[228:231], a[208:211], a[248:251], v[228:231]\n\t"
"s_add_u32 s46, s46, s75\n\t"
"ds_read_b64_tr_b16 a[204:205], v18 offset:21088\n\t"
"s_add_u32 s46, s46, 32505856\n\t"
"v_mfma_f32_16x16x32_bf16 v[244:247], a[212:215], a[248:251], v[244:247]\n\t"
"ds_read_b64_tr_b16 a[206:207], v18 offset:21600\n\t"
"v_lshrrev_b32 v26, 1, v1\n\t"
"v_mfma_f32_16x16x32_bf16 v[240:243], a[212:215], a[244:247], v[240:243]\n\t"
"v_mov_b32 v24, 1073741824\n\t"
"v_add_u32 v26, s46, v26\n\t"
"v_mfma_f32_16x16x32_bf16 v[224:227], a[208:211], a[244:247], v[224:227]\n\t"
"s_waitcnt lgkmcnt(4)\n\t"
"buffer_load_dwordx4 a[232:235], v1, s[8:11], s57 offen offset:3072 sc1\n\t"
"v_mfma_f32_16x16x32_bf16 v[64:67], a[192:195], a[244:247], v[64:67]\n\t"
"ds_read_b64_tr_b16 a[208:209], v18 offset:21120\n\t"
"v_and_b32 v30, 7, %[vT]\n\t"
"v_mfma_f32_16x16x32_bf16 v[96:99], a[196:199], a[244:247], v[96:99]\n\t"
"s_addk_i32 s58, 4096\n\t"
"ds_read_b64_tr_b16 a[210:211], v18 offset:21632\n\t"
"v_mov_b32_dpp v26, v24 quad_perm:[0,1,2,3] row_mask:0xe bank_mask:0xf\n\t"
"v_mfma_f32_16x16x32_bf16 v[100:103], a[196:199], a[248:251], v[100:103]\n\t"
"s_barrier\n\t"
"v_mfma_f32_16x16x32_bf16 v[68:71], a[192:195], a[248:251], v[68:71]\n\t"
"buffer_load_dwordx4 a[168:171], v1, s[8:11], s58 offen offset:0 sc1\n\t"
"v_mfma_f32_16x16x32_bf16 v[72:75], a[192:195], a[252:255], v[72:75]\n\t"
"ds_read_b64_tr_b16 a[212:213], v18 offset:21152\n\t"
"v_lshlrev_b32 v30, 11, v30\n\t"
"v_mfma_f32_16x16x32_bf16 v[104:107], a[196:199], a[252:255], v[104:107]\n\t"
"ds_read_b64_tr_b16 a[214:215], v18 offset:21664\n\t"
"v_xor_b32 v30, s46, v30\n\t"
"v_mfma_f32_16x16x32_bf16 v[108:111], a[196:199], a[96:99], v[108:111]\n\t"
"s_add_u32 s62, s62, s56\n\t"
"v_mfma_f32_16x16x32_bf16 v[76:79], a[192:195], a[96:99], v[76:79]\n\t"
"s_addk_i32 m0, -29440\n\t"
"s_waitcnt lgkmcnt(4)\n\t"
"v_mfma_f32_16x16x32_bf16 v[128:131], a[200:203], a[100:103], v[128:131]\n\t"
"ds_read_b64_tr_b16 a[192:193], v19 offset:29504\n\t"
"s_memrealtime s[40:41]\n\t"
"v_mfma_f32_16x16x32_bf16 v[144:147], a[204:207], a[100:103], v[144:147]\n\t"
"ds_read_b64_tr_b16 a[194:195], v19 offset:30016\n\t"
"v_mov_b32_dpp v30, v24 quad_perm:[0,1,2,3] row_mask:0xf bank_mask:0xc\n\t"
"v_mfma_f32_16x16x32_bf16 v[148:151], a[204:207], a[104:107], v[148:151]\n\t"
"buffer_load_dwordx4 v2, s[24:27], s62 offen offset:0 lds\n\t"
"v_mfma_f32_16x16x32_bf16 v[132:135], a[200:203], a[104:107], v[132:135]\n\t"
"v_mov_b32_dpp v30, v24 quad_perm:[0,1,2,3] row_mask:0xe bank_mask:0xf\n\t"
"v_mfma_f32_16x16x32_bf16 v[136:139], a[200:203], a[108:111], v[136:139]\n\t"
"ds_read_b64_tr_b16 a[196:197], v18 offset:21184\n\t"
"v_mfma_f32_16x16x32_bf16 v[152:155], a[204:207], a[108:111], v[152:155]\n\t"
"ds_read_b64_tr_b16 a[198:199], v18 offset:21696\n\t"
"s_addk_i32 m0, 832\n\t"
"v_mfma_f32_16x16x32_bf16 v[156:159], a[204:207], a[112:115], v[156:159]\n\t"
"buffer_load_dwordx4 v2, s[24:27], s62 offen offset:1280 lds\n\t"
"v_mfma_f32_16x16x32_bf16 v[140:143], a[200:203], a[112:115], v[140:143]\n\t"
"s_waitcnt lgkmcnt(4)\n\t"
"v_mfma_f32_16x16x32_bf16 v[172:175], a[208:211], a[112:115], v[172:175]\n\t"
"ds_read_b64_tr_b16 a[200:201], v18 offset:21216\n\t"
"v_mfma_f32_16x16x32_bf16 v[188:191], a[212:215], a[112:115], v[188:191]\n\t"
"ds_read_b64_tr_b16 a[202:203], v18 offset:21728\n\t"
"v_mfma_f32_16x16x32_bf16 v[184:187], a[212:215], a[108:111], v[184:187]\n\t"
"v_mfma_f32_16x16x32_bf16 v[168:171], a[208:211], a[108:111], v[168:171]\n\t"
"s_addk_i32 m0, 832\n\t"
"ds_read_b64_tr_b16 a[204:205], v18 offset:21248\n\t"
"v_mfma_f32_16x16x32_bf16 v[164:167], a[208:211], a[104:107], v[164:167]\n\t"
"buffer_load_dwordx4 v2, s[24:27], s62 offen offset:2560 lds\n\t"
"v_mfma_f32_16x16x32_bf16 v[180:183], a[212:215], a[104:107], v[180:183]\n\t"
"ds_read_b64_tr_b16 a[206:207], v18 offset:21760\n\t"
"v_mfma_f32_16x16x32_bf16 v[176:179], a[212:215], a[100:103], v[176:179]\n\t"
"v_mov_b32 v24, s40\n\t"
"v_mfma_f32_16x16x32_bf16 v[160:163], a[208:211], a[100:103], v[160:163]\n\t"
"s_waitcnt lgkmcnt(4)\n\t"
"ds_read_b64_tr_b16 a[208:209], v18 offset:21280\n\t"
"v_mfma_f32_16x16x32_bf16 v[192:195], a[192:195], a[100:103], v[192:195]\n\t"
"buffer_store_dwordx2 v[24:25], v26, s[8:11], 0 offen offset:0 sc1\n\t"
"v_mfma_f32_16x16x32_bf16 v[208:211], a[196:199], a[100:103], v[208:211]\n\t"
"ds_read_b64_tr_b16 a[210:211], v18 offset:21792\n\t"
"s_addk_i32 m0, 832\n\t"
"v_mfma_f32_16x16x32_bf16 v[212:215], a[196:199], a[104:107], v[212:215]\n\t"
"buffer_load_dwordx4 v2, s[24:27], s62 offen offset:3840 lds\n\t"
"v_mfma_f32_16x16x32_bf16 v[196:199], a[192:195], a[104:107], v[196:199]\n\t"
".Lpg14%=:\n"
"s_cbranch_execz .Lhr0%=\n\t"
"v_mfma_f32_16x16x32_bf16 v[200:203], a[192:195], a[108:111], v[200:203]\n\t"
"ds_read_b64_tr_b16 a[212:213], v19 offset:29536\n\t"
"v_mfma_f32_16x16x32_bf16 v[216:219], a[196:199], a[108:111], v[216:219]\n\t"
"ds_read_b64_tr_b16 a[214:215], v19 offset:30048\n\t"
"v_mfma_f32_16x16x32_bf16 v[220:223], a[196:199], a[112:115], v[220:223]\n\t"
"v_mfma_f32_16x16x32_bf16 v[204:207], a[192:195], a[112:115], v[204:207]\n\t"
"s_addk_i32 m0, 5888\n\t"
"s_waitcnt lgkmcnt(4)\n\t"
"ds_read_b64_tr_b16 a[192:193], v18 offset:31584\n\t"
"v_mfma_f32_16x16x32_bf16 v[236:239], a[200:203], a[112:115], v[236:239]\n\t"
"buffer_load_dwordx4 v9, s[24:27], s62 offen offset:64 lds\n\t"
"v_mfma_f32_16x16x32_bf16 v[252:255], a[204:207], a[112:115], v[252:255]\n\t"
"ds_read_b64_tr_b16 a[194:195], v18 offset:32096\n\t"
"v_mfma_f32_16x16x32_bf16 v[248:251], a[204:207], a[108:111], v[248:251]\n\t"
"v_mfma_f32_16x16x32_bf16 v[232:235], a[200:203], a[108:111], v[232:235]\n\t"
"v_mfma_f32_16x16x32_bf16 v[228:231], a[200:203], a[104:107], v[228:231]\n\t"
"ds_read_b64_tr_b16 a[196:197], v18 offset:31616\n\t"
"v_mfma_f32_16x16x32_bf16 v[244:247], a[204:207], a[104:107], v[244:247]\n\t"
"ds_read_b64_tr_b16 a[198:199], v18 offset:32128\n\t"
"v_mfma_f32_16x16x32_bf16 v[240:243], a[204:207], a[100:103], v[240:243]\n\t"
"buffer_load_dwordx4 a[172:175], v1, s[8:11], s58 offen offset:1024 sc1\n\t"
"v_mfma_f32_16x16x32_bf16 v[224:227], a[200:203], a[100:103], v[224:227]\n\t"
"s_waitcnt lgkmcnt(4)\n\t"
"v_mfma_f32_16x16x32_bf16 v[64:67], a[208:211], a[100:103], v[64:67]\n\t"
"ds_read_b64_tr_b16 a[200:201], v18 offset:31648\n\t"
"v_mfma_f32_16x16x32_bf16 v[96:99], a[212:215], a[100:103], v[96:99]\n\t"
"ds_read_b64_tr_b16 a[202:203], v18 offset:32160\n\t"
"v_mfma_f32_16x16x32_bf16 v[100:103], a[212:215], a[104:107], v[100:103]\n\t"
"v_mfma_f32_16x16x32_bf16 v[68:71], a[208:211], a[104:107], v[68:71]\n\t"
"buffer_load_dwordx4 a[176:179], v1, s[8:11], s58 offen offset:2048 sc1\n\t"
"v_mfma_f32_16x16x32_bf16 v[72:75], a[208:211], a[108:111], v[72:75]\n\t"
"ds_read_b64_tr_b16 a[204:205], v18 offset:31680\n\t"
"v_mfma_f32_16x16x32_bf16 v[104:107], a[212:215], a[108:111], v[104:107]\n\t"
"ds_read_b64_tr_b16 a[206:207], v18 offset:32192\n\t"
"v_mfma_f32_16x16x32_bf16 v[108:111], a[212:215], a[112:115], v[108:111]\n\t"
"buffer_load_dwordx4 a[180:183], v1, s[8:11], s58 offen offset:3072 sc1\n\t"
"v_mfma_f32_16x16x32_bf16 v[76:79], a[208:211], a[112:115], v[76:79]\n\t"
"s_waitcnt lgkmcnt(4)\n\t"
"v_mfma_f32_16x16x32_bf16 v[128:131], a[192:195], a[116:119], v[128:131]\n\t"
"ds_read_b64_tr_b16 a[208:209], v19 offset:40032\n\t"
"v_mfma_f32_16x16x32_bf16 v[144:147], a[196:199], a[116:119], v[144:147]\n\t"
"ds_read_b64_tr_b16 a[210:211], v19 offset:40544\n\t"
"v_mfma_f32_16x16x32_bf16 v[148:151], a[196:199], a[120:123], v[148:151]\n\t"
"v_mfma_f32_16x16x32_bf16 v[132:135], a[192:195], a[120:123], v[132:135]\n\t"
"s_addk_i32 s59, 4096\n\t"
"ds_read_b64_tr_b16 a[212:213], v18 offset:31712\n\t"
"v_mfma_f32_16x16x32_bf16 v[136:139], a[192:195], a[124:127], v[136:139]\n\t"
"buffer_load_dwordx4 a[184:187], v1, s[8:11], s59 offen offset:0 sc1\n\t"
"v_mfma_f32_16x16x32_bf16 v[152:155], a[196:199], a[124:127], v[152:155]\n\t"
"ds_read_b64_tr_b16 a[214:215], v18 offset:32224\n\t"
"v_mfma_f32_16x16x32_bf16 v[156:159], a[196:199], a[236:239], v[156:159]\n\t"
"v_mfma_f32_16x16x32_bf16 v[140:143], a[192:195], a[236:239], v[140:143]\n\t"
"s_waitcnt lgkmcnt(4)\n\t"
"ds_read_b64_tr_b16 a[192:193], v18 offset:31744\n\t"
"v_mfma_f32_16x16x32_bf16 v[172:175], a[200:203], a[236:239], v[172:175]\n\t"
"buffer_load_dwordx4 a[188:191], v1, s[8:11], s59 offen offset:1024 sc1\n\t"
"v_mfma_f32_16x16x32_bf16 v[188:191], a[204:207], a[236:239], v[188:191]\n\t"
"ds_read_b64_tr_b16 a[194:195], v18 offset:32256\n\t"
"v_mfma_f32_16x16x32_bf16 v[184:187], a[204:207], a[124:127], v[184:187]\n\t"
"v_mfma_f32_16x16x32_bf16 v[168:171], a[200:203], a[124:127], v[168:171]\n\t"
"v_mfma_f32_16x16x32_bf16 v[164:167], a[200:203], a[120:123], v[164:167]\n\t"
"ds_read_b64_tr_b16 a[196:197], v18 offset:31776\n\t"
"v_mfma_f32_16x16x32_bf16 v[180:183], a[204:207], a[120:123], v[180:183]\n\t"
"buffer_load_dwordx4 v[52:55], v1, s[8:11], s59 offen offset:2048 sc1\n\t"
"v_mfma_f32_16x16x32_bf16 v[176:179], a[204:207], a[116:119], v[176:179]\n\t"
"ds_read_b64_tr_b16 a[198:199], v18 offset:32288\n\t"
"v_mfma_f32_16x16x32_bf16 v[160:163], a[200:203], a[116:119], v[160:163]\n\t"
"s_cmp_eq_u32 s99, 0\n\t"
"s_cbranch_scc1 .Ly1%=\n\t"
".Lyb1%=:\n"
"s_waitcnt lgkmcnt(4)\n\t"
"v_mfma_f32_16x16x32_bf16 v[192:195], a[208:211], a[116:119], v[192:195]\n\t"
"ds_read_b64_tr_b16 a[200:201], v18 offset:31808\n\t"
"v_mfma_f32_16x16x32_bf16 v[208:211], a[212:215], a[116:119], v[208:211]\n\t"
"ds_read_b64_tr_b16 a[202:203], v18 offset:32320\n\t"
"v_mfma_f32_16x16x32_bf16 v[212:215], a[212:215], a[120:123], v[212:215]\n\t"
"buffer_load_dwordx4 v[56:59], v1, s[8:11], s59 offen offset:3072 sc1\n\t"
"v_mfma_f32_16x16x32_bf16 v[196:199], a[208:211], a[120:123], v[196:199]\n\t"
"v_mfma_f32_16x16x32_bf16 v[200:203], a[208:211], a[124:127], v[200:203]\n\t"
"ds_read_b64_tr_b16 a[204:205], v19 offset:40064\n\t"
"v_mfma_f32_16x16x32_bf16 v[216:219], a[212:215], a[124:127], v[216:219]\n\t"
"ds_read_b64_tr_b16 a[206:207], v19 offset:40576\n\t"
"s_addk_i32 s57, 4096\n\t"
"v_mfma_f32_16x16x32_bf16 v[220:223], a[212:215], a[236:239], v[220:223]\n\t"
"buffer_load_dwordx4 v[60:63], v1, s[8:11], s57 offen offset:0 sc1\n\t"
"v_mfma_f32_16x16x32_bf16 v[204:207], a[208:211], a[236:239], v[204:207]\n\t"
"s_waitcnt lgkmcnt(4)\n\t"
"v_mfma_f32_16x16x32_bf16 v[236:239], a[192:195], a[236:239], v[236:239]\n\t"
"ds_read_b64_tr_b16 a[208:209], v18 offset:0\n\t"
"v_mfma_f32_16x16x32_bf16 v[252:255], a[196:199], a[236:239], v[252:255]\n\t"
"ds_read_b64_tr_b16 a[210:211], v18 offset:512\n\t"
"v_mfma_f32_16x16x32_bf16 v[248:251], a[196:199], a[124:127], v[248:251]\n\t"
"v_mfma_f32_16x16x32_bf16 v[232:235], a[192:195], a[124:127], v[232:235]\n\t"
"buffer_load_dwordx4 v[32:35], v1, s[8:11], s57 offen offset:1024 sc1\n\t"
"v_mfma_f32_16x16x32_bf16 v[228:231], a[192:195], a[120:123], v[228:231]\n\t"
"ds_read_b64_tr_b16 a[212:213], v18 offset:32\n\t"
"v_mfma_f32_16x16x32_bf16 v[244:247], a[196:199], a[120:123], v[244:247]\n\t"
"ds_read_b64_tr_b16 a[214:215], v18 offset:544\n\t"
"v_mfma_f32_16x16x32_bf16 v[240:243], a[196:199], a[116:119], v[240:243]\n\t"
"v_mfma_f32_16x16x32_bf16 v[224:227], a[192:195], a[116:119], v[224:227]\n\t"
"s_waitcnt lgkmcnt(4)\n\t"
"buffer_load_dwordx4 v[36:39], v1, s[8:11], s57 offen offset:2048 sc1\n\t"
"v_mfma_f32_16x16x32_bf16 v[64:67], a[200:203], a[116:119], v[64:67]\n\t"
"ds_read_b64_tr_b16 a[192:193], v18 offset:64\n\t"
"v_mfma_f32_16x16x32_bf16 v[96:99], a[204:207], a[116:119], v[96:99]\n\t"
"ds_read_b64_tr_b16 a[194:195], v18 offset:576\n\t"
"v_mfma_f32_16x16x32_bf16 v[100:103], a[204:207], a[120:123], v[100:103]\n\t"
"s_waitcnt vmcnt(27)\n\t"
"s_barrier\n\t"
"v_mfma_f32_16x16x32_bf16 v[68:71], a[200:203], a[120:123], v[68:71]\n\t"
"s_add_u32 s63, s63, s56\n\t"
"s_addk_i32 m0, -29440\n\t"
"ds_read_b64_tr_b16 a[196:197], v18 offset:96\n\t"
"v_mfma_f32_16x16x32_bf16 v[72:75], a[200:203], a[124:127], v[72:75]\n\t"
"buffer_load_dwordx4 v2, s[24:27], s63 offen offset:0 lds\n\t"
"v_mfma_f32_16x16x32_bf16 v[104:107], a[204:207], a[124:127], v[104:107]\n\t"
"ds_read_b64_tr_b16 a[198:199], v18 offset:608\n\t"
"v_mfma_f32_16x16x32_bf16 v[108:111], a[204:207], a[236:239], v[108:111]\n\t"
"v_mfma_f32_16x16x32_bf16 v[76:79], a[200:203], a[236:239], v[76:79]\n\t"
"s_waitcnt lgkmcnt(4)\n\t"
"v_mfma_f32_16x16x32_bf16 v[128:131], a[208:211], a[240:243], v[128:131]\n\t"
"ds_read_b64_tr_b16 a[200:201], v18 offset:10528\n\t"
"v_mfma_f32_16x16x32_bf16 v[144:147], a[212:215], a[240:243], v[144:147]\n\t"
"ds_read_b64_tr_b16 a[202:203], v18 offset:11040\n\t"
"s_addk_i32 m0, 832\n\t"
"v_mfma_f32_16x16x32_bf16 v[148:151], a[212:215], a[0:3], v[148:151]\n\t"
"buffer_load_dwordx4 v2, s[24:27], s63 offen offset:1280 lds\n\t"
"v_mfma_f32_16x16x32_bf16 v[132:135], a[208:211], a[0:3], v[132:135]\n\t"
"v_mfma_f32_16x16x32_bf16 v[136:139], a[208:211], a[4:7], v[136:139]\n\t"
"ds_read_b64_tr_b16 a[204:205], v18 offset:10560\n\t"
"v_mfma_f32_16x16x32_bf16 v[152:155], a[212:215], a[4:7], v[152:155]\n\t"
"ds_read_b64_tr_b16 a[206:207], v18 offset:11072\n\t"
"v_mfma_f32_16x16x32_bf16 v[156:159], a[212:215], a[8:11], v[156:159]\n\t"
"v_mfma_f32_16x16x32_bf16 v[140:143], a[208:211], a[8:11], v[140:143]\n\t"
"s_addk_i32 m0, 832\n\t"
"s_waitcnt lgkmcnt(4)\n\t"
"ds_read_b64_tr_b16 a[208:209], v13 offset:16448\n\t"
"v_mfma_f32_16x16x32_bf16 v[172:175], a[192:195], a[8:11], v[172:175]\n\t"
"buffer_load_dwordx4 v2, s[24:27], s63 offen offset:2560 lds\n\t"
"v_mfma_f32_16x16x32_bf16 v[188:191], a[196:199], a[8:11], v[188:191]\n\t"
"ds_read_b64_tr_b16 a[210:211], v13 offset:16960\n\t"
"v_mfma_f32_16x16x32_bf16 v[184:187], a[196:199], a[4:7], v[184:187]\n\t"
"v_mfma_f32_16x16x32_bf16 v[168:171], a[192:195], a[4:7], v[168:171]\n\t"
"v_mfma_f32_16x16x32_bf16 v[164:167], a[192:195], a[0:3], v[164:167]\n\t"
"ds_read_b64_tr_b16 a[212:213], v13 offset:16480\n\t"
"v_mfma_f32_16x16x32_bf16 v[180:183], a[196:199], a[0:3], v[180:183]\n\t"
"ds_read_b64_tr_b16 a[214:215], v13 offset:16992\n\t"
"s_addk_i32 m0, 832\n\t"
"v_mfma_f32_16x16x32_bf16 v[176:179], a[196:199], a[240:243], v[176:179]\n\t"
"buffer_load_dwordx4 v2, s[24:27], s63 offen offset:3840 lds\n\t"
"v_mfma_f32_16x16x32_bf16 v[160:163], a[192:195], a[240:243], v[160:163]\n\t"
"s_waitcnt lgkmcnt(4)\n\t"
"v_mfma_f32_16x16x32_bf16 v[128:131], a[200:203], a[12:15], v[128:131]\n\t"
"ds_read_b64_tr_b16 a[192:193], v13 offset:26976\n\t"
"v_mfma_f32_16x16x32_bf16 v[144:147], a[204:207], a[12:15], v[144:147]\n\t"
"ds_read_b64_tr_b16 a[194:195], v13 offset:27488\n\t"
"v_mfma_f32_16x16x32_bf16 v[148:151], a[204:207], a[16:19], v[148:151]\n\t"
"v_mfma_f32_16x16x32_bf16 v[132:135], a[200:203], a[16:19], v[132:135]\n\t"
"s_addk_i32 m0, 5888\n\t"
"ds_read_b64_tr_b16 a[196:197], v13 offset:27008\n\t"
"v_mfma_f32_16x16x32_bf16 v[136:139], a[200:203], a[20:23], v[136:139]\n\t"
"buffer_load_dwordx4 v9, s[24:27], s63 offen offset:64 lds\n\t"
"v_mfma_f32_16x16x32_bf16 v[152:155], a[204:207], a[20:23], v[152:155]\n\t"
"ds_read_b64_tr_b16 a[198:199], v13 offset:27520\n\t"
"v_mfma_f32_16x16x32_bf16 v[156:159], a[204:207], a[24:27], v[156:159]\n\t"
"v_mfma_f32_16x16x32_bf16 v[140:143], a[200:203], a[24:27], v[140:143]\n\t"
"s_waitcnt lgkmcnt(4)\n\t"
"v_mfma_f32_16x16x32_bf16 v[128:131], a[208:211], a[28:31], v[128:131]\n\t"
"ds_read_b64_tr_b16 a[200:201], v16 offset:42112\n\t"
"v_mfma_f32_16x16x32_bf16 v[144:147], a[212:215], a[28:31], v[144:147]\n\t"
"ds_read_b64_tr_b16 a[202:203], v16 offset:42624\n\t"
"v_mfma_f32_16x16x32_bf16 v[148:151], a[212:215], a[32:35], v[148:151]\n\t"
"buffer_load_dwordx4 v[40:43], v1, s[8:11], s57 offen offset:3072 sc1\n\t"
"v_mfma_f32_16x16x32_bf16 v[132:135], a[208:211], a[32:35], v[132:135]\n\t"
"v_mfma_f32_16x16x32_bf16 v[136:139], a[208:211], a[36:39], v[136:139]\n\t"
"ds_read_b64_tr_b16 a[204:205], v16 offset:42144\n\t"
"v_mfma_f32_16x16x32_bf16 v[152:155], a[212:215], a[36:39], v[152:155]\n\t"
"ds_read_b64_tr_b16 a[206:207], v16 offset:42656\n\t"
"v_mfma_f32_16x16x32_bf16 v[156:159], a[212:215], a[40:43], v[156:159]\n\t"
"s_addk_i32 s58, 4096\n\t"
"v_mfma_f32_16x16x32_bf16 v[140:143], a[208:211], a[40:43], v[140:143]\n\t"
"s_waitcnt lgkmcnt(4)\n\t"
"v_mfma_f32_16x16x32_bf16 v[128:131], a[192:195], a[44:47], v[128:131]\n\t"
"ds_read_b64_tr_b16 a[208:209], v16 offset:52640\n\t"
"v_mfma_f32_16x16x32_bf16 v[144:147], a[196:199], a[44:47], v[144:147]\n\t"
"ds_read_b64_tr_b16 a[210:211], v16 offset:53152\n\t"
"v_mfma_f32_16x16x32_bf16 v[148:151], a[196:199], a[48:51], v[148:151]\n\t"
"buffer_load_dwordx4 a[216:219], v1, s[8:11], s58 offen offset:0 sc1\n\t"
"v_mfma_f32_16x16x32_bf16 v[132:135], a[192:195], a[48:51], v[132:135]\n\t"
"buffer_load_dwordx4 a[220:223], v1, s[8:11], s58 offen offset:1024 sc1\n\t"
"v_mfma_f32_16x16x32_bf16 v[136:139], a[192:195], a[52:55], v[136:139]\n\t"
"ds_read_b64_tr_b16 a[212:213], v16 offset:52672\n\t"
"v_mfma_f32_16x16x32_bf16 v[152:155], a[196:199], a[52:55], v[152:155]\n\t"
"ds_read_b64_tr_b16 a[214:215], v16 offset:53184\n\t"
"v_mfma_f32_16x16x32_bf16 v[156:159], a[196:199], a[56:59], v[156:159]\n\t"
"v_mfma_f32_16x16x32_bf16 v[140:143], a[192:195], a[56:59], v[140:143]\n\t"
"s_waitcnt lgkmcnt(4)\n\t"
"ds_read_b64_tr_b16 a[192:193], v16 offset:21056\n\t"
"v_mfma_f32_16x16x32_bf16 v[128:131], a[200:203], a[60:63], v[128:131]\n\t"
"buffer_load_dwordx4 a[224:227], v1, s[8:11], s58 offen offset:2048 sc1\n\t"
"v_mfma_f32_16x16x32_bf16 v[144:147], a[204:207], a[60:63], v[144:147]\n\t"
"ds_read_b64_tr_b16 a[194:195], v16 offset:21568\n\t"
"v_mfma_f32_16x16x32_bf16 v[148:151], a[204:207], a[64:67], v[148:151]\n\t"
"v_mfma_f32_16x16x32_bf16 v[132:135], a[200:203], a[64:67], v[132:135]\n\t"
"ds_read_b64_tr_b16 a[196:197], v16 offset:21088\n\t"
"v_mfma_f32_16x16x32_bf16 v[136:139], a[200:203], a[68:71], v[136:139]\n\t"
"buffer_load_dwordx4 a[228:231], v1, s[8:11], s58 offen offset:3072 sc1\n\t"
"v_mfma_f32_16x16x32_bf16 v[152:155], a[204:207], a[68:71], v[152:155]\n\t"
"ds_read_b64_tr_b16 a[198:199], v16 offset:21600\n\t"
"v_mfma_f32_16x16x32_bf16 v[156:159], a[204:207], a[72:75], v[156:159]\n\t"
"v_mfma_f32_16x16x32_bf16 v[140:143], a[200:203], a[72:75], v[140:143]\n\t"
"s_waitcnt lgkmcnt(4)\n\t"
"v_mfma_f32_16x16x32_bf16 v[128:131], a[208:211], a[76:79], v[128:131]\n\t"
"ds_read_b64_tr_b16 a[200:201], v16 offset:31584\n\t"
"s_addk_i32 s59, 4096\n\t"
"v_mfma_f32_16x16x32_bf16 v[144:147], a[212:215], a[76:79], v[144:147]\n\t"
"buffer_load_dwordx4 a[244:247], v1, s[8:11], s59 offen offset:0 sc1\n\t"
"v_mfma_f32_16x16x32_bf16 v[148:151], a[212:215], a[80:83], v[148:151]\n\t"
"ds_read_b64_tr_b16 a[202:203], v16 offset:32096\n\t"
"v_mfma_f32_16x16x32_bf16 v[132:135], a[208:211], a[80:83], v[132:135]\n\t"
"v_mfma_f32_16x16x32_bf16 v[136:139], a[208:211], a[84:87], v[136:139]\n\t"
"ds_read_b64_tr_b16 a[204:205], v16 offset:31616\n\t"
"v_mfma_f32_16x16x32_bf16 v[152:155], a[212:215], a[84:87], v[152:155]\n\t"
"buffer_load_dwordx4 a[248:251], v1, s[8:11], s59 offen offset:1024 sc1\n\t"
"v_mfma_f32_16x16x32_bf16 v[156:159], a[212:215], a[88:91], v[156:159]\n\t"
"ds_read_b64_tr_b16 a[206:207], v16 offset:32128\n\t"
"v_mfma_f32_16x16x32_bf16 v[140:143], a[208:211], a[88:91], v[140:143]\n\t"
"s_waitcnt lgkmcnt(4)\n\t"
"s_waitcnt vmcnt(37)\n\t"
"v_mfma_f32_16x16x32_bf16 v[128:131], a[192:195], a[92:95], v[128:131]\n\t"
"ds_read_b64_tr_b16 a[208:209], v16 offset:0\n\t"
"v_mfma_f32_16x16x32_bf16 v[144:147], a[196:199], a[92:95], v[144:147]\n\t"
"ds_read_b64_tr_b16 a[210:211], v16 offset:512\n\t"
"v_mfma_f32_16x16x32_bf16 v[148:151], a[196:199], a[128:131], v[148:151]\n\t"
"buffer_load_dwordx4 a[252:255], v1, s[8:11], s59 offen offset:2048 sc1\n\t"
"v_mfma_f32_16x16x32_bf16 v[132:135], a[192:195], a[128:131], v[132:135]\n\t"
"v_mfma_f32_16x16x32_bf16 v[136:139], a[192:195], a[132:135], v[136:139]\n\t"
"ds_read_b64_tr_b16 a[212:213], v16 offset:32\n\t"
"v_mfma_f32_16x16x32_bf16 v[152:155], a[196:199], a[132:135], v[152:155]\n\t"
"ds_read_b64_tr_b16 a[214:215], v16 offset:544\n\t"
"v_mfma_f32_16x16x32_bf16 v[156:159], a[196:199], a[136:139], v[156:159]\n\t"
"buffer_load_dwordx4 a[96:99], v1, s[8:11], s59 offen offset:3072 sc1\n\t"
"v_mfma_f32_16x16x32_bf16 v[140:143], a[192:195], a[136:139], v[140:143]\n\t"
"s_waitcnt lgkmcnt(4)\n\t"
"s_waitcnt vmcnt(35)\n\t"
"v_mfma_f32_16x16x32_bf16 v[128:131], a[200:203], a[140:143], v[128:131]\n\t"
"ds_read_b64_tr_b16 a[192:193], v16 offset:10528\n\t"
"v_mfma_f32_16x16x32_bf16 v[144:147], a[204:207], a[140:143], v[144:147]\n\t"
"ds_read_b64_tr_b16 a[194:195], v16 offset:11040\n\t"
"v_mfma_f32_16x16x32_bf16 v[148:151], a[204:207], a[144:147], v[148:151]\n\t"
"s_add_u32 m0, s43, 0\n\t"
"v_mfma_f32_16x16x32_bf16 v[132:135], a[200:203], a[144:147], v[132:135]\n\t"
"buffer_load_dwordx4 v92, s[4:7], s47 offen offset:0 lds\n\t"
"v_mfma_f32_16x16x32_bf16 v[136:139], a[200:203], a[148:151], v[136:139]\n\t"
"ds_read_b64_tr_b16 a[196:197], v16 offset:10560\n\t"
"v_mfma_f32_16x16x32_bf16 v[152:155], a[204:207], a[148:151], v[152:155]\n\t"
"ds_read_b64_tr_b16 a[198:199], v16 offset:11072\n\t"
"v_mfma_f32_16x16x32_bf16 v[156:159], a[204:207], a[152:155], v[156:159]\n\t"
"s_add_u32 m0, s43, 64\n\t"
"s_waitcnt vmcnt(25)\n\t"
"v_mfma_f32_16x16x32_bf16 v[140:143], a[200:203], a[152:155], v[140:143]\n\t"
"s_barrier\n\t"
"s_waitcnt lgkmcnt(4)\n\t"
"v_mfma_f32_16x16x32_bf16 v[128:131], a[208:211], a[156:159], v[128:131]\n\t"
"buffer_load_dwordx4 v93, s[4:7], s47 offen offset:1024 lds\n\t"
"ds_read_b64_tr_b16 a[200:201], v18 offset:42112\n\t"
"v_mfma_f32_16x16x32_bf16 v[144:147], a[212:215], a[156:159], v[144:147]\n\t"
"ds_read_b64_tr_b16 a[202:203], v18 offset:42624\n\t"
"v_mfma_f32_16x16x32_bf16 v[148:151], a[212:215], a[160:163], v[148:151]\n\t"
"s_addk_i32 m0, 2112\n\t"
"v_mfma_f32_16x16x32_bf16 v[132:135], a[208:211], a[160:163], v[132:135]\n\t"
"buffer_load_dwordx4 v94, s[4:7], s47 offen offset:0 lds\n\t"
"v_mfma_f32_16x16x32_bf16 v[136:139], a[208:211], a[164:167], v[136:139]\n\t"
"ds_read_b64_tr_b16 a[204:205], v18 offset:42144\n\t"
"v_mfma_f32_16x16x32_bf16 v[152:155], a[212:215], a[164:167], v[152:155]\n\t"
"ds_read_b64_tr_b16 a[206:207], v18 offset:42656\n\t"
"v_mfma_f32_16x16x32_bf16 v[156:159], a[212:215], a[232:235], v[156:159]\n\t"
"buffer_load_dwordx2 v[90:91], v7, s[8:11], 0 offen offset:0 sc1\n\t"
"v_mfma_f32_16x16x32_bf16 v[140:143], a[208:211], a[232:235], v[140:143]\n\t"
"s_waitcnt lgkmcnt(4)\n\t"
"s_waitcnt vmcnt(25)\n\t"
"v_mfma_f32_16x16x32_bf16 v[128:131], a[192:195], a[168:171], v[128:131]\n\t"
"ds_read_b64_tr_b16 a[208:209], v18 offset:52640\n\t"
"v_mfma_f32_16x16x32_bf16 v[144:147], a[196:199], a[168:171], v[144:147]\n\t"
"ds_read_b64_tr_b16 a[210:211], v18 offset:53152\n\t"
"v_mfma_f32_16x16x32_bf16 v[148:151], a[196:199], a[172:175], v[148:151]\n\t"
"v_mfma_f32_16x16x32_bf16 v[132:135], a[192:195], a[172:175], v[132:135]\n\t"
"v_mfma_f32_16x16x32_bf16 v[136:139], a[192:195], a[176:179], v[136:139]\n\t"
"ds_read_b64_tr_b16 a[212:213], v18 offset:52672\n\t"
"v_mfma_f32_16x16x32_bf16 v[152:155], a[196:199], a[176:179], v[152:155]\n\t"
"ds_read_b64_tr_b16 a[214:215], v18 offset:53184\n\t"
"v_mfma_f32_16x16x32_bf16 v[156:159], a[196:199], a[180:183], v[156:159]\n\t"
"s_waitcnt vmcnt(13)\n\t"
"v_mfma_f32_16x16x32_bf16 v[140:143], a[192:195], a[180:183], v[140:143]\n\t"
"s_barrier\n\t"
"s_waitcnt lgkmcnt(4)\n\t"
"v_mfma_f32_16x16x32_bf16 v[128:131], a[200:203], a[184:187], v[128:131]\n\t"
"ds_read_b64_tr_b16 a[192:193], v18 offset:21056\n\t"
"v_mfma_f32_16x16x32_bf16 v[144:147], a[204:207], a[184:187], v[144:147]\n\t"
"ds_read_b64_tr_b16 a[194:195], v18 offset:21568\n\t"
"v_mfma_f32_16x16x32_bf16 v[148:151], a[204:207], a[188:191], v[148:151]\n\t"
"v_mfma_f32_16x16x32_bf16 v[132:135], a[200:203], a[188:191], v[132:135]\n\t"
"v_mfma_f32_16x16x32_bf16 v[136:139], a[200:203], v[52:55], v[136:139]\n\t"
"ds_read_b64_tr_b16 a[196:197], v18 offset:21088\n\t"
"v_mfma_f32_16x16x32_bf16 v[152:155], a[204:207], v[52:55], v[152:155]\n\t"
"ds_read_b64_tr_b16 a[198:199], v18 offset:21600\n\t"
"v_mfma_f32_16x16x32_bf16 v[156:159], a[204:207], v[56:59], v[156:159]\n\t"
"v_mfma_f32_16x16x32_bf16 v[140:143], a[200:203], v[56:59], v[140:143]\n\t"
"s_waitcnt lgkmcnt(4)\n\t"
"s_waitcnt vmcnt(12)\n\t"
"v_mfma_f32_16x16x32_bf16 v[128:131], a[208:211], v[60:63], v[128:131]\n\t"
"ds_read_b64_tr_b16 a[200:201], v18 offset:31584\n\t"
"v_mfma_f32_16x16x32_bf16 v[144:147], a[212:215], v[60:63], v[144:147]\n\t"
"ds_read_b64_tr_b16 a[202:203], v18 offset:32096\n\t"
"v_mfma_f32_16x16x32_bf16 v[148:151], a[212:215], v[32:35], v[148:151]\n\t"
"v_mov_b32 v28, 0\n\t"
"v_mov_b32 v29, 0\n\t"
"v_mfma_f32_16x16x32_bf16 v[132:135], a[208:211], v[32:35], v[132:135]\n\t"
"v_mfma_f32_16x16x32_bf16 v[136:139], a[208:211], v[36:39], v[136:139]\n\t"
"ds_read_b64_tr_b16 a[204:205], v18 offset:31616\n\t"
"v_mfma_f32_16x16x32_bf16 v[152:155], a[212:215], v[36:39], v[152:155]\n\t"
"buffer_atomic_add_x2 v[28:29], v30, s[8:11], 0 offen offset:0 sc0 sc1\n\t"
"v_mfma_f32_16x16x32_bf16 v[156:159], a[212:215], v[40:43], v[156:159]\n\t"
"ds_read_b64_tr_b16 a[206:207], v18 offset:32128\n\t"
"v_mfma_f32_16x16x32_bf16 v[140:143], a[208:211], v[40:43], v[140:143]\n\t"
"s_waitcnt lgkmcnt(4)\n\t"
"s_waitcnt vmcnt(9)\n\t"
"v_mfma_f32_16x16x32_bf16 v[128:131], a[192:195], a[216:219], v[128:131]\n\t"
"ds_read_b64_tr_b16 a[208:209], v18 offset:10592\n\t"
"v_mfma_f32_16x16x32_bf16 v[144:147], a[196:199], a[216:219], v[144:147]\n\t"
"ds_read_b64_tr_b16 a[210:211], v18 offset:11104\n\t"
"v_mfma_f32_16x16x32_bf16 v[148:151], a[196:199], a[220:223], v[148:151]\n\t"
"v_mfma_f32_16x16x32_bf16 v[132:135], a[192:195], a[220:223], v[132:135]\n\t"
"v_mfma_f32_16x16x32_bf16 v[136:139], a[192:195], a[224:227], v[136:139]\n\t"
"ds_read_b64_tr_b16 a[212:213], v18 offset:10624\n\t"
"v_mfma_f32_16x16x32_bf16 v[152:155], a[196:199], a[224:227], v[152:155]\n\t"
"ds_read_b64_tr_b16 a[214:215], v18 offset:11136\n\t"
"v_mfma_f32_16x16x32_bf16 v[156:159], a[196:199], a[228:231], v[156:159]\n\t"
"v_mfma_f32_16x16x32_bf16 v[140:143], a[192:195], a[228:231], v[140:143]\n\t"
"s_waitcnt lgkmcnt(4)\n\t"
"s_waitcnt vmcnt(5)\n\t"
"v_mfma_f32_16x16x32_bf16 v[128:131], a[200:203], a[244:247], v[128:131]\n\t"
"ds_read_b64_tr_b16 a[192:193], v13 offset:16512\n\t"
"v_mfma_f32_16x16x32_bf16 v[144:147], a[204:207], a[244:247], v[144:147]\n\t"
"ds_read_b64_tr_b16 a[194:195], v13 offset:17024\n\t"
"v_mfma_f32_16x16x32_bf16 v[148:151], a[204:207], a[248:251], v[148:151]\n\t"
"v_mfma_f32_16x16x32_bf16 v[132:135], a[200:203], a[248:251], v[132:135]\n\t"
"v_mfma_f32_16x16x32_bf16 v[136:139], a[200:203], a[252:255], v[136:139]\n\t"
"ds_read_b64_tr_b16 a[196:197], v13 offset:16544\n\t"
"v_mfma_f32_16x16x32_bf16 v[152:155], a[204:207], a[252:255], v[152:155]\n\t"
"ds_read_b64_tr_b16 a[198:199], v13 offset:17056\n\t"
"v_mfma_f32_16x16x32_bf16 v[156:159], a[204:207], a[96:99], v[156:159]\n\t"
"v_mfma_f32_16x16x32_bf16 v[140:143], a[200:203], a[96:99], v[140:143]\n\t"
"s_waitcnt lgkmcnt(4)\n\t"
"v_mfma_f32_16x16x32_bf16 v[160:163], a[208:211], a[12:15], v[160:163]\n\t"
"ds_read_b64_tr_b16 a[200:201], v13 offset:27040\n\t"
"v_mfma_f32_16x16x32_bf16 v[176:179], a[212:215], a[12:15], v[176:179]\n\t"
"ds_read_b64_tr_b16 a[202:203], v13 offset:27552\n\t"
"v_cvt_pk_bf16_f32 v128, v128, v129\n\t"
"v_mfma_f32_16x16x32_bf16 v[180:183], a[212:215], a[16:19], v[180:183]\n\t"
"v_cvt_pk_bf16_f32 v129, v130, v131\n\t"
"v_add3_u32 v24, v128, v129, s39\n\t"
"v_mfma_f32_16x16x32_bf16 v[164:167], a[208:211], a[16:19], v[164:167]\n\t"
"v_cvt_pk_bf16_f32 v130, v144, v145\n\t"
"v_cvt_pk_bf16_f32 v131, v146, v147\n\t"
"v_mfma_f32_16x16x32_bf16 v[168:171], a[208:211], a[20:23], v[168:171]\n\t"
"s_xor_b32 s82, s74, 4718592\n\t"
"ds_read_b64_tr_b16 a[204:205], v13 offset:27072\n\t"
"v_add3_u32 v24, v130, v131, v24\n\t"
"v_mfma_f32_16x16x32_bf16 v[184:187], a[212:215], a[20:23], v[184:187]\n\t"
"ds_read_b64_tr_b16 a[206:207], v13 offset:27584\n\t"
"buffer_store_dwordx4 v[128:131], v1, s[8:11], s82 offen offset:0 sc1\n\t"
"v_mfma_f32_16x16x32_bf16 v[188:191], a[212:215], a[24:27], v[188:191]\n\t"
"v_cvt_pk_bf16_f32 v132, v132, v133\n\t"
"v_cvt_pk_bf16_f32 v133, v134, v135\n\t"
"v_mfma_f32_16x16x32_bf16 v[172:175], a[208:211], a[24:27], v[172:175]\n\t"
"s_waitcnt lgkmcnt(4)\n\t"
"v_add3_u32 v25, v132, v133, s39\n\t"
"v_cvt_pk_bf16_f32 v134, v148, v149\n\t"
"v_mfma_f32_16x16x32_bf16 v[160:163], a[192:195], a[28:31], v[160:163]\n\t"
"ds_read_b64_tr_b16 a[208:209], v16 offset:42176\n\t"
"v_cvt_pk_bf16_f32 v135, v150, v151\n\t"
"v_mfma_f32_16x16x32_bf16 v[176:179], a[196:199], a[28:31], v[176:179]\n\t"
"ds_read_b64_tr_b16 a[210:211], v16 offset:42688\n\t"
"v_add3_u32 v25, v134, v135, v25\n\t"
"v_mfma_f32_16x16x32_bf16 v[180:183], a[196:199], a[32:35], v[180:183]\n\t"
"v_cvt_pk_bf16_f32 v136, v136, v137\n\t"
"v_cvt_pk_bf16_f32 v137, v138, v139\n\t"
"v_mfma_f32_16x16x32_bf16 v[164:167], a[192:195], a[32:35], v[164:167]\n\t"
"v_add3_u32 v26, v136, v137, s39\n\t"
"v_cvt_pk_bf16_f32 v138, v152, v153\n\t"
"v_mfma_f32_16x16x32_bf16 v[168:171], a[192:195], a[36:39], v[168:171]\n\t"
"s_xor_b32 s82, s82, s51\n\t"
"ds_read_b64_tr_b16 a[212:213], v16 offset:42208\n\t"
"v_cvt_pk_bf16_f32 v139, v154, v155\n\t"
"v_mfma_f32_16x16x32_bf16 v[184:187], a[196:199], a[36:39], v[184:187]\n\t"
"ds_read_b64_tr_b16 a[214:215], v16 offset:42720\n\t"
"buffer_store_dwordx4 v[132:135], v1, s[8:11], s82 offen offset:0 sc1\n\t"
"v_mfma_f32_16x16x32_bf16 v[188:191], a[196:199], a[40:43], v[188:191]\n\t"
"v_add3_u32 v26, v138, v139, v26\n\t"
"v_cvt_pk_bf16_f32 v140, v140, v141\n\t"
"v_mfma_f32_16x16x32_bf16 v[172:175], a[192:195], a[40:43], v[172:175]\n\t"
"s_waitcnt lgkmcnt(4)\n\t"
"v_cvt_pk_bf16_f32 v141, v142, v143\n\t"
"v_add3_u32 v27, v140, v141, s39\n\t"
"v_mfma_f32_16x16x32_bf16 v[160:163], a[200:203], a[44:47], v[160:163]\n\t"
"ds_read_b64_tr_b16 a[192:193], v16 offset:52704\n\t"
"v_cvt_pk_bf16_f32 v142, v156, v157\n\t"
"v_mfma_f32_16x16x32_bf16 v[176:179], a[204:207], a[44:47], v[176:179]\n\t"
"ds_read_b64_tr_b16 a[194:195], v16 offset:53216\n\t"
"v_cvt_pk_bf16_f32 v143, v158, v159\n\t"
"v_mfma_f32_16x16x32_bf16 v[180:183], a[204:207], a[48:51], v[180:183]\n\t"
"s_waitcnt vmcnt(3)\n\t"
"v_add3_u32 v27, v142, v143, v27\n\t"
"v_cmp_ne_u32 vcc, s39, v91\n\t"
"s_cbranch_vccnz .LslX%=\n\t"
".LbkX%=:\n"
"v_mfma_f32_16x16x32_bf16 v[164:167], a[200:203], a[48:51], v[164:167]\n\t"
"s_mov_b64 s[4:5], s[36:37]\n\t"
"ds_read_b64 v[80:81], v4 offset:0\n\t"
"ds_read_b64 v[82:83], v6 offset:0\n\t"
"v_mfma_f32_16x16x32_bf16 v[168:171], a[200:203], a[52:55], v[168:171]\n\t"
"s_xor_b32 s82, s82, s52\n\t"
"ds_read_b64_tr_b16 a[196:197], v16 offset:52736\n\t"
"ds_read_b64 v[84:85], v4 offset:64\n\t"
"v_mfma_f32_16x16x32_bf16 v[184:187], a[204:207], a[52:55], v[184:187]\n\t"
"ds_read_b64_tr_b16 a[198:199], v16 offset:53248\n\t"
"buffer_store_dwordx4 v[136:139], v1, s[8:11], s82 offen offset:0 sc1\n\t"
"v_mfma_f32_16x16x32_bf16 v[188:191], a[204:207], a[56:59], v[188:191]\n\t"
"ds_read_b64 v[86:87], v6 offset:64\n\t"
"ds_read_b64 v[88:89], v5 offset:2176\n\t"
"v_mfma_f32_16x16x32_bf16 v[172:175], a[200:203], a[56:59], v[172:175]\n\t"
"v_lshlrev_b32 v91, 16, v90\n\t"
"s_waitcnt lgkmcnt(9)\n\t"
"v_mfma_f32_16x16x32_bf16 v[160:163], a[208:211], a[60:63], v[160:163]\n\t"
"ds_read_b64_tr_b16 a[200:201], v16 offset:21120\n\t"
"v_mfma_f32_16x16x32_bf16 v[176:179], a[212:215], a[60:63], v[176:179]\n\t"
"ds_read_b64_tr_b16 a[202:203], v16 offset:21632\n\t"
"v_mfma_f32_16x16x32_bf16 v[180:183], a[212:215], a[64:67], v[180:183]\n\t"
"v_rcp_f32 v91, v91\n\t"
"v_mfma_f32_16x16x32_bf16 v[164:167], a[208:211], a[64:67], v[164:167]\n\t"
"v_and_b32 v90, 4294901760, v90\n\t"
"v_mfma_f32_16x16x32_bf16 v[168:171], a[208:211], a[68:71], v[168:171]\n\t"
"s_xor_b32 s82, s82, s51\n\t"
"ds_read_b64_tr_b16 a[204:205], v16 offset:21152\n\t"
"v_mul_f32 v90, -v90, v91\n\t"
"v_mfma_f32_16x16x32_bf16 v[184:187], a[212:215], a[68:71], v[184:187]\n\t"
"buffer_store_dwordx4 v[140:143], v1, s[8:11], s82 offen offset:0 sc1\n\t"
"v_mfma_f32_16x16x32_bf16 v[188:191], a[212:215], a[72:75], v[188:191]\n\t"
"ds_read_b64_tr_b16 a[206:207], v16 offset:21664\n\t"
"v_mfma_f32_16x16x32_bf16 v[172:175], a[208:211], a[72:75], v[172:175]\n\t"
"s_waitcnt lgkmcnt(6)\n\t"
"v_mfma_f32_16x16x32_bf16 v[160:163], a[192:195], a[76:79], v[160:163]\n\t"
"ds_read_b64_tr_b16 a[208:209], v16 offset:31648\n\t"
"v_mfma_f32_16x16x32_bf16 v[176:179], a[196:199], a[76:79], v[176:179]\n\t"
"ds_read_b64_tr_b16 a[210:211], v16 offset:32160\n\t"
"v_mfma_f32_16x16x32_bf16 v[180:183], a[196:199], a[80:83], v[180:183]\n\t"
"v_mfma_f32_16x16x32_bf16 v[164:167], a[192:195], a[80:83], v[164:167]\n\t"
"v_mfma_f32_16x16x32_bf16 v[168:171], a[192:195], a[84:87], v[168:171]\n\t"
"ds_read_b64_tr_b16 a[212:213], v16 offset:31680\n\t"
"v_mfma_f32_16x16x32_bf16 v[184:187], a[196:199], a[84:87], v[184:187]\n\t"
"ds_read_b64_tr_b16 a[214:215], v16 offset:32192\n\t"
"s_memrealtime s[84:85]\n\t"
"v_mfma_f32_16x16x32_bf16 v[188:191], a[196:199], a[88:91], v[188:191]\n\t"
"v_mfma_f32_16x16x32_bf16 v[172:175], a[192:195], a[88:91], v[172:175]\n\t"
"s_waitcnt lgkmcnt(4)\n\t"
"v_mfma_f32_16x16x32_bf16 v[160:163], a[200:203], a[92:95], v[160:163]\n\t"
"ds_read_b64_tr_b16 a[192:193], v16 offset:64\n\t"
"v_mfma_f32_16x16x32_bf16 v[176:179], a[204:207], a[92:95], v[176:179]\n\t"
"ds_read_b64_tr_b16 a[194:195], v16 offset:576\n\t"
"v_mfma_f32_16x16x32_bf16 v[180:183], a[204:207], a[128:131], v[180:183]\n\t"
"v_mfma_f32_16x16x32_bf16 v[164:167], a[200:203], a[128:131], v[164:167]\n\t"
"v_mfma_f32_16x16x32_bf16 v[168:171], a[200:203], a[132:135], v[168:171]\n\t"
"ds_read_b64_tr_b16 a[196:197], v16 offset:96\n\t"
"v_mfma_f32_16x16x32_bf16 v[184:187], a[204:207], a[132:135], v[184:187]\n\t"
"ds_read_b64_tr_b16 a[198:199], v16 offset:608\n\t"
"v_mfma_f32_16x16x32_bf16 v[188:191], a[204:207], a[136:139], v[188:191]\n\t"
"v_mfma_f32_16x16x32_bf16 v[172:175], a[200:203], a[136:139], v[172:175]\n\t"
"s_waitcnt lgkmcnt(4)\n\t"
"v_mfma_f32_16x16x32_bf16 v[160:163], a[208:211], a[140:143], v[160:163]\n\t"
"ds_read_b64_tr_b16 a[200:201], v16 offset:10592\n\t"
"v_mfma_f32_16x16x32_bf16 v[176:179], a[212:215], a[140:143], v[176:179]\n\t"
"ds_read_b64_tr_b16 a[202:203], v16 offset:11104\n\t"
"v_mfma_f32_16x16x32_bf16 v[180:183], a[212:215], a[144:147], v[180:183]\n\t"
"v_mfma_f32_16x16x32_bf16 v[164:167], a[208:211], a[144:147], v[164:167]\n\t"
"s_waitcnt vmcnt(4)\n\t"
"v_cmp_ne_u32 vcc, s39, v29\n\t"
"s_and_b64 vcc, vcc, 255\n\t"
"s_cbranch_vccz .LbkhA%=\n\t"
".LslhA%=:\n"
"s_sleep 0\n\t"
"v_mov_b32 v28, 0\n\t"
"v_mov_b32 v29, 0\n\t"
"buffer_atomic_add_x2 v[28:29], v30, s[8:11], 0 offen offset:0 sc0 sc1\n\t"
"s_waitcnt vmcnt(0)\n\t"
"v_cmp_ne_u32 vcc, s39, v29\n\t"
"s_and_b64 vcc, vcc, 255\n\t"
"s_cbranch_vccz .LbkhA%=\n\t"
"s_sub_u32 s55, s55, 1\n\t"
"s_cbranch_scc0 .LslhA%=\n\t"
"s_mov_b32 s55, 0\n\t"
".LbkhA%=:\n"
"v_mfma_f32_16x16x32_bf16 v[168:171], a[208:211], a[148:151], v[168:171]\n\t"
"ds_read_b64_tr_b16 a[204:205], v16 offset:10624\n\t"
"v_subrev_u32 v28, s40, v28\n\t"
"v_mfma_f32_16x16x32_bf16 v[184:187], a[212:215], a[148:151], v[184:187]\n\t"
"ds_read_b64_tr_b16 a[206:207], v16 offset:11136\n\t"
"v_mfma_f32_16x16x32_bf16 v[188:191], a[212:215], a[152:155], v[188:191]\n\t"
"v_max_i32_dpp v28, v28, v28 quad_perm:[1,0,3,2] row_mask:0xf bank_mask:0xf\n\t"
"v_mfma_f32_16x16x32_bf16 v[172:175], a[208:211], a[152:155], v[172:175]\n\t"
"s_waitcnt lgkmcnt(4)\n\t"
"v_mfma_f32_16x16x32_bf16 v[160:163], a[192:195], a[156:159], v[160:163]\n\t"
"ds_read_b64_tr_b16 a[208:209], v18 offset:42176\n\t"
"v_max_i32_dpp v28, v28, v28 quad_perm:[2,3,0,1] row_mask:0xf bank_mask:0xf\n\t"
"v_mfma_f32_16x16x32_bf16 v[176:179], a[196:199], a[156:159], v[176:179]\n\t"
"ds_read_b64_tr_b16 a[210:211], v18 offset:42688\n\t"
"v_mfma_f32_16x16x32_bf16 v[180:183], a[196:199], a[160:163], v[180:183]\n\t"
"v_max_i32_dpp v28, v28, v28 row_half_mirror row_mask:0xf bank_mask:0xf\n\t"
"v_mfma_f32_16x16x32_bf16 v[164:167], a[192:195], a[160:163], v[164:167]\n\t"
"v_readfirstlane_b32 s81, v28\n\t"
"v_readfirstlane_b32 s79, v28\n\t"
"v_mfma_f32_16x16x32_bf16 v[168:171], a[192:195], a[164:167], v[168:171]\n\t"
"ds_read_b64_tr_b16 a[212:213], v18 offset:42208\n\t"
"v_mfma_f32_16x16x32_bf16 v[184:187], a[196:199], a[164:167], v[184:187]\n\t"
"s_cmp_gt_i32 s81, 15\n\t"
"ds_read_b64_tr_b16 a[214:215], v18 offset:42720\n\t"
"s_cbranch_scc0 .LpbA%=\n\t"
"s_min_i32 s81, s81, 400\n\t"
"s_waitcnt lgkmcnt(0)\n\t"
"s_add_u32 s81, s81, s84\n\t"
".LplA%=:\n"
"s_memrealtime s[86:87]\n\t"
"s_waitcnt lgkmcnt(0)\n\t"
"s_sub_u32 s87, s81, s86\n\t"
"s_cmp_gt_i32 s87, -9\n\t"
"s_cbranch_scc1 .LplA%=\n\t"
".LpbA%=:\n"
"v_mfma_f32_16x16x32_bf16 v[188:191], a[196:199], a[232:235], v[188:191]\n\t"
"v_mfma_f32_16x16x32_bf16 v[172:175], a[192:195], a[232:235], v[172:175]\n\t"
"s_min_i32 s79, s79, 200\n\t"
"s_waitcnt lgkmcnt(4)\n\t"
"v_mfma_f32_16x16x32_bf16 v[160:163], a[200:203], a[168:171], v[160:163]\n\t"
"ds_read_b64_tr_b16 a[192:193], v18 offset:52704\n\t"
"v_mfma_f32_16x16x32_bf16 v[176:179], a[204:207], a[168:171], v[176:179]\n\t"
"ds_read_b64_tr_b16 a[194:195], v18 offset:53216\n\t"
"v_mfma_f32_16x16x32_bf16 v[180:183], a[204:207], a[172:175], v[180:183]\n\t"
"v_mfma_f32_16x16x32_bf16 v[164:167], a[200:203], a[172:175], v[164:167]\n\t"
"v_mfma_f32_16x16x32_bf16 v[168:171], a[200:203], a[176:179], v[168:171]\n\t"
"ds_read_b64_tr_b16 a[196:197], v18 offset:52736\n\t"
"v_mfma_f32_16x16x32_bf16 v[184:187], a[204:207], a[176:179], v[184:187]\n\t"
"ds_read_b64_tr_b16 a[198:199], v18 offset:53248\n\t"
"v_mfma_f32_16x16x32_bf16 v[188:191], a[204:207], a[180:183], v[188:191]\n\t"
"v_mfma_f32_16x16x32_bf16 v[172:175], a[200:203], a[180:183], v[172:175]\n\t"
"s_xor_b32 s83, s74, 4194304\n\t"
"s_waitcnt lgkmcnt(4)\n\t"
"ds_read_b64_tr_b16 a[200:201], v18 offset:21120\n\t"
"v_mfma_f32_16x16x32_bf16 v[160:163], a[208:211], a[184:187], v[160:163]\n\t"
"buffer_load_dwordx4 v[128:131], v1, s[8:11], s83 offen offset:0 sc1\n\t"
"v_mfma_f32_16x16x32_bf16 v[176:179], a[212:215], a[184:187], v[176:179]\n\t"
"ds_read_b64_tr_b16 a[202:203], v18 offset:21632\n\t"
"v_mfma_f32_16x16x32_bf16 v[180:183], a[212:215], a[188:191], v[180:183]\n\t"
"v_mfma_f32_16x16x32_bf16 v[164:167], a[208:211], a[188:191], v[164:167]\n\t"
"v_mfma_f32_16x16x32_bf16 v[168:171], a[208:211], v[52:55], v[168:171]\n\t"
"ds_read_b64_tr_b16 a[204:205], v18 offset:21152\n\t"
".Lpg15%=:\n"
"s_cbranch_execz .Lhr0%=\n\t"
"v_mfma_f32_16x16x32_bf16 v[184:187], a[212:215], v[52:55], v[184:187]\n\t"
"ds_read_b64_tr_b16 a[206:207], v18 offset:21664\n\t"
"v_mfma_f32_16x16x32_bf16 v[188:191], a[212:215], v[56:59], v[188:191]\n\t"
"v_mfma_f32_16x16x32_bf16 v[172:175], a[208:211], v[56:59], v[172:175]\n\t"
"s_waitcnt lgkmcnt(4)\n\t"
"v_mfma_f32_16x16x32_bf16 v[160:163], a[192:195], v[60:63], v[160:163]\n\t"
"ds_read_b64_tr_b16 a[208:209], v18 offset:31648\n\t"
"s_add_u32 s83, s83, s53\n\t"
"v_mfma_f32_16x16x32_bf16 v[176:179], a[196:199], v[60:63], v[176:179]\n\t"
"buffer_load_dwordx4 v[132:135], v1, s[8:11], s83 offen offset:0 sc1\n\t"
"v_mfma_f32_16x16x32_bf16 v[180:183], a[196:199], v[32:35], v[180:183]\n\t"
"ds_read_b64_tr_b16 a[210:211], v18 offset:32160\n\t"
"v_mfma_f32_16x16x32_bf16 v[164:167], a[192:195], v[32:35], v[164:167]\n\t"
"v_mfma_f32_16x16x32_bf16 v[168:171], a[192:195], v[36:39], v[168:171]\n\t"
"ds_read_b64_tr_b16 a[212:213], v18 offset:31680\n\t"
"v_mfma_f32_16x16x32_bf16 v[184:187], a[196:199], v[36:39], v[184:187]\n\t"
"ds_read_b64_tr_b16 a[214:215], v18 offset:32192\n\t"
"v_mfma_f32_16x16x32_bf16 v[188:191], a[196:199], v[40:43], v[188:191]\n\t"
"v_mfma_f32_16x16x32_bf16 v[172:175], a[192:195], v[40:43], v[172:175]\n\t"
"s_waitcnt lgkmcnt(4)\n\t"
"v_mfma_f32_16x16x32_bf16 v[160:163], a[200:203], a[216:219], v[160:163]\n\t"
"ds_read_b64_tr_b16 a[192:193], v19 offset:8448\n\t"
"v_mfma_f32_16x16x32_bf16 v[176:179], a[204:207], a[216:219], v[176:179]\n\t"
"ds_read_b64_tr_b16 a[194:195], v19 offset:8960\n\t"
"s_add_u32 s83, s83, s53\n\t"
"v_mfma_f32_16x16x32_bf16 v[180:183], a[204:207], a[220:223], v[180:183]\n\t"
"buffer_load_dwordx4 v[136:139], v1, s[8:11], s83 offen offset:0 sc1\n\t"
"v_mfma_f32_16x16x32_bf16 v[164:167], a[200:203], a[220:223], v[164:167]\n\t"
"v_mfma_f32_16x16x32_bf16 v[168:171], a[200:203], a[224:227], v[168:171]\n\t"
"ds_read_b64_tr_b16 a[196:197], v19 offset:18976\n\t"
"v_mfma_f32_16x16x32_bf16 v[184:187], a[204:207], a[224:227], v[184:187]\n\t"
"ds_read_b64_tr_b16 a[198:199], v19 offset:19488\n\t"
"v_mfma_f32_16x16x32_bf16 v[188:191], a[204:207], a[228:231], v[188:191]\n\t"
"v_mfma_f32_16x16x32_bf16 v[172:175], a[200:203], a[228:231], v[172:175]\n\t"
"s_waitcnt lgkmcnt(4)\n\t"
"v_mfma_f32_16x16x32_bf16 v[160:163], a[208:211], a[244:247], v[160:163]\n\t"
"ds_read_b64_tr_b16 a[200:201], v14 offset:24896\n\t"
"v_mfma_f32_16x16x32_bf16 v[176:179], a[212:215], a[244:247], v[176:179]\n\t"
"ds_read_b64_tr_b16 a[202:203], v14 offset:25408\n\t"
"v_mfma_f32_16x16x32_bf16 v[180:183], a[212:215], a[248:251], v[180:183]\n\t"
"s_add_u32 s83, s83, s53\n\t"
"v_mfma_f32_16x16x32_bf16 v[164:167], a[208:211], a[248:251], v[164:167]\n\t"
"buffer_load_dwordx4 v[140:143], v1, s[8:11], s83 offen offset:0 sc1\n\t"
"v_mfma_f32_16x16x32_bf16 v[168:171], a[208:211], a[252:255], v[168:171]\n\t"
"ds_read_b64_tr_b16 a[204:205], v14 offset:35424\n\t"
"v_mfma_f32_16x16x32_bf16 v[184:187], a[212:215], a[252:255], v[184:187]\n\t"
"ds_read_b64_tr_b16 a[206:207], v14 offset:35936\n\t"
"v_mfma_f32_16x16x32_bf16 v[188:191], a[212:215], a[96:99], v[188:191]\n\t"
"v_mfma_f32_16x16x32_bf16 v[172:175], a[208:211], a[96:99], v[172:175]\n\t"
"s_waitcnt lgkmcnt(4)\n\t"
"v_mfma_f32_16x16x32_bf16 v[192:195], a[192:195], a[240:243], v[192:195]\n\t"
"ds_read_b64_tr_b16 a[208:209], v17 offset:50560\n\t"
"v_mfma_f32_16x16x32_bf16 v[196:199], a[192:195], a[0:3], v[196:199]\n\t"
"ds_read_b64_tr_b16 a[210:211], v17 offset:51072\n\t"
"v_cvt_pk_bf16_f32 v160, v160, v161\n\t"
"v_mfma_f32_16x16x32_bf16 v[200:203], a[192:195], a[4:7], v[200:203]\n\t"
"v_cvt_pk_bf16_f32 v161, v162, v163\n\t"
"v_add3_u32 v24, v160, v161, v24\n\t"
"v_mfma_f32_16x16x32_bf16 v[204:207], a[192:195], a[8:11], v[204:207]\n\t"
"v_cvt_pk_bf16_f32 v162, v176, v177\n\t"
"v_cvt_pk_bf16_f32 v163, v178, v179\n\t"
"v_mfma_f32_16x16x32_bf16 v[192:195], a[196:199], a[12:15], v[192:195]\n\t"
"s_xor_b32 s82, s74, 13107200\n\t"
"ds_read_b64_tr_b16 a[212:213], v17 offset:61088\n\t"
"v_add3_u32 v24, v162, v163, v24\n\t"
"v_mfma_f32_16x16x32_bf16 v[196:199], a[196:199], a[16:19], v[196:199]\n\t"
"ds_read_b64_tr_b16 a[214:215], v17 offset:61600\n\t"
"buffer_store_dwordx4 v[160:163], v1, s[8:11], s82 offen offset:0 sc1\n\t"
"v_mfma_f32_16x16x32_bf16 v[200:203], a[196:199], a[20:23], v[200:203]\n\t"
"v_cvt_pk_bf16_f32 v164, v164, v165\n\t"
"v_cvt_pk_bf16_f32 v165, v166, v167\n\t"
"v_mfma_f32_16x16x32_bf16 v[204:207], a[196:199], a[24:27], v[204:207]\n\t"
"s_waitcnt lgkmcnt(4)\n\t"
"v_add3_u32 v25, v164, v165, v25\n\t"
"v_cvt_pk_bf16_f32 v166, v180, v181\n\t"
"v_mfma_f32_16x16x32_bf16 v[192:195], a[200:203], a[28:31], v[192:195]\n\t"
"ds_read_b64_tr_b16 a[192:193], v17 offset:29504\n\t"
"v_cvt_pk_bf16_f32 v167, v182, v183\n\t"
"v_mfma_f32_16x16x32_bf16 v[196:199], a[200:203], a[32:35], v[196:199]\n\t"
"ds_read_b64_tr_b16 a[194:195], v17 offset:30016\n\t"
"v_add3_u32 v25, v166, v167, v25\n\t"
"v_mfma_f32_16x16x32_bf16 v[200:203], a[200:203], a[36:39], v[200:203]\n\t"
"v_cvt_pk_bf16_f32 v168, v168, v169\n\t"
"v_cvt_pk_bf16_f32 v169, v170, v171\n\t"
"v_mfma_f32_16x16x32_bf16 v[204:207], a[200:203], a[40:43], v[204:207]\n\t"
"v_add3_u32 v26, v168, v169, v26\n\t"
"v_cvt_pk_bf16_f32 v170, v184, v185\n\t"
"v_mfma_f32_16x16x32_bf16 v[192:195], a[204:207], a[44:47], v[192:195]\n\t"
"ds_read_b64_tr_b16 a[196:197], v17 offset:40032\n\t"
"v_cvt_pk_bf16_f32 v171, v186, v187\n\t"
"v_mfma_f32_16x16x32_bf16 v[196:199], a[204:207], a[48:51], v[196:199]\n\t"
"ds_read_b64_tr_b16 a[198:199], v17 offset:40544\n\t"
"v_add3_u32 v26, v170, v171, v26\n\t"
"v_mfma_f32_16x16x32_bf16 v[200:203], a[204:207], a[52:55], v[200:203]\n\t"
"v_cvt_pk_bf16_f32 v172, v172, v173\n\t"
"v_cvt_pk_bf16_f32 v173, v174, v175\n\t"
"v_mfma_f32_16x16x32_bf16 v[204:207], a[204:207], a[56:59], v[204:207]\n\t"
"s_waitcnt lgkmcnt(4)\n\t"
"v_add3_u32 v27, v172, v173, v27\n\t"
"v_cvt_pk_bf16_f32 v174, v188, v189\n\t"
"v_mfma_f32_16x16x32_bf16 v[192:195], a[208:211], a[60:63], v[192:195]\n\t"
"s_xor_b32 s82, s82, s51\n\t"
"ds_read_b64_tr_b16 a[200:201], v17 offset:8448\n\t"
"v_cvt_pk_bf16_f32 v175, v190, v191\n\t"
"v_mfma_f32_16x16x32_bf16 v[196:199], a[208:211], a[64:67], v[196:199]\n\t"
"buffer_store_dwordx4 v[164:167], v1, s[8:11], s82 offen offset:0 sc1\n\t"
"v_mfma_f32_16x16x32_bf16 v[200:203], a[208:211], a[68:71], v[200:203]\n\t"
"ds_read_b64_tr_b16 a[202:203], v17 offset:8960\n\t"
"v_add3_u32 v27, v174, v175, v27\n\t"
"v_mfma_f32_16x16x32_bf16 v[204:207], a[208:211], a[72:75], v[204:207]\n\t"
"v_mfma_f32_16x16x32_bf16 v[192:195], a[212:215], a[76:79], v[192:195]\n\t"
"ds_read_b64_tr_b16 a[204:205], v17 offset:18976\n\t"
"v_mfma_f32_16x16x32_bf16 v[196:199], a[212:215], a[80:83], v[196:199]\n\t"
"ds_read_b64_tr_b16 a[206:207], v17 offset:19488\n\t"
"v_mfma_f32_16x16x32_bf16 v[200:203], a[212:215], a[84:87], v[200:203]\n\t"
"v_mfma_f32_16x16x32_bf16 v[204:207], a[212:215], a[88:91], v[204:207]\n\t"
"s_waitcnt lgkmcnt(4)\n\t"
"v_mfma_f32_16x16x32_bf16 v[192:195], a[192:195], a[92:95], v[192:195]\n\t"
"ds_read_b64_tr_b16 a[208:209], v19 offset:50560\n\t"
"v_mfma_f32_16x16x32_bf16 v[196:199], a[192:195], a[128:131], v[196:199]\n\t"
"ds_read_b64_tr_b16 a[210:211], v19 offset:51072\n\t"
"v_mfma_f32_16x16x32_bf16 v[200:203], a[192:195], a[132:135], v[200:203]\n\t"
"v_mfma_f32_16x16x32_bf16 v[204:207], a[192:195], a[136:139], v[204:207]\n\t"
"v_mfma_f32_16x16x32_bf16 v[192:195], a[196:199], a[140:143], v[192:195]\n\t"
"ds_read_b64_tr_b16 a[212:213], v19 offset:61088\n\t"
"s_xor_b32 s82, s82, s52\n\t"
"v_mfma_f32_16x16x32_bf16 v[196:199], a[196:199], a[144:147], v[196:199]\n\t"
"buffer_store_dwordx4 v[168:171], v1, s[8:11], s82 offen offset:0 sc1\n\t"
"v_mfma_f32_16x16x32_bf16 v[200:203], a[196:199], a[148:151], v[200:203]\n\t"
"ds_read_b64_tr_b16 a[214:215], v19 offset:61600\n\t"
"v_mfma_f32_16x16x32_bf16 v[204:207], a[196:199], a[152:155], v[204:207]\n\t"
"s_waitcnt lgkmcnt(4)\n\t"
"v_mfma_f32_16x16x32_bf16 v[192:195], a[200:203], a[156:159], v[192:195]\n\t"
"ds_read_b64_tr_b16 a[192:193], v19 offset:29504\n\t"
"v_mfma_f32_16x16x32_bf16 v[196:199], a[200:203], a[160:163], v[196:199]\n\t"
"ds_read_b64_tr_b16 a[194:195], v19 offset:30016\n\t"
"v_mfma_f32_16x16x32_bf16 v[200:203], a[200:203], a[164:167], v[200:203]\n\t"
"v_mfma_f32_16x16x32_bf16 v[204:207], a[200:203], a[232:235], v[204:207]\n\t"
"v_mfma_f32_16x16x32_bf16 v[192:195], a[204:207], a[168:171], v[192:195]\n\t"
"ds_read_b64_tr_b16 a[196:197], v19 offset:40032\n\t"
"v_mfma_f32_16x16x32_bf16 v[196:199], a[204:207], a[172:175], v[196:199]\n\t"
"ds_read_b64_tr_b16 a[198:199], v19 offset:40544\n\t"
"v_mfma_f32_16x16x32_bf16 v[200:203], a[204:207], a[176:179], v[200:203]\n\t"
"v_mfma_f32_16x16x32_bf16 v[204:207], a[204:207], a[180:183], v[204:207]\n\t"
"s_waitcnt lgkmcnt(4)\n\t"
"v_mfma_f32_16x16x32_bf16 v[192:195], a[208:211], a[184:187], v[192:195]\n\t"
"ds_read_b64_tr_b16 a[200:201], v18 offset:128\n\t"
"s_xor_b32 s82, s82, s51\n\t"
"v_mfma_f32_16x16x32_bf16 v[196:199], a[208:211], a[188:191], v[196:199]\n\t"
"buffer_store_dwordx4 v[172:175], v1, s[8:11], s82 offen offset:0 sc1\n\t"
"v_mfma_f32_16x16x32_bf16 v[200:203], a[208:211], v[52:55], v[200:203]\n\t"
"ds_read_b64_tr_b16 a[202:203], v18 offset:640\n\t"
"v_mfma_f32_16x16x32_bf16 v[204:207], a[208:211], v[56:59], v[204:207]\n\t"
"v_mfma_f32_16x16x32_bf16 v[192:195], a[212:215], v[60:63], v[192:195]\n\t"
"ds_read_b64_tr_b16 a[204:205], v18 offset:160\n\t"
"v_mfma_f32_16x16x32_bf16 v[196:199], a[212:215], v[32:35], v[196:199]\n\t"
"ds_read_b64_tr_b16 a[206:207], v18 offset:672\n\t"
"v_mfma_f32_16x16x32_bf16 v[200:203], a[212:215], v[36:39], v[200:203]\n\t"
"v_mfma_f32_16x16x32_bf16 v[204:207], a[212:215], v[40:43], v[204:207]\n\t"
"s_waitcnt lgkmcnt(4)\n\t"
"v_mfma_f32_16x16x32_bf16 v[192:195], a[192:195], a[216:219], v[192:195]\n\t"
"ds_read_b64_tr_b16 a[208:209], v18 offset:10656\n\t"
"v_mfma_f32_16x16x32_bf16 v[196:199], a[192:195], a[220:223], v[196:199]\n\t"
"ds_read_b64_tr_b16 a[210:211], v18 offset:11168\n\t"
"v_mfma_f32_16x16x32_bf16 v[200:203], a[192:195], a[224:227], v[200:203]\n\t"
"v_mfma_f32_16x16x32_bf16 v[204:207], a[192:195], a[228:231], v[204:207]\n\t"
"v_mfma_f32_16x16x32_bf16 v[192:195], a[196:199], a[244:247], v[192:195]\n\t"
"ds_read_b64_tr_b16 a[212:213], v18 offset:10688\n\t"
"v_mfma_f32_16x16x32_bf16 v[196:199], a[196:199], a[248:251], v[196:199]\n\t"
"ds_read_b64_tr_b16 a[214:215], v18 offset:11200\n\t"
"v_mfma_f32_16x16x32_bf16 v[200:203], a[196:199], a[252:255], v[200:203]\n\t"
"v_mfma_f32_16x16x32_bf16 v[204:207], a[196:199], a[96:99], v[204:207]\n\t"
"s_waitcnt lgkmcnt(4)\n\t"
"v_mfma_f32_16x16x32_bf16 v[212:215], a[200:203], a[0:3], v[212:215]\n\t"
"ds_read_b64_tr_b16 a[192:193], v13 offset:16576\n\t"
"v_mfma_f32_16x16x32_bf16 v[228:231], a[204:207], a[0:3], v[228:231]\n\t"
"ds_read_b64_tr_b16 a[194:195], v13 offset:17088\n\t"
"v_mfma_f32_16x16x32_bf16 v[232:235], a[204:207], a[4:7], v[232:235]\n\t"
"v_mfma_f32_16x16x32_bf16 v[216:219], a[200:203], a[4:7], v[216:219]\n\t"
"ds_read_b64_tr_b16 a[196:197], v13 offset:16608\n\t"
"v_mfma_f32_16x16x32_bf16 v[220:223], a[200:203], a[8:11], v[220:223]\n\t"
"s_waitcnt lgkmcnt(3)\n\t"
"ds_read_b64_tr_b16 a[198:199], v13 offset:17120\n\t"
"v_cvt_pk_bf16_f32 v192, v192, v193\n\t"
"v_mfma_f32_16x16x32_bf16 v[236:239], a[204:207], a[8:11], v[236:239]\n\t"
"s_xor_b32 s82, s74, 21495808\n\t"
"v_cvt_pk_bf16_f32 v193, v194, v195\n\t"
"v_add3_u32 v194, v192, v193, v24\n\t"
"v_mfma_f32_16x16x32_bf16 v[212:215], a[208:211], a[16:19], v[212:215]\n\t"
"ds_read_b64_tr_b16 a[200:201], v13 offset:27104\n\t"
"buffer_store_dwordx3 v[192:194], v20, s[8:11], s82 offen offset:0 sc1\n\t"
"v_mfma_f32_16x16x32_bf16 v[228:231], a[212:215], a[16:19], v[228:231]\n\t"
"ds_read_b64_tr_b16 a[202:203], v13 offset:27616\n\t"
"v_cvt_pk_bf16_f32 v196, v196, v197\n\t"
"v_mfma_f32_16x16x32_bf16 v[232:235], a[212:215], a[20:23], v[232:235]\n\t"
"v_cvt_pk_bf16_f32 v197, v198, v199\n\t"
"v_add3_u32 v198, v196, v197, v25\n\t"
"v_mfma_f32_16x16x32_bf16 v[216:219], a[208:211], a[20:23], v[216:219]\n\t"
"ds_read_b64_tr_b16 a[204:205], v13 offset:27136\n\t"
"v_cvt_pk_bf16_f32 v200, v200, v201\n\t"
"v_mfma_f32_16x16x32_bf16 v[220:223], a[208:211], a[24:27], v[220:223]\n\t"
"ds_read_b64_tr_b16 a[206:207], v13 offset:27648\n\t"
"v_cvt_pk_bf16_f32 v201, v202, v203\n\t"
"v_mfma_f32_16x16x32_bf16 v[236:239], a[212:215], a[24:27], v[236:239]\n\t"
"s_waitcnt lgkmcnt(4)\n\t"
"v_add3_u32 v202, v200, v201, v26\n\t"
"v_cvt_pk_bf16_f32 v204, v204, v205\n\t"
"v_mfma_f32_16x16x32_bf16 v[212:215], a[192:195], a[32:35], v[212:215]\n\t"
"ds_read_b64_tr_b16 a[208:209], v16 offset:42240\n\t"
"v_cvt_pk_bf16_f32 v205, v206, v207\n\t"
"v_mfma_f32_16x16x32_bf16 v[228:231], a[196:199], a[32:35], v[228:231]\n\t"
"ds_read_b64_tr_b16 a[210:211], v16 offset:42752\n\t"
"v_add3_u32 v206, v204, v205, v27\n\t"
"v_mfma_f32_16x16x32_bf16 v[232:235], a[196:199], a[36:39], v[232:235]\n\t"
"v_mfma_f32_16x16x32_bf16 v[216:219], a[192:195], a[36:39], v[216:219]\n\t"
"ds_read_b64_tr_b16 a[212:213], v16 offset:42272\n\t"
"v_mfma_f32_16x16x32_bf16 v[220:223], a[192:195], a[40:43], v[220:223]\n\t"
"ds_read_b64_tr_b16 a[214:215], v16 offset:42784\n\t"
"s_xor_b32 s82, s82, s51\n\t"
"v_mfma_f32_16x16x32_bf16 v[236:239], a[196:199], a[40:43], v[236:239]\n\t"
"s_waitcnt lgkmcnt(4)\n\t"
"buffer_store_dwordx3 v[196:198], v20, s[8:11], s82 offen offset:0 sc1\n\t"
"v_mfma_f32_16x16x32_bf16 v[212:215], a[200:203], a[48:51], v[212:215]\n\t"
"ds_read_b64_tr_b16 a[192:193], v16 offset:52768\n\t"
"v_mfma_f32_16x16x32_bf16 v[228:231], a[204:207], a[48:51], v[228:231]\n\t"
"ds_read_b64_tr_b16 a[194:195], v16 offset:53280\n\t"
"v_mfma_f32_16x16x32_bf16 v[232:235], a[204:207], a[52:55], v[232:235]\n\t"
"v_mfma_f32_16x16x32_bf16 v[216:219], a[200:203], a[52:55], v[216:219]\n\t"
"ds_read_b64_tr_b16 a[196:197], v16 offset:52800\n\t"
"v_mfma_f32_16x16x32_bf16 v[220:223], a[200:203], a[56:59], v[220:223]\n\t"
"ds_read_b64_tr_b16 a[198:199], v16 offset:53312\n\t"
"v_mfma_f32_16x16x32_bf16 v[236:239], a[204:207], a[56:59], v[236:239]\n\t"
"s_waitcnt lgkmcnt(4)\n\t"
"v_mfma_f32_16x16x32_bf16 v[212:215], a[208:211], a[64:67], v[212:215]\n\t"
"ds_read_b64_tr_b16 a[200:201], v16 offset:21184\n\t"
"v_mfma_f32_16x16x32_bf16 v[228:231], a[212:215], a[64:67], v[228:231]\n\t"
"ds_read_b64_tr_b16 a[202:203], v16 offset:21696\n\t"
"v_mfma_f32_16x16x32_bf16 v[232:235], a[212:215], a[68:71], v[232:235]\n\t"
"v_mfma_f32_16x16x32_bf16 v[216:219], a[208:211], a[68:71], v[216:219]\n\t"
"ds_read_b64_tr_b16 a[204:205], v16 offset:21216\n\t"
"s_xor_b32 s82, s82, s52\n\t"
"v_mfma_f32_16x16x32_bf16 v[220:223], a[208:211], a[72:75], v[220:223]\n\t"
"buffer_store_dwordx3 v[200:202], v20, s[8:11], s82 offen offset:0 sc1\n\t"
"v_mfma_f32_16x16x32_bf16 v[236:239], a[212:215], a[72:75], v[236:239]\n\t"
"ds_read_b64_tr_b16 a[206:207], v16 offset:21728\n\t"
"s_waitcnt lgkmcnt(4)\n\t"
"v_mfma_f32_16x16x32_bf16 v[212:215], a[192:195], a[80:83], v[212:215]\n\t"
"ds_read_b64_tr_b16 a[208:209], v16 offset:31712\n\t"
"v_mfma_f32_16x16x32_bf16 v[228:231], a[196:199], a[80:83], v[228:231]\n\t"
"ds_read_b64_tr_b16 a[210:211], v16 offset:32224\n\t"
"v_mfma_f32_16x16x32_bf16 v[232:235], a[196:199], a[84:87], v[232:235]\n\t"
"v_mfma_f32_16x16x32_bf16 v[216:219], a[192:195], a[84:87], v[216:219]\n\t"
"ds_read_b64_tr_b16 a[212:213], v16 offset:31744\n\t"
"v_mfma_f32_16x16x32_bf16 v[220:223], a[192:195], a[88:91], v[220:223]\n\t"
"s_mul_i32 s64, s79, 2\n\t"
"ds_read_b64_tr_b16 a[214:215], v16 offset:32256\n\t"
"s_ashr_i32 s64, s64, 3\n\t"
"v_mfma_f32_16x16x32_bf16 v[236:239], a[196:199], a[88:91], v[236:239]\n\t"
"s_waitcnt lgkmcnt(4)\n\t"
"v_mfma_f32_16x16x32_bf16 v[212:215], a[200:203], a[128:131], v[212:215]\n\t"
"ds_read_b64_tr_b16 a[192:193], v16 offset:128\n\t"
"v_mfma_f32_16x16x32_bf16 v[228:231], a[204:207], a[128:131], v[228:231]\n\t"
"ds_read_b64_tr_b16 a[194:195], v16 offset:640\n\t"
"v_mfma_f32_16x16x32_bf16 v[232:235], a[204:207], a[132:135], v[232:235]\n\t"
"s_xor_b32 s82, s82, s51\n\t"
"ds_read_b64_tr_b16 a[196:197], v16 offset:160\n\t"
"v_mfma_f32_16x16x32_bf16 v[216:219], a[200:203], a[132:135], v[216:219]\n\t"
"buffer_store_dwordx3 v[204:206], v20, s[8:11], s82 offen offset:0 sc1\n\t"
"v_mfma_f32_16x16x32_bf16 v[220:223], a[200:203], a[136:139], v[220:223]\n\t"
"ds_read_b64_tr_b16 a[198:199], v16 offset:672\n\t"
"v_mfma_f32_16x16x32_bf16 v[236:239], a[204:207], a[136:139], v[236:239]\n\t"
"s_waitcnt lgkmcnt(4)\n\t"
"v_mfma_f32_16x16x32_bf16 v[212:215], a[208:211], a[144:147], v[212:215]\n\t"
"s_cmp_gt_i32 s64, 0\n\t"
"ds_read_b64_tr_b16 a[200:201], v16 offset:10656\n\t"
"s_cbranch_scc0 .Lmq0%=\n\t"
".Lmp0%=:\n"
"s_sleep 1\n\t"
"s_sub_i32 s64, s64, 4\n\t"
"s_cmp_gt_i32 s64, 0\n\t"
"s_cbranch_scc1 .Lmp0%=\n\t"
".Lmq0%=:\n"
"v_mfma_f32_16x16x32_bf16 v[228:231], a[212:215], a[144:147], v[228:231]\n\t"
"ds_read_b64_tr_b16 a[202:203], v16 offset:11168\n\t"
"v_mfma_f32_16x16x32_bf16 v[232:235], a[212:215], a[148:151], v[232:235]\n\t"
"s_xor_b32 s83, s74, 12582912\n\t"
"ds_read_b64_tr_b16 a[204:205], v16 offset:10688\n\t"
"v_mfma_f32_16x16x32_bf16 v[216:219], a[208:211], a[148:151], v[216:219]\n\t"
"buffer_load_dwordx4 v[160:163], v1, s[8:11], s83 offen offset:0 sc1\n\t"
"v_mfma_f32_16x16x32_bf16 v[220:223], a[208:211], a[152:155], v[220:223]\n\t"
"ds_read_b64_tr_b16 a[206:207], v16 offset:11200\n\t"
"v_mfma_f32_16x16x32_bf16 v[236:239], a[212:215], a[152:155], v[236:239]\n\t"
"s_waitcnt lgkmcnt(4)\n\t"
"v_mfma_f32_16x16x32_bf16 v[212:215], a[192:195], a[160:163], v[212:215]\n\t"
"ds_read_b64_tr_b16 a[100:101], v18 offset:42240\n\t"
"v_mfma_f32_16x16x32_bf16 v[228:231], a[196:199], a[160:163], v[228:231]\n\t"
"ds_read_b64_tr_b16 a[102:103], v18 offset:42752\n\t"
"v_mfma_f32_16x16x32_bf16 v[232:235], a[196:199], a[164:167], v[232:235]\n\t"
"v_mfma_f32_16x16x32_bf16 v[216:219], a[192:195], a[164:167], v[216:219]\n\t"
"ds_read_b64_tr_b16 a[104:105], v18 offset:42272\n\t"
"v_mfma_f32_16x16x32_bf16 v[220:223], a[192:195], a[232:235], v[220:223]\n\t"
"ds_read_b64_tr_b16 a[106:107], v18 offset:42784\n\t"
"v_mfma_f32_16x16x32_bf16 v[236:239], a[196:199], a[232:235], v[236:239]\n\t"
"s_waitcnt lgkmcnt(4)\n\t"
"v_mfma_f32_16x16x32_bf16 v[212:215], a[200:203], a[172:175], v[212:215]\n\t"
"ds_read_b64_tr_b16 a[108:109], v18 offset:52768\n\t"
"v_mfma_f32_16x16x32_bf16 v[228:231], a[204:207], a[172:175], v[228:231]\n\t"
"ds_read_b64_tr_b16 a[110:111], v18 offset:53280\n\t"
"v_mfma_f32_16x16x32_bf16 v[232:235], a[204:207], a[176:179], v[232:235]\n\t"
"s_add_u32 s83, s83, s53\n\t"
"ds_read_b64_tr_b16 a[112:113], v18 offset:52800\n\t"
"v_mfma_f32_16x16x32_bf16 v[216:219], a[200:203], a[176:179], v[216:219]\n\t"
"buffer_load_dwordx4 v[164:167], v1, s[8:11], s83 offen offset:0 sc1\n\t"
"v_mfma_f32_16x16x32_bf16 v[220:223], a[200:203], a[180:183], v[220:223]\n\t"
"ds_read_b64_tr_b16 a[114:115], v18 offset:53312\n\t"
"v_mfma_f32_16x16x32_bf16 v[236:239], a[204:207], a[180:183], v[236:239]\n\t"
"s_waitcnt lgkmcnt(4)\n\t"
"v_mfma_f32_16x16x32_bf16 v[212:215], a[100:103], a[188:191], v[212:215]\n\t"
"ds_read_b64_tr_b16 a[116:117], v18 offset:21184\n\t"
"v_mfma_f32_16x16x32_bf16 v[228:231], a[104:107], a[188:191], v[228:231]\n\t"
"ds_read_b64_tr_b16 a[118:119], v18 offset:21696\n\t"
"v_mfma_f32_16x16x32_bf16 v[232:235], a[104:107], v[52:55], v[232:235]\n\t"
"v_mfma_f32_16x16x32_bf16 v[216:219], a[100:103], v[52:55], v[216:219]\n\t"
"ds_read_b64_tr_b16 a[120:121], v18 offset:21216\n\t"
"v_mfma_f32_16x16x32_bf16 v[220:223], a[100:103], v[56:59], v[220:223]\n\t"
"ds_read_b64_tr_b16 a[122:123], v18 offset:21728\n\t"
"v_mfma_f32_16x16x32_bf16 v[236:239], a[104:107], v[56:59], v[236:239]\n\t"
"s_waitcnt lgkmcnt(4)\n\t"
"v_mfma_f32_16x16x32_bf16 v[212:215], a[108:111], v[32:35], v[212:215]\n\t"
"ds_read_b64_tr_b16 a[124:125], v18 offset:31712\n\t"
"v_mfma_f32_16x16x32_bf16 v[228:231], a[112:115], v[32:35], v[228:231]\n\t"
"ds_read_b64_tr_b16 a[126:127], v18 offset:32224\n\t"
"v_mfma_f32_16x16x32_bf16 v[232:235], a[112:115], v[36:39], v[232:235]\n\t"
"s_add_u32 s83, s83, s53\n\t"
"ds_read_b64_tr_b16 a[236:237], v18 offset:31744\n\t"
"v_mfma_f32_16x16x32_bf16 v[216:219], a[108:111], v[36:39], v[216:219]\n\t"
"buffer_load_dwordx4 v[168:171], v1, s[8:11], s83 offen offset:0 sc1\n\t"
"v_mfma_f32_16x16x32_bf16 v[220:223], a[108:111], v[40:43], v[220:223]\n\t"
"ds_read_b64_tr_b16 a[238:239], v18 offset:32256\n\t"
"v_mfma_f32_16x16x32_bf16 v[236:239], a[112:115], v[40:43], v[236:239]\n\t"
"s_waitcnt lgkmcnt(4)\n\t"
"v_mfma_f32_16x16x32_bf16 v[212:215], a[116:119], a[220:223], v[212:215]\n\t"
"ds_read_b64_tr_b16 a[192:193], v18 offset:192\n\t"
"v_mfma_f32_16x16x32_bf16 v[228:231], a[120:123], a[220:223], v[228:231]\n\t"
"ds_read_b64_tr_b16 a[194:195], v18 offset:704\n\t"
"v_mfma_f32_16x16x32_bf16 v[232:235], a[120:123], a[224:227], v[232:235]\n\t"
"v_mfma_f32_16x16x32_bf16 v[216:219], a[116:119], a[224:227], v[216:219]\n\t"
"ds_read_b64_tr_b16 a[196:197], v18 offset:224\n\t"
"v_mfma_f32_16x16x32_bf16 v[220:223], a[116:119], a[228:231], v[220:223]\n\t"
"ds_read_b64_tr_b16 a[198:199], v18 offset:736\n\t"
"v_mfma_f32_16x16x32_bf16 v[236:239], a[120:123], a[228:231], v[236:239]\n\t"
"s_waitcnt lgkmcnt(4)\n\t"
"v_mfma_f32_16x16x32_bf16 v[212:215], a[124:127], a[248:251], v[212:215]\n\t"
"ds_read_b64_tr_b16 a[200:201], v18 offset:10720\n\t"
"v_mfma_f32_16x16x32_bf16 v[228:231], a[236:239], a[248:251], v[228:231]\n\t"
"ds_read_b64_tr_b16 a[202:203], v18 offset:11232\n\t"
"v_mfma_f32_16x16x32_bf16 v[232:235], a[236:239], a[252:255], v[232:235]\n\t"
"s_add_u32 s83, s83, s53\n\t"
"ds_read_b64_tr_b16 a[204:205], v18 offset:10752\n\t"
"v_mfma_f32_16x16x32_bf16 v[216:219], a[124:127], a[252:255], v[216:219]\n\t"
"buffer_load_dwordx4 v[172:175], v1, s[8:11], s83 offen offset:0 sc1\n\t"
"v_mfma_f32_16x16x32_bf16 v[220:223], a[124:127], a[96:99], v[220:223]\n\t"
"ds_read_b64_tr_b16 a[206:207], v18 offset:11264\n\t"
"v_mfma_f32_16x16x32_bf16 v[236:239], a[236:239], a[96:99], v[236:239]\n\t"
"s_waitcnt lgkmcnt(4)\n\t"
"v_mfma_f32_16x16x32_bf16 v[244:247], a[192:195], a[0:3], v[244:247]\n\t"
"ds_read_b64_tr_b16 a[208:209], v13 offset:16640\n\t"
"v_mfma_f32_16x16x32_bf16 v[68:71], a[196:199], a[0:3], v[68:71]\n\t"
"ds_read_b64_tr_b16 a[210:211], v13 offset:17152\n\t"
"v_mfma_f32_16x16x32_bf16 v[72:75], a[196:199], a[4:7], v[72:75]\n\t"
"v_mfma_f32_16x16x32_bf16 v[248:251], a[192:195], a[4:7], v[248:251]\n\t"
"ds_read_b64_tr_b16 a[212:213], v13 offset:16672\n\t"
"v_cvt_pk_bf16_f32 v212, v212, v213\n\t"
"v_mfma_f32_16x16x32_bf16 v[252:255], a[192:195], a[8:11], v[252:255]\n\t"
"ds_read_b64_tr_b16 a[214:215], v13 offset:17184\n\t"
"v_cvt_pk_bf16_f32 v213, v214, v215\n\t"
"v_mfma_f32_16x16x32_bf16 v[76:79], a[196:199], a[8:11], v[76:79]\n\t"
"s_waitcnt lgkmcnt(4)\n\t"
"v_add3_u32 v12, v212, v213, s39\n\t"
"v_cvt_pk_bf16_f32 v214, v228, v229\n\t"
"v_mfma_f32_16x16x32_bf16 v[244:247], a[200:203], a[16:19], v[244:247]\n\t"
"ds_read_b64_tr_b16 a[192:193], v13 offset:27168\n\t"
"v_cvt_pk_bf16_f32 v215, v230, v231\n\t"
"v_mfma_f32_16x16x32_bf16 v[68:71], a[204:207], a[16:19], v[68:71]\n\t"
"s_xor_b32 s82, s74, 1179648\n\t"
"ds_read_b64_tr_b16 a[194:195], v13 offset:27680\n\t"
"v_add3_u32 v12, v214, v215, v12\n\t"
"v_mfma_f32_16x16x32_bf16 v[72:75], a[204:207], a[20:23], v[72:75]\n\t"
"buffer_store_dwordx4 v[212:215], v1, s[8:11], s82 offen offset:0 sc1\n\t"
"v_mfma_f32_16x16x32_bf16 v[248:251], a[200:203], a[20:23], v[248:251]\n\t"
"ds_read_b64_tr_b16 a[196:197], v13 offset:27200\n\t"
"v_cvt_pk_bf16_f32 v216, v216, v217\n\t"
"v_mfma_f32_16x16x32_bf16 v[252:255], a[200:203], a[24:27], v[252:255]\n\t"
"ds_read_b64_tr_b16 a[198:199], v13 offset:27712\n\t"
"v_cvt_pk_bf16_f32 v217, v218, v219\n\t"
"v_mfma_f32_16x16x32_bf16 v[76:79], a[204:207], a[24:27], v[76:79]\n\t"
"s_waitcnt lgkmcnt(4)\n\t"
"v_add3_u32 v15, v216, v217, s39\n\t"
"v_cvt_pk_bf16_f32 v218, v232, v233\n\t"
"v_mfma_f32_16x16x32_bf16 v[244:247], a[208:211], a[32:35], v[244:247]\n\t"
"ds_read_b64_tr_b16 a[200:201], v16 offset:42304\n\t"
"v_cvt_pk_bf16_f32 v219, v234, v235\n\t"
"v_mfma_f32_16x16x32_bf16 v[68:71], a[212:215], a[32:35], v[68:71]\n\t"
"ds_read_b64_tr_b16 a[202:203], v16 offset:42816\n\t"
"v_add3_u32 v15, v218, v219, v15\n\t"
"v_mfma_f32_16x16x32_bf16 v[72:75], a[212:215], a[36:39], v[72:75]\n\t"
"v_cvt_pk_bf16_f32 v220, v220, v221\n\t"
"v_cvt_pk_bf16_f32 v221, v222, v223\n\t"
"v_mfma_f32_16x16x32_bf16 v[248:251], a[208:211], a[36:39], v[248:251]\n\t"
"ds_read_b64_tr_b16 a[204:205], v16 offset:42336\n\t"
"v_add3_u32 v9, v220, v221, s39\n\t"
"v_mfma_f32_16x16x32_bf16 v[252:255], a[208:211], a[40:43], v[252:255]\n\t"
"ds_read_b64_tr_b16 a[206:207], v16 offset:42848\n\t"
"v_cvt_pk_bf16_f32 v222, v236, v237\n\t"
"v_mfma_f32_16x16x32_bf16 v[76:79], a[212:215], a[40:43], v[76:79]\n\t"
"s_waitcnt lgkmcnt(4)\n\t"
"v_cvt_pk_bf16_f32 v223, v238, v239\n\t"
"v_add3_u32 v9, v222, v223, v9\n\t"
"v_mfma_f32_16x16x32_bf16 v[244:247], a[192:195], a[48:51], v[244:247]\n\t"
"ds_read_b64_tr_b16 a[208:209], v16 offset:52832\n\t"
"v_mfma_f32_16x16x32_bf16 v[68:71], a[196:199], a[48:51], v[68:71]\n\t"
"ds_read_b64_tr_b16 a[210:211], v16 offset:53344\n\t"
"s_xor_b32 s82, s82, s52\n\t"
"v_mfma_f32_16x16x32_bf16 v[72:75], a[196:199], a[52:55], v[72:75]\n\t"
"buffer_store_dwordx4 v[216:219], v1, s[8:11], s82 offen offset:0 sc1\n\t"
"v_mfma_f32_16x16x32_bf16 v[248:251], a[192:195], a[52:55], v[248:251]\n\t"
"ds_read_b64_tr_b16 a[212:213], v16 offset:52864\n\t"
"v_mfma_f32_16x16x32_bf16 v[252:255], a[192:195], a[56:59], v[252:255]\n\t"
"ds_read_b64_tr_b16 a[214:215], v16 offset:53376\n\t"
"v_mfma_f32_16x16x32_bf16 v[76:79], a[196:199], a[56:59], v[76:79]\n\t"
"s_waitcnt lgkmcnt(4)\n\t"
"v_mfma_f32_16x16x32_bf16 v[244:247], a[200:203], a[64:67], v[244:247]\n\t"
"ds_read_b64_tr_b16 a[192:193], v16 offset:21248\n\t"
"v_mfma_f32_16x16x32_bf16 v[68:71], a[204:207], a[64:67], v[68:71]\n\t"
"ds_read_b64_tr_b16 a[194:195], v16 offset:21760\n\t"
"v_mfma_f32_16x16x32_bf16 v[72:75], a[204:207], a[68:71], v[72:75]\n\t"
"v_mfma_f32_16x16x32_bf16 v[248:251], a[200:203], a[68:71], v[248:251]\n\t"
"ds_read_b64_tr_b16 a[196:197], v16 offset:21280\n\t"
"v_mfma_f32_16x16x32_bf16 v[252:255], a[200:203], a[72:75], v[252:255]\n\t"
"ds_read_b64_tr_b16 a[198:199], v16 offset:21792\n\t"
"v_mfma_f32_16x16x32_bf16 v[76:79], a[204:207], a[72:75], v[76:79]\n\t"
"s_waitcnt lgkmcnt(4)\n\t"
"v_mfma_f32_16x16x32_bf16 v[244:247], a[208:211], a[80:83], v[244:247]\n\t"
"ds_read_b64_tr_b16 a[200:201], v16 offset:31776\n\t"
"v_mfma_f32_16x16x32_bf16 v[68:71], a[212:215], a[80:83], v[68:71]\n\t"
"ds_read_b64_tr_b16 a[202:203], v16 offset:32288\n\t"
"s_xor_b32 s82, s82, s51\n\t"
"v_mfma_f32_16x16x32_bf16 v[72:75], a[212:215], a[84:87], v[72:75]\n\t"
"buffer_store_dwordx4 v[220:223], v1, s[8:11], s82 offen offset:0 sc1\n\t"
"v_mfma_f32_16x16x32_bf16 v[248:251], a[208:211], a[84:87], v[248:251]\n\t"
"ds_read_b64_tr_b16 a[204:205], v16 offset:31808\n\t"
"v_mfma_f32_16x16x32_bf16 v[252:255], a[208:211], a[88:91], v[252:255]\n\t"
"ds_read_b64_tr_b16 a[206:207], v16 offset:32320\n\t"
"v_mfma_f32_16x16x32_bf16 v[76:79], a[212:215], a[88:91], v[76:79]\n\t"
"s_waitcnt lgkmcnt(4)\n\t"
"v_mfma_f32_16x16x32_bf16 v[244:247], a[192:195], a[128:131], v[244:247]\n\t"
"ds_read_b64_tr_b16 v[144:145], v16 offset:192\n\t"
"v_mfma_f32_16x16x32_bf16 v[68:71], a[196:199], a[128:131], v[68:71]\n\t"
"ds_read_b64_tr_b16 v[146:147], v16 offset:704\n\t"
"v_mfma_f32_16x16x32_bf16 v[72:75], a[196:199], a[132:135], v[72:75]\n\t"
"v_mfma_f32_16x16x32_bf16 v[248:251], a[192:195], a[132:135], v[248:251]\n\t"
"ds_read_b64_tr_b16 v[148:149], v16 offset:224\n\t"
"v_mfma_f32_16x16x32_bf16 v[252:255], a[192:195], a[136:139], v[252:255]\n\t"
"ds_read_b64_tr_b16 v[150:151], v16 offset:736\n\t"
"v_mfma_f32_16x16x32_bf16 v[76:79], a[196:199], a[136:139], v[76:79]\n\t"
"s_waitcnt lgkmcnt(4)\n\t"
"v_mfma_f32_16x16x32_bf16 v[244:247], a[200:203], a[144:147], v[244:247]\n\t"
"ds_read_b64_tr_b16 v[152:153], v16 offset:10720\n\t"
"s_xor_b32 s83, s74, 20971520\n\t"
"v_mfma_f32_16x16x32_bf16 v[68:71], a[204:207], a[144:147], v[68:71]\n\t"
"buffer_load_dwordx3 v[192:194], v20, s[8:11], s83 offen offset:0 sc1\n\t"
"v_mfma_f32_16x16x32_bf16 v[72:75], a[204:207], a[148:151], v[72:75]\n\t"
"ds_read_b64_tr_b16 v[154:155], v16 offset:11232\n\t"
"v_mfma_f32_16x16x32_bf16 v[248:251], a[200:203], a[148:151], v[248:251]\n\t"
"ds_read_b64_tr_b16 v[156:157], v16 offset:10752\n\t"
"v_mfma_f32_16x16x32_bf16 v[252:255], a[200:203], a[152:155], v[252:255]\n\t"
"ds_read_b64_tr_b16 v[158:159], v16 offset:11264\n\t"
"v_mfma_f32_16x16x32_bf16 v[76:79], a[204:207], a[152:155], v[76:79]\n\t"
"s_waitcnt lgkmcnt(4)\n\t"
"v_mfma_f32_16x16x32_bf16 v[244:247], v[144:147], a[160:163], v[244:247]\n\t"
"ds_read_b64_tr_b16 v[176:177], v18 offset:42304\n\t"
"v_mfma_f32_16x16x32_bf16 v[68:71], v[148:151], a[160:163], v[68:71]\n\t"
"ds_read_b64_tr_b16 v[178:179], v18 offset:42816\n\t"
"v_mfma_f32_16x16x32_bf16 v[72:75], v[148:151], a[164:167], v[72:75]\n\t"
"v_mfma_f32_16x16x32_bf16 v[248:251], v[144:147], a[164:167], v[248:251]\n\t"
"ds_read_b64_tr_b16 v[180:181], v18 offset:42336\n\t"
"v_mfma_f32_16x16x32_bf16 v[252:255], v[144:147], a[232:235], v[252:255]\n\t"
"ds_read_b64_tr_b16 v[182:183], v18 offset:42848\n\t"
"v_mfma_f32_16x16x32_bf16 v[76:79], v[148:151], a[232:235], v[76:79]\n\t"
"s_add_u32 s83, s83, s53\n\t"
"s_waitcnt lgkmcnt(4)\n\t"
"ds_read_b64_tr_b16 v[184:185], v18 offset:52832\n\t"
"v_mfma_f32_16x16x32_bf16 v[244:247], v[152:155], a[172:175], v[244:247]\n\t"
"buffer_load_dwordx3 v[196:198], v20, s[8:11], s83 offen offset:0 sc1\n\t"
"v_mfma_f32_16x16x32_bf16 v[68:71], v[156:159], a[172:175], v[68:71]\n\t"
"ds_read_b64_tr_b16 v[186:187], v18 offset:53344\n\t"
"v_mfma_f32_16x16x32_bf16 v[72:75], v[156:159], a[176:179], v[72:75]\n\t"
"v_mfma_f32_16x16x32_bf16 v[248:251], v[152:155], a[176:179], v[248:251]\n\t"
"ds_read_b64_tr_b16 v[188:189], v18 offset:52864\n\t"
"v_mfma_f32_16x16x32_bf16 v[252:255], v[152:155], a[180:183], v[252:255]\n\t"
"ds_read_b64_tr_b16 v[190:191], v18 offset:53376\n\t"
"v_mfma_f32_16x16x32_bf16 v[76:79], v[156:159], a[180:183], v[76:79]\n\t"
"s_waitcnt lgkmcnt(4)\n\t"
"v_mfma_f32_16x16x32_bf16 v[244:247], v[176:179], a[188:191], v[244:247]\n\t"
"ds_read_b64_tr_b16 v[228:229], v18 offset:21248\n\t"
"v_mfma_f32_16x16x32_bf16 v[68:71], v[180:183], a[188:191], v[68:71]\n\t"
"ds_read_b64_tr_b16 v[230:231], v18 offset:21760\n\t"
"v_mfma_f32_16x16x32_bf16 v[72:75], v[180:183], v[52:55], v[72:75]\n\t"
"v_mfma_f32_16x16x32_bf16 v[248:251], v[176:179], v[52:55], v[248:251]\n\t"
"ds_read_b64_tr_b16 v[232:233], v18 offset:21280\n\t"
"v_mfma_f32_16x16x32_bf16 v[252:255], v[176:179], v[56:59], v[252:255]\n\t"
"ds_read_b64_tr_b16 v[234:235], v18 offset:21792\n\t"
"s_add_u32 s83, s83, s53\n\t"
"v_mfma_f32_16x16x32_bf16 v[76:79], v[180:183], v[56:59], v[76:79]\n\t"
"s_waitcnt lgkmcnt(4)\n\t"
"buffer_load_dwordx3 v[200:202], v20, s[8:11], s83 offen offset:0 sc1\n\t"
"v_mfma_f32_16x16x32_bf16 v[244:247], v[184:187], v[32:35], v[244:247]\n\t"
"ds_read_b64_tr_b16 v[236:237], v18 offset:31776\n\t"
"v_mfma_f32_16x16x32_bf16 v[68:71], v[188:191], v[32:35], v[68:71]\n\t"
"ds_read_b64_tr_b16 v[238:239], v18 offset:32288\n\t"
"v_mfma_f32_16x16x32_bf16 v[72:75], v[188:191], v[36:39], v[72:75]\n\t"
"v_mfma_f32_16x16x32_bf16 v[248:251], v[184:187], v[36:39], v[248:251]\n\t"
"ds_read_b64_tr_b16 a[208:209], v18 offset:31808\n\t"
"v_mfma_f32_16x16x32_bf16 v[252:255], v[184:187], v[40:43], v[252:255]\n\t"
"ds_read_b64_tr_b16 a[210:211], v18 offset:32320\n\t"
"v_mfma_f32_16x16x32_bf16 v[76:79], v[188:191], v[40:43], v[76:79]\n\t"
"s_waitcnt lgkmcnt(4)\n\t"
"v_mfma_f32_16x16x32_bf16 v[244:247], v[228:231], a[220:223], v[244:247]\n\t"
"ds_read_b64_tr_b16 a[212:213], v19 offset:8480\n\t"
"v_mfma_f32_16x16x32_bf16 v[68:71], v[232:235], a[220:223], v[68:71]\n\t"
"ds_read_b64_tr_b16 a[214:215], v19 offset:8992\n\t"
"v_mfma_f32_16x16x32_bf16 v[72:75], v[232:235], a[224:227], v[72:75]\n\t"
"v_mfma_f32_16x16x32_bf16 v[248:251], v[228:231], a[224:227], v[248:251]\n\t"
"ds_read_b64_tr_b16 a[192:193], v19 offset:19008\n\t"
"s_add_u32 s83, s83, s53\n\t"
"v_mfma_f32_16x16x32_bf16 v[252:255], v[228:231], a[228:231], v[252:255]\n\t"
"buffer_load_dwordx3 v[204:206], v20, s[8:11], s83 offen offset:0 sc1\n\t"
"v_mfma_f32_16x16x32_bf16 v[76:79], v[232:235], a[228:231], v[76:79]\n\t"
"ds_read_b64_tr_b16 a[194:195], v19 offset:19520\n\t"
"s_waitcnt lgkmcnt(4)\n\t"
"v_mfma_f32_16x16x32_bf16 v[244:247], v[236:239], a[248:251], v[244:247]\n\t"
"ds_read_b64_tr_b16 a[196:197], v14 offset:24928\n\t"
"v_mfma_f32_16x16x32_bf16 v[68:71], a[208:211], a[248:251], v[68:71]\n\t"
"ds_read_b64_tr_b16 a[198:199], v14 offset:25440\n\t"
"v_mfma_f32_16x16x32_bf16 v[72:75], a[208:211], a[252:255], v[72:75]\n\t"
".Lpg16%=:\n"
"s_cbranch_execz .Lhr0%=\n\t"
"v_mfma_f32_16x16x32_bf16 v[248:251], v[236:239], a[252:255], v[248:251]\n\t"
"ds_read_b64_tr_b16 a[200:201], v14 offset:35456\n\t"
"v_mfma_f32_16x16x32_bf16 v[252:255], v[236:239], a[96:99], v[252:255]\n\t"
"ds_read_b64_tr_b16 a[202:203], v14 offset:35968\n\t"
"v_mfma_f32_16x16x32_bf16 v[76:79], a[208:211], a[96:99], v[76:79]\n\t"
"s_waitcnt lgkmcnt(4)\n\t"
"v_mfma_f32_16x16x32_bf16 v[100:103], a[212:215], a[0:3], v[100:103]\n\t"
"ds_read_b64_tr_b16 a[204:205], v17 offset:50592\n\t"
"v_mfma_f32_16x16x32_bf16 v[104:107], a[212:215], a[4:7], v[104:107]\n\t"
"ds_read_b64_tr_b16 a[206:207], v17 offset:51104\n\t"
"v_mfma_f32_16x16x32_bf16 v[108:111], a[212:215], a[8:11], v[108:111]\n\t"
"v_mfma_f32_16x16x32_bf16 v[100:103], a[192:195], a[16:19], v[100:103]\n\t"
"ds_read_b64_tr_b16 a[0:1], v17 offset:61120\n\t"
"v_cvt_pk_bf16_f32 v244, v244, v245\n\t"
"v_mfma_f32_16x16x32_bf16 v[104:107], a[192:195], a[20:23], v[104:107]\n\t"
"ds_read_b64_tr_b16 a[2:3], v17 offset:61632\n\t"
"v_cvt_pk_bf16_f32 v245, v246, v247\n\t"
"v_mfma_f32_16x16x32_bf16 v[108:111], a[192:195], a[24:27], v[108:111]\n\t"
"s_waitcnt lgkmcnt(4)\n\t"
"v_add3_u32 v12, v244, v245, v12\n\t"
"v_cvt_pk_bf16_f32 v246, v68, v69\n\t"
"v_mfma_f32_16x16x32_bf16 v[100:103], a[196:199], a[32:35], v[100:103]\n\t"
"ds_read_b64_tr_b16 a[4:5], v17 offset:29536\n\t"
"v_cvt_pk_bf16_f32 v247, v70, v71\n\t"
"v_mfma_f32_16x16x32_bf16 v[104:107], a[196:199], a[36:39], v[104:107]\n\t"
"s_xor_b32 s82, s74, 9568256\n\t"
"ds_read_b64_tr_b16 a[6:7], v17 offset:30048\n\t"
"v_add3_u32 v12, v246, v247, v12\n\t"
"v_mfma_f32_16x16x32_bf16 v[108:111], a[196:199], a[40:43], v[108:111]\n\t"
"buffer_store_dwordx4 v[244:247], v1, s[8:11], s82 offen offset:0 sc1\n\t"
"v_mfma_f32_16x16x32_bf16 v[100:103], a[200:203], a[48:51], v[100:103]\n\t"
"ds_read_b64_tr_b16 a[8:9], v17 offset:40064\n\t"
"v_cvt_pk_bf16_f32 v248, v248, v249\n\t"
"v_mfma_f32_16x16x32_bf16 v[104:107], a[200:203], a[52:55], v[104:107]\n\t"
"ds_read_b64_tr_b16 a[10:11], v17 offset:40576\n\t"
"v_cvt_pk_bf16_f32 v249, v250, v251\n\t"
"v_mfma_f32_16x16x32_bf16 v[108:111], a[200:203], a[56:59], v[108:111]\n\t"
"s_waitcnt lgkmcnt(4)\n\t"
"v_add3_u32 v15, v248, v249, v15\n\t"
"v_cvt_pk_bf16_f32 v250, v72, v73\n\t"
"v_mfma_f32_16x16x32_bf16 v[100:103], a[204:207], a[64:67], v[100:103]\n\t"
"ds_read_b64_tr_b16 a[16:17], v17 offset:8480\n\t"
"v_cvt_pk_bf16_f32 v251, v74, v75\n\t"
"v_mfma_f32_16x16x32_bf16 v[104:107], a[204:207], a[68:71], v[104:107]\n\t"
"ds_read_b64_tr_b16 a[18:19], v17 offset:8992\n\t"
"v_add3_u32 v15, v250, v251, v15\n\t"
"v_mfma_f32_16x16x32_bf16 v[108:111], a[204:207], a[72:75], v[108:111]\n\t"
"v_cvt_pk_bf16_f32 v252, v252, v253\n\t"
"v_cvt_pk_bf16_f32 v253, v254, v255\n\t"
"v_mfma_f32_16x16x32_bf16 v[100:103], a[0:3], a[80:83], v[100:103]\n\t"
"ds_read_b64_tr_b16 a[20:21], v17 offset:19008\n\t"
"v_add3_u32 v9, v252, v253, v9\n\t"
"v_mfma_f32_16x16x32_bf16 v[104:107], a[0:3], a[84:87], v[104:107]\n\t"
"ds_read_b64_tr_b16 a[22:23], v17 offset:19520\n\t"
"v_cvt_pk_bf16_f32 v254, v76, v77\n\t"
"v_mfma_f32_16x16x32_bf16 v[108:111], a[0:3], a[88:91], v[108:111]\n\t"
"s_waitcnt lgkmcnt(4)\n\t"
"v_cvt_pk_bf16_f32 v255, v78, v79\n\t"
"v_add3_u32 v9, v254, v255, v9\n\t"
"v_mfma_f32_16x16x32_bf16 v[100:103], a[4:7], a[128:131], v[100:103]\n\t"
"ds_read_b64_tr_b16 a[24:25], v19 offset:50592\n\t"
"v_mfma_f32_16x16x32_bf16 v[104:107], a[4:7], a[132:135], v[104:107]\n\t"
"ds_read_b64_tr_b16 a[26:27], v19 offset:51104\n\t"
"s_xor_b32 s82, s82, s52\n\t"
"v_mfma_f32_16x16x32_bf16 v[108:111], a[4:7], a[136:139], v[108:111]\n\t"
"buffer_store_dwordx4 v[248:251], v1, s[8:11], s82 offen offset:0 sc1\n\t"
"v_mfma_f32_16x16x32_bf16 v[100:103], a[8:11], a[144:147], v[100:103]\n\t"
"ds_read_b64_tr_b16 a[32:33], v19 offset:61120\n\t"
"v_mfma_f32_16x16x32_bf16 v[104:107], a[8:11], a[148:151], v[104:107]\n\t"
"ds_read_b64_tr_b16 a[34:35], v19 offset:61632\n\t"
"v_mfma_f32_16x16x32_bf16 v[108:111], a[8:11], a[152:155], v[108:111]\n\t"
"s_waitcnt lgkmcnt(4)\n\t"
"v_mfma_f32_16x16x32_bf16 v[100:103], a[16:19], a[160:163], v[100:103]\n\t"
"ds_read_b64_tr_b16 a[36:37], v19 offset:29536\n\t"
"v_mfma_f32_16x16x32_bf16 v[104:107], a[16:19], a[164:167], v[104:107]\n\t"
"ds_read_b64_tr_b16 a[38:39], v19 offset:30048\n\t"
"v_mfma_f32_16x16x32_bf16 v[108:111], a[16:19], a[232:235], v[108:111]\n\t"
"v_mfma_f32_16x16x32_bf16 v[100:103], a[20:23], a[172:175], v[100:103]\n\t"
"ds_read_b64_tr_b16 v[68:69], v19 offset:40064\n\t"
"v_mfma_f32_16x16x32_bf16 v[104:107], a[20:23], a[176:179], v[104:107]\n\t"
"ds_read_b64_tr_b16 v[70:71], v19 offset:40576\n\t"
"v_mfma_f32_16x16x32_bf16 v[108:111], a[20:23], a[180:183], v[108:111]\n\t"
"s_waitcnt lgkmcnt(4)\n\t"
"v_mfma_f32_16x16x32_bf16 v[100:103], a[24:27], a[188:191], v[100:103]\n\t"
"ds_read_b64_tr_b16 a[40:41], v18 offset:128\n\t"
"v_mfma_f32_16x16x32_bf16 v[104:107], a[24:27], v[52:55], v[104:107]\n\t"
"ds_read_b64_tr_b16 a[42:43], v18 offset:640\n\t"
"s_xor_b32 s82, s82, s51\n\t"
"v_mfma_f32_16x16x32_bf16 v[108:111], a[24:27], v[56:59], v[108:111]\n\t"
"ds_read_b64_tr_b16 a[48:49], v18 offset:160\n\t"
"buffer_store_dwordx4 v[252:255], v1, s[8:11], s82 offen offset:0 sc1\n\t"
"v_mfma_f32_16x16x32_bf16 v[100:103], a[32:35], v[32:35], v[100:103]\n\t"
"ds_read_b64_tr_b16 a[50:51], v18 offset:672\n\t"
"v_mfma_f32_16x16x32_bf16 v[104:107], a[32:35], v[36:39], v[104:107]\n\t"
"ds_read_b64_tr_b16 a[52:53], v18 offset:192\n\t"
"v_mfma_f32_16x16x32_bf16 v[108:111], a[32:35], v[40:43], v[108:111]\n\t"
"ds_read_b64_tr_b16 a[54:55], v18 offset:704\n\t"
"s_waitcnt lgkmcnt(6)\n\t"
"v_mfma_f32_16x16x32_bf16 v[100:103], a[36:39], a[220:223], v[100:103]\n\t"
"ds_read_b64_tr_b16 a[56:57], v18 offset:224\n\t"
"v_mfma_f32_16x16x32_bf16 v[104:107], a[36:39], a[224:227], v[104:107]\n\t"
"ds_read_b64_tr_b16 a[58:59], v18 offset:736\n\t"
"v_mfma_f32_16x16x32_bf16 v[108:111], a[36:39], a[228:231], v[108:111]\n\t"
"ds_read_b64_tr_b16 a[64:65], v18 offset:10656\n\t"
"v_mfma_f32_16x16x32_bf16 v[100:103], v[68:71], a[248:251], v[100:103]\n\t"
"s_nop 2\n\t"
"ds_read_b64_tr_b16 a[66:67], v18 offset:11168\n\t"
"v_mfma_f32_16x16x32_bf16 v[104:107], v[68:71], a[252:255], v[104:107]\n\t"
"s_nop 2\n\t"
"ds_read_b64_tr_b16 a[68:69], v18 offset:10688\n\t"
"v_mfma_f32_16x16x32_bf16 v[108:111], v[68:71], a[96:99], v[108:111]\n\t"
"ds_read_b64_tr_b16 a[70:71], v18 offset:11200\n\t"
"s_waitcnt lgkmcnt(8)\n\t"
"v_mfma_f32_16x16x32_bf16 v[208:211], a[40:43], a[240:243], v[208:211]\n\t"
"ds_read_b64_tr_b16 v[72:73], v18 offset:10720\n\t"
"v_cvt_pk_bf16_f32 v100, v100, v101\n\t"
"v_mfma_f32_16x16x32_bf16 v[224:227], a[48:51], a[240:243], v[224:227]\n\t"
"ds_read_b64_tr_b16 v[74:75], v18 offset:11232\n\t"
"v_cvt_pk_bf16_f32 v101, v102, v103\n\t"
"v_mfma_f32_16x16x32_bf16 v[96:99], a[212:215], a[240:243], v[96:99]\n\t"
"s_xor_b32 s82, s74, 17956864\n\t"
"ds_read_b64_tr_b16 a[72:73], v18 offset:10752\n\t"
"v_add3_u32 v102, v100, v101, v12\n\t"
"v_mfma_f32_16x16x32_bf16 v[240:243], v[144:147], a[156:159], v[240:243]\n\t"
"s_waitcnt lgkmcnt(7)\n\t"
"ds_read_b64_tr_b16 a[74:75], v18 offset:11264\n\t"
"buffer_store_dwordx3 v[100:102], v20, s[8:11], s82 offen offset:0 sc1\n\t"
"v_mfma_f32_16x16x32_bf16 v[64:67], a[56:59], a[240:243], v[64:67]\n\t"
"ds_read_b64_tr_b16 a[80:81], v13 offset:16576\n\t"
"v_cvt_pk_bf16_f32 v104, v104, v105\n\t"
"v_mfma_f32_16x16x32_bf16 v[96:99], a[192:195], a[12:15], v[96:99]\n\t"
"ds_read_b64_tr_b16 a[82:83], v13 offset:17088\n\t"
"v_cvt_pk_bf16_f32 v105, v106, v107\n\t"
"v_mfma_f32_16x16x32_bf16 v[240:243], a[52:55], a[240:243], v[240:243]\n\t"
"ds_read_b64_tr_b16 a[84:85], v13 offset:16608\n\t"
"v_add3_u32 v106, v104, v105, v15\n\t"
"v_mfma_f32_16x16x32_bf16 v[64:67], v[148:151], a[156:159], v[64:67]\n\t"
"s_waitcnt lgkmcnt(7)\n\t"
"ds_read_b64_tr_b16 a[86:87], v13 offset:17120\n\t"
"v_cvt_pk_bf16_f32 v108, v108, v109\n\t"
"v_mfma_f32_16x16x32_bf16 v[208:211], a[64:67], a[12:15], v[208:211]\n\t"
"ds_read_b64_tr_b16 a[88:89], v13 offset:16640\n\t"
"v_cvt_pk_bf16_f32 v109, v110, v111\n\t"
"v_mfma_f32_16x16x32_bf16 v[224:227], a[68:71], a[12:15], v[224:227]\n\t"
"ds_read_b64_tr_b16 a[90:91], v13 offset:17152\n\t"
"v_add3_u32 v110, v108, v109, v9\n\t"
"v_mfma_f32_16x16x32_bf16 v[96:99], a[196:199], a[28:31], v[96:99]\n\t"
"ds_read_b64_tr_b16 v[76:77], v13 offset:16672\n\t"
"s_xor_b32 s82, s82, s52\n\t"
"v_mfma_f32_16x16x32_bf16 v[240:243], v[152:155], a[168:171], v[240:243]\n\t"
"s_waitcnt lgkmcnt(7)\n\t"
"ds_read_b64_tr_b16 v[78:79], v13 offset:17184\n\t"
"buffer_store_dwordx3 v[104:106], v20, s[8:11], s82 offen offset:0 sc1\n\t"
"v_mfma_f32_16x16x32_bf16 v[64:67], a[72:75], a[12:15], v[64:67]\n\t"
"s_waitcnt vmcnt(12)\n\t"
"ds_read_b64_tr_b16 a[128:129], v13 offset:27104\n\t"
"v_add3_u32 v24, v128, v129, s39\n\t"
"v_mfma_f32_16x16x32_bf16 v[96:99], a[200:203], a[44:47], v[96:99]\n\t"
"ds_read_b64_tr_b16 a[130:131], v13 offset:27616\n\t"
"v_add3_u32 v25, v132, v133, s39\n\t"
"v_mfma_f32_16x16x32_bf16 v[240:243], v[72:75], a[12:15], v[240:243]\n\t"
"ds_read_b64_tr_b16 a[132:133], v13 offset:27136\n\t"
"v_add3_u32 v26, v136, v137, s39\n\t"
"v_mfma_f32_16x16x32_bf16 v[64:67], v[156:159], a[168:171], v[64:67]\n\t"
"s_waitcnt lgkmcnt(7)\n\t"
"ds_read_b64_tr_b16 a[134:135], v13 offset:27648\n\t"
"v_add3_u32 v27, v140, v141, s39\n\t"
"v_mfma_f32_16x16x32_bf16 v[208:211], a[80:83], a[28:31], v[208:211]\n\t"
"ds_read_b64_tr_b16 a[136:137], v13 offset:27168\n\t"
"v_add3_u32 v24, v130, v131, v24\n\t"
"v_mfma_f32_16x16x32_bf16 v[224:227], a[84:87], a[28:31], v[224:227]\n\t"
"ds_read_b64_tr_b16 a[138:139], v13 offset:27680\n\t"
"v_add3_u32 v25, v134, v135, v25\n\t"
"v_mfma_f32_16x16x32_bf16 v[96:99], a[204:207], a[60:63], v[96:99]\n\t"
"s_xor_b32 s82, s82, s51\n\t"
"ds_read_b64_tr_b16 a[144:145], v13 offset:27200\n\t"
"v_add3_u32 v26, v138, v139, v26\n\t"
"v_mfma_f32_16x16x32_bf16 v[208:211], a[100:103], a[184:187], v[208:211]\n\t"
"s_waitcnt lgkmcnt(7)\n\t"
"ds_read_b64_tr_b16 a[146:147], v13 offset:27712\n\t"
"buffer_store_dwordx3 v[108:110], v20, s[8:11], s82 offen offset:0 sc1\n\t"
"v_mfma_f32_16x16x32_bf16 v[240:243], a[88:91], a[28:31], v[240:243]\n\t"
"ds_read_b64_tr_b16 a[148:149], v16 offset:42240\n\t"
"v_add3_u32 v27, v142, v143, v27\n\t"
"v_mfma_f32_16x16x32_bf16 v[64:67], v[76:79], a[28:31], v[64:67]\n\t"
"ds_read_b64_tr_b16 a[150:151], v16 offset:42752\n\t"
"v_add3_u32 v24, v160, v161, v24\n\t"
"v_mfma_f32_16x16x32_bf16 v[96:99], a[0:3], a[76:79], v[96:99]\n\t"
"ds_read_b64_tr_b16 a[152:153], v16 offset:42272\n\t"
"v_add3_u32 v25, v164, v165, v25\n\t"
"v_mfma_f32_16x16x32_bf16 v[224:227], a[104:107], a[184:187], v[224:227]\n\t"
"s_waitcnt lgkmcnt(7)\n\t"
"ds_read_b64_tr_b16 a[154:155], v16 offset:42784\n\t"
"v_add3_u32 v26, v168, v169, v26\n\t"
"v_mfma_f32_16x16x32_bf16 v[208:211], a[128:131], a[44:47], v[208:211]\n\t"
"ds_read_b64_tr_b16 a[160:161], v16 offset:42304\n\t"
"v_add3_u32 v27, v172, v173, v27\n\t"
"v_mfma_f32_16x16x32_bf16 v[96:99], a[4:7], a[92:95], v[96:99]\n\t"
"ds_read_b64_tr_b16 a[162:163], v16 offset:42816\n\t"
"v_add3_u32 v24, v162, v163, v24\n\t"
"v_mfma_f32_16x16x32_bf16 v[224:227], a[132:135], a[44:47], v[224:227]\n\t"
"ds_read_b64_tr_b16 a[164:165], v16 offset:42336\n\t"
"v_add3_u32 v25, v166, v167, v25\n\t"
"v_mfma_f32_16x16x32_bf16 v[240:243], v[176:179], a[184:187], v[240:243]\n\t"
"s_waitcnt lgkmcnt(7)\n\t"
"ds_read_b64_tr_b16 a[166:167], v16 offset:42848\n\t"
"v_add3_u32 v26, v170, v171, v26\n\t"
"v_mfma_f32_16x16x32_bf16 v[64:67], a[144:147], a[44:47], v[64:67]\n\t"
"ds_read_b64_tr_b16 a[232:233], v16 offset:52768\n\t"
"v_add3_u32 v27, v174, v175, v27\n\t"
"v_mfma_f32_16x16x32_bf16 v[96:99], a[8:11], a[140:143], v[96:99]\n\t"
"ds_read_b64_tr_b16 a[234:235], v16 offset:53280\n\t"
"v_mfma_f32_16x16x32_bf16 v[240:243], a[136:139], a[44:47], v[240:243]\n\t"
"ds_read_b64_tr_b16 a[172:173], v16 offset:52800\n\t"
"v_mfma_f32_16x16x32_bf16 v[64:67], v[180:183], a[184:187], v[64:67]\n\t"
"ds_read_b64_tr_b16 a[174:175], v16 offset:53312\n\t"
"s_waitcnt lgkmcnt(8)\n\t"
"v_mfma_f32_16x16x32_bf16 v[208:211], a[148:151], a[60:63], v[208:211]\n\t"
"ds_read_b64_tr_b16 a[176:177], v16 offset:52832\n\t"
"v_mfma_f32_16x16x32_bf16 v[224:227], a[152:155], a[60:63], v[224:227]\n\t"
"ds_read_b64_tr_b16 a[178:179], v16 offset:53344\n\t"
"v_mfma_f32_16x16x32_bf16 v[96:99], a[16:19], a[156:159], v[96:99]\n\t"
"ds_read_b64_tr_b16 a[180:181], v16 offset:52864\n\t"
"v_mfma_f32_16x16x32_bf16 v[208:211], a[108:111], v[60:63], v[208:211]\n\t"
"ds_read_b64_tr_b16 a[182:183], v16 offset:53376\n\t"
"s_waitcnt lgkmcnt(8)\n\t"
"v_mfma_f32_16x16x32_bf16 v[240:243], a[160:163], a[60:63], v[240:243]\n\t"
"ds_read_b64_tr_b16 a[188:189], v16 offset:21184\n\t"
"v_mfma_f32_16x16x32_bf16 v[64:67], a[164:167], a[60:63], v[64:67]\n\t"
"ds_read_b64_tr_b16 a[190:191], v16 offset:21696\n\t"
"v_mfma_f32_16x16x32_bf16 v[96:99], a[20:23], a[168:171], v[96:99]\n\t"
"ds_read_b64_tr_b16 v[52:53], v16 offset:21216\n\t"
"v_mfma_f32_16x16x32_bf16 v[224:227], a[112:115], v[60:63], v[224:227]\n\t"
"ds_read_b64_tr_b16 v[54:55], v16 offset:21728\n\t"
"s_waitcnt lgkmcnt(8)\n\t"
"v_mfma_f32_16x16x32_bf16 v[208:211], a[232:235], a[76:79], v[208:211]\n\t"
"s_waitcnt vmcnt(6)\n\t"
"ds_read_b64_tr_b16 v[56:57], v16 offset:21248\n\t"
"v_add3_u32 v24, v192, v193, v24\n\t"
"v_mfma_f32_16x16x32_bf16 v[96:99], a[24:27], a[184:187], v[96:99]\n\t"
"ds_read_b64_tr_b16 v[58:59], v16 offset:21760\n\t"
"v_add3_u32 v25, v196, v197, v25\n\t"
"v_mfma_f32_16x16x32_bf16 v[224:227], a[172:175], a[76:79], v[224:227]\n\t"
"ds_read_b64_tr_b16 v[32:33], v16 offset:21280\n\t"
"v_add3_u32 v26, v200, v201, v26\n\t"
"v_mfma_f32_16x16x32_bf16 v[240:243], v[184:187], v[60:63], v[240:243]\n\t"
"s_waitcnt lgkmcnt(7)\n\t"
"ds_read_b64_tr_b16 v[34:35], v16 offset:21792\n\t"
"v_add3_u32 v27, v204, v205, v27\n\t"
"v_mfma_f32_16x16x32_bf16 v[64:67], a[180:183], a[76:79], v[64:67]\n\t"
"s_mov_b32 s65, 0\n\t"
"ds_read_b64_tr_b16 v[36:37], v16 offset:31712\n\t"
"v_xor_b32 v24, v24, v194\n\t"
"v_mfma_f32_16x16x32_bf16 v[96:99], a[32:35], v[60:63], v[96:99]\n\t"
"ds_read_b64_tr_b16 v[38:39], v16 offset:32224\n\t"
"v_xor_b32 v25, v25, v198\n\t"
"v_mfma_f32_16x16x32_bf16 v[240:243], a[176:179], a[76:79], v[240:243]\n\t"
"ds_read_b64_tr_b16 v[40:41], v16 offset:31744\n\t"
"v_xor_b32 v26, v26, v202\n\t"
"v_mfma_f32_16x16x32_bf16 v[64:67], v[188:191], v[60:63], v[64:67]\n\t"
"s_waitcnt lgkmcnt(7)\n\t"
"ds_read_b64_tr_b16 v[42:43], v16 offset:32256\n\t"
"v_xor_b32 v27, v27, v206\n\t"
"v_mfma_f32_16x16x32_bf16 v[208:211], a[188:191], a[92:95], v[208:211]\n\t"
"ds_read_b64_tr_b16 a[220:221], v16 offset:31776\n\t"
"v_or3_b32 v7, v24, v25, v26\n\t"
"v_mfma_f32_16x16x32_bf16 v[224:227], v[52:55], a[92:95], v[224:227]\n\t"
"ds_read_b64_tr_b16 a[222:223], v16 offset:32288\n\t"
"v_or_b32 v7, v7, v27\n\t"
"v_mfma_f32_16x16x32_bf16 v[240:243], v[228:231], a[216:219], v[240:243]\n\t"
"ds_read_b64_tr_b16 a[224:225], v16 offset:31808\n\t"
"v_cmp_ne_u32 vcc, 0, v7\n\t"
"s_cbranch_vccnz .LslpA%=\n\t"
".LbkpA%=:\n"
"v_mfma_f32_16x16x32_bf16 v[208:211], a[116:119], a[216:219], v[208:211]\n\t"
"ds_read_b64_tr_b16 a[226:227], v16 offset:32320\n\t"
"s_waitcnt lgkmcnt(8)\n\t"
"v_mfma_f32_16x16x32_bf16 v[64:67], v[32:35], a[92:95], v[64:67]\n\t"
"ds_read_b64_tr_b16 a[228:229], v16 offset:128\n\t"
"v_mfma_f32_16x16x32_bf16 v[240:243], v[56:59], a[92:95], v[240:243]\n\t"
"ds_read_b64_tr_b16 a[230:231], v16 offset:640\n\t"
"s_xor_b32 s83, s74, 1048576\n\t"
"v_mfma_f32_16x16x32_bf16 v[224:227], a[120:123], a[216:219], v[224:227]\n\t"
"ds_read_b64_tr_b16 a[248:249], v16 offset:160\n\t"
"buffer_load_dwordx4 v[212:215], v1, s[8:11], s83 offen offset:0 sc1\n\t"
"v_mfma_f32_16x16x32_bf16 v[64:67], v[232:235], a[216:219], v[64:67]\n\t"
"ds_read_b64_tr_b16 a[250:251], v16 offset:672\n\t"
"s_waitcnt lgkmcnt(8)\n\t"
"v_mfma_f32_16x16x32_bf16 v[208:211], v[36:39], a[140:143], v[208:211]\n\t"
"s_add_u32 s83, s83, s53\n\t"
"ds_read_b64_tr_b16 a[252:253], v16 offset:10656\n\t"
"v_mfma_f32_16x16x32_bf16 v[224:227], v[40:43], a[140:143], v[224:227]\n\t"
"ds_read_b64_tr_b16 a[254:255], v16 offset:11168\n\t"
"buffer_load_dwordx4 v[216:219], v1, s[8:11], s83 offen offset:0 sc1\n\t"
"v_mfma_f32_16x16x32_bf16 v[96:99], a[36:39], a[216:219], v[96:99]\n\t"
"ds_read_b64_tr_b16 a[96:97], v16 offset:10688\n\t"
"v_lshlrev_b32 v176, 16, v80\n\t"
"v_mfma_f32_16x16x32_bf16 v[208:211], a[124:127], a[244:247], v[208:211]\n\t"
"s_add_u32 s83, s83, s53\n\t"
"ds_read_b64_tr_b16 a[98:99], v16 offset:11200\n\t"
"s_waitcnt lgkmcnt(8)\n\t"
"v_mfma_f32_16x16x32_bf16 v[240:243], a[220:223], a[140:143], v[240:243]\n\t"
"buffer_load_dwordx4 v[220:223], v1, s[8:11], s83 offen offset:0 sc1\n\t"
"v_mfma_f32_16x16x32_bf16 v[64:67], a[224:227], a[140:143], v[64:67]\n\t"
"v_and_b32 v177, s80, v80\n\t"
"v_lshlrev_b32 v178, 16, v81\n\t"
"v_mfma_f32_16x16x32_bf16 v[224:227], a[236:239], a[244:247], v[224:227]\n\t"
"v_and_b32 v179, s80, v81\n\t"
"v_fma_f32 v144, v176, v91, v90\n\t"
"v_mfma_f32_16x16x32_bf16 v[240:243], v[236:239], a[244:247], v[240:243]\n\t"
"s_waitcnt lgkmcnt(4)\n\t"
"v_fma_f32 v145, v177, v91, v90\n\t"
"v_fma_f32 v146, v178, v91, v90\n\t"
"v_mfma_f32_16x16x32_bf16 v[208:211], a[228:231], a[156:159], v[208:211]\n\t"
"v_fma_f32 v147, v179, v91, v90\n\t"
"v_lshlrev_b32 v180, 16, v82\n\t"
"v_mfma_f32_16x16x32_bf16 v[224:227], a[248:251], a[156:159], v[224:227]\n\t"
"v_and_b32 v181, s80, v82\n\t"
"v_lshlrev_b32 v182, 16, v83\n\t"
"v_mfma_f32_16x16x32_bf16 v[64:67], a[208:211], a[244:247], v[64:67]\n\t"
"v_and_b32 v183, s80, v83\n\t"
"v_fma_f32 v148, v180, v91, v90\n\t"
"v_mfma_f32_16x16x32_bf16 v[96:99], v[68:71], a[244:247], v[96:99]\n\t"
"s_waitcnt lgkmcnt(0)\n\t"
"v_fma_f32 v149, v181, v91, v90\n\t"
"v_fma_f32 v150, v182, v91, v90\n\t"
"v_mfma_f32_16x16x32_bf16 v[208:211], a[252:255], a[168:171], v[208:211]\n\t"
"v_fma_f32 v151, v183, v91, v90\n\t"
"v_lshlrev_b32 v184, 16, v84\n\t"
"v_mfma_f32_16x16x32_bf16 v[224:227], a[96:99], a[168:171], v[224:227]\n\t"
"v_and_b32 v185, s80, v84\n\t"
"v_lshlrev_b32 v186, 16, v85\n\t"
"v_and_b32 v187, s80, v85\n\t"
"v_pk_fma_f32 v[152:153], v[184:185], v[90:91], v[90:91] op_sel:[0,1,0] op_sel_hi:[1,1,0]\n\t"
"v_pk_fma_f32 v[154:155], v[186:187], v[90:91], v[90:91] op_sel:[0,1,0] op_sel_hi:[1,1,0]\n\t"
"v_lshlrev_b32 v188, 16, v86\n\t"
"v_and_b32 v189, s80, v86\n\t"
"v_lshlrev_b32 v190, 16, v87\n\t"
"v_and_b32 v191, s80, v87\n\t"
"v_pk_fma_f32 v[156:157], v[188:189], v[90:91], v[90:91] op_sel:[0,1,0] op_sel_hi:[1,1,0]\n\t"
"v_pk_fma_f32 v[158:159], v[190:191], v[90:91], v[90:91] op_sel:[0,1,0] op_sel_hi:[1,1,0]\n\t"
"v_lshlrev_b32 v232, 16, v88\n\t"
"v_and_b32 v233, s80, v88\n\t"
"v_lshlrev_b32 v234, 16, v89\n\t"
"v_and_b32 v235, s80, v89\n\t"
"v_pk_fma_f32 v[228:229], v[232:233], v[90:91], v[90:91] op_sel:[0,1,0] op_sel_hi:[1,1,0]\n\t"
"v_pk_fma_f32 v[230:231], v[234:235], v[90:91], v[90:91] op_sel:[0,1,0] op_sel_hi:[1,1,0]\n\t"
"s_cmp_eq_u32 s99, 0\n\t"
"s_cbranch_scc1 .Ly2%=\n\t"
".Lyb2%=:\n"
"v_mul_f32 v112, s70, v144\n\t"
"v_mul_f32 v113, s70, v145\n\t"
"s_cmp_lg_u32 s65, 0\n\t"
"s_cbranch_scc1 .LreA%=\n\t"
".LrbA%=:\n"
"v_mfma_f32_16x16x16_bf16 v[208:211], v[10:11], v[128:129], v[208:211]\n\t"
"v_mul_f32 v114, s70, v146\n\t"
"v_mul_f32 v115, s70, v147\n\t"
"v_exp_f32 v112, v112\n\t"
"v_exp_f32 v113, v113\n\t"
"v_exp_f32 v114, v114\n\t"
"v_mfma_f32_16x16x16_bf16 v[224:227], v[10:11], v[130:131], v[224:227]\n\t"
"v_exp_f32 v115, v115\n\t"
"v_add_f32 v112, 1.0, v112\n\t"
"v_add_f32 v113, 1.0, v113\n\t"
"v_add_f32 v114, 1.0, v114\n\t"
"v_add_f32 v115, 1.0, v115\n\t"
"v_mfma_f32_16x16x16_bf16 v[240:243], v[10:11], v[160:161], v[240:243]\n\t"
"v_rcp_f32 v112, v112\n\t"
"v_rcp_f32 v113, v113\n\t"
"v_rcp_f32 v114, v114\n\t"
"v_rcp_f32 v115, v115\n\t"
"v_pk_mul_f32 v[144:145], v[144:145], v[112:113]\n\t"
"v_mfma_f32_16x16x16_bf16 v[64:67], v[10:11], v[162:163], v[64:67]\n\t"
"v_pk_mul_f32 v[146:147], v[146:147], v[114:115]\n\t"
"v_mul_f32 v116, s70, v148\n\t"
"v_mul_f32 v117, s70, v149\n\t"
"v_mul_f32 v118, s70, v150\n\t"
"v_mul_f32 v119, s70, v151\n\t"
"v_mfma_f32_16x16x16_bf16 v[96:99], v[10:11], v[192:193], v[96:99]\n\t"
"v_exp_f32 v116, v116\n\t"
"v_exp_f32 v117, v117\n\t"
"s_xor_b32 s83, s74, 9437184\n\t"
"buffer_load_dwordx4 v[244:247], v1, s[8:11], s83 offen offset:0 sc1\n\t"
"v_exp_f32 v118, v118\n\t"
"v_mfma_f32_16x16x16_bf16 v[208:211], v[10:11], v[132:133], v[208:211]\n\t"
"v_exp_f32 v119, v119\n\t"
"v_add_f32 v116, 1.0, v116\n\t"
"s_add_u32 s83, s83, s53\n\t"
"buffer_load_dwordx4 v[248:251], v1, s[8:11], s83 offen offset:0 sc1\n\t"
"v_add_f32 v117, 1.0, v117\n\t"
"v_mfma_f32_16x16x16_bf16 v[224:227], v[10:11], v[134:135], v[224:227]\n\t"
"v_add_f32 v118, 1.0, v118\n\t"
"v_add_f32 v119, 1.0, v119\n\t"
"s_add_u32 s83, s83, s53\n\t"
"buffer_load_dwordx4 v[252:255], v1, s[8:11], s83 offen offset:0 sc1\n\t"
"v_rcp_f32 v116, v116\n\t"
"v_mfma_f32_16x16x16_bf16 v[240:243], v[10:11], v[164:165], v[240:243]\n\t"
"v_rcp_f32 v117, v117\n\t"
"v_rcp_f32 v118, v118\n\t"
"v_rcp_f32 v119, v119\n\t"
"v_pk_mul_f32 v[148:149], v[148:149], v[116:117]\n\t"
"v_pk_mul_f32 v[150:151], v[150:151], v[118:119]\n\t"
"v_mfma_f32_16x16x16_bf16 v[64:67], v[10:11], v[166:167], v[64:67]\n\t"
"v_mul_f32 v120, s70, v152\n\t"
"v_mul_f32 v121, s70, v153\n\t"
"v_mul_f32 v122, s70, v154\n\t"
"v_mul_f32 v123, s70, v155\n\t"
"v_exp_f32 v120, v120\n\t"
"v_mfma_f32_16x16x16_bf16 v[96:99], v[10:11], v[196:197], v[96:99]\n\t"
"v_exp_f32 v121, v121\n\t"
"v_exp_f32 v122, v122\n\t"
"v_exp_f32 v123, v123\n\t"
"s_xor_b32 s83, s74, 17825792\n\t"
"buffer_load_dwordx3 v[100:102], v20, s[8:11], s83 offen offset:0 sc1\n\t"
"v_mfma_f32_16x16x16_bf16 v[208:211], v[10:11], v[136:137], v[208:211]\n\t"
"v_add_f32 v120, 1.0, v120\n\t"
"v_add_f32 v121, 1.0, v121\n\t"
"v_add_f32 v122, 1.0, v122\n\t"
"s_add_u32 s83, s83, s53\n\t"
"buffer_load_dwordx3 v[104:106], v20, s[8:11], s83 offen offset:0 sc1\n\t"
"v_mfma_f32_16x16x16_bf16 v[224:227], v[10:11], v[138:139], v[224:227]\n\t"
"v_add_f32 v123, 1.0, v123\n\t"
"v_rcp_f32 v120, v120\n\t"
"v_rcp_f32 v121, v121\n\t"
"s_add_u32 s83, s83, s53\n\t"
"buffer_load_dwordx3 v[108:110], v20, s[8:11], s83 offen offset:0 sc1\n\t"
"v_mfma_f32_16x16x16_bf16 v[240:243], v[10:11], v[168:169], v[240:243]\n\t"
"v_rcp_f32 v122, v122\n\t"
"v_rcp_f32 v123, v123\n\t"
"v_pk_mul_f32 v[152:153], v[152:153], v[120:121]\n\t"
"v_pk_mul_f32 v[154:155], v[154:155], v[122:123]\n\t"
"v_mul_f32 v124, s70, v156\n\t"
"v_mfma_f32_16x16x16_bf16 v[64:67], v[10:11], v[170:171], v[64:67]\n\t"
"v_mul_f32 v125, s70, v157\n\t"
"v_mul_f32 v126, s70, v158\n\t"
"v_mul_f32 v127, s70, v159\n\t"
"v_exp_f32 v124, v124\n\t"
"v_exp_f32 v125, v125\n\t"
"v_mfma_f32_16x16x16_bf16 v[96:99], v[10:11], v[200:201], v[96:99]\n\t"
"v_exp_f32 v126, v126\n\t"
"v_exp_f32 v127, v127\n\t"
"v_add_f32 v124, 1.0, v124\n\t"
"v_add_f32 v125, 1.0, v125\n\t"
"v_add_f32 v126, 1.0, v126\n\t"
"v_mfma_f32_16x16x16_bf16 v[208:211], v[10:11], v[140:141], v[208:211]\n\t"
"v_add_f32 v127, 1.0, v127\n\t"
"v_rcp_f32 v124, v124\n\t"
"v_rcp_f32 v125, v125\n\t"
"v_rcp_f32 v126, v126\n\t"
"v_rcp_f32 v127, v127\n\t"
"v_mfma_f32_16x16x16_bf16 v[224:227], v[10:11], v[142:143], v[224:227]\n\t"
"v_pk_mul_f32 v[156:157], v[156:157], v[124:125]\n\t"
"v_pk_mul_f32 v[158:159], v[158:159], v[126:127]\n\t"
"v_mul_f32 v48, s70, v228\n\t"
"v_mul_f32 v49, s70, v229\n\t"
"v_mul_f32 v50, s70, v230\n\t"
"v_mfma_f32_16x16x16_bf16 v[240:243], v[10:11], v[172:173], v[240:243]\n\t"
"v_mul_f32 v51, s70, v231\n\t"
"v_exp_f32 v48, v48\n\t"
"v_exp_f32 v49, v49\n\t"
"v_exp_f32 v50, v50\n\t"
"v_exp_f32 v51, v51\n\t"
"v_mfma_f32_16x16x16_bf16 v[64:67], v[10:11], v[174:175], v[64:67]\n\t"
"v_add_f32 v48, 1.0, v48\n\t"
"v_add_f32 v49, 1.0, v49\n\t"
"v_add_f32 v50, 1.0, v50\n\t"
"v_add_f32 v51, 1.0, v51\n\t"
"v_rcp_f32 v48, v48\n\t"
"v_mfma_f32_16x16x16_bf16 v[96:99], v[10:11], v[204:205], v[96:99]\n\t"
"v_rcp_f32 v49, v49\n\t"
"v_rcp_f32 v50, v50\n\t"
"v_rcp_f32 v51, v51\n\t"
"v_pk_mul_f32 v[228:229], v[228:229], v[48:49]\n\t"
"v_pk_mul_f32 v[230:231], v[230:231], v[50:51]\n\t"
"s_nop 3\n\t"
"v_pk_fma_f32 v[176:177], v[208:209], v[144:145], v[176:177]\n\t"
"v_pk_fma_f32 v[178:179], v[210:211], v[146:147], v[178:179]\n\t"
"v_pk_fma_f32 v[180:181], v[224:225], v[148:149], v[180:181]\n\t"
"v_pk_fma_f32 v[182:183], v[226:227], v[150:151], v[182:183]\n\t"
"v_pk_fma_f32 v[184:185], v[240:241], v[152:153], v[184:185]\n\t"
"v_pk_fma_f32 v[186:187], v[242:243], v[154:155], v[186:187]\n\t"
"v_pk_fma_f32 v[188:189], v[64:65], v[156:157], v[188:189]\n\t"
"v_pk_fma_f32 v[190:191], v[66:67], v[158:159], v[190:191]\n\t"
"v_pk_fma_f32 v[232:233], v[96:97], v[228:229], v[232:233]\n\t"
"v_pk_fma_f32 v[234:235], v[98:99], v[230:231], v[234:235]\n\t"
".LB0%=:\n"
"s_waitcnt vmcnt(6)\n\t"
"v_mfma_f32_16x16x16_bf16 v[112:115], v[10:11], v[212:213], 0\n\t"
"v_add3_u32 v12, v212, v213, s39\n\t"
"v_mfma_f32_16x16x16_bf16 v[116:119], v[10:11], v[214:215], 0\n\t"
"v_add3_u32 v12, v214, v215, v12\n\t"
"v_mfma_f32_16x16x16_bf16 v[112:115], v[10:11], v[216:217], v[112:115]\n\t"
"v_add3_u32 v15, v216, v217, s39\n\t"
"v_mfma_f32_16x16x16_bf16 v[116:119], v[10:11], v[218:219], v[116:119]\n\t"
"v_add3_u32 v15, v218, v219, v15\n\t"
"v_mfma_f32_16x16x16_bf16 v[112:115], v[10:11], v[220:221], v[112:115]\n\t"
"v_add3_u32 v9, v220, v221, s39\n\t"
"v_mfma_f32_16x16x16_bf16 v[116:119], v[10:11], v[222:223], v[116:119]\n\t"
"v_add3_u32 v9, v222, v223, v9\n\t"
"s_nop 4\n\t"
"v_pk_fma_f32 v[112:113], v[112:113], v[144:145], v[176:177]\n\t"
"v_pk_fma_f32 v[114:115], v[114:115], v[146:147], v[178:179]\n\t"
"v_cvt_pk_bf16_f32 v112, v112, v113\n\t"
"v_cvt_pk_bf16_f32 v113, v114, v115\n\t"
"ds_write_b64 v4, v[112:113] offset:0\n\t"
"v_pk_fma_f32 v[116:117], v[116:117], v[148:149], v[180:181]\n\t"
"v_pk_fma_f32 v[118:119], v[118:119], v[150:151], v[182:183]\n\t"
"v_cvt_pk_bf16_f32 v116, v116, v117\n\t"
"v_cvt_pk_bf16_f32 v117, v118, v119\n\t"
"ds_write_b64 v6, v[116:117] offset:0\n\t"
"s_waitcnt vmcnt(3)\n\t"
"v_mfma_f32_16x16x16_bf16 v[120:123], v[10:11], v[244:245], 0\n\t"
"v_add3_u32 v12, v244, v245, v12\n\t"
"v_mfma_f32_16x16x16_bf16 v[124:127], v[10:11], v[246:247], 0\n\t"
"v_add3_u32 v12, v246, v247, v12\n\t"
"v_mfma_f32_16x16x16_bf16 v[120:123], v[10:11], v[248:249], v[120:123]\n\t"
"v_add3_u32 v15, v248, v249, v15\n\t"
"v_mfma_f32_16x16x16_bf16 v[124:127], v[10:11], v[250:251], v[124:127]\n\t"
"v_add3_u32 v15, v250, v251, v15\n\t"
"v_mfma_f32_16x16x16_bf16 v[120:123], v[10:11], v[252:253], v[120:123]\n\t"
"v_add3_u32 v9, v252, v253, v9\n\t"
"v_mfma_f32_16x16x16_bf16 v[124:127], v[10:11], v[254:255], v[124:127]\n\t"
"v_add3_u32 v9, v254, v255, v9\n\t"
"s_nop 4\n\t"
"v_pk_fma_f32 v[120:121], v[120:121], v[152:153], v[184:185]\n\t"
"v_pk_fma_f32 v[122:123], v[122:123], v[154:155], v[186:187]\n\t"
"v_cvt_pk_bf16_f32 v120, v120, v121\n\t"
"v_cvt_pk_bf16_f32 v121, v122, v123\n\t"
"ds_write_b64 v4, v[120:121] offset:64\n\t"
"v_pk_fma_f32 v[124:125], v[124:125], v[156:157], v[188:189]\n\t"
"v_pk_fma_f32 v[126:127], v[126:127], v[158:159], v[190:191]\n\t"
"v_cvt_pk_bf16_f32 v124, v124, v125\n\t"
"v_cvt_pk_bf16_f32 v125, v126, v127\n\t"
"ds_write_b64 v6, v[124:125] offset:64\n\t"
"ds_read_b128 v[112:115], v95 offset:0\n\t"
"ds_read_b128 v[116:119], v95 offset:1088\n\t"
"s_waitcnt lgkmcnt(1)\n\t"
"buffer_store_dwordx4 v[112:115], v92, s[4:7], s47 offen offset:0\n\t"
"s_waitcnt lgkmcnt(0)\n\t"
"buffer_store_dwordx4 v[116:119], v93, s[4:7], s47 offen offset:1024\n\t"
"s_waitcnt vmcnt(2)\n\t"
"v_mfma_f32_16x16x16_bf16 v[48:51], v[10:11], v[100:101], 0\n\t"
"v_add3_u32 v12, v100, v101, v12\n\t"
"v_add3_u32 v15, v104, v105, v15\n\t"
"v_mfma_f32_16x16x16_bf16 v[48:51], v[10:11], v[104:105], v[48:51]\n\t"
"v_add3_u32 v9, v108, v109, v9\n\t"
"v_xor_b32 v12, v12, v102\n\t"
"v_mfma_f32_16x16x16_bf16 v[48:51], v[10:11], v[108:109], v[48:51]\n\t"
"v_xor_b32 v15, v15, v106\n\t"
"v_xor_b32 v9, v9, v110\n\t"
"v_or3_b32 v7, v12, v15, v9\n\t"
"v_cmp_ne_u32 vcc, 0, v7\n\t"
"s_cbranch_vccnz .LslpB%=\n\t"
".LbkpB%=:\n"
"s_nop 2\n\t"
"v_pk_fma_f32 v[48:49], v[48:49], v[228:229], v[232:233]\n\t"
"v_pk_fma_f32 v[50:51], v[50:51], v[230:231], v[234:235]\n\t"
"v_cvt_pk_bf16_f32 v48, v48, v49\n\t"
"v_cvt_pk_bf16_f32 v49, v50, v51\n\t"
"ds_write_b64 v5, v[48:49] offset:2176\n\t"
"ds_read_b128 v[60:63], v95 offset:2176\n\t"
"s_waitcnt lgkmcnt(0)\n\t"
"buffer_store_dwordx4 v[60:63], v94, s[4:7], s47 offen offset:0\n\t"
"s_endpgm\n\t"
".LslpB%=:\n"
"v_cmp_ne_u32 vcc, 0, v12\n\t"
"s_cbranch_vccz .Lsk1%=\n\t"
"s_xor_b32 s66, s74, 1048576\n\t"
"buffer_load_dwordx4 v[212:215], v1, s[8:11], s66 offen offset:0 sc1\n\t"
"s_lshl3_add_u32 s66, s53, s66\n\t"
"buffer_load_dwordx4 v[244:247], v1, s[8:11], s66 offen offset:0 sc1\n\t"
"s_lshl3_add_u32 s66, s53, s66\n\t"
"buffer_load_dwordx3 v[100:102], v20, s[8:11], s66 offen offset:0 sc1\n\t"
".Lsk1%=:\n"
"v_cmp_ne_u32 vcc, 0, v15\n\t"
"s_cbranch_vccz .Lsk2%=\n\t"
"s_xor_b32 s66, s74, 2097152\n\t"
"buffer_load_dwordx4 v[216:219], v1, s[8:11], s66 offen offset:0 sc1\n\t"
"s_lshl3_add_u32 s66, s53, s66\n\t"
"buffer_load_dwordx4 v[248:251], v1, s[8:11], s66 offen offset:0 sc1\n\t"
"s_lshl3_add_u32 s66, s53, s66\n\t"
"buffer_load_dwordx3 v[104:106], v20, s[8:11], s66 offen offset:0 sc1\n\t"
".Lsk2%=:\n"
"v_cmp_ne_u32 vcc, 0, v9\n\t"
"s_cbranch_vccz .Lsk3%=\n\t"
"s_xor_b32 s66, s74, 3145728\n\t"
"buffer_load_dwordx4 v[220:223], v1, s[8:11], s66 offen offset:0 sc1\n\t"
"s_lshl3_add_u32 s66, s53, s66\n\t"
"buffer_load_dwordx4 v[252:255], v1, s[8:11], s66 offen offset:0 sc1\n\t"
"s_lshl3_add_u32 s66, s53, s66\n\t"
"buffer_load_dwordx3 v[108:110], v20, s[8:11], s66 offen offset:0 sc1\n\t"
".Lsk3%=:\n"
"s_waitcnt vmcnt(0)\n\t"
"s_sub_u32 s55, s55, 1\n\t"
"s_cbranch_scc0 .LB0%=\n\t"
"s_mov_b32 s55, 0\n\t"
"s_branch .LbkpB%=\n\t"
".LslpA%=:\n"
"s_add_u32 s65, s65, 1\n\t"
"v_cmp_ne_u32 vcc, 0, v24\n\t"
"s_cbranch_vccz .Lsk4%=\n\t"
"s_xor_b32 s66, s74, 4194304\n\t"
"buffer_load_dwordx4 v[128:131], v1, s[8:11], s66 offen offset:0 sc1\n\t"
"s_lshl3_add_u32 s66, s53, s66\n\t"
"buffer_load_dwordx4 v[160:163], v1, s[8:11], s66 offen offset:0 sc1\n\t"
"s_lshl3_add_u32 s66, s53, s66\n\t"
"buffer_load_dwordx3 v[192:194], v20, s[8:11], s66 offen offset:0 sc1\n\t"
".Lsk4%=:\n"
"v_cmp_ne_u32 vcc, 0, v25\n\t"
"s_cbranch_vccz .Lsk5%=\n\t"
"s_xor_b32 s66, s74, 5242880\n\t"
"buffer_load_dwordx4 v[132:135], v1, s[8:11], s66 offen offset:0 sc1\n\t"
"s_lshl3_add_u32 s66, s53, s66\n\t"
"buffer_load_dwordx4 v[164:167], v1, s[8:11], s66 offen offset:0 sc1\n\t"
"s_lshl3_add_u32 s66, s53, s66\n\t"
"buffer_load_dwordx3 v[196:198], v20, s[8:11], s66 offen offset:0 sc1\n\t"
".Lsk5%=:\n"
"v_cmp_ne_u32 vcc, 0, v26\n\t"
"s_cbranch_vccz .Lsk6%=\n\t"
"s_xor_b32 s66, s74, 6291456\n\t"
"buffer_load_dwordx4 v[136:139], v1, s[8:11], s66 offen offset:0 sc1\n\t"
"s_lshl3_add_u32 s66, s53, s66\n\t"
"buffer_load_dwordx4 v[168:171], v1, s[8:11], s66 offen offset:0 sc1\n\t"
"s_lshl3_add_u32 s66, s53, s66\n\t"
"buffer_load_dwordx3 v[200:202], v20, s[8:11], s66 offen offset:0 sc1\n\t"
".Lsk6%=:\n"
"v_cmp_ne_u32 vcc, 0, v27\n\t"
"s_cbranch_vccz .Lsk7%=\n\t"
"s_xor_b32 s66, s74, 7340032\n\t"
"buffer_load_dwordx4 v[140:143], v1, s[8:11], s66 offen offset:0 sc1\n\t"
"s_lshl3_add_u32 s66, s53, s66\n\t"
"buffer_load_dwordx4 v[172:175], v1, s[8:11], s66 offen offset:0 sc1\n\t"
"s_lshl3_add_u32 s66, s53, s66\n\t"
"buffer_load_dwordx3 v[204:206], v20, s[8:11], s66 offen offset:0 sc1\n\t"
".Lsk7%=:\n"
"s_cmp_eq_u32 s65, 1\n\t"
"s_cbranch_scc1 .LbkpA%=\n\t"
"s_waitcnt vmcnt(0)\n\t"
".LreA%=:\n"
"s_waitcnt vmcnt(3)\n\t"
"v_add3_u32 v24, v128, v129, s39\n\t"
"v_add3_u32 v25, v132, v133, s39\n\t"
"v_add3_u32 v26, v136, v137, s39\n\t"
"v_add3_u32 v27, v140, v141, s39\n\t"
"v_add3_u32 v24, v130, v131, v24\n\t"
"v_add3_u32 v25, v134, v135, v25\n\t"
"v_add3_u32 v26, v138, v139, v26\n\t"
"v_add3_u32 v27, v142, v143, v27\n\t"
"v_add3_u32 v24, v160, v161, v24\n\t"
"v_add3_u32 v25, v164, v165, v25\n\t"
"v_add3_u32 v26, v168, v169, v26\n\t"
"v_add3_u32 v27, v172, v173, v27\n\t"
"v_add3_u32 v24, v162, v163, v24\n\t"
"v_add3_u32 v25, v166, v167, v25\n\t"
"v_add3_u32 v26, v170, v171, v26\n\t"
"v_add3_u32 v27, v174, v175, v27\n\t"
"v_add3_u32 v24, v192, v193, v24\n\t"
"v_add3_u32 v25, v196, v197, v25\n\t"
"v_add3_u32 v26, v200, v201, v26\n\t"
"v_add3_u32 v27, v204, v205, v27\n\t"
"v_xor_b32 v24, v24, v194\n\t"
"v_xor_b32 v25, v25, v198\n\t"
"v_xor_b32 v26, v26, v202\n\t"
"v_xor_b32 v27, v27, v206\n\t"
"v_or3_b32 v7, v24, v25, v26\n\t"
"v_or_b32 v7, v7, v27\n\t"
"v_cmp_ne_u32 vcc, 0, v7\n\t"
"s_cbranch_vccz .LrbA%=\n\t"
"s_sleep 2\n\t"
"s_sub_u32 s55, s55, 1\n\t"
"s_cbranch_scc0 .LslpA%=\n\t"
"s_mov_b32 s55, 0\n\t"
"s_branch .LrbA%=\n\t"
".Ly0%=:\n"
"v_bfe_u32 v22, %[vT], 3, 3\n\t"
"v_min_u32 v22, 5, v22\n\t"
"v_mul_u32_u24 v8, 40, v22\n\t"
"v_and_b32 v22, 7, %[vT]\n\t"
"v_min_u32 v22, 4, v22\n\t"
"v_add_u32 v8, v8, v22\n\t"
"s_lshr_b32 s76, s72, 21\n\t"
"s_sub_u32 s77, s76, 4\n\t"
"v_bfe_u32 v22, %[vT], 2, 1\n\t"
"v_mad_u32_u24 v8, v22, s77, v8\n\t"
"s_and_b32 s48, s72, 7\n\t"
"s_lshl_b32 s77, s48, 12\n\t"
"s_add_u32 s77, s77, 28340224\n\t"
"v_lshl_add_u32 v8, v8, 10, s77\n\t"
"s_mul_i32 s48, s48, 3072\n\t"
"s_add_u32 s48, s48, 28700672\n\t"
"s_mul_i32 s76, s76, 768\n\t"
"s_add_u32 s76, s76, s48\n\t"
"buffer_load_dword v23, v8, s[8:11], 0 offen offset:0 sc1\n\t"
"v_lshrrev_b32 v22, 1, v1\n\t"
"v_mov_b32 v44, 1073741824\n\t"
"s_nop 1\n\t"
"v_mov_b32_dpp v22, v44 quad_perm:[0,1,2,3] row_mask:0x8 bank_mask:0xf\n\t"
"s_branch .Lyb0%=\n\t"
".Ly1%=:\n"
"s_waitcnt vmcnt(15)\n\t"
"v_sub_u32 v23, s39, v23\n\t"
"v_cmp_lt_u32 vcc, 4, v23\n\t"
"s_cbranch_vccnz .LslY%=\n\t"
".LbkY%=:\n"
"buffer_load_dwordx2 v[112:113], v22, s[8:11], s48 offen offset:0 sc1\n\t"
"buffer_load_dwordx2 v[114:115], v22, s[8:11], s48 offen offset:384 sc1\n\t"
"buffer_load_dwordx2 v[116:117], v22, s[8:11], s48 offen offset:768 sc1\n\t"
"buffer_load_dwordx2 v[118:119], v22, s[8:11], s48 offen offset:1152 sc1\n\t"
"buffer_load_dwordx2 v[120:121], v22, s[8:11], s48 offen offset:1536 sc1\n\t"
"buffer_load_dwordx2 v[122:123], v22, s[8:11], s48 offen offset:1920 sc1\n\t"
"buffer_load_dwordx2 v[124:125], v22, s[8:11], s48 offen offset:2304 sc1\n\t"
"buffer_load_dwordx2 v[126:127], v22, s[8:11], s48 offen offset:2688 sc1\n\t"
"buffer_load_dwordx2 v[48:49], v22, s[8:11], s76 offen offset:0 sc1\n\t"
"buffer_load_dwordx2 v[50:51], v22, s[8:11], s76 offen offset:384 sc1\n\t"
"s_branch .Lyb1%=\n\t"
".Ly2%=:\n"
"s_ashr_i32 s46, s28, 31\n\t"
"s_ashr_i32 s75, s29, 31\n\t"
"s_orn2_b32 s46, 2147483648, s46\n\t"
"s_orn2_b32 s75, 2147483648, s75\n\t"
"s_xor_b32 s46, s46, s28\n\t"
"s_xor_b32 s75, s75, s29\n\t"
"s_and_b32 s46, s46, 4294901760\n\t"
"s_and_b32 s75, s75, 4294901760\n\t"
"v_mov_b32 v44, 0xbfb8aa3b\n\t"
"v_mov_b32 v45, 0xbfb8aa3b\n\t"
"v_mul_f32 v44, s46, v44\n\t"
"v_mul_f32 v45, s75, v45\n\t"
"v_exp_f32 v44, v44\n\t"
"v_exp_f32 v45, v45\n\t"
"s_andn2_b32 s46, 2047, s28\n\t"
"s_andn2_b32 s75, 2047, s29\n\t"
"v_add_f32 v44, 1.0, v44\n\t"
"v_add_f32 v45, 1.0, v45\n\t"
"v_rcp_f32 v44, v44\n\t"
"v_rcp_f32 v45, v45\n\t"
"v_cmp_eq_u32 s[68:69], s46, v21\n\t"
"v_cmp_eq_u32 vcc, s75, v21\n\t"
"v_cvt_pk_bf16_f32 v44, v44, v44\n\t"
"v_cvt_pk_bf16_f32 v45, v45, v45\n\t"
"v_cndmask_b32 v44, 0, v44, s[68:69]\n\t"
"v_cndmask_b32 v45, 0, v45, vcc\n\t"
"v_mov_b32 v46, v44\n\t"
"v_mov_b32 v47, v45\n\t"
"s_waitcnt vmcnt(59)\n\t"
"s_nop 0\n\t"
"v_mfma_f32_16x16x32_bf16 v[144:147], v[112:115], v[44:47], v[144:147]\n\t"
"v_mfma_f32_16x16x32_bf16 v[148:151], v[116:119], v[44:47], v[148:151]\n\t"
"v_mfma_f32_16x16x32_bf16 v[152:155], v[120:123], v[44:47], v[152:155]\n\t"
"v_mfma_f32_16x16x32_bf16 v[156:159], v[124:127], v[44:47], v[156:159]\n\t"
"v_mfma_f32_16x16x32_bf16 v[228:231], v[48:51], v[44:47], v[228:231]\n\t"
"s_nop 7\n\t"
"s_branch .Lyb2%=\n\t"
".Lnoln%=:\n"
"s_add_u32 m0, s42, 12320\n\t"
"s_nop 0\n\t"
"buffer_load_dwordx4 v2, s[12:15], s41 offen offset:0 lds\n\t"
"buffer_load_dwordx4 v8, s[12:15], s41 offen offset:1024 lds\n\t"
"buffer_load_dwordx4 v9, s[12:15], s41 offen offset:2048 lds\n\t"
"s_add_u32 s41, s41, s56\n\t"
"s_cmp_eq_u32 s41, s79\n\t"
"s_cselect_b32 s41, 0, s41\n\t"
"s_add_u32 m0, s51, 32768\n\t"
"s_add_u32 s47, s40, 0\n\t"
"buffer_load_dwordx4 v13, s[4:7], s47 offen offset:0 lds\n\t"
"buffer_load_dwordx4 v14, s[4:7], s47 offen offset:1024 lds\n\t"
"s_addk_i32 m0, 2048\n\t"
"s_add_u32 s47, s40, s98\n\t"
"buffer_load_dwordx4 v13, s[4:7], s47 offen offset:0 lds\n\t"
"buffer_load_dwordx4 v14, s[4:7], s47 offen offset:1024 lds\n\t"
"s_addk_i32 m0, 2048\n\t"
"s_add_u32 s47, s40, s99\n\t"
"buffer_load_dwordx4 v13, s[4:7], s47 offen offset:0 lds\n\t"
"buffer_load_dwordx4 v14, s[4:7], s47 offen offset:1024 lds\n\t"
"s_addk_i32 m0, 2048\n\t"
"s_add_u32 s47, s47, s98\n\t"
"buffer_load_dwordx4 v13, s[4:7], s47 offen offset:0 lds\n\t"
"buffer_load_dwordx4 v14, s[4:7], s47 offen offset:1024 lds\n\t"
"s_addk_i32 s40, 128\n\t"
"s_cmp_eq_u32 s40, s48\n\t"
"s_cselect_b32 s40, s76, s40\n\t"
"s_mul_i32 s46, s38, 3\n\t"
"s_lshr_b32 s75, s46, 1\n\t"
"s_lshl_b32 s75, s75, 9\n\t"
"s_and_b32 s80, s46, 1\n\t"
"s_lshl_b32 s80, s80, 3\n\t"
"s_add_u32 s75, s75, s80\n\t"
"v_add_u32 v17, s75, v5\n\t"
"s_add_u32 s46, s46, 1\n\t"
"s_lshr_b32 s75, s46, 1\n\t"
"s_lshl_b32 s75, s75, 9\n\t"
"s_and_b32 s80, s46, 1\n\t"
"s_lshl_b32 s80, s80, 3\n\t"
"s_add_u32 s75, s75, s80\n\t"
"v_add_u32 v18, s75, v5\n\t"
"s_add_u32 s46, s46, 1\n\t"
"s_lshr_b32 s75, s46, 1\n\t"
"s_lshl_b32 s75, s75, 9\n\t"
"s_and_b32 s80, s46, 1\n\t"
"s_lshl_b32 s80, s80, 3\n\t"
"s_add_u32 s75, s75, s80\n\t"
"v_add_u32 v19, s75, v5\n\t"
"s_add_u32 s46, s46, 1\n\t"
"s_lshl_b32 s80, s45, 9\n\t"
"s_add_u32 s80, s80, 2621440\n\t"
"v_lshrrev_b32 v38, 1, v1\n\t"
"s_and_b32 s81, %[sB], 3\n\t"
"s_lshl_b32 s81, s81, 7\n\t"
"v_xor_b32 v38, s81, v38\n\t"
"v_add_u32 v38, s80, v38\n\t"
"s_lshl_b32 s80, s38, 11\n\t"
"s_add_u32 s80, s80, 2637824\n\t"
"v_add_u32 v39, s80, v1\n\t"
"s_min_u32 s81, s38, 2\n\t"
"s_lshl_b32 s81, s81, 9\n\t"
"v_lshrrev_b32 v42, 1, v1\n\t"
"v_add_u32 v42, s81, v42\n\t"
"v_lshrrev_b32 v41, 1, v42\n\t"
"v_add_u32 v41, 74944, v41\n\t"
"s_mul_i32 s80, s78, 7680\n\t"
"s_add_u32 s80, s80, 0\n\t"
"v_add_u32 v42, s80, v42\n\t"
"v_add_u32 v43, 4608, v42\n\t"
"v_mov_b32 v63, 1073741824\n\t"
"s_branch .Ljoin%=\n\t"
".Lredo%=:\n"
"s_waitcnt vmcnt(0) lgkmcnt(0)\n\t"
"s_barrier\n\t"
"s_mov_b32 s49, 1\n\t"
"s_nop 7\n\t"
"s_nop 7\n\t"
"v_add_u32 v2, 896, v8\n\t"
"v_subrev_u32 v9, 896, v8\n\t"
"v_bfe_u32 v50, %[vT], 3, 3\n\t"
"v_mul_u32_u24 v13, 1280, v50\n\t"
"v_bfe_u32 v50, %[vT], 4, 2\n\t"
"v_and_b32 v51, 7, %[vT]\n\t"
"v_xor_b32 v51, v50, v51\n\t"
"v_lshl_add_u32 v13, v51, 4, v13\n\t"
"v_xor_b32 v14, 64, v13\n\t"
"v_add_u32 v14, 9216, v14\n\t"
"v_bfe_u32 v51, %[vT], 1, 3\n\t"
"v_xor_b32 v50, v50, v51\n\t"
"v_and_b32 v51, 15, %[vT]\n\t"
"v_lshlrev_b32 v51, 7, v51\n\t"
"v_lshl_add_u32 v15, v50, 4, v51\n\t"
"v_and_b32 v51, 192, %[vT]\n\t"
"v_lshl_add_u32 v15, v51, 7, v15\n\t"
"v_add_u32 v15, 77824, v15\n\t"
"v_xor_b32 v16, 64, v15\n\t"
"s_branch .Lstart%=\n\t"
".Lexact%=:\n"
"s_waitcnt vmcnt(0) lgkmcnt(0)\n\t"
"s_barrier\n\t"
"s_mov_b32 s49, 2\n\t"
"s_mov_b32 s47, 0\n\t"
"s_nop 7\n\t"
".Lexl%=:\n"
"s_add_u32 m0, s43, 0\n\t"
"s_nop 0\n\t"
"buffer_load_dwordx4 v3, s[16:19], s47 offen offset:0 lds\n\t"
"buffer_load_dwordx4 v10, s[16:19], s47 offen offset:1024 lds\n\t"
"buffer_load_dwordx4 v11, s[16:19], s47 offen offset:2048 lds\n\t"
"s_waitcnt vmcnt(0)\n\t"
"s_barrier\n\t"
"ds_read_b128 a[192:195], v6 offset:0\n\t"
"ds_read_b128 a[196:199], v12 offset:0\n\t"
"ds_read_b128 a[200:203], v6 offset:1024\n\t"
"ds_read_b128 a[204:207], v12 offset:1024\n\t"
"ds_read_b128 a[208:211], v6 offset:2048\n\t"
"ds_read_b128 a[212:215], v12 offset:2048\n\t"
"s_waitcnt lgkmcnt(0)\n\t"
"v_mfma_f32_16x16x32_bf16 v[64:67], a[192:195], a[96:99], 0\n\t"
"v_mfma_f32_16x16x32_bf16 v[68:71], a[192:195], a[100:103], 0\n\t"
"v_mfma_f32_16x16x32_bf16 v[72:75], a[192:195], a[104:107], 0\n\t"
"v_mfma_f32_16x16x32_bf16 v[76:79], a[192:195], a[108:111], 0\n\t"
"v_mfma_f32_16x16x32_bf16 v[64:67], a[196:199], a[112:115], v[64:67]\n\t"
"v_mfma_f32_16x16x32_bf16 v[68:71], a[196:199], a[116:119], v[68:71]\n\t"
"v_mfma_f32_16x16x32_bf16 v[72:75], a[196:199], a[120:123], v[72:75]\n\t"
"v_mfma_f32_16x16x32_bf16 v[76:79], a[196:199], a[124:127], v[76:79]\n\t"
"v_mfma_f32_16x16x32_bf16 v[64:67], a[200:203], a[128:131], v[64:67]\n\t"
"v_mfma_f32_16x16x32_bf16 v[68:71], a[200:203], a[132:135], v[68:71]\n\t"
"v_mfma_f32_16x16x32_bf16 v[72:75], a[200:203], a[136:139], v[72:75]\n\t"
"v_mfma_f32_16x16x32_bf16 v[76:79], a[200:203], a[140:143], v[76:79]\n\t"
"v_mfma_f32_16x16x32_bf16 v[64:67], a[204:207], a[144:147], v[64:67]\n\t"
"v_mfma_f32_16x16x32_bf16 v[68:71], a[204:207], a[148:151], v[68:71]\n\t"
"v_mfma_f32_16x16x32_bf16 v[72:75], a[204:207], a[152:155], v[72:75]\n\t"
"v_mfma_f32_16x16x32_bf16 v[76:79], a[204:207], a[156:159], v[76:79]\n\t"
"v_mfma_f32_16x16x32_bf16 v[64:67], a[208:211], a[160:163], v[64:67]\n\t"
"v_mfma_f32_16x16x32_bf16 v[68:71], a[208:211], a[164:167], v[68:71]\n\t"
"v_mfma_f32_16x16x32_bf16 v[72:75], a[208:211], a[168:171], v[72:75]\n\t"
"v_mfma_f32_16x16x32_bf16 v[76:79], a[208:211], a[172:175], v[76:79]\n\t"
"v_mfma_f32_16x16x32_bf16 v[64:67], a[212:215], a[176:179], v[64:67]\n\t"
"v_mfma_f32_16x16x32_bf16 v[68:71], a[212:215], a[180:183], v[68:71]\n\t"
"v_mfma_f32_16x16x32_bf16 v[72:75], a[212:215], a[184:187], v[72:75]\n\t"
"v_mfma_f32_16x16x32_bf16 v[76:79], a[212:215], a[188:191], v[76:79]\n\t"
"ds_read_b128 a[192:195], v6 offset:6144\n\t"
"ds_read_b128 a[196:199], v12 offset:6144\n\t"
"ds_read_b128 a[200:203], v6 offset:7168\n\t"
"ds_read_b128 a[204:207], v12 offset:7168\n\t"
"ds_read_b128 a[208:211], v6 offset:8192\n\t"
"ds_read_b128 a[212:215], v12 offset:8192\n\t"
"s_waitcnt lgkmcnt(0)\n\t"
"v_mfma_f32_16x16x32_bf16 v[80:83], a[192:195], a[96:99], 0\n\t"
"v_mfma_f32_16x16x32_bf16 v[84:87], a[192:195], a[100:103], 0\n\t"
"v_mfma_f32_16x16x32_bf16 v[88:91], a[192:195], a[104:107], 0\n\t"
"v_mfma_f32_16x16x32_bf16 v[92:95], a[192:195], a[108:111], 0\n\t"
"v_mfma_f32_16x16x32_bf16 v[80:83], a[196:199], a[112:115], v[80:83]\n\t"
"v_mfma_f32_16x16x32_bf16 v[84:87], a[196:199], a[116:119], v[84:87]\n\t"
"v_mfma_f32_16x16x32_bf16 v[88:91], a[196:199], a[120:123], v[88:91]\n\t"
"v_mfma_f32_16x16x32_bf16 v[92:95], a[196:199], a[124:127], v[92:95]\n\t"
"v_mfma_f32_16x16x32_bf16 v[80:83], a[200:203], a[128:131], v[80:83]\n\t"
"v_mfma_f32_16x16x32_bf16 v[84:87], a[200:203], a[132:135], v[84:87]\n\t"
"v_mfma_f32_16x16x32_bf16 v[88:91], a[200:203], a[136:139], v[88:91]\n\t"
"v_mfma_f32_16x16x32_bf16 v[92:95], a[200:203], a[140:143], v[92:95]\n\t"
"v_mfma_f32_16x16x32_bf16 v[80:83], a[204:207], a[144:147], v[80:83]\n\t"
"v_mfma_f32_16x16x32_bf16 v[84:87], a[204:207], a[148:151], v[84:87]\n\t"
"v_mfma_f32_16x16x32_bf16 v[88:91], a[204:207], a[152:155], v[88:91]\n\t"
"v_mfma_f32_16x16x32_bf16 v[92:95], a[204:207], a[156:159], v[92:95]\n\t"
"v_mfma_f32_16x16x32_bf16 v[80:83], a[208:211], a[160:163], v[80:83]\n\t"
"v_mfma_f32_16x16x32_bf16 v[84:87], a[208:211], a[164:167], v[84:87]\n\t"
"v_mfma_f32_16x16x32_bf16 v[88:91], a[208:211], a[168:171], v[88:91]\n\t"
"v_mfma_f32_16x16x32_bf16 v[92:95], a[208:211], a[172:175], v[92:95]\n\t"
"v_mfma_f32_16x16x32_bf16 v[80:83], a[212:215], a[176:179], v[80:83]\n\t"
"v_mfma_f32_16x16x32_bf16 v[84:87], a[212:215], a[180:183], v[84:87]\n\t"
"v_mfma_f32_16x16x32_bf16 v[88:91], a[212:215], a[184:187], v[88:91]\n\t"
"v_mfma_f32_16x16x32_bf16 v[92:95], a[212:215], a[188:191], v[92:95]\n\t"
"s_nop 7\n\t"
"s_nop 3\n\t"
"v_max3_f32 v24, v24, v64, v65\n\t"
"v_max3_f32 v24, v24, v66, v67\n\t"
"v_max3_f32 v24, v24, v80, v81\n\t"
"v_max3_f32 v24, v24, v82, v83\n\t"
"v_max3_f32 v25, v25, v68, v69\n\t"
"v_max3_f32 v25, v25, v70, v71\n\t"
"v_max3_f32 v25, v25, v84, v85\n\t"
"v_max3_f32 v25, v25, v86, v87\n\t"
"v_max3_f32 v26, v26, v72, v73\n\t"
"v_max3_f32 v26, v26, v74, v75\n\t"
"v_max3_f32 v26, v26, v88, v89\n\t"
"v_max3_f32 v26, v26, v90, v91\n\t"
"v_max3_f32 v27, v27, v76, v77\n\t"
"v_max3_f32 v27, v27, v78, v79\n\t"
"v_max3_f32 v27, v27, v92, v93\n\t"
"v_max3_f32 v27, v27, v94, v95\n\t"
"s_barrier\n\t"
"s_add_u32 s47, s47, 12288\n\t"
"s_cmp_lt_u32 s47, 196608\n\t"
"s_cbranch_scc1 .Lexl%=\n\t"
"s_nop 3\n\t"
"v_add_u32 v2, 896, v8\n\t"
"v_subrev_u32 v9, 896, v8\n\t"
"v_bfe_u32 v50, %[vT], 3, 3\n\t"
"v_mul_u32_u24 v13, 1280, v50\n\t"
"v_bfe_u32 v50, %[vT], 4, 2\n\t"
"v_and_b32 v51, 7, %[vT]\n\t"
"v_xor_b32 v51, v50, v51\n\t"
"v_lshl_add_u32 v13, v51, 4, v13\n\t"
"v_xor_b32 v14, 64, v13\n\t"
"v_add_u32 v14, 9216, v14\n\t"
"v_bfe_u32 v51, %[vT], 1, 3\n\t"
"v_xor_b32 v50, v50, v51\n\t"
"v_and_b32 v51, 15, %[vT]\n\t"
"v_lshlrev_b32 v51, 7, v51\n\t"
"v_lshl_add_u32 v15, v50, 4, v51\n\t"
"v_and_b32 v51, 192, %[vT]\n\t"
"v_lshl_add_u32 v15, v51, 7, v15\n\t"
"v_add_u32 v15, 77824, v15\n\t"
"v_xor_b32 v16, 64, v15\n\t"
"s_branch .Lstart%=\n\t"
".LslK%=:\n"
"s_sleep 4\n\t"
"buffer_load_dwordx2 v[36:37], v38, s[8:11], 0 offen offset:0 sc1\n\t"
"buffer_load_dwordx4 v[48:51], v39, s[8:11], 0 offen offset:0 sc1\n\t"
"buffer_load_dwordx4 v[52:55], v39, s[8:11], 0 offen offset:1024 sc1\n\t"
"s_waitcnt vmcnt(0)\n\t"
"v_xor_b32 v49, s39, v49\n\t"
"v_xor_b32 v51, s39, v51\n\t"
"v_xor_b32 v53, s39, v53\n\t"
"v_xor_b32 v55, s39, v55\n\t"
"v_xor_b32 v37, s39, v37\n\t"
"v_or3_b32 v49, v49, v51, v53\n\t"
"v_or3_b32 v49, v49, v55, v37\n\t"
"v_cmp_ne_u32 vcc, 0, v49\n\t"
"s_cbranch_vccz .LbkK%=\n\t"
"s_sub_u32 s55, s55, 1\n\t"
"s_cbranch_scc0 .LslK%=\n\t"
"s_mov_b32 s55, 0\n\t"
"s_branch .LbkK%=\n\t"
".LslG%=:\n"
"s_sleep 4\n\t"
"buffer_load_dwordx2 a[96:97], v42, s[8:11], 0 offen offset:0 sc1\n\t"
"buffer_load_dwordx2 a[98:99], v42, s[8:11], 0 offen offset:1536 sc1\n\t"
"buffer_load_dwordx2 a[100:101], v42, s[8:11], 0 offen offset:3072 sc1\n\t"
"buffer_load_dwordx2 a[102:103], v43, s[8:11], 0 offen offset:0 sc1\n\t"
"buffer_load_dwordx2 a[104:105], v43, s[8:11], 0 offen offset:1536 sc1\n\t"
"s_waitcnt vmcnt(0)\n\t"
"v_accvgpr_read_b32 v48, a96\n\t"
"v_accvgpr_read_b32 v49, a97\n\t"
"v_accvgpr_read_b32 v50, a98\n\t"
"v_accvgpr_read_b32 v51, a99\n\t"
"v_accvgpr_read_b32 v52, a100\n\t"
"v_accvgpr_read_b32 v53, a101\n\t"
"v_accvgpr_read_b32 v54, a102\n\t"
"v_accvgpr_read_b32 v55, a103\n\t"
"v_accvgpr_read_b32 v56, a104\n\t"
"v_accvgpr_read_b32 v57, a105\n\t"
"v_xor_b32 v49, s39, v49\n\t"
"v_xor_b32 v51, s39, v51\n\t"
"v_xor_b32 v53, s39, v53\n\t"
"v_xor_b32 v55, s39, v55\n\t"
"v_xor_b32 v57, s39, v57\n\t"
"v_or3_b32 v49, v49, v51, v53\n\t"
"v_or3_b32 v49, v49, v55, v57\n\t"
"v_cmp_ne_u32 vcc, 0, v49\n\t"
"s_cbranch_vccz .LbkG%=\n\t"
"s_sub_u32 s55, s55, 1\n\t"
"s_cbranch_scc0 .LslG%=\n\t"
"s_mov_b32 s55, 0\n\t"
"s_branch .LbkG%=\n\t"
".LslS%=:\n"
"s_sleep 4\n\t"
"buffer_load_dwordx2 v[44:45], v34, s[8:11], 0 offen offset:0 sc1\n\t"
"s_waitcnt vmcnt(0)\n\t"
"v_cmp_ne_u32 vcc, s39, v45\n\t"
"s_cbranch_vccz .LbkS%=\n\t"
"s_sub_u32 s55, s55, 1\n\t"
"s_cbranch_scc0 .LslS%=\n\t"
"s_mov_b32 s55, 0\n\t"
"s_branch .LbkS%=\n\t"
".LslB%=:\n"
"s_sleep 4\n\t"
"buffer_load_dword v30, v10, s[8:11], 0 offen offset:0 sc1\n\t"
"s_waitcnt vmcnt(0)\n\t"
"v_sub_u32 v30, s39, v30\n\t"
"v_cmp_lt_u32 vcc, 6, v30\n\t"
"s_cbranch_vccz .LbkB%=\n\t"
"s_sub_u32 s55, s55, 1\n\t"
"s_cbranch_scc0 .LslB%=\n\t"
"s_mov_b32 s55, 0\n\t"
"s_branch .LbkB%=\n\t"
".LslC%=:\n"
"s_sleep 4\n\t"
"buffer_load_dword v30, v29, s[8:11], 0 offen offset:0 sc1\n\t"
"s_waitcnt vmcnt(0)\n\t"
"v_sub_u32 v30, s39, v30\n\t"
"v_cmp_lt_u32 vcc, 5, v30\n\t"
"s_cbranch_vccz .LbkC%=\n\t"
"s_sub_u32 s55, s55, 1\n\t"
"s_cbranch_scc0 .LslC%=\n\t"
"s_mov_b32 s55, 0\n\t"
"s_branch .LbkC%=\n\t"
".LslD%=:\n"
"s_sleep 4\n\t"
"buffer_load_dword v30, v29, s[8:11], 0 offen offset:0 sc1\n\t"
"s_waitcnt vmcnt(0)\n\t"
"v_sub_u32 v30, s39, v30\n\t"
"v_cmp_lt_u32 vcc, 4, v30\n\t"
"s_cbranch_vccz .LbkD%=\n\t"
"s_sub_u32 s55, s55, 1\n\t"
"s_cbranch_scc0 .LslD%=\n\t"
"s_mov_b32 s55, 0\n\t"
"s_branch .LbkD%=\n\t"
".LslY%=:\n"
"s_sleep 4\n\t"
"buffer_load_dword v23, v8, s[8:11], 0 offen offset:0 sc1\n\t"
"s_waitcnt vmcnt(0)\n\t"
"v_sub_u32 v23, s39, v23\n\t"
"v_cmp_lt_u32 vcc, 4, v23\n\t"
"s_cbranch_vccz .LbkY%=\n\t"
"s_sub_u32 s55, s55, 1\n\t"
"s_cbranch_scc0 .LslY%=\n\t"
"s_mov_b32 s55, 0\n\t"
"s_branch .LbkY%=\n\t"
".LslX%=:\n"
"s_sleep 4\n\t"
"buffer_load_dwordx2 v[90:91], v7, s[8:11], 0 offen offset:0 sc1\n\t"
"s_waitcnt vmcnt(0)\n\t"
"v_cmp_ne_u32 vcc, s39, v91\n\t"
"s_cbranch_vccz .LbkX%=\n\t"
"s_sub_u32 s55, s55, 1\n\t"
"s_cbranch_scc0 .LslX%=\n\t"
"s_mov_b32 s55, 0\n\t"
"s_branch .LbkX%=\n\t"
".Lend%=:\n"
".if (.Lend%=-.Lpn%=) < 61836\n.error \"png\"\n.endif\n\t"
    : [karg] "+s"(karg) : [sB] "s"(blockIdx.x), [vT] "v"(threadIdx.x)
    : "memory", "scc", "vcc", CLOB_V, CLOB_A, CLOB_S);
}

extern "C" void kernel_launch(void* const* d_in, const int* in_sizes, int n_in, void* d_out, int out_size, void* d_ws, size_t ws_size, hipStream_t stream) {
  const unsigned F = 0x00020000;
  (void)hipFuncSetAttribute((const void*)k_fused, hipFuncAttributeMaxDynamicSharedMemorySize, 163840);
  k_fused<<<256, 256, 163840, stream>>>(d_in[0], 2621440u, F, d_ws, 67108864u, F, d_in[3], 640u * 12288u + 2048u, F, d_in[1], 196608u + 2048u, F, d_in[2], 196608u, F,
                                        d_in[4], 7864320u, F, d_in[6], d_in[7], d_in[8], d_in[9], d_out);
}
```
